```python
import torch

def _softmax(x, axis=-1):
    return torch.softmax(x, dim=axis)
def _gelu(x):
    return torch.nn.functional.gelu(x, approximate="tanh")
def _silu(x):
    return torch.nn.functional.silu(x)
def _sigmoid(x):
    return torch.sigmoid(x)
def _layernorm(x, axis=-1, eps=1e-5):
    m = x.mean(dim=axis, keepdim=True)
    v = x.var(dim=axis, keepdim=True, unbiased=False)
    return (x - m) / torch.sqrt(v + eps)
def _scatter_add(tgt, idx, src, axis=0):
    return tgt.clone().index_add_(axis, idx, src)
def _segment_sum(src, seg, n):
    out = torch.zeros((n,) + src.shape[1:], dtype=src.dtype)
    return out.index_add_(0, seg, src)
def _pad(x, pw):
    flat = tuple(v for pair in reversed(pw) for v in pair)
    return torch.nn.functional.pad(x, flat)

kernel_name = 'concat_heads__compressed_kv_cla__linear_kernel_hedgehog__prompt'

def setup_inputs():

    _w = (lambda a: (lambda t: t.to(torch.bfloat16) if t.is_floating_point() else t)(torch.as_tensor(a)))
    def _A_setup():
        B, H, Sq, Sk, D, M = 1, 32, 2048, 2048, 128, 640
        Dc = 4 * D
        return {
            "x": _w((lambda *s: torch.randn(*s, dtype=torch.bfloat16) * 0.8)(B, Sq, M)),
            "c_kv": _w((lambda *s: torch.randn(*s, dtype=torch.bfloat16) * 0.4)(B, Sk, Dc)),
            "w_q": _w((lambda *s: torch.randn(*s, dtype=torch.bfloat16) * 6.4)(M, H, D) * (M ** -0.5)),
            "w_uk": _w((lambda *s: torch.randn(*s, dtype=torch.bfloat16) * 6.4)(Dc, H, D) * (Dc ** -0.5)),
            "w_uv": _w((lambda *s: torch.randn(*s, dtype=torch.bfloat16) * 0.4)(Dc, H, D) * (Dc ** -0.5)),
            "w_o": _w((lambda *s: torch.randn(*s, dtype=torch.bfloat16) * 0.4)(H, D, M) * ((H * D) ** -0.5)),
        }
    _A_in = _A_setup()

    def _B_setup():
        B, S, D, H = 1, 2048, 640, 32
        Dh = D // H
        return {
            "x": _w((lambda *s: torch.randn(*s, dtype=torch.bfloat16) * 0.4)(B, S, D)),
            "wqkv": _w((lambda *s: torch.randn(*s, dtype=torch.bfloat16) * 0.4)(D, 3 * D)),
            "w_phi": _w((lambda *s: torch.randn(*s, dtype=torch.bfloat16) * 0.4)(H, Dh, Dh) * (Dh ** -0.5)),
            "wo": _w((lambda *s: torch.randn(*s, dtype=torch.bfloat16) * 0.05)(D, D)),
        }
    _B_in = _B_setup()

    return {
        "A_x": _A_in["x"],
        "A_c_kv": _A_in["c_kv"],
        "A_w_q": _A_in["w_q"],
        "A_w_uk": _A_in["w_uk"],
        "A_w_uv": _A_in["w_uv"],
        "A_w_o": _A_in["w_o"],
        "B_x": _B_in["x"],
        "B_wqkv": _B_in["wqkv"],
        "B_w_phi": _B_in["w_phi"],
        "B_wo": _B_in["wo"],
    }

def reference(A_x, A_c_kv, A_w_q, A_w_uk, A_w_uv, A_w_o, B_x, B_wqkv, B_w_phi, B_wo):
    def _A_ref(x, c_kv, w_q, w_uk, w_uv, w_o):
        B, Sq, M = x.shape[0], x.shape[1], x.shape[2]
        Sk = c_kv.shape[1]
        H, D = w_q.shape[1], w_q.shape[2]
        q = torch.einsum("bqm,mhd->bhqd", x, w_q)
        k = torch.einsum("bkc,chd->bhkd", c_kv, w_uk)
        v = torch.einsum("bkc,chd->bhkd", c_kv, w_uv)
        scores = torch.einsum("bhqd,bhkd->bhqk", q, k) * (D ** -0.5)
        off = Sk - Sq
        qi = torch.arange(Sq)[:, None]
        kj = torch.arange(Sk)[None, :]
        cmask = kj <= (qi + off)
        scores = torch.where(cmask[None, None], scores, -1e9)
        p = _softmax(scores, axis=-1)
        ctx = torch.einsum("bhqk,bhkd->bhqd", p, v)
        return torch.einsum("bhqd,hdm->bqm", ctx, w_o)

    def _B_ref(x, wqkv, w_phi, wo):
        B, S, D = x.shape
        H = 32; Dh = D // H
        qkv = (lambda x, *d: x.permute(*d))((x @ wqkv).reshape(B, S, 3, H, Dh), 2, 0, 3, 1, 4)
        q, k, v = qkv[0], qkv[1], qkv[2]
        pq = torch.einsum("bhsd,hdf->bhsf", q, w_phi)
        pk = torch.einsum("bhsd,hdf->bhsf", k, w_phi)
        phi_q = torch.concatenate([_softmax(pq, axis=-1), _softmax(-pq, axis=-1)], axis=-1)
        phi_k = torch.concatenate([_softmax(pk, axis=-1), _softmax(-pk, axis=-1)], axis=-1)
        kv = torch.einsum("bhsf,bhsd->bhfd", phi_k, v)
        z  = torch.einsum("bhsf->bhf", phi_k)
        num = torch.einsum("bhqf,bhfd->bhqd", phi_q, kv)
        den = torch.einsum("bhqf,bhf->bhq", phi_q, z)[..., None] + 1e-6
        o = num / den
        return (lambda x, *d: x.permute(*d))(o, 0, 2, 1, 3).reshape(B, S, D) @ wo

    a = _A_ref(A_x, A_c_kv, A_w_q, A_w_uk, A_w_uv, A_w_o)
    b = _B_ref(B_x, B_wqkv, B_w_phi, B_wo)
    return torch.concatenate([a, b], axis=-1)
```

```cpp
#include <hip/hip_runtime.h>
#include <hip/hip_bf16.h>
#include <stdint.h>
typedef unsigned short u16;
__device__ __forceinline__ float bf2f(u16 v){ return __uint_as_float(((unsigned)v)<<16); }
__device__ __forceinline__ u16 f2bf(float f){ __hip_bfloat16 b = __float2bfloat16(f); return *(u16*)&b; }

__global__ void gemm_naive(const u16* A, int lda, const u16* B, int ldb, u16* C, int ldc, int M, int N, int K, float scale){
  __shared__ float As[64][33]; __shared__ float Bs[32][65];
  int tx = threadIdx.x & 15, ty = threadIdx.x >> 4;
  int r0 = blockIdx.y*64, c0 = blockIdx.x*64;
  float acc[4][4] = {};
  for(int k0=0;k0<K;k0+=32){
    for(int i=threadIdx.x;i<64*32;i+=256){ int r=i>>5,k=i&31; As[r][k]=bf2f(A[(size_t)(r0+r)*lda+k0+k]); }
    for(int i=threadIdx.x;i<32*64;i+=256){ int k=i>>6,c=i&63; Bs[k][c]=(c0+c<N)?bf2f(B[(size_t)(k0+k)*ldb+c0+c]):0.f; }
    __syncthreads();
    for(int k=0;k<32;k++){ float a[4],b[4];
      for(int i=0;i<4;i++) a[i]=As[ty*4+i][k];
      for(int j=0;j<4;j++) b[j]=Bs[k][tx*4+j];
      for(int i=0;i<4;i++) for(int j=0;j<4;j++) acc[i][j]+=a[i]*b[j]; }
    __syncthreads();
  }
  for(int i=0;i<4;i++) for(int j=0;j<4;j++){ int c=c0+tx*4+j; if(c<N) C[(size_t)(r0+ty*4+i)*ldc+c]=f2bf(acc[i][j]*scale); }
}
__global__ void attn_naive(const u16* Q,const u16* Kp,const u16* V,u16* O){
  int h = blockIdx.y; int row = blockIdx.x*4 + (threadIdx.x>>6); int lane = threadIdx.x&63;
  __shared__ float sc[4][2048]; __shared__ float qs[4][128];
  int w = threadIdx.x>>6;
  qs[w][lane]=bf2f(Q[(size_t)row*4096+h*128+lane]); qs[w][lane+64]=bf2f(Q[(size_t)row*4096+h*128+64+lane]);
  __syncthreads();
  float m=-1e30f;
  for(int k=lane;k<=row;k+=64){ const u16* kp=Kp+(size_t)k*4096+h*128; float s=0; for(int d=0;d<128;d++) s+=qs[w][d]*bf2f(kp[d]); s*=0.08838834764831845f; sc[w][k]=s; m=fmaxf(m,s);}
  for(int o=32;o>0;o>>=1) m=fmaxf(m,__shfl_xor(m,o));
  float l=0; for(int k=lane;k<=row;k+=64){ float p=__expf(sc[w][k]-m); sc[w][k]=p; l+=p; }
  for(int o=32;o>0;o>>=1) l+=__shfl_xor(l,o);
  __syncthreads();
  float o0=0,o1=0; for(int k=0;k<=row;k++){ float p=sc[w][k]; const u16* vp=V+(size_t)k*4096+h*128; o0+=p*bf2f(vp[lane]); o1+=p*bf2f(vp[lane+64]); }
  O[(size_t)row*4096+h*128+lane]=f2bf(o0/l); O[(size_t)row*4096+h*128+64+lane]=f2bf(o1/l);
}
__device__ void phi40(const u16* src, const u16* wphi, float* out){
  float p[20]; float x[20]; for(int d=0;d<20;d++) x[d]=bf2f(src[d]);
  float mx=-1e30f,mn=1e30f;
  for(int f=0;f<20;f++){ float s=0; for(int d=0;d<20;d++) s+=x[d]*bf2f(wphi[d*20+f]); p[f]=s; mx=fmaxf(mx,s); mn=fminf(mn,s);}
  float s1=0,s2=0; for(int f=0;f<20;f++){ out[f]=__expf(p[f]-mx); s1+=out[f]; out[20+f]=__expf(mn-p[f]); s2+=out[20+f]; }
  for(int f=0;f<20;f++){ out[f]/=s1; out[20+f]/=s2; }
}
__global__ __launch_bounds__(256) void b_kv(const u16* qkv,const u16* wphi,float* kv){
  int h=blockIdx.x; __shared__ float ph[256][41]; __shared__ float vs[256][21];
  float acc[4]={0,0,0,0};
  for(int s0=0;s0<2048;s0+=256){ int s=s0+threadIdx.x; float o[40]; phi40(qkv+(size_t)s*1920+640+h*20,wphi+h*400,o);
    for(int f=0;f<40;f++) ph[threadIdx.x][f]=o[f]; for(int d=0;d<20;d++) vs[threadIdx.x][d]=bf2f(qkv[(size_t)s*1920+1280+h*20+d]); vs[threadIdx.x][20]=1.f;
    __syncthreads();
    for(int i=0;i<4;i++){ int e=threadIdx.x+256*i; if(e<840){ int f=e/21,d=e%21; float a=0; for(int r=0;r<256;r++) a+=ph[r][f]*vs[r][d]; acc[i]+=a; } }
    __syncthreads(); }
  for(int i=0;i<4;i++){ int e=threadIdx.x+256*i; if(e<840) kv[h*840+e]=acc[i]; }
}
__global__ __launch_bounds__(256) void b_o(const u16* qkv,const u16* wphi,const float* kv,u16* o){
  int h=blockIdx.y; int s=blockIdx.x*256+threadIdx.x; float ph[40]; phi40(qkv+(size_t)s*1920+h*20,wphi+h*400,ph);
  const float* k=kv+h*840; float den=1e-6f; for(int f=0;f<40;f++) den+=ph[f]*k[f*21+20];
  for(int d=0;d<20;d++){ float n=0; for(int f=0;f<40;f++) n+=ph[f]*k[f*21+d]; o[(size_t)s*640+h*20+d]=f2bf(n/den); }
}
extern "C" void kernel_launch(void* const* d_in, const int* in_sizes, int n_in, void* d_out, int out_size, void* d_ws, size_t ws_size, hipStream_t stream){
  const u16* x=(const u16*)d_in[0]; const u16* ckv=(const u16*)d_in[1]; const u16* wq=(const u16*)d_in[2]; const u16* wuk=(const u16*)d_in[3];
  const u16* wuv=(const u16*)d_in[4]; const u16* wo=(const u16*)d_in[5]; const u16* bx=(const u16*)d_in[6]; const u16* wqkv=(const u16*)d_in[7];
  const u16* wphi=(const u16*)d_in[8]; const u16* bwo=(const u16*)d_in[9]; u16* out=(u16*)d_out;
  char* ws=(char*)d_ws; u16* q=(u16*)ws; u16* k=q+2048*4096; u16* v=k+2048*4096; u16* ctx=v+2048*4096; u16* qkv=ctx+2048*4096; u16* ob=qkv+2048*1920; float* kv=(float*)(ob+2048*640);
  gemm_naive<<<dim3(64,32),256,0,stream>>>(x,640,wq,4096,q,4096,2048,4096,640,1.f);
  gemm_naive<<<dim3(64,32),256,0,stream>>>(ckv,512,wuk,4096,k,4096,2048,4096,512,1.f);
  gemm_naive<<<dim3(64,32),256,0,stream>>>(ckv,512,wuv,4096,v,4096,2048,4096,512,1.f);
  attn_naive<<<dim3(512,32),256,0,stream>>>(q,k,v,ctx);
  gemm_naive<<<dim3(10,32),256,0,stream>>>(ctx,4096,wo,640,out,1280,2048,640,4096,1.f);
  gemm_naive<<<dim3(30,32),256,0,stream>>>(bx,640,wqkv,1920,qkv,1920,2048,1920,640,1.f);
  b_kv<<<32,256,0,stream>>>(qkv,wphi,kv);
  b_o<<<dim3(8,32),256,0,stream>>>(qkv,wphi,kv,ob);
  gemm_naive<<<dim3(10,32),256,0,stream>>>(ob,640,bwo,640,out+640,1280,2048,640,640,1.f);
}
```

```cpp
#include <hip/hip_runtime.h>
#include <stdint.h>

__global__ __launch_bounds__(256,1) void k_all(const void* x,const void* ckv,const void* wq,const void* wuk,const void* wuv,const void* wo,const void* bx,const void* wqkv,const void* wphi,const void* bwo,void* out,void* ws){
  __shared__ __attribute__((aligned(16))) char smem[163840];
  unsigned tid = threadIdx.x; unsigned wg = blockIdx.x;
  auto kp = __builtin_amdgcn_kernarg_segment_ptr();
  unsigned lb = (unsigned)(size_t)smem;
  asm volatile(
R"A(.macro M_ d,a,b,c
v_mfma_f32_16x16x32_bf16 \d,\a,\b,\c
.endm
.macro C_ d,a,b
v_cvt_pk_bf16_f32 \d,\a,\b
.endm
.macro AR_ d,a
v_accvgpr_read_b32 \d,\a
.endm
.macro AW_ d,a
v_accvgpr_write_b32 \d,\a
.endm
.macro E_ d,a
v_exp_f32 \d,\a
.endm
.macro R_ d,a,o
ds_read_b128 \d,\a offset:\o
.endm
.macro T_ d,a,o
ds_read_b64_tr_b16 \d,\a offset:\o
.endm
LSTART_%=:
s_load_dwordx2 s[88:89],s[0:1],88
s_load_dwordx2 s[4:5],s[0:1],8
s_load_dwordx2 s[8:9],s[0:1],24
s_load_dwordx2 s[12:13],s[0:1],32
s_load_dwordx2 s[24:25],s[0:1],56
s_load_dwordx2 s[28:29],s[0:1],64
s_load_dwordx2 s[32:33],s[0:1],40
s_load_dwordx2 s[36:37],s[0:1],72
s_load_dwordx2 s[68:69],s[0:1],0
s_load_dwordx2 s[70:71],s[0:1],16
s_load_dwordx2 s[72:73],s[0:1],48
v_lshrrev_b32 v1,6,v0
s_nop 0
v_readfirstlane_b32 s3,v1
s_waitcnt lgkmcnt(0)
v_mov_b32 v246,0x6404000
global_load_dword v247,v246,s[88:89] sc1
v_mbcnt_lo_u32_b32 v248,-1,0
v_mbcnt_hi_u32_b32 v248,-1,v248
v_lshlrev_b32 v248,4,v248
s_getpc_b64 s[60:61]
LPF1_%=:
s_sub_u32 s60,s60,LPF1_%=-LSTART_%=
s_subb_u32 s61,s61,0
s_mov_b32 s64,s60
s_mov_b32 s65,s61
s_and_b32 s65,s65,0xffff
s_mov_b32 s66,0x28000
s_mov_b32 s67,0x20000
s_mov_b32 s66,LCODEEND_%=-LSTART_%=
s_lshr_b32 s62,s2,3
s_lshl_b32 s62,s62,2
s_add_u32 s62,s62,s3
s_lshl_b32 s62,s62,10
buffer_load_dwordx4 v[240:243],v248,s[64:67],s62 offen
s_add_u32 s62,s62,0x20000
buffer_load_dwordx4 v[240:243],v248,s[64:67],s62 offen
v_mbcnt_lo_u32_b32 v146,-1,0
v_mbcnt_hi_u32_b32 v146,-1,v146
v_lshlrev_b32 v144,4,v146
v_and_b32 v147,15,v146
v_lshrrev_b32 v148,4,v146
s_and_b32 s54,s2,7
s_lshr_b32 s55,s2,3
s_and_b32 s19,s55,7
s_lshr_b32 s55,s55,3
s_lshl_b32 s55,s55,3
s_add_u32 s18,s55,s54
s_lshl_b32 s54,s19,2
s_add_u32 s54,s54,s3
s_lshl_b32 s20,s54,16
s_lshl_b32 s23,s3,12
s_add_u32 s22,s23,16384
s_and_b32 s21,s3,1
s_lshl_b32 s21,s21,17
s_lshl_b32 s54,s18,8
s_add_u32 s21,s21,s54
v_lshrrev_b32 v149,2,v146
v_lshlrev_b32 v149,10,v149
v_sub_u32 v150,4,v148
v_xor_b32 v150,v150,v146
v_and_b32 v150,3,v150
v_lshl_add_u32 v149,v150,4,v149
v_add_u32 v128,0x0,v149
v_add_u32 v129,0x3c00,v149
v_add_u32 v130,0x7800,v149
v_add_u32 v131,0xb400,v149
v_lshrrev_b32 v149,2,v146
v_and_b32 v149,3,v149
v_lshlrev_b32 v149,13,v149
v_and_b32 v151,3,v146
v_xor_b32 v150,0,v151
v_lshl_add_u32 v150,v148,2,v150
v_lshl_add_u32 v150,v150,4,v149
v_add_u32 v132,0x0,v150
v_add_u32 v133,0x7c00,v150
v_xor_b32 v150,2,v151
v_lshl_add_u32 v150,v148,2,v150
v_lshl_add_u32 v150,v150,4,v149
v_add_u32 v134,0xf800,v150
v_add_u32 v135,0x17400,v150
s_and_b32 s5,s5,0xffff
s_mov_b32 s6,0x200000
s_mov_b32 s7,0x20000
s_and_b32 s9,s9,0xffff
s_mov_b32 s10,0x400000
s_mov_b32 s11,0x20000
s_and_b32 s13,s13,0xffff
s_mov_b32 s14,0x400000
s_mov_b32 s15,0x20000
s_cmp_ge_u32 s3,2
s_cselect_b32 s8,s12,s8
s_cselect_b32 s9,s13,s9
s_add_u32 s55,s21,0x0
s_add_u32 m0,s22,0x0
s_nop 0
buffer_load_dwordx4 v132,s[8:11],s55 offen lds
buffer_load_dwordx4 v133,s[8:11],s55 offen offset:1024 lds
buffer_load_dwordx4 v134,s[8:11],s55 offen offset:2048 lds
buffer_load_dwordx4 v135,s[8:11],s55 offen offset:3072 lds
s_add_u32 s55,s20,0x0
s_add_u32 m0,s23,0x0
s_nop 0
buffer_load_dwordx4 v128,s[4:7],s55 offen lds
buffer_load_dwordx4 v129,s[4:7],s55 offen offset:1024 lds
buffer_load_dwordx4 v130,s[4:7],s55 offen offset:2048 lds
buffer_load_dwordx4 v131,s[4:7],s55 offen offset:3072 lds
s_add_u32 s55,s21,0x40000
s_add_u32 m0,s22,0x8000
s_nop 0
buffer_load_dwordx4 v132,s[8:11],s55 offen lds
buffer_load_dwordx4 v133,s[8:11],s55 offen offset:1024 lds
buffer_load_dwordx4 v134,s[8:11],s55 offen offset:2048 lds
buffer_load_dwordx4 v135,s[8:11],s55 offen offset:3072 lds
s_add_u32 s55,s20,0x40
s_add_u32 m0,s23,0x8000
s_nop 0
buffer_load_dwordx4 v128,s[4:7],s55 offen lds
buffer_load_dwordx4 v129,s[4:7],s55 offen offset:1024 lds
buffer_load_dwordx4 v130,s[4:7],s55 offen offset:2048 lds
buffer_load_dwordx4 v131,s[4:7],s55 offen offset:3072 lds
s_add_u32 s55,s21,0x80000
s_add_u32 m0,s22,0x10000
s_nop 0
buffer_load_dwordx4 v132,s[8:11],s55 offen lds
buffer_load_dwordx4 v133,s[8:11],s55 offen offset:1024 lds
buffer_load_dwordx4 v134,s[8:11],s55 offen offset:2048 lds
buffer_load_dwordx4 v135,s[8:11],s55 offen offset:3072 lds
s_add_u32 s55,s20,0x80
s_add_u32 m0,s23,0x10000
s_nop 0
buffer_load_dwordx4 v128,s[4:7],s55 offen lds
buffer_load_dwordx4 v129,s[4:7],s55 offen offset:1024 lds
buffer_load_dwordx4 v130,s[4:7],s55 offen offset:2048 lds
buffer_load_dwordx4 v131,s[4:7],s55 offen offset:3072 lds
s_add_u32 s55,s21,0xc0000
s_add_u32 m0,s22,0x18000
s_nop 0
buffer_load_dwordx4 v132,s[8:11],s55 offen lds
buffer_load_dwordx4 v133,s[8:11],s55 offen offset:1024 lds
buffer_load_dwordx4 v134,s[8:11],s55 offen offset:2048 lds
buffer_load_dwordx4 v135,s[8:11],s55 offen offset:3072 lds
s_add_u32 s55,s20,0xc0
s_add_u32 m0,s23,0x18000
s_nop 0
buffer_load_dwordx4 v128,s[4:7],s55 offen lds
buffer_load_dwordx4 v129,s[4:7],s55 offen offset:1024 lds
buffer_load_dwordx4 v130,s[4:7],s55 offen offset:2048 lds
buffer_load_dwordx4 v131,s[4:7],s55 offen offset:3072 lds
s_and_b32 s54,s3,1
s_lshl_b32 s54,s54,13
v_lshrrev_b32 v149,2,v147
v_sub_u32 v149,4,v149
v_xor_b32 v149,v149,v148
v_and_b32 v149,3,v149
v_lshlrev_b32 v149,4,v149
v_lshl_add_u32 v149,v147,6,v149
v_add_u32 v136,s54,v149
v_add_u32 v137,0x10000,v136
s_lshr_b32 s54,s3,1
s_lshl_b32 s54,s54,13
s_add_u32 s54,s54,0x4000
v_lshlrev_b32 v149,11,v148
v_lshrrev_b32 v150,2,v147
v_lshl_add_u32 v149,v150,6,v149
v_and_b32 v150,3,v146
v_lshl_add_u32 v149,v150,3,v149
v_add_u32 v149,s54,v149
v_and_b32 v150,1,v148
v_xor_b32 v150,0,v150
v_lshl_add_u32 v140,v150,5,v149
v_add_u32 v142,0x10000,v140
v_and_b32 v150,1,v148
v_xor_b32 v150,1,v150
v_lshl_add_u32 v141,v150,5,v149
v_add_u32 v143,0x10000,v141
s_waitcnt vmcnt(24)
s_barrier
v_readfirstlane_b32 s86,v247
s_nop 3
s_add_u32 s86,s86,1
s_cmp_ge_u32 s3,2
s_cbranch_scc1 LKV_V6_%=
ds_read_b64_tr_b16 v[0:1],v140
T_ v[2:3],v140,1024
ds_read_b128 v[32:35],v136
R_ v[36:39],v136,1024
R_ v[40:43],v136,2048
R_ v[44:47],v136,3072
R_ v[48:51],v136,4096
R_ v[52:55],v136,5120
R_ v[56:59],v136,6144
R_ v[60:63],v136,7168
ds_read_b64_tr_b16 v[4:5],v141
T_ v[6:7],v141,1024
T_ v[8:9],v140,256
T_ v[10:11],v140,1280
T_ v[12:13],v141,256
T_ v[14:15],v141,1280
T_ v[16:17],v140,512
T_ v[18:19],v140,1536
T_ v[20:21],v141,512
T_ v[22:23],v141,1536
T_ v[24:25],v140,768
T_ v[26:27],v140,1792
T_ v[28:29],v141,768
T_ v[30:31],v141,1792
s_waitcnt vmcnt(16)
s_waitcnt lgkmcnt(0)
s_barrier
M_ a[0:3],v[0:3],v[32:35],0
T_ v[64:65],v140,32768
M_ a[4:7],v[0:3],v[36:39],0
T_ v[66:67],v140,33792
M_ a[8:11],v[0:3],v[40:43],0
R_ v[96:99],v136,32768
M_ a[12:15],v[0:3],v[44:47],0
R_ v[100:103],v136,33792
M_ a[16:19],v[0:3],v[48:51],0
R_ v[104:107],v136,34816
v_mbcnt_lo_u32_b32 v248,-1,0
M_ a[20:23],v[0:3],v[52:55],0
R_ v[108:111],v136,35840
M_ a[24:27],v[0:3],v[56:59],0
R_ v[112:115],v136,36864
M_ a[28:31],v[0:3],v[60:63],0
R_ v[116:119],v136,37888
M_ a[32:35],v[4:7],v[32:35],0
R_ v[120:123],v136,38912
v_mbcnt_hi_u32_b32 v248,-1,v248
M_ a[36:39],v[4:7],v[36:39],0
R_ v[124:127],v136,39936
M_ a[40:43],v[4:7],v[40:43],0
T_ v[68:69],v141,32768
M_ a[44:47],v[4:7],v[44:47],0
T_ v[70:71],v141,33792
M_ a[48:51],v[4:7],v[48:51],0
T_ v[72:73],v140,33024
v_lshlrev_b32 v248,4,v248
M_ a[52:55],v[4:7],v[52:55],0
T_ v[74:75],v140,34048
M_ a[56:59],v[4:7],v[56:59],0
T_ v[76:77],v141,33024
M_ a[60:63],v[4:7],v[60:63],0
T_ v[78:79],v141,34048
M_ a[64:67],v[8:11],v[32:35],0
T_ v[80:81],v140,33280
s_mov_b32 s60,s72
M_ a[68:71],v[8:11],v[36:39],0
T_ v[82:83],v140,34304
M_ a[72:75],v[8:11],v[40:43],0
T_ v[84:85],v141,33280
M_ a[76:79],v[8:11],v[44:47],0
T_ v[86:87],v141,34304
M_ a[80:83],v[8:11],v[48:51],0
T_ v[88:89],v140,33536
s_mov_b32 s61,s73
M_ a[84:87],v[8:11],v[52:55],0
T_ v[90:91],v140,34560
M_ a[88:91],v[8:11],v[56:59],0
T_ v[92:93],v141,33536
M_ a[92:95],v[8:11],v[60:63],0
T_ v[94:95],v141,34560
M_ a[96:99],v[12:15],v[32:35],0
s_add_u32 s55,s21,0x100000
s_mov_b32 s64,s60
M_ a[100:103],v[12:15],v[36:39],0
s_add_u32 m0,s22,0x0
M_ a[104:107],v[12:15],v[40:43],0
buffer_load_dwordx4 v132,s[8:11],s55 offen lds
M_ a[108:111],v[12:15],v[44:47],0
M_ a[112:115],v[12:15],v[48:51],0
s_mov_b32 s65,s61
M_ a[116:119],v[12:15],v[52:55],0
M_ a[120:123],v[12:15],v[56:59],0
M_ a[124:127],v[12:15],v[60:63],0
buffer_load_dwordx4 v133,s[8:11],s55 offen offset:1024 lds
M_ a[128:131],v[16:19],v[32:35],0
M_ a[132:135],v[16:19],v[36:39],0
s_and_b32 s65,s65,0xffff
M_ a[136:139],v[16:19],v[40:43],0
M_ a[140:143],v[16:19],v[44:47],0
M_ a[144:147],v[16:19],v[48:51],0
buffer_load_dwordx4 v134,s[8:11],s55 offen offset:2048 lds
M_ a[148:151],v[16:19],v[52:55],0
s_mov_b32 s66,0x280000
M_ a[152:155],v[16:19],v[56:59],0
M_ a[156:159],v[16:19],v[60:63],0
M_ a[160:163],v[20:23],v[32:35],0
M_ a[164:167],v[20:23],v[36:39],0
buffer_load_dwordx4 v135,s[8:11],s55 offen offset:3072 lds
s_mov_b32 s67,0x20000
M_ a[168:171],v[20:23],v[40:43],0
M_ a[172:175],v[20:23],v[44:47],0
M_ a[176:179],v[20:23],v[48:51],0
M_ a[180:183],v[20:23],v[52:55],0
s_add_u32 s55,s20,0x100
s_lshl_b32 s62,s2,2
M_ a[184:187],v[20:23],v[56:59],0
s_add_u32 m0,s23,0x0
M_ a[188:191],v[20:23],v[60:63],0
buffer_load_dwordx4 v128,s[4:7],s55 offen lds
M_ a[192:195],v[24:27],v[32:35],0
M_ a[196:199],v[24:27],v[36:39],0
s_add_u32 s62,s62,s3
M_ a[200:203],v[24:27],v[40:43],0
M_ a[204:207],v[24:27],v[44:47],0
M_ a[208:211],v[24:27],v[48:51],0
buffer_load_dwordx4 v129,s[4:7],s55 offen offset:1024 lds
M_ a[212:215],v[24:27],v[52:55],0
s_lshl_b32 s62,s62,10
M_ a[216:219],v[24:27],v[56:59],0
M_ a[220:223],v[24:27],v[60:63],0
M_ a[224:227],v[28:31],v[32:35],0
M_ a[228:231],v[28:31],v[36:39],0
buffer_load_dwordx4 v130,s[4:7],s55 offen offset:2048 lds
buffer_load_dwordx4 v[240:243],v248,s[64:67],s62 offen
M_ a[232:235],v[28:31],v[40:43],0
M_ a[236:239],v[28:31],v[44:47],0
M_ a[240:243],v[28:31],v[48:51],0
M_ a[244:247],v[28:31],v[52:55],0
M_ a[248:251],v[28:31],v[56:59],0
buffer_load_dwordx4 v131,s[4:7],s55 offen offset:3072 lds
M_ a[252:255],v[28:31],v[60:63],0
s_waitcnt vmcnt(16)
s_waitcnt lgkmcnt(0)
s_barrier
M_ a[0:3],v[64:67],v[96:99],a[0:3]
ds_read_b64_tr_b16 v[0:1],v142
M_ a[4:7],v[64:67],v[100:103],a[4:7]
T_ v[2:3],v142,1024
M_ a[8:11],v[64:67],v[104:107],a[8:11]
ds_read_b128 v[32:35],v137
M_ a[12:15],v[64:67],v[108:111],a[12:15]
R_ v[36:39],v137,1024
M_ a[16:19],v[64:67],v[112:115],a[16:19]
R_ v[40:43],v137,2048
v_mbcnt_lo_u32_b32 v161,-1,0
M_ a[20:23],v[64:67],v[116:119],a[20:23]
R_ v[44:47],v137,3072
M_ a[24:27],v[64:67],v[120:123],a[24:27]
R_ v[48:51],v137,4096
M_ a[28:31],v[64:67],v[124:127],a[28:31]
R_ v[52:55],v137,5120
v_mbcnt_hi_u32_b32 v161,-1,v161
M_ a[32:35],v[68:71],v[96:99],a[32:35]
R_ v[56:59],v137,6144
M_ a[36:39],v[68:71],v[100:103],a[36:39]
R_ v[60:63],v137,7168
M_ a[40:43],v[68:71],v[104:107],a[40:43]
ds_read_b64_tr_b16 v[4:5],v143
M_ a[44:47],v[68:71],v[108:111],a[44:47]
T_ v[6:7],v143,1024
v_lshlrev_b32 v160,4,v161
M_ a[48:51],v[68:71],v[112:115],a[48:51]
T_ v[8:9],v142,256
M_ a[52:55],v[68:71],v[116:119],a[52:55]
T_ v[10:11],v142,1280
M_ a[56:59],v[68:71],v[120:123],a[56:59]
T_ v[12:13],v143,256
M_ a[60:63],v[68:71],v[124:127],a[60:63]
T_ v[14:15],v143,1280
s_and_b32 s25,s25,0xffff
M_ a[64:67],v[72:75],v[96:99],a[64:67]
T_ v[16:17],v142,512
M_ a[68:71],v[72:75],v[100:103],a[68:71]
T_ v[18:19],v142,1536
M_ a[72:75],v[72:75],v[104:107],a[72:75]
T_ v[20:21],v143,512
M_ a[76:79],v[72:75],v[108:111],a[76:79]
T_ v[22:23],v143,1536
s_mov_b32 s26,0x258000
M_ a[80:83],v[72:75],v[112:115],a[80:83]
T_ v[24:25],v142,768
M_ a[84:87],v[72:75],v[116:119],a[84:87]
T_ v[26:27],v142,1792
M_ a[88:91],v[72:75],v[120:123],a[88:91]
T_ v[28:29],v143,768
M_ a[92:95],v[72:75],v[124:127],a[92:95]
T_ v[30:31],v143,1792
s_mov_b32 s27,0x20000
M_ a[96:99],v[76:79],v[96:99],a[96:99]
s_add_u32 s55,s21,0x140000
M_ a[100:103],v[76:79],v[100:103],a[100:103]
s_add_u32 m0,s22,0x8000
M_ a[104:107],v[76:79],v[104:107],a[104:107]
buffer_load_dwordx4 v132,s[8:11],s55 offen lds
M_ a[108:111],v[76:79],v[108:111],a[108:111]
s_and_b32 s29,s29,0xffff
M_ a[112:115],v[76:79],v[112:115],a[112:115]
M_ a[116:119],v[76:79],v[116:119],a[116:119]
M_ a[120:123],v[76:79],v[120:123],a[120:123]
M_ a[124:127],v[76:79],v[124:127],a[124:127]
buffer_load_dwordx4 v133,s[8:11],s55 offen offset:1024 lds
s_mov_b32 s30,0x6400
M_ a[128:131],v[80:83],v[96:99],a[128:131]
M_ a[132:135],v[80:83],v[100:103],a[132:135]
M_ a[136:139],v[80:83],v[104:107],a[136:139]
s_mov_b32 s31,0x20000
M_ a[140:143],v[80:83],v[108:111],a[140:143]
M_ a[144:147],v[80:83],v[112:115],a[144:147]
buffer_load_dwordx4 v134,s[8:11],s55 offen offset:2048 lds
M_ a[148:151],v[80:83],v[116:119],a[148:151]
M_ a[152:155],v[80:83],v[120:123],a[152:155]
s_and_b32 s37,s37,0xffff
M_ a[156:159],v[80:83],v[124:127],a[156:159]
M_ a[160:163],v[84:87],v[96:99],a[160:163]
M_ a[164:167],v[84:87],v[100:103],a[164:167]
buffer_load_dwordx4 v135,s[8:11],s55 offen offset:3072 lds
M_ a[168:171],v[84:87],v[104:107],a[168:171]
s_mov_b32 s38,0xc8000
M_ a[172:175],v[84:87],v[108:111],a[172:175]
M_ a[176:179],v[84:87],v[112:115],a[176:179]
M_ a[180:183],v[84:87],v[116:119],a[180:183]
s_add_u32 s55,s20,0x140
M_ a[184:187],v[84:87],v[120:123],a[184:187]
s_add_u32 m0,s23,0x8000
s_mov_b32 s39,0x20000
M_ a[188:191],v[84:87],v[124:127],a[188:191]
buffer_load_dwordx4 v128,s[4:7],s55 offen lds
M_ a[192:195],v[88:91],v[96:99],a[192:195]
M_ a[196:199],v[88:91],v[100:103],a[196:199]
M_ a[200:203],v[88:91],v[104:107],a[200:203]
s_lshl_b32 s40,s2,2
M_ a[204:207],v[88:91],v[108:111],a[204:207]
M_ a[208:211],v[88:91],v[112:115],a[208:211]
buffer_load_dwordx4 v129,s[4:7],s55 offen offset:1024 lds
M_ a[212:215],v[88:91],v[116:119],a[212:215]
M_ a[216:219],v[88:91],v[120:123],a[216:219]
s_add_u32 s40,s40,s3
M_ a[220:223],v[88:91],v[124:127],a[220:223]
M_ a[224:227],v[92:95],v[96:99],a[224:227]
M_ a[228:231],v[92:95],v[100:103],a[228:231]
buffer_load_dwordx4 v130,s[4:7],s55 offen offset:2048 lds
M_ a[232:235],v[92:95],v[104:107],a[232:235]
s_cmp_ge_u32 s40,640
s_cbranch_scc1 LS2x7_%=
s_mul_i32 s41,s40,0xcccd
s_lshr_b32 s41,s41,20
s_mul_i32 s42,s41,20
s_sub_u32 s42,s40,s42
)A"
R"A(v_and_b32 v162,15,v161
v_lshlrev_b32 v163,3,v162
v_mov_b32 v164,0
v_cmp_le_u32 vcc,5,v162
v_mov_b32 v165,0x9d8
s_nop 0
v_cndmask_b32 v164,v164,v165,vcc
v_cmp_le_u32 vcc,8,v162
v_mov_b32 v165,0x4c0
s_nop 0
v_cndmask_b32 v164,v164,v165,vcc
v_cmp_le_u32 vcc,13,v162
v_mov_b32 v165,0x9b0
s_nop 0
v_cndmask_b32 v164,v164,v165,vcc
v_cmp_le_u32 vcc,15,v162
v_mov_b32 v165,0x7000000
s_nop 0
v_cndmask_b32 v164,v164,v165,vcc
v_add_u32 v163,v163,v164
v_lshrrev_b32 v164,4,v161
v_mul_u32_u24 v164,0xf00,v164
v_add_u32 v163,v163,v164
s_mul_i32 s43,s42,122880
s_mul_i32 s41,s41,40
s_add_u32 s43,s43,s41
buffer_load_dwordx2 v[170:171],v163,s[24:27],s43 offen
s_add_u32 s43,s43,0x3c00
buffer_load_dwordx2 v[172:173],v163,s[24:27],s43 offen
s_add_u32 s43,s43,0x3c00
buffer_load_dwordx2 v[174:175],v163,s[24:27],s43 offen
s_add_u32 s43,s43,0x3c00
buffer_load_dwordx2 v[176:177],v163,s[24:27],s43 offen
s_add_u32 s43,s43,0x3c00
buffer_load_dwordx2 v[178:179],v163,s[24:27],s43 offen
s_add_u32 s43,s43,0x3c00
buffer_load_dwordx2 v[180:181],v163,s[24:27],s43 offen
s_add_u32 s43,s43,0x3c00
buffer_load_dwordx2 v[182:183],v163,s[24:27],s43 offen
s_add_u32 s43,s43,0x3c00
buffer_load_dwordx2 v[184:185],v163,s[24:27],s43 offen
s_branch LT2x7_%=
LS2x7_%=:
s_cmp_ge_u32 s40,672
s_cbranch_scc1 LU2x7_%=
v_and_b32 v162,15,v161
v_lshrrev_b32 v163,4,v161
v_lshrrev_b32 v164,2,v162
v_mul_u32_u24 v164,5,v164
v_and_b32 v165,3,v162
v_add_u32 v166,v164,v165
v_add_u32 v167,4,v164
v_cmp_eq_u32 vcc,0,v165
v_mov_b32 v165,0x1000000
s_nop 0
v_cndmask_b32 v167,v165,v167,vcc
s_sub_u32 s41,s40,640
s_mul_i32 s41,s41,800
v_lshl_add_u32 v162,v163,2,0
v_mul_u32_u24 v162,20,v162
v_add_u32 v162,v162,v166
v_lshlrev_b32 v162,1,v162
buffer_load_ushort v170,v162,s[28:31],s41 offen
v_lshl_add_u32 v162,v163,2,1
v_mul_u32_u24 v162,20,v162
v_add_u32 v162,v162,v166
v_lshlrev_b32 v162,1,v162
buffer_load_ushort v171,v162,s[28:31],s41 offen
v_lshl_add_u32 v162,v163,2,2
v_mul_u32_u24 v162,20,v162
v_add_u32 v162,v162,v166
v_lshlrev_b32 v162,1,v162
buffer_load_ushort v172,v162,s[28:31],s41 offen
v_lshl_add_u32 v162,v163,2,3
v_mul_u32_u24 v162,20,v162
v_add_u32 v162,v162,v166
v_lshlrev_b32 v162,1,v162
buffer_load_ushort v173,v162,s[28:31],s41 offen
v_cmp_eq_u32 vcc,0,v163
v_mov_b32 v162,0x100000
v_mov_b32 v164,16
s_nop 0
v_cndmask_b32 v162,v162,v164,vcc
v_mul_u32_u24 v162,20,v162
v_add_u32 v162,v162,v166
v_lshlrev_b32 v162,1,v162
buffer_load_ushort v174,v162,s[28:31],s41 offen
v_cmp_eq_u32 vcc,0,v163
v_mov_b32 v162,0x100000
v_mov_b32 v164,17
s_nop 0
v_cndmask_b32 v162,v162,v164,vcc
v_mul_u32_u24 v162,20,v162
v_add_u32 v162,v162,v166
v_lshlrev_b32 v162,1,v162
buffer_load_ushort v175,v162,s[28:31],s41 offen
v_cmp_eq_u32 vcc,0,v163
v_mov_b32 v162,0x100000
v_mov_b32 v164,18
s_nop 0
v_cndmask_b32 v162,v162,v164,vcc
v_mul_u32_u24 v162,20,v162
v_add_u32 v162,v162,v166
v_lshlrev_b32 v162,1,v162
buffer_load_ushort v176,v162,s[28:31],s41 offen
v_cmp_eq_u32 vcc,0,v163
v_mov_b32 v162,0x100000
v_mov_b32 v164,19
s_nop 0
v_cndmask_b32 v162,v162,v164,vcc
v_mul_u32_u24 v162,20,v162
v_add_u32 v162,v162,v166
v_lshlrev_b32 v162,1,v162
buffer_load_ushort v177,v162,s[28:31],s41 offen
v_lshl_add_u32 v162,v163,2,0
v_mul_u32_u24 v162,20,v162
v_add_u32 v162,v162,v167
v_lshlrev_b32 v162,1,v162
buffer_load_ushort v178,v162,s[28:31],s41 offen
v_lshl_add_u32 v162,v163,2,1
v_mul_u32_u24 v162,20,v162
v_add_u32 v162,v162,v167
v_lshlrev_b32 v162,1,v162
buffer_load_ushort v179,v162,s[28:31],s41 offen
v_lshl_add_u32 v162,v163,2,2
v_mul_u32_u24 v162,20,v162
v_add_u32 v162,v162,v167
v_lshlrev_b32 v162,1,v162
buffer_load_ushort v180,v162,s[28:31],s41 offen
v_lshl_add_u32 v162,v163,2,3
v_mul_u32_u24 v162,20,v162
v_add_u32 v162,v162,v167
v_lshlrev_b32 v162,1,v162
buffer_load_ushort v181,v162,s[28:31],s41 offen
v_cmp_eq_u32 vcc,0,v163
v_mov_b32 v162,0x100000
v_mov_b32 v164,16
s_nop 0
v_cndmask_b32 v162,v162,v164,vcc
v_mul_u32_u24 v162,20,v162
v_add_u32 v162,v162,v167
v_lshlrev_b32 v162,1,v162
buffer_load_ushort v182,v162,s[28:31],s41 offen
v_cmp_eq_u32 vcc,0,v163
v_mov_b32 v162,0x100000
v_mov_b32 v164,17
s_nop 0
v_cndmask_b32 v162,v162,v164,vcc
v_mul_u32_u24 v162,20,v162
v_add_u32 v162,v162,v167
v_lshlrev_b32 v162,1,v162
buffer_load_ushort v183,v162,s[28:31],s41 offen
v_cmp_eq_u32 vcc,0,v163
v_mov_b32 v162,0x100000
v_mov_b32 v164,18
s_nop 0
v_cndmask_b32 v162,v162,v164,vcc
v_mul_u32_u24 v162,20,v162
v_add_u32 v162,v162,v167
v_lshlrev_b32 v162,1,v162
buffer_load_ushort v184,v162,s[28:31],s41 offen
v_cmp_eq_u32 vcc,0,v163
v_mov_b32 v162,0x100000
v_mov_b32 v164,19
s_nop 0
v_cndmask_b32 v162,v162,v164,vcc
v_mul_u32_u24 v162,20,v162
v_add_u32 v162,v162,v167
v_lshlrev_b32 v162,1,v162
buffer_load_ushort v185,v162,s[28:31],s41 offen
s_branch LT2x7_%=
LU2x7_%=:
s_cmp_ge_u32 s40,832
s_cbranch_scc1 LT2x7_%=
s_sub_u32 s43,s40,672
s_lshr_b32 s41,s43,3
s_lshr_b32 s42,s43,1
s_and_b32 s42,s42,3
s_and_b32 s43,s43,1
s_mul_i32 s41,s41,40960
s_mul_i32 s42,s42,320
s_add_u32 s41,s41,s42
s_lshl_b32 s43,s43,7
s_add_u32 s41,s41,s43
v_add_u32 v163,0,v161
v_mul_u32_u24 v164,0xaaab,v163
v_lshrrev_b32 v164,19,v164
v_mul_u32_u24 v165,12,v164
v_sub_u32 v165,v163,v165
v_mul_u32_u24 v164,0x500,v164
v_lshl_add_u32 v164,v165,4,v164
buffer_load_dwordx4 v[170:173],v164,s[36:39],s41 offen
v_add_u32 v163,64,v161
v_mul_u32_u24 v164,0xaaab,v163
v_lshrrev_b32 v164,19,v164
v_mul_u32_u24 v165,12,v164
v_sub_u32 v165,v163,v165
v_mul_u32_u24 v164,0x500,v164
v_lshl_add_u32 v164,v165,4,v164
buffer_load_dwordx4 v[174:177],v164,s[36:39],s41 offen
v_add_u32 v163,128,v161
v_mul_u32_u24 v164,0xaaab,v163
v_lshrrev_b32 v164,19,v164
v_mul_u32_u24 v165,12,v164
v_sub_u32 v165,v163,v165
v_mul_u32_u24 v164,0x500,v164
v_lshl_add_u32 v164,v165,4,v164
buffer_load_dwordx4 v[178:181],v164,s[36:39],s41 offen
v_add_u32 v163,192,v161
v_mul_u32_u24 v164,0xaaab,v163
v_lshrrev_b32 v164,19,v164
v_mul_u32_u24 v165,12,v164
v_sub_u32 v165,v163,v165
v_mul_u32_u24 v164,0x500,v164
v_lshl_add_u32 v164,v165,4,v164
buffer_load_dwordx4 v[182:185],v164,s[36:39],s41 offen
v_add_u32 v163,256,v161
v_mul_u32_u24 v164,0xaaab,v163
v_lshrrev_b32 v164,19,v164
v_mul_u32_u24 v165,12,v164
v_sub_u32 v165,v163,v165
v_mul_u32_u24 v164,0x500,v164
v_lshl_add_u32 v164,v165,4,v164
buffer_load_dwordx4 v[186:189],v164,s[36:39],s41 offen
v_add_u32 v163,320,v161
v_mul_u32_u24 v164,0xaaab,v163
v_lshrrev_b32 v164,19,v164
v_mul_u32_u24 v165,12,v164
v_sub_u32 v165,v163,v165
v_mul_u32_u24 v164,0x500,v164
v_lshl_add_u32 v164,v165,4,v164
buffer_load_dwordx4 v[190:193],v164,s[36:39],s41 offen
LT2x7_%=:
M_ a[236:239],v[92:95],v[108:111],a[236:239]
M_ a[240:243],v[92:95],v[112:115],a[240:243]
M_ a[244:247],v[92:95],v[116:119],a[244:247]
M_ a[248:251],v[92:95],v[120:123],a[248:251]
buffer_load_dwordx4 v131,s[4:7],s55 offen offset:3072 lds
M_ a[252:255],v[92:95],v[124:127],a[252:255]
s_waitcnt vmcnt(16)
s_waitcnt lgkmcnt(0)
s_barrier
M_ a[0:3],v[0:3],v[32:35],a[0:3]
T_ v[64:65],v142,32768
M_ a[4:7],v[0:3],v[36:39],a[4:7]
T_ v[66:67],v142,33792
M_ a[8:11],v[0:3],v[40:43],a[8:11]
R_ v[96:99],v137,32768
M_ a[12:15],v[0:3],v[44:47],a[12:15]
R_ v[100:103],v137,33792
M_ a[16:19],v[0:3],v[48:51],a[16:19]
R_ v[104:107],v137,34816
s_add_u32 s62,s62,0x100000
M_ a[20:23],v[0:3],v[52:55],a[20:23]
R_ v[108:111],v137,35840
M_ a[24:27],v[0:3],v[56:59],a[24:27]
R_ v[112:115],v137,36864
M_ a[28:31],v[0:3],v[60:63],a[28:31]
R_ v[116:119],v137,37888
M_ a[32:35],v[4:7],v[32:35],a[32:35]
R_ v[120:123],v137,38912
M_ a[36:39],v[4:7],v[36:39],a[36:39]
R_ v[124:127],v137,39936
M_ a[40:43],v[4:7],v[40:43],a[40:43]
T_ v[68:69],v143,32768
M_ a[44:47],v[4:7],v[44:47],a[44:47]
T_ v[70:71],v143,33792
M_ a[48:51],v[4:7],v[48:51],a[48:51]
T_ v[72:73],v142,33024
M_ a[52:55],v[4:7],v[52:55],a[52:55]
T_ v[74:75],v142,34048
M_ a[56:59],v[4:7],v[56:59],a[56:59]
T_ v[76:77],v143,33024
M_ a[60:63],v[4:7],v[60:63],a[60:63]
T_ v[78:79],v143,34048
M_ a[64:67],v[8:11],v[32:35],a[64:67]
T_ v[80:81],v142,33280
M_ a[68:71],v[8:11],v[36:39],a[68:71]
T_ v[82:83],v142,34304
M_ a[72:75],v[8:11],v[40:43],a[72:75]
T_ v[84:85],v143,33280
M_ a[76:79],v[8:11],v[44:47],a[76:79]
T_ v[86:87],v143,34304
M_ a[80:83],v[8:11],v[48:51],a[80:83]
T_ v[88:89],v142,33536
M_ a[84:87],v[8:11],v[52:55],a[84:87]
T_ v[90:91],v142,34560
M_ a[88:91],v[8:11],v[56:59],a[88:91]
T_ v[92:93],v143,33536
M_ a[92:95],v[8:11],v[60:63],a[92:95]
T_ v[94:95],v143,34560
M_ a[96:99],v[12:15],v[32:35],a[96:99]
s_add_u32 s55,s21,0x180000
M_ a[100:103],v[12:15],v[36:39],a[100:103]
s_add_u32 m0,s22,0x10000
M_ a[104:107],v[12:15],v[40:43],a[104:107]
buffer_load_dwordx4 v132,s[8:11],s55 offen lds
M_ a[108:111],v[12:15],v[44:47],a[108:111]
M_ a[112:115],v[12:15],v[48:51],a[112:115]
M_ a[116:119],v[12:15],v[52:55],a[116:119]
M_ a[120:123],v[12:15],v[56:59],a[120:123]
M_ a[124:127],v[12:15],v[60:63],a[124:127]
buffer_load_dwordx4 v133,s[8:11],s55 offen offset:1024 lds
M_ a[128:131],v[16:19],v[32:35],a[128:131]
M_ a[132:135],v[16:19],v[36:39],a[132:135]
buffer_load_dwordx4 v[240:243],v248,s[64:67],s62 offen
M_ a[136:139],v[16:19],v[40:43],a[136:139]
M_ a[140:143],v[16:19],v[44:47],a[140:143]
M_ a[144:147],v[16:19],v[48:51],a[144:147]
buffer_load_dwordx4 v134,s[8:11],s55 offen offset:2048 lds
M_ a[148:151],v[16:19],v[52:55],a[148:151]
M_ a[152:155],v[16:19],v[56:59],a[152:155]
M_ a[156:159],v[16:19],v[60:63],a[156:159]
M_ a[160:163],v[20:23],v[32:35],a[160:163]
M_ a[164:167],v[20:23],v[36:39],a[164:167]
buffer_load_dwordx4 v135,s[8:11],s55 offen offset:3072 lds
M_ a[168:171],v[20:23],v[40:43],a[168:171]
M_ a[172:175],v[20:23],v[44:47],a[172:175]
M_ a[176:179],v[20:23],v[48:51],a[176:179]
M_ a[180:183],v[20:23],v[52:55],a[180:183]
s_add_u32 s55,s20,0x180
M_ a[184:187],v[20:23],v[56:59],a[184:187]
s_add_u32 m0,s23,0x10000
M_ a[188:191],v[20:23],v[60:63],a[188:191]
buffer_load_dwordx4 v128,s[4:7],s55 offen lds
M_ a[192:195],v[24:27],v[32:35],a[192:195]
M_ a[196:199],v[24:27],v[36:39],a[196:199]
M_ a[200:203],v[24:27],v[40:43],a[200:203]
M_ a[204:207],v[24:27],v[44:47],a[204:207]
M_ a[208:211],v[24:27],v[48:51],a[208:211]
buffer_load_dwordx4 v129,s[4:7],s55 offen offset:1024 lds
M_ a[212:215],v[24:27],v[52:55],a[212:215]
M_ a[216:219],v[24:27],v[56:59],a[216:219]
M_ a[220:223],v[24:27],v[60:63],a[220:223]
M_ a[224:227],v[28:31],v[32:35],a[224:227]
M_ a[228:231],v[28:31],v[36:39],a[228:231]
buffer_load_dwordx4 v130,s[4:7],s55 offen offset:2048 lds
M_ a[232:235],v[28:31],v[40:43],a[232:235]
M_ a[236:239],v[28:31],v[44:47],a[236:239]
M_ a[240:243],v[28:31],v[48:51],a[240:243]
M_ a[244:247],v[28:31],v[52:55],a[244:247]
M_ a[248:251],v[28:31],v[56:59],a[248:251]
buffer_load_dwordx4 v131,s[4:7],s55 offen offset:3072 lds
M_ a[252:255],v[28:31],v[60:63],a[252:255]
s_waitcnt vmcnt(17)
s_waitcnt lgkmcnt(0)
s_barrier
M_ a[0:3],v[64:67],v[96:99],a[0:3]
ds_read_b64_tr_b16 v[0:1],v140
M_ a[4:7],v[64:67],v[100:103],a[4:7]
T_ v[2:3],v140,1024
M_ a[8:11],v[64:67],v[104:107],a[8:11]
ds_read_b128 v[32:35],v136
M_ a[12:15],v[64:67],v[108:111],a[12:15]
R_ v[36:39],v136,1024
M_ a[16:19],v[64:67],v[112:115],a[16:19]
R_ v[40:43],v136,2048
s_add_u32 s62,s62,0x100000
M_ a[20:23],v[64:67],v[116:119],a[20:23]
R_ v[44:47],v136,3072
M_ a[24:27],v[64:67],v[120:123],a[24:27]
R_ v[48:51],v136,4096
M_ a[28:31],v[64:67],v[124:127],a[28:31]
R_ v[52:55],v136,5120
M_ a[32:35],v[68:71],v[96:99],a[32:35]
R_ v[56:59],v136,6144
M_ a[36:39],v[68:71],v[100:103],a[36:39]
R_ v[60:63],v136,7168
M_ a[40:43],v[68:71],v[104:107],a[40:43]
ds_read_b64_tr_b16 v[4:5],v141
M_ a[44:47],v[68:71],v[108:111],a[44:47]
T_ v[6:7],v141,1024
M_ a[48:51],v[68:71],v[112:115],a[48:51]
T_ v[8:9],v140,256
M_ a[52:55],v[68:71],v[116:119],a[52:55]
T_ v[10:11],v140,1280
M_ a[56:59],v[68:71],v[120:123],a[56:59]
T_ v[12:13],v141,256
M_ a[60:63],v[68:71],v[124:127],a[60:63]
T_ v[14:15],v141,1280
M_ a[64:67],v[72:75],v[96:99],a[64:67]
T_ v[16:17],v140,512
M_ a[68:71],v[72:75],v[100:103],a[68:71]
T_ v[18:19],v140,1536
M_ a[72:75],v[72:75],v[104:107],a[72:75]
T_ v[20:21],v141,512
M_ a[76:79],v[72:75],v[108:111],a[76:79]
T_ v[22:23],v141,1536
M_ a[80:83],v[72:75],v[112:115],a[80:83]
T_ v[24:25],v140,768
M_ a[84:87],v[72:75],v[116:119],a[84:87]
T_ v[26:27],v140,1792
M_ a[88:91],v[72:75],v[120:123],a[88:91]
T_ v[28:29],v141,768
M_ a[92:95],v[72:75],v[124:127],a[92:95]
T_ v[30:31],v141,1792
M_ a[96:99],v[76:79],v[96:99],a[96:99]
s_add_u32 s55,s21,0x1c0000
M_ a[100:103],v[76:79],v[100:103],a[100:103]
s_add_u32 m0,s22,0x18000
M_ a[104:107],v[76:79],v[104:107],a[104:107]
buffer_load_dwordx4 v132,s[8:11],s55 offen lds
M_ a[108:111],v[76:79],v[108:111],a[108:111]
M_ a[112:115],v[76:79],v[112:115],a[112:115]
M_ a[116:119],v[76:79],v[116:119],a[116:119]
M_ a[120:123],v[76:79],v[120:123],a[120:123]
M_ a[124:127],v[76:79],v[124:127],a[124:127]
buffer_load_dwordx4 v133,s[8:11],s55 offen offset:1024 lds
M_ a[128:131],v[80:83],v[96:99],a[128:131]
M_ a[132:135],v[80:83],v[100:103],a[132:135]
buffer_load_dwordx4 v[240:243],v248,s[64:67],s62 offen
M_ a[136:139],v[80:83],v[104:107],a[136:139]
M_ a[140:143],v[80:83],v[108:111],a[140:143]
M_ a[144:147],v[80:83],v[112:115],a[144:147]
buffer_load_dwordx4 v134,s[8:11],s55 offen offset:2048 lds
M_ a[148:151],v[80:83],v[116:119],a[148:151]
M_ a[152:155],v[80:83],v[120:123],a[152:155]
M_ a[156:159],v[80:83],v[124:127],a[156:159]
M_ a[160:163],v[84:87],v[96:99],a[160:163]
M_ a[164:167],v[84:87],v[100:103],a[164:167]
buffer_load_dwordx4 v135,s[8:11],s55 offen offset:3072 lds
M_ a[168:171],v[84:87],v[104:107],a[168:171]
M_ a[172:175],v[84:87],v[108:111],a[172:175]
M_ a[176:179],v[84:87],v[112:115],a[176:179]
M_ a[180:183],v[84:87],v[116:119],a[180:183]
s_add_u32 s55,s20,0x1c0
M_ a[184:187],v[84:87],v[120:123],a[184:187]
s_add_u32 m0,s23,0x18000
M_ a[188:191],v[84:87],v[124:127],a[188:191]
buffer_load_dwordx4 v128,s[4:7],s55 offen lds
M_ a[192:195],v[88:91],v[96:99],a[192:195]
M_ a[196:199],v[88:91],v[100:103],a[196:199]
M_ a[200:203],v[88:91],v[104:107],a[200:203]
M_ a[204:207],v[88:91],v[108:111],a[204:207]
M_ a[208:211],v[88:91],v[112:115],a[208:211]
buffer_load_dwordx4 v129,s[4:7],s55 offen offset:1024 lds
)A"
R"A(M_ a[212:215],v[88:91],v[116:119],a[212:215]
M_ a[216:219],v[88:91],v[120:123],a[216:219]
M_ a[220:223],v[88:91],v[124:127],a[220:223]
M_ a[224:227],v[92:95],v[96:99],a[224:227]
M_ a[228:231],v[92:95],v[100:103],a[228:231]
buffer_load_dwordx4 v130,s[4:7],s55 offen offset:2048 lds
M_ a[232:235],v[92:95],v[104:107],a[232:235]
M_ a[236:239],v[92:95],v[108:111],a[236:239]
M_ a[240:243],v[92:95],v[112:115],a[240:243]
M_ a[244:247],v[92:95],v[116:119],a[244:247]
M_ a[248:251],v[92:95],v[120:123],a[248:251]
buffer_load_dwordx4 v131,s[4:7],s55 offen offset:3072 lds
M_ a[252:255],v[92:95],v[124:127],a[252:255]
s_waitcnt vmcnt(18)
s_waitcnt lgkmcnt(0)
s_barrier
M_ a[0:3],v[0:3],v[32:35],a[0:3]
T_ v[64:65],v140,32768
M_ a[4:7],v[0:3],v[36:39],a[4:7]
T_ v[66:67],v140,33792
M_ a[8:11],v[0:3],v[40:43],a[8:11]
R_ v[96:99],v136,32768
M_ a[12:15],v[0:3],v[44:47],a[12:15]
R_ v[100:103],v136,33792
M_ a[16:19],v[0:3],v[48:51],a[16:19]
R_ v[104:107],v136,34816
v_mbcnt_lo_u32_b32 v248,-1,0
M_ a[20:23],v[0:3],v[52:55],a[20:23]
R_ v[108:111],v136,35840
M_ a[24:27],v[0:3],v[56:59],a[24:27]
R_ v[112:115],v136,36864
M_ a[28:31],v[0:3],v[60:63],a[28:31]
R_ v[116:119],v136,37888
M_ a[32:35],v[4:7],v[32:35],a[32:35]
R_ v[120:123],v136,38912
v_mbcnt_hi_u32_b32 v248,-1,v248
M_ a[36:39],v[4:7],v[36:39],a[36:39]
R_ v[124:127],v136,39936
M_ a[40:43],v[4:7],v[40:43],a[40:43]
T_ v[68:69],v141,32768
M_ a[44:47],v[4:7],v[44:47],a[44:47]
T_ v[70:71],v141,33792
M_ a[48:51],v[4:7],v[48:51],a[48:51]
T_ v[72:73],v140,33024
v_lshlrev_b32 v248,4,v248
M_ a[52:55],v[4:7],v[52:55],a[52:55]
T_ v[74:75],v140,34048
M_ a[56:59],v[4:7],v[56:59],a[56:59]
T_ v[76:77],v141,33024
M_ a[60:63],v[4:7],v[60:63],a[60:63]
T_ v[78:79],v141,34048
M_ a[64:67],v[8:11],v[32:35],a[64:67]
T_ v[80:81],v140,33280
s_mov_b32 s60,s68
M_ a[68:71],v[8:11],v[36:39],a[68:71]
T_ v[82:83],v140,34304
M_ a[72:75],v[8:11],v[40:43],a[72:75]
T_ v[84:85],v141,33280
M_ a[76:79],v[8:11],v[44:47],a[76:79]
T_ v[86:87],v141,34304
M_ a[80:83],v[8:11],v[48:51],a[80:83]
T_ v[88:89],v140,33536
s_mov_b32 s61,s69
M_ a[84:87],v[8:11],v[52:55],a[84:87]
T_ v[90:91],v140,34560
M_ a[88:91],v[8:11],v[56:59],a[88:91]
T_ v[92:93],v141,33536
M_ a[92:95],v[8:11],v[60:63],a[92:95]
T_ v[94:95],v141,34560
M_ a[96:99],v[12:15],v[32:35],a[96:99]
s_add_u32 s55,s21,0x200000
s_mov_b32 s64,s60
M_ a[100:103],v[12:15],v[36:39],a[100:103]
s_add_u32 m0,s22,0x0
M_ a[104:107],v[12:15],v[40:43],a[104:107]
buffer_load_dwordx4 v132,s[8:11],s55 offen lds
M_ a[108:111],v[12:15],v[44:47],a[108:111]
M_ a[112:115],v[12:15],v[48:51],a[112:115]
s_mov_b32 s65,s61
M_ a[116:119],v[12:15],v[52:55],a[116:119]
M_ a[120:123],v[12:15],v[56:59],a[120:123]
M_ a[124:127],v[12:15],v[60:63],a[124:127]
buffer_load_dwordx4 v133,s[8:11],s55 offen offset:1024 lds
M_ a[128:131],v[16:19],v[32:35],a[128:131]
M_ a[132:135],v[16:19],v[36:39],a[132:135]
s_and_b32 s65,s65,0xffff
M_ a[136:139],v[16:19],v[40:43],a[136:139]
M_ a[140:143],v[16:19],v[44:47],a[140:143]
M_ a[144:147],v[16:19],v[48:51],a[144:147]
buffer_load_dwordx4 v134,s[8:11],s55 offen offset:2048 lds
M_ a[148:151],v[16:19],v[52:55],a[148:151]
s_mov_b32 s66,0x280000
M_ a[152:155],v[16:19],v[56:59],a[152:155]
M_ a[156:159],v[16:19],v[60:63],a[156:159]
M_ a[160:163],v[20:23],v[32:35],a[160:163]
M_ a[164:167],v[20:23],v[36:39],a[164:167]
buffer_load_dwordx4 v135,s[8:11],s55 offen offset:3072 lds
s_mov_b32 s67,0x20000
M_ a[168:171],v[20:23],v[40:43],a[168:171]
M_ a[172:175],v[20:23],v[44:47],a[172:175]
M_ a[176:179],v[20:23],v[48:51],a[176:179]
M_ a[180:183],v[20:23],v[52:55],a[180:183]
s_add_u32 s55,s20,0x200
s_lshl_b32 s62,s2,2
M_ a[184:187],v[20:23],v[56:59],a[184:187]
s_add_u32 m0,s23,0x0
M_ a[188:191],v[20:23],v[60:63],a[188:191]
buffer_load_dwordx4 v128,s[4:7],s55 offen lds
M_ a[192:195],v[24:27],v[32:35],a[192:195]
M_ a[196:199],v[24:27],v[36:39],a[196:199]
s_add_u32 s62,s62,s3
M_ a[200:203],v[24:27],v[40:43],a[200:203]
M_ a[204:207],v[24:27],v[44:47],a[204:207]
M_ a[208:211],v[24:27],v[48:51],a[208:211]
buffer_load_dwordx4 v129,s[4:7],s55 offen offset:1024 lds
M_ a[212:215],v[24:27],v[52:55],a[212:215]
s_lshl_b32 s62,s62,10
M_ a[216:219],v[24:27],v[56:59],a[216:219]
M_ a[220:223],v[24:27],v[60:63],a[220:223]
M_ a[224:227],v[28:31],v[32:35],a[224:227]
M_ a[228:231],v[28:31],v[36:39],a[228:231]
buffer_load_dwordx4 v130,s[4:7],s55 offen offset:2048 lds
buffer_load_dwordx4 v[240:243],v248,s[64:67],s62 offen
M_ a[232:235],v[28:31],v[40:43],a[232:235]
M_ a[236:239],v[28:31],v[44:47],a[236:239]
M_ a[240:243],v[28:31],v[48:51],a[240:243]
M_ a[244:247],v[28:31],v[52:55],a[244:247]
M_ a[248:251],v[28:31],v[56:59],a[248:251]
buffer_load_dwordx4 v131,s[4:7],s55 offen offset:3072 lds
M_ a[252:255],v[28:31],v[60:63],a[252:255]
s_waitcnt vmcnt(18)
s_waitcnt lgkmcnt(0)
s_barrier
M_ a[0:3],v[64:67],v[96:99],a[0:3]
ds_read_b64_tr_b16 v[0:1],v142
M_ a[4:7],v[64:67],v[100:103],a[4:7]
T_ v[2:3],v142,1024
M_ a[8:11],v[64:67],v[104:107],a[8:11]
ds_read_b128 v[32:35],v137
M_ a[12:15],v[64:67],v[108:111],a[12:15]
R_ v[36:39],v137,1024
M_ a[16:19],v[64:67],v[112:115],a[16:19]
R_ v[40:43],v137,2048
s_cmp_ge_u32 s40,640
s_cbranch_scc1 LS3x8_%=
s_mul_i32 s42,s3,8192
s_add_u32 s42,s42,0x20000
v_lshlrev_b32 v164,3,v161
v_add_u32 v164,s42,v164
ds_write_b64 v164,v[170:171]
ds_write_b64 v164,v[172:173] offset:512
ds_write_b64 v164,v[174:175] offset:1024
ds_write_b64 v164,v[176:177] offset:1536
ds_write_b64 v164,v[178:179] offset:2048
ds_write_b64 v164,v[180:181] offset:2560
ds_write_b64 v164,v[182:183] offset:3072
ds_write_b64 v164,v[184:185] offset:3584
v_lshrrev_b32 v162,4,v161
v_lshlrev_b32 v162,10,v162
v_lshrrev_b32 v165,2,v161
v_and_b32 v165,3,v165
v_lshl_add_u32 v162,v165,7,v162
v_and_b32 v165,3,v161
v_lshl_add_u32 v162,v165,3,v162
v_add_u32 v162,s42,v162
s_waitcnt lgkmcnt(0)
ds_read_b64_tr_b16 v[194:195],v162
T_ v[196:197],v162,512
T_ v[198:199],v162,32
T_ v[200:201],v162,544
T_ v[202:203],v162,64
T_ v[204:205],v162,576
T_ v[206:207],v162,96
T_ v[208:209],v162,608
s_lshl_b32 s41,s40,12
s_add_u32 s41,s41,0x3000000
s_add_u32 s44,s88,s41
s_addc_u32 s45,s89,0
s_waitcnt lgkmcnt(6)
global_store_dwordx4 v160,v[194:197],s[44:45] sc1
s_waitcnt lgkmcnt(4)
global_store_dwordx4 v160,v[198:201],s[44:45] offset:1024 sc1
s_waitcnt lgkmcnt(2)
global_store_dwordx4 v160,v[202:205],s[44:45] offset:2048 sc1
s_waitcnt lgkmcnt(0)
global_store_dwordx4 v160,v[206:209],s[44:45] offset:3072 sc1
s_branch LT3x8_%=
LS3x8_%=:
s_cmp_ge_u32 s40,672
s_cbranch_scc1 LU3x8_%=
v_lshl_or_b32 v194,v171,16,v170
v_lshl_or_b32 v195,v173,16,v172
v_lshl_or_b32 v196,v175,16,v174
v_lshl_or_b32 v197,v177,16,v176
v_lshl_or_b32 v198,v179,16,v178
v_lshl_or_b32 v199,v181,16,v180
v_lshl_or_b32 v200,v183,16,v182
v_lshl_or_b32 v201,v185,16,v184
s_sub_u32 s41,s40,640
s_lshl_b32 s41,s41,11
s_add_u32 s41,s41,0x3300000
s_add_u32 s44,s88,s41
s_addc_u32 s45,s89,0
global_store_dwordx4 v160,v[194:197],s[44:45] sc1
global_store_dwordx4 v160,v[198:201],s[44:45] offset:1024 sc1
s_branch LT3x8_%=
LU3x8_%=:
s_cmp_ge_u32 s40,832
s_cbranch_scc1 LT3x8_%=
s_mul_i32 s42,s3,8192
s_add_u32 s42,s42,0x20000
v_add_u32 v162,s42,v160
ds_write_b128 v162,v[170:173]
ds_write_b128 v162,v[174:177] offset:1024
ds_write_b128 v162,v[178:181] offset:2048
ds_write_b128 v162,v[182:185] offset:3072
ds_write_b128 v162,v[186:189] offset:4096
ds_write_b128 v162,v[190:193] offset:5120
v_lshrrev_b32 v162,4,v161
v_lshlrev_b32 v162,3,v162
v_lshrrev_b32 v165,2,v161
v_and_b32 v165,3,v165
v_add_u32 v162,v162,v165
v_mul_u32_u24 v162,0xc0,v162
v_and_b32 v165,3,v161
v_lshl_add_u32 v162,v165,4,v162
v_add_u32 v162,s42,v162
s_sub_u32 s43,s40,672
s_lshr_b32 s41,s43,3
s_lshr_b32 s42,s43,1
s_and_b32 s42,s42,3
s_and_b32 s43,s43,1
s_lshl_b32 s44,s43,3
v_add_u32 v162,s44,v162
s_mul_i32 s44,s43,48
v_add_u32 v163,s44,v162
s_waitcnt lgkmcnt(0)
ds_read_b64_tr_b16 v[194:195],v162
T_ v[196:197],v162,768
T_ v[198:199],v163,8
T_ v[200:201],v163,776
T_ v[202:203],v162,64
T_ v[204:205],v162,832
T_ v[206:207],v163,72
T_ v[208:209],v163,840
T_ v[210:211],v162,128
T_ v[212:213],v162,896
s_mul_i32 s42,s42,20
s_add_u32 s41,s41,s42
s_mul_i32 s41,s41,10
s_mul_i32 s43,s43,5
s_add_u32 s41,s41,s43
s_lshl_b32 s41,s41,10
s_add_u32 s41,s41,0x3a00000
s_add_u32 s44,s88,s41
s_addc_u32 s45,s89,0
v_add_u32 v164,4096,v160
s_waitcnt lgkmcnt(8)
global_store_dwordx4 v160,v[194:197],s[44:45] sc1
s_waitcnt lgkmcnt(6)
global_store_dwordx4 v160,v[198:201],s[44:45] offset:1024 sc1
s_waitcnt lgkmcnt(4)
global_store_dwordx4 v160,v[202:205],s[44:45] offset:2048 sc1
s_waitcnt lgkmcnt(2)
global_store_dwordx4 v160,v[206:209],s[44:45] offset:3072 sc1
s_waitcnt lgkmcnt(0)
global_store_dwordx4 v164,v[210:213],s[44:45] sc1
LT3x8_%=:
M_ a[20:23],v[64:67],v[116:119],a[20:23]
R_ v[44:47],v137,3072
M_ a[24:27],v[64:67],v[120:123],a[24:27]
R_ v[48:51],v137,4096
M_ a[28:31],v[64:67],v[124:127],a[28:31]
R_ v[52:55],v137,5120
M_ a[32:35],v[68:71],v[96:99],a[32:35]
R_ v[56:59],v137,6144
M_ a[36:39],v[68:71],v[100:103],a[36:39]
R_ v[60:63],v137,7168
M_ a[40:43],v[68:71],v[104:107],a[40:43]
ds_read_b64_tr_b16 v[4:5],v143
M_ a[44:47],v[68:71],v[108:111],a[44:47]
T_ v[6:7],v143,1024
M_ a[48:51],v[68:71],v[112:115],a[48:51]
T_ v[8:9],v142,256
M_ a[52:55],v[68:71],v[116:119],a[52:55]
T_ v[10:11],v142,1280
M_ a[56:59],v[68:71],v[120:123],a[56:59]
T_ v[12:13],v143,256
M_ a[60:63],v[68:71],v[124:127],a[60:63]
T_ v[14:15],v143,1280
M_ a[64:67],v[72:75],v[96:99],a[64:67]
T_ v[16:17],v142,512
M_ a[68:71],v[72:75],v[100:103],a[68:71]
T_ v[18:19],v142,1536
M_ a[72:75],v[72:75],v[104:107],a[72:75]
T_ v[20:21],v143,512
M_ a[76:79],v[72:75],v[108:111],a[76:79]
T_ v[22:23],v143,1536
M_ a[80:83],v[72:75],v[112:115],a[80:83]
T_ v[24:25],v142,768
M_ a[84:87],v[72:75],v[116:119],a[84:87]
T_ v[26:27],v142,1792
M_ a[88:91],v[72:75],v[120:123],a[88:91]
T_ v[28:29],v143,768
M_ a[92:95],v[72:75],v[124:127],a[92:95]
T_ v[30:31],v143,1792
M_ a[96:99],v[76:79],v[96:99],a[96:99]
s_add_u32 s55,s21,0x240000
M_ a[100:103],v[76:79],v[100:103],a[100:103]
s_add_u32 m0,s22,0x8000
M_ a[104:107],v[76:79],v[104:107],a[104:107]
buffer_load_dwordx4 v132,s[8:11],s55 offen lds
M_ a[108:111],v[76:79],v[108:111],a[108:111]
M_ a[112:115],v[76:79],v[112:115],a[112:115]
M_ a[116:119],v[76:79],v[116:119],a[116:119]
M_ a[120:123],v[76:79],v[120:123],a[120:123]
M_ a[124:127],v[76:79],v[124:127],a[124:127]
buffer_load_dwordx4 v133,s[8:11],s55 offen offset:1024 lds
M_ a[128:131],v[80:83],v[96:99],a[128:131]
M_ a[132:135],v[80:83],v[100:103],a[132:135]
M_ a[136:139],v[80:83],v[104:107],a[136:139]
M_ a[140:143],v[80:83],v[108:111],a[140:143]
M_ a[144:147],v[80:83],v[112:115],a[144:147]
buffer_load_dwordx4 v134,s[8:11],s55 offen offset:2048 lds
M_ a[148:151],v[80:83],v[116:119],a[148:151]
M_ a[152:155],v[80:83],v[120:123],a[152:155]
M_ a[156:159],v[80:83],v[124:127],a[156:159]
M_ a[160:163],v[84:87],v[96:99],a[160:163]
M_ a[164:167],v[84:87],v[100:103],a[164:167]
buffer_load_dwordx4 v135,s[8:11],s55 offen offset:3072 lds
M_ a[168:171],v[84:87],v[104:107],a[168:171]
M_ a[172:175],v[84:87],v[108:111],a[172:175]
M_ a[176:179],v[84:87],v[112:115],a[176:179]
M_ a[180:183],v[84:87],v[116:119],a[180:183]
s_add_u32 s55,s20,0x240
M_ a[184:187],v[84:87],v[120:123],a[184:187]
s_add_u32 m0,s23,0x8000
M_ a[188:191],v[84:87],v[124:127],a[188:191]
buffer_load_dwordx4 v128,s[4:7],s55 offen lds
M_ a[192:195],v[88:91],v[96:99],a[192:195]
M_ a[196:199],v[88:91],v[100:103],a[196:199]
M_ a[200:203],v[88:91],v[104:107],a[200:203]
M_ a[204:207],v[88:91],v[108:111],a[204:207]
M_ a[208:211],v[88:91],v[112:115],a[208:211]
buffer_load_dwordx4 v129,s[4:7],s55 offen offset:1024 lds
M_ a[212:215],v[88:91],v[116:119],a[212:215]
M_ a[216:219],v[88:91],v[120:123],a[216:219]
M_ a[220:223],v[88:91],v[124:127],a[220:223]
M_ a[224:227],v[92:95],v[96:99],a[224:227]
M_ a[228:231],v[92:95],v[100:103],a[228:231]
buffer_load_dwordx4 v130,s[4:7],s55 offen offset:2048 lds
M_ a[232:235],v[92:95],v[104:107],a[232:235]
M_ a[236:239],v[92:95],v[108:111],a[236:239]
M_ a[240:243],v[92:95],v[112:115],a[240:243]
M_ a[244:247],v[92:95],v[116:119],a[244:247]
M_ a[248:251],v[92:95],v[120:123],a[248:251]
buffer_load_dwordx4 v131,s[4:7],s55 offen offset:3072 lds
M_ a[252:255],v[92:95],v[124:127],a[252:255]
s_waitcnt vmcnt(17)
s_waitcnt lgkmcnt(0)
s_barrier
M_ a[0:3],v[0:3],v[32:35],a[0:3]
T_ v[64:65],v142,32768
M_ a[4:7],v[0:3],v[36:39],a[4:7]
T_ v[66:67],v142,33792
M_ a[8:11],v[0:3],v[40:43],a[8:11]
R_ v[96:99],v137,32768
M_ a[12:15],v[0:3],v[44:47],a[12:15]
R_ v[100:103],v137,33792
M_ a[16:19],v[0:3],v[48:51],a[16:19]
R_ v[104:107],v137,34816
s_add_u32 s62,s62,0x100000
M_ a[20:23],v[0:3],v[52:55],a[20:23]
R_ v[108:111],v137,35840
M_ a[24:27],v[0:3],v[56:59],a[24:27]
R_ v[112:115],v137,36864
M_ a[28:31],v[0:3],v[60:63],a[28:31]
R_ v[116:119],v137,37888
M_ a[32:35],v[4:7],v[32:35],a[32:35]
R_ v[120:123],v137,38912
M_ a[36:39],v[4:7],v[36:39],a[36:39]
R_ v[124:127],v137,39936
M_ a[40:43],v[4:7],v[40:43],a[40:43]
T_ v[68:69],v143,32768
M_ a[44:47],v[4:7],v[44:47],a[44:47]
T_ v[70:71],v143,33792
M_ a[48:51],v[4:7],v[48:51],a[48:51]
T_ v[72:73],v142,33024
M_ a[52:55],v[4:7],v[52:55],a[52:55]
T_ v[74:75],v142,34048
M_ a[56:59],v[4:7],v[56:59],a[56:59]
T_ v[76:77],v143,33024
M_ a[60:63],v[4:7],v[60:63],a[60:63]
T_ v[78:79],v143,34048
M_ a[64:67],v[8:11],v[32:35],a[64:67]
T_ v[80:81],v142,33280
M_ a[68:71],v[8:11],v[36:39],a[68:71]
T_ v[82:83],v142,34304
M_ a[72:75],v[8:11],v[40:43],a[72:75]
T_ v[84:85],v143,33280
M_ a[76:79],v[8:11],v[44:47],a[76:79]
T_ v[86:87],v143,34304
M_ a[80:83],v[8:11],v[48:51],a[80:83]
T_ v[88:89],v142,33536
M_ a[84:87],v[8:11],v[52:55],a[84:87]
T_ v[90:91],v142,34560
M_ a[88:91],v[8:11],v[56:59],a[88:91]
T_ v[92:93],v143,33536
M_ a[92:95],v[8:11],v[60:63],a[92:95]
T_ v[94:95],v143,34560
M_ a[96:99],v[12:15],v[32:35],a[96:99]
s_add_u32 s55,s21,0x280000
M_ a[100:103],v[12:15],v[36:39],a[100:103]
s_add_u32 m0,s22,0x10000
M_ a[104:107],v[12:15],v[40:43],a[104:107]
buffer_load_dwordx4 v132,s[8:11],s55 offen lds
M_ a[108:111],v[12:15],v[44:47],a[108:111]
M_ a[112:115],v[12:15],v[48:51],a[112:115]
)A"
R"A(M_ a[116:119],v[12:15],v[52:55],a[116:119]
M_ a[120:123],v[12:15],v[56:59],a[120:123]
M_ a[124:127],v[12:15],v[60:63],a[124:127]
buffer_load_dwordx4 v133,s[8:11],s55 offen offset:1024 lds
M_ a[128:131],v[16:19],v[32:35],a[128:131]
M_ a[132:135],v[16:19],v[36:39],a[132:135]
buffer_load_dwordx4 v[240:243],v248,s[64:67],s62 offen
M_ a[136:139],v[16:19],v[40:43],a[136:139]
M_ a[140:143],v[16:19],v[44:47],a[140:143]
M_ a[144:147],v[16:19],v[48:51],a[144:147]
buffer_load_dwordx4 v134,s[8:11],s55 offen offset:2048 lds
M_ a[148:151],v[16:19],v[52:55],a[148:151]
M_ a[152:155],v[16:19],v[56:59],a[152:155]
M_ a[156:159],v[16:19],v[60:63],a[156:159]
M_ a[160:163],v[20:23],v[32:35],a[160:163]
M_ a[164:167],v[20:23],v[36:39],a[164:167]
buffer_load_dwordx4 v135,s[8:11],s55 offen offset:3072 lds
M_ a[168:171],v[20:23],v[40:43],a[168:171]
M_ a[172:175],v[20:23],v[44:47],a[172:175]
M_ a[176:179],v[20:23],v[48:51],a[176:179]
M_ a[180:183],v[20:23],v[52:55],a[180:183]
s_add_u32 s55,s20,0x280
M_ a[184:187],v[20:23],v[56:59],a[184:187]
s_add_u32 m0,s23,0x10000
M_ a[188:191],v[20:23],v[60:63],a[188:191]
buffer_load_dwordx4 v128,s[4:7],s55 offen lds
M_ a[192:195],v[24:27],v[32:35],a[192:195]
M_ a[196:199],v[24:27],v[36:39],a[196:199]
M_ a[200:203],v[24:27],v[40:43],a[200:203]
M_ a[204:207],v[24:27],v[44:47],a[204:207]
M_ a[208:211],v[24:27],v[48:51],a[208:211]
buffer_load_dwordx4 v129,s[4:7],s55 offen offset:1024 lds
M_ a[212:215],v[24:27],v[52:55],a[212:215]
M_ a[216:219],v[24:27],v[56:59],a[216:219]
M_ a[220:223],v[24:27],v[60:63],a[220:223]
M_ a[224:227],v[28:31],v[32:35],a[224:227]
M_ a[228:231],v[28:31],v[36:39],a[228:231]
buffer_load_dwordx4 v130,s[4:7],s55 offen offset:2048 lds
M_ a[232:235],v[28:31],v[40:43],a[232:235]
M_ a[236:239],v[28:31],v[44:47],a[236:239]
M_ a[240:243],v[28:31],v[48:51],a[240:243]
M_ a[244:247],v[28:31],v[52:55],a[244:247]
M_ a[248:251],v[28:31],v[56:59],a[248:251]
buffer_load_dwordx4 v131,s[4:7],s55 offen offset:3072 lds
M_ a[252:255],v[28:31],v[60:63],a[252:255]
s_waitcnt vmcnt(17)
s_waitcnt lgkmcnt(0)
s_barrier
M_ a[0:3],v[64:67],v[96:99],a[0:3]
ds_read_b64_tr_b16 v[0:1],v140
M_ a[4:7],v[64:67],v[100:103],a[4:7]
T_ v[2:3],v140,1024
M_ a[8:11],v[64:67],v[104:107],a[8:11]
ds_read_b128 v[32:35],v136
M_ a[12:15],v[64:67],v[108:111],a[12:15]
R_ v[36:39],v136,1024
M_ a[16:19],v[64:67],v[112:115],a[16:19]
R_ v[40:43],v136,2048
s_add_u32 s62,s62,0x100000
M_ a[20:23],v[64:67],v[116:119],a[20:23]
R_ v[44:47],v136,3072
M_ a[24:27],v[64:67],v[120:123],a[24:27]
R_ v[48:51],v136,4096
M_ a[28:31],v[64:67],v[124:127],a[28:31]
R_ v[52:55],v136,5120
M_ a[32:35],v[68:71],v[96:99],a[32:35]
R_ v[56:59],v136,6144
M_ a[36:39],v[68:71],v[100:103],a[36:39]
R_ v[60:63],v136,7168
M_ a[40:43],v[68:71],v[104:107],a[40:43]
ds_read_b64_tr_b16 v[4:5],v141
M_ a[44:47],v[68:71],v[108:111],a[44:47]
T_ v[6:7],v141,1024
M_ a[48:51],v[68:71],v[112:115],a[48:51]
T_ v[8:9],v140,256
M_ a[52:55],v[68:71],v[116:119],a[52:55]
T_ v[10:11],v140,1280
M_ a[56:59],v[68:71],v[120:123],a[56:59]
T_ v[12:13],v141,256
M_ a[60:63],v[68:71],v[124:127],a[60:63]
T_ v[14:15],v141,1280
M_ a[64:67],v[72:75],v[96:99],a[64:67]
T_ v[16:17],v140,512
M_ a[68:71],v[72:75],v[100:103],a[68:71]
T_ v[18:19],v140,1536
M_ a[72:75],v[72:75],v[104:107],a[72:75]
T_ v[20:21],v141,512
M_ a[76:79],v[72:75],v[108:111],a[76:79]
T_ v[22:23],v141,1536
M_ a[80:83],v[72:75],v[112:115],a[80:83]
T_ v[24:25],v140,768
M_ a[84:87],v[72:75],v[116:119],a[84:87]
T_ v[26:27],v140,1792
M_ a[88:91],v[72:75],v[120:123],a[88:91]
T_ v[28:29],v141,768
M_ a[92:95],v[72:75],v[124:127],a[92:95]
T_ v[30:31],v141,1792
M_ a[96:99],v[76:79],v[96:99],a[96:99]
s_add_u32 s55,s21,0x2c0000
M_ a[100:103],v[76:79],v[100:103],a[100:103]
s_add_u32 m0,s22,0x18000
M_ a[104:107],v[76:79],v[104:107],a[104:107]
buffer_load_dwordx4 v132,s[8:11],s55 offen lds
M_ a[108:111],v[76:79],v[108:111],a[108:111]
M_ a[112:115],v[76:79],v[112:115],a[112:115]
M_ a[116:119],v[76:79],v[116:119],a[116:119]
M_ a[120:123],v[76:79],v[120:123],a[120:123]
M_ a[124:127],v[76:79],v[124:127],a[124:127]
buffer_load_dwordx4 v133,s[8:11],s55 offen offset:1024 lds
M_ a[128:131],v[80:83],v[96:99],a[128:131]
M_ a[132:135],v[80:83],v[100:103],a[132:135]
buffer_load_dwordx4 v[240:243],v248,s[64:67],s62 offen
M_ a[136:139],v[80:83],v[104:107],a[136:139]
M_ a[140:143],v[80:83],v[108:111],a[140:143]
M_ a[144:147],v[80:83],v[112:115],a[144:147]
buffer_load_dwordx4 v134,s[8:11],s55 offen offset:2048 lds
M_ a[148:151],v[80:83],v[116:119],a[148:151]
M_ a[152:155],v[80:83],v[120:123],a[152:155]
M_ a[156:159],v[80:83],v[124:127],a[156:159]
M_ a[160:163],v[84:87],v[96:99],a[160:163]
M_ a[164:167],v[84:87],v[100:103],a[164:167]
buffer_load_dwordx4 v135,s[8:11],s55 offen offset:3072 lds
M_ a[168:171],v[84:87],v[104:107],a[168:171]
M_ a[172:175],v[84:87],v[108:111],a[172:175]
M_ a[176:179],v[84:87],v[112:115],a[176:179]
M_ a[180:183],v[84:87],v[116:119],a[180:183]
s_add_u32 s55,s20,0x2c0
M_ a[184:187],v[84:87],v[120:123],a[184:187]
s_add_u32 m0,s23,0x18000
M_ a[188:191],v[84:87],v[124:127],a[188:191]
buffer_load_dwordx4 v128,s[4:7],s55 offen lds
M_ a[192:195],v[88:91],v[96:99],a[192:195]
M_ a[196:199],v[88:91],v[100:103],a[196:199]
M_ a[200:203],v[88:91],v[104:107],a[200:203]
M_ a[204:207],v[88:91],v[108:111],a[204:207]
M_ a[208:211],v[88:91],v[112:115],a[208:211]
buffer_load_dwordx4 v129,s[4:7],s55 offen offset:1024 lds
M_ a[212:215],v[88:91],v[116:119],a[212:215]
M_ a[216:219],v[88:91],v[120:123],a[216:219]
M_ a[220:223],v[88:91],v[124:127],a[220:223]
M_ a[224:227],v[92:95],v[96:99],a[224:227]
M_ a[228:231],v[92:95],v[100:103],a[228:231]
buffer_load_dwordx4 v130,s[4:7],s55 offen offset:2048 lds
M_ a[232:235],v[92:95],v[104:107],a[232:235]
M_ a[236:239],v[92:95],v[108:111],a[236:239]
M_ a[240:243],v[92:95],v[112:115],a[240:243]
M_ a[244:247],v[92:95],v[116:119],a[244:247]
M_ a[248:251],v[92:95],v[120:123],a[248:251]
buffer_load_dwordx4 v131,s[4:7],s55 offen offset:3072 lds
M_ a[252:255],v[92:95],v[124:127],a[252:255]
s_waitcnt vmcnt(18)
s_waitcnt lgkmcnt(0)
s_barrier
M_ a[0:3],v[0:3],v[32:35],a[0:3]
T_ v[64:65],v140,32768
M_ a[4:7],v[0:3],v[36:39],a[4:7]
T_ v[66:67],v140,33792
M_ a[8:11],v[0:3],v[40:43],a[8:11]
R_ v[96:99],v136,32768
M_ a[12:15],v[0:3],v[44:47],a[12:15]
R_ v[100:103],v136,33792
M_ a[16:19],v[0:3],v[48:51],a[16:19]
R_ v[104:107],v136,34816
v_mbcnt_lo_u32_b32 v248,-1,0
M_ a[20:23],v[0:3],v[52:55],a[20:23]
R_ v[108:111],v136,35840
M_ a[24:27],v[0:3],v[56:59],a[24:27]
R_ v[112:115],v136,36864
M_ a[28:31],v[0:3],v[60:63],a[28:31]
R_ v[116:119],v136,37888
M_ a[32:35],v[4:7],v[32:35],a[32:35]
R_ v[120:123],v136,38912
v_mbcnt_hi_u32_b32 v248,-1,v248
M_ a[36:39],v[4:7],v[36:39],a[36:39]
R_ v[124:127],v136,39936
M_ a[40:43],v[4:7],v[40:43],a[40:43]
T_ v[68:69],v141,32768
M_ a[44:47],v[4:7],v[44:47],a[44:47]
T_ v[70:71],v141,33792
M_ a[48:51],v[4:7],v[48:51],a[48:51]
T_ v[72:73],v140,33024
v_lshlrev_b32 v248,4,v248
M_ a[52:55],v[4:7],v[52:55],a[52:55]
T_ v[74:75],v140,34048
M_ a[56:59],v[4:7],v[56:59],a[56:59]
T_ v[76:77],v141,33024
M_ a[60:63],v[4:7],v[60:63],a[60:63]
T_ v[78:79],v141,34048
M_ a[64:67],v[8:11],v[32:35],a[64:67]
T_ v[80:81],v140,33280
s_mov_b32 s60,s70
M_ a[68:71],v[8:11],v[36:39],a[68:71]
T_ v[82:83],v140,34304
M_ a[72:75],v[8:11],v[40:43],a[72:75]
T_ v[84:85],v141,33280
M_ a[76:79],v[8:11],v[44:47],a[76:79]
T_ v[86:87],v141,34304
M_ a[80:83],v[8:11],v[48:51],a[80:83]
T_ v[88:89],v140,33536
s_mov_b32 s61,s71
M_ a[84:87],v[8:11],v[52:55],a[84:87]
T_ v[90:91],v140,34560
M_ a[88:91],v[8:11],v[56:59],a[88:91]
T_ v[92:93],v141,33536
M_ a[92:95],v[8:11],v[60:63],a[92:95]
T_ v[94:95],v141,34560
M_ a[96:99],v[12:15],v[32:35],a[96:99]
s_add_u32 s55,s21,0x300000
s_mov_b32 s64,s60
M_ a[100:103],v[12:15],v[36:39],a[100:103]
s_add_u32 m0,s22,0x0
M_ a[104:107],v[12:15],v[40:43],a[104:107]
buffer_load_dwordx4 v132,s[8:11],s55 offen lds
M_ a[108:111],v[12:15],v[44:47],a[108:111]
M_ a[112:115],v[12:15],v[48:51],a[112:115]
s_mov_b32 s65,s61
M_ a[116:119],v[12:15],v[52:55],a[116:119]
M_ a[120:123],v[12:15],v[56:59],a[120:123]
M_ a[124:127],v[12:15],v[60:63],a[124:127]
buffer_load_dwordx4 v133,s[8:11],s55 offen offset:1024 lds
M_ a[128:131],v[16:19],v[32:35],a[128:131]
M_ a[132:135],v[16:19],v[36:39],a[132:135]
s_and_b32 s65,s65,0xffff
M_ a[136:139],v[16:19],v[40:43],a[136:139]
M_ a[140:143],v[16:19],v[44:47],a[140:143]
M_ a[144:147],v[16:19],v[48:51],a[144:147]
buffer_load_dwordx4 v134,s[8:11],s55 offen offset:2048 lds
M_ a[148:151],v[16:19],v[52:55],a[148:151]
s_mov_b32 s66,0x500000
M_ a[152:155],v[16:19],v[56:59],a[152:155]
M_ a[156:159],v[16:19],v[60:63],a[156:159]
M_ a[160:163],v[20:23],v[32:35],a[160:163]
M_ a[164:167],v[20:23],v[36:39],a[164:167]
buffer_load_dwordx4 v135,s[8:11],s55 offen offset:3072 lds
s_mov_b32 s67,0x20000
M_ a[168:171],v[20:23],v[40:43],a[168:171]
M_ a[172:175],v[20:23],v[44:47],a[172:175]
M_ a[176:179],v[20:23],v[48:51],a[176:179]
M_ a[180:183],v[20:23],v[52:55],a[180:183]
s_add_u32 s55,s20,0x300
s_lshl_b32 s62,s2,2
M_ a[184:187],v[20:23],v[56:59],a[184:187]
s_add_u32 m0,s23,0x0
M_ a[188:191],v[20:23],v[60:63],a[188:191]
buffer_load_dwordx4 v128,s[4:7],s55 offen lds
M_ a[192:195],v[24:27],v[32:35],a[192:195]
M_ a[196:199],v[24:27],v[36:39],a[196:199]
s_add_u32 s62,s62,s3
M_ a[200:203],v[24:27],v[40:43],a[200:203]
M_ a[204:207],v[24:27],v[44:47],a[204:207]
M_ a[208:211],v[24:27],v[48:51],a[208:211]
buffer_load_dwordx4 v129,s[4:7],s55 offen offset:1024 lds
M_ a[212:215],v[24:27],v[52:55],a[212:215]
s_lshl_b32 s62,s62,10
M_ a[216:219],v[24:27],v[56:59],a[216:219]
M_ a[220:223],v[24:27],v[60:63],a[220:223]
M_ a[224:227],v[28:31],v[32:35],a[224:227]
M_ a[228:231],v[28:31],v[36:39],a[228:231]
buffer_load_dwordx4 v130,s[4:7],s55 offen offset:2048 lds
buffer_load_dwordx4 v[240:243],v248,s[64:67],s62 offen
M_ a[232:235],v[28:31],v[40:43],a[232:235]
M_ a[236:239],v[28:31],v[44:47],a[236:239]
M_ a[240:243],v[28:31],v[48:51],a[240:243]
M_ a[244:247],v[28:31],v[52:55],a[244:247]
M_ a[248:251],v[28:31],v[56:59],a[248:251]
buffer_load_dwordx4 v131,s[4:7],s55 offen offset:3072 lds
M_ a[252:255],v[28:31],v[60:63],a[252:255]
s_waitcnt vmcnt(18)
s_waitcnt lgkmcnt(0)
s_barrier
M_ a[0:3],v[64:67],v[96:99],a[0:3]
ds_read_b64_tr_b16 v[0:1],v142
M_ a[4:7],v[64:67],v[100:103],a[4:7]
T_ v[2:3],v142,1024
M_ a[8:11],v[64:67],v[104:107],a[8:11]
ds_read_b128 v[32:35],v137
M_ a[12:15],v[64:67],v[108:111],a[12:15]
R_ v[36:39],v137,1024
M_ a[16:19],v[64:67],v[112:115],a[16:19]
R_ v[40:43],v137,2048
s_cmp_lg_u32 s3,0
s_cbranch_scc1 LPB4x9_%=
s_lshl_b32 s54,s2,2
s_add_u32 s54,s54,0x6400000
v_mov_b32 v162,s54
v_mov_b32 v163,s86
s_mov_b64 exec,1
global_store_dword v162,v163,s[88:89] sc1
s_mov_b64 exec,-1
LPB4x9_%=:
M_ a[20:23],v[64:67],v[116:119],a[20:23]
R_ v[44:47],v137,3072
M_ a[24:27],v[64:67],v[120:123],a[24:27]
R_ v[48:51],v137,4096
M_ a[28:31],v[64:67],v[124:127],a[28:31]
R_ v[52:55],v137,5120
M_ a[32:35],v[68:71],v[96:99],a[32:35]
R_ v[56:59],v137,6144
M_ a[36:39],v[68:71],v[100:103],a[36:39]
R_ v[60:63],v137,7168
M_ a[40:43],v[68:71],v[104:107],a[40:43]
ds_read_b64_tr_b16 v[4:5],v143
M_ a[44:47],v[68:71],v[108:111],a[44:47]
T_ v[6:7],v143,1024
M_ a[48:51],v[68:71],v[112:115],a[48:51]
T_ v[8:9],v142,256
M_ a[52:55],v[68:71],v[116:119],a[52:55]
T_ v[10:11],v142,1280
M_ a[56:59],v[68:71],v[120:123],a[56:59]
T_ v[12:13],v143,256
M_ a[60:63],v[68:71],v[124:127],a[60:63]
T_ v[14:15],v143,1280
M_ a[64:67],v[72:75],v[96:99],a[64:67]
T_ v[16:17],v142,512
M_ a[68:71],v[72:75],v[100:103],a[68:71]
T_ v[18:19],v142,1536
M_ a[72:75],v[72:75],v[104:107],a[72:75]
T_ v[20:21],v143,512
M_ a[76:79],v[72:75],v[108:111],a[76:79]
T_ v[22:23],v143,1536
M_ a[80:83],v[72:75],v[112:115],a[80:83]
T_ v[24:25],v142,768
M_ a[84:87],v[72:75],v[116:119],a[84:87]
T_ v[26:27],v142,1792
M_ a[88:91],v[72:75],v[120:123],a[88:91]
T_ v[28:29],v143,768
M_ a[92:95],v[72:75],v[124:127],a[92:95]
T_ v[30:31],v143,1792
M_ a[96:99],v[76:79],v[96:99],a[96:99]
s_add_u32 s55,s21,0x340000
M_ a[100:103],v[76:79],v[100:103],a[100:103]
s_add_u32 m0,s22,0x8000
M_ a[104:107],v[76:79],v[104:107],a[104:107]
buffer_load_dwordx4 v132,s[8:11],s55 offen lds
M_ a[108:111],v[76:79],v[108:111],a[108:111]
M_ a[112:115],v[76:79],v[112:115],a[112:115]
M_ a[116:119],v[76:79],v[116:119],a[116:119]
M_ a[120:123],v[76:79],v[120:123],a[120:123]
M_ a[124:127],v[76:79],v[124:127],a[124:127]
buffer_load_dwordx4 v133,s[8:11],s55 offen offset:1024 lds
M_ a[128:131],v[80:83],v[96:99],a[128:131]
M_ a[132:135],v[80:83],v[100:103],a[132:135]
M_ a[136:139],v[80:83],v[104:107],a[136:139]
M_ a[140:143],v[80:83],v[108:111],a[140:143]
M_ a[144:147],v[80:83],v[112:115],a[144:147]
buffer_load_dwordx4 v134,s[8:11],s55 offen offset:2048 lds
M_ a[148:151],v[80:83],v[116:119],a[148:151]
M_ a[152:155],v[80:83],v[120:123],a[152:155]
M_ a[156:159],v[80:83],v[124:127],a[156:159]
M_ a[160:163],v[84:87],v[96:99],a[160:163]
M_ a[164:167],v[84:87],v[100:103],a[164:167]
buffer_load_dwordx4 v135,s[8:11],s55 offen offset:3072 lds
M_ a[168:171],v[84:87],v[104:107],a[168:171]
M_ a[172:175],v[84:87],v[108:111],a[172:175]
M_ a[176:179],v[84:87],v[112:115],a[176:179]
M_ a[180:183],v[84:87],v[116:119],a[180:183]
s_add_u32 s55,s20,0x340
M_ a[184:187],v[84:87],v[120:123],a[184:187]
s_add_u32 m0,s23,0x8000
M_ a[188:191],v[84:87],v[124:127],a[188:191]
buffer_load_dwordx4 v128,s[4:7],s55 offen lds
M_ a[192:195],v[88:91],v[96:99],a[192:195]
M_ a[196:199],v[88:91],v[100:103],a[196:199]
M_ a[200:203],v[88:91],v[104:107],a[200:203]
M_ a[204:207],v[88:91],v[108:111],a[204:207]
M_ a[208:211],v[88:91],v[112:115],a[208:211]
buffer_load_dwordx4 v129,s[4:7],s55 offen offset:1024 lds
M_ a[212:215],v[88:91],v[116:119],a[212:215]
M_ a[216:219],v[88:91],v[120:123],a[216:219]
M_ a[220:223],v[88:91],v[124:127],a[220:223]
M_ a[224:227],v[92:95],v[96:99],a[224:227]
M_ a[228:231],v[92:95],v[100:103],a[228:231]
buffer_load_dwordx4 v130,s[4:7],s55 offen offset:2048 lds
M_ a[232:235],v[92:95],v[104:107],a[232:235]
M_ a[236:239],v[92:95],v[108:111],a[236:239]
M_ a[240:243],v[92:95],v[112:115],a[240:243]
)A"
R"A(M_ a[244:247],v[92:95],v[116:119],a[244:247]
M_ a[248:251],v[92:95],v[120:123],a[248:251]
buffer_load_dwordx4 v131,s[4:7],s55 offen offset:3072 lds
M_ a[252:255],v[92:95],v[124:127],a[252:255]
s_waitcnt vmcnt(17)
s_waitcnt lgkmcnt(0)
s_barrier
M_ a[0:3],v[0:3],v[32:35],a[0:3]
T_ v[64:65],v142,32768
M_ a[4:7],v[0:3],v[36:39],a[4:7]
T_ v[66:67],v142,33792
M_ a[8:11],v[0:3],v[40:43],a[8:11]
R_ v[96:99],v137,32768
M_ a[12:15],v[0:3],v[44:47],a[12:15]
R_ v[100:103],v137,33792
M_ a[16:19],v[0:3],v[48:51],a[16:19]
R_ v[104:107],v137,34816
s_add_u32 s62,s62,0x100000
buffer_load_dwordx4 v[240:243],v248,s[64:67],s62 offen
M_ a[20:23],v[0:3],v[52:55],a[20:23]
R_ v[108:111],v137,35840
v_mbcnt_lo_u32_b32 v210,-1,0
M_ a[24:27],v[0:3],v[56:59],a[24:27]
R_ v[112:115],v137,36864
v_mbcnt_hi_u32_b32 v210,-1,v210
M_ a[28:31],v[0:3],v[60:63],a[28:31]
R_ v[116:119],v137,37888
v_lshlrev_b32 v208,4,v210
s_and_b32 s44,s2,7
M_ a[32:35],v[4:7],v[32:35],a[32:35]
R_ v[120:123],v137,38912
s_lshr_b32 s37,s2,3
M_ a[36:39],v[4:7],v[36:39],a[36:39]
R_ v[124:127],v137,39936
s_lshr_b32 s38,s44,1
M_ a[40:43],v[4:7],v[40:43],a[40:43]
T_ v[68:69],v143,32768
s_lshl_b32 s38,s38,2
M_ a[44:47],v[4:7],v[44:47],a[44:47]
T_ v[70:71],v143,33792
s_and_b32 s32,s37,3
s_add_u32 s32,s32,s38
M_ a[48:51],v[4:7],v[48:51],a[48:51]
T_ v[72:73],v142,33024
s_and_b32 s44,s44,1
M_ a[52:55],v[4:7],v[52:55],a[52:55]
T_ v[74:75],v142,34048
s_lshl_b32 s44,s44,3
M_ a[56:59],v[4:7],v[56:59],a[56:59]
T_ v[76:77],v143,33024
s_lshr_b32 s33,s37,2
s_add_u32 s33,s33,s44
M_ a[60:63],v[4:7],v[60:63],a[60:63]
T_ v[78:79],v143,34048
s_lshr_b32 s34,s3,1
M_ a[64:67],v[8:11],v[32:35],a[64:67]
T_ v[80:81],v142,33280
s_and_b32 s35,s3,1
M_ a[68:71],v[8:11],v[36:39],a[68:71]
T_ v[82:83],v142,34304
s_lshl_b32 s36,s33,1
M_ a[72:75],v[8:11],v[40:43],a[72:75]
T_ v[84:85],v143,33280
s_add_u32 s36,s36,s35
v_and_b32 v211,15,v210
M_ a[76:79],v[8:11],v[44:47],a[76:79]
T_ v[86:87],v143,34304
v_lshrrev_b32 v212,4,v210
M_ a[80:83],v[8:11],v[48:51],a[80:83]
T_ v[88:89],v142,33536
s_lshl_b32 s44,s32,2
M_ a[84:87],v[8:11],v[52:55],a[84:87]
T_ v[90:91],v142,34560
s_add_u32 s44,s44,s3
M_ a[88:91],v[8:11],v[56:59],a[88:91]
T_ v[92:93],v143,33536
s_mul_i32 s37,s44,0xa000
s_lshl_b32 s39,s3,12
M_ a[92:95],v[8:11],v[60:63],a[92:95]
T_ v[94:95],v143,34560
v_lshrrev_b32 v213,3,v210
M_ a[96:99],v[12:15],v[32:35],a[96:99]
s_add_u32 s55,s21,0x380000
v_mul_u32_u24 v213,0x500,v213
M_ a[100:103],v[12:15],v[36:39],a[100:103]
s_add_u32 m0,s22,0x10000
v_and_b32 v215,7,v210
v_add_u32 v214,0,v212
M_ a[104:107],v[12:15],v[40:43],a[104:107]
buffer_load_dwordx4 v132,s[8:11],s55 offen lds
v_xor_b32 v214,v214,v215
M_ a[108:111],v[12:15],v[44:47],a[108:111]
v_lshl_add_u32 v214,v214,4,v213
M_ a[112:115],v[12:15],v[48:51],a[112:115]
v_add_u32 v192,0x0,v214
M_ a[116:119],v[12:15],v[52:55],a[116:119]
v_add_u32 v194,0x4800,v214
v_add_u32 v214,4,v212
M_ a[120:123],v[12:15],v[56:59],a[120:123]
v_xor_b32 v214,v214,v215
M_ a[124:127],v[12:15],v[60:63],a[124:127]
buffer_load_dwordx4 v133,s[8:11],s55 offen offset:1024 lds
v_lshl_add_u32 v214,v214,4,v213
M_ a[128:131],v[16:19],v[32:35],a[128:131]
v_add_u32 v193,0x2400,v214
M_ a[132:135],v[16:19],v[36:39],a[132:135]
v_add_u32 v195,0x6c00,v214
s_lshl_b32 s44,s33,1
M_ a[136:139],v[16:19],v[40:43],a[136:139]
s_add_u32 s44,s44,s34
M_ a[140:143],v[16:19],v[44:47],a[140:143]
s_mul_i32 s44,s44,20
M_ a[144:147],v[16:19],v[48:51],a[144:147]
buffer_load_dwordx4 v134,s[8:11],s55 offen offset:2048 lds
s_add_u32 s44,s44,s35
s_lshl_b32 s38,s44,12
M_ a[148:151],v[16:19],v[52:55],a[148:151]
s_lshl_b32 s40,s3,12
M_ a[152:155],v[16:19],v[56:59],a[152:155]
s_add_u32 s40,s40,16384
M_ a[156:159],v[16:19],v[60:63],a[156:159]
s_lshl_b32 s44,s34,13
M_ a[160:163],v[20:23],v[32:35],a[160:163]
v_add_u32 v213,0,v212
v_lshrrev_b32 v214,1,v211
M_ a[164:167],v[20:23],v[36:39],a[164:167]
buffer_load_dwordx4 v135,s[8:11],s55 offen offset:3072 lds
v_xor_b32 v213,v213,v214
M_ a[168:171],v[20:23],v[40:43],a[168:171]
v_lshlrev_b32 v213,4,v213
M_ a[172:175],v[20:23],v[44:47],a[172:175]
v_lshl_add_u32 v213,v211,7,v213
v_add_u32 v196,s44,v213
M_ a[176:179],v[20:23],v[48:51],a[176:179]
v_add_u32 v198,0x10000,v196
M_ a[180:183],v[20:23],v[52:55],a[180:183]
s_add_u32 s55,s20,0x380
v_add_u32 v213,4,v212
M_ a[184:187],v[20:23],v[56:59],a[184:187]
s_add_u32 m0,s23,0x10000
v_lshrrev_b32 v214,1,v211
M_ a[188:191],v[20:23],v[60:63],a[188:191]
buffer_load_dwordx4 v128,s[4:7],s55 offen lds
v_xor_b32 v213,v213,v214
v_lshlrev_b32 v213,4,v213
M_ a[192:195],v[24:27],v[32:35],a[192:195]
v_lshl_add_u32 v213,v211,7,v213
M_ a[196:199],v[24:27],v[36:39],a[196:199]
v_add_u32 v197,s44,v213
M_ a[200:203],v[24:27],v[40:43],a[200:203]
v_add_u32 v199,0x10000,v197
M_ a[204:207],v[24:27],v[44:47],a[204:207]
s_lshl_b32 s44,s35,13
s_add_u32 s44,s44,16384
M_ a[208:211],v[24:27],v[48:51],a[208:211]
buffer_load_dwordx4 v129,s[4:7],s55 offen offset:1024 lds
v_add_u32 v200,s44,v208
M_ a[212:215],v[24:27],v[52:55],a[212:215]
v_add_u32 v201,0x10000,v200
M_ a[216:219],v[24:27],v[56:59],a[216:219]
s_mov_b32 s41,0x3fb8aa3b
s_mov_b32 s24,s72
M_ a[220:223],v[24:27],v[60:63],a[220:223]
s_mov_b32 s25,s73
M_ a[224:227],v[28:31],v[32:35],a[224:227]
s_and_b32 s25,s25,0xffff
M_ a[228:231],v[28:31],v[36:39],a[228:231]
buffer_load_dwordx4 v130,s[4:7],s55 offen offset:2048 lds
s_mov_b32 s26,0x280000
M_ a[232:235],v[28:31],v[40:43],a[232:235]
s_mov_b32 s27,0x20000
s_add_u32 s28,s88,0x3000000
s_addc_u32 s29,s89,0
M_ a[236:239],v[28:31],v[44:47],a[236:239]
s_and_b32 s29,s29,0xffff
M_ a[240:243],v[28:31],v[48:51],a[240:243]
s_mov_b32 s30,0x280000
M_ a[244:247],v[28:31],v[52:55],a[244:247]
s_mov_b32 s31,0x20000
M_ a[248:251],v[28:31],v[56:59],a[248:251]
buffer_load_dwordx4 v131,s[4:7],s55 offen offset:3072 lds
M_ a[252:255],v[28:31],v[60:63],a[252:255]
s_waitcnt vmcnt(17)
s_waitcnt lgkmcnt(0)
s_barrier
M_ a[0:3],v[64:67],v[96:99],a[0:3]
ds_read_b64_tr_b16 v[0:1],v140
M_ a[4:7],v[64:67],v[100:103],a[4:7]
T_ v[2:3],v140,1024
M_ a[8:11],v[64:67],v[104:107],a[8:11]
ds_read_b128 v[32:35],v136
M_ a[12:15],v[64:67],v[108:111],a[12:15]
R_ v[36:39],v136,1024
M_ a[16:19],v[64:67],v[112:115],a[16:19]
R_ v[40:43],v136,2048
s_add_u32 s62,s62,0x100000
M_ a[20:23],v[64:67],v[116:119],a[20:23]
R_ v[44:47],v136,3072
M_ a[24:27],v[64:67],v[120:123],a[24:27]
R_ v[48:51],v136,4096
M_ a[28:31],v[64:67],v[124:127],a[28:31]
R_ v[52:55],v136,5120
M_ a[32:35],v[68:71],v[96:99],a[32:35]
R_ v[56:59],v136,6144
M_ a[36:39],v[68:71],v[100:103],a[36:39]
R_ v[60:63],v136,7168
M_ a[40:43],v[68:71],v[104:107],a[40:43]
ds_read_b64_tr_b16 v[4:5],v141
M_ a[44:47],v[68:71],v[108:111],a[44:47]
T_ v[6:7],v141,1024
M_ a[48:51],v[68:71],v[112:115],a[48:51]
T_ v[8:9],v140,256
M_ a[52:55],v[68:71],v[116:119],a[52:55]
T_ v[10:11],v140,1280
M_ a[56:59],v[68:71],v[120:123],a[56:59]
T_ v[12:13],v141,256
M_ a[60:63],v[68:71],v[124:127],a[60:63]
T_ v[14:15],v141,1280
M_ a[64:67],v[72:75],v[96:99],a[64:67]
T_ v[16:17],v140,512
M_ a[68:71],v[72:75],v[100:103],a[68:71]
T_ v[18:19],v140,1536
M_ a[72:75],v[72:75],v[104:107],a[72:75]
T_ v[20:21],v141,512
M_ a[76:79],v[72:75],v[108:111],a[76:79]
T_ v[22:23],v141,1536
M_ a[80:83],v[72:75],v[112:115],a[80:83]
T_ v[24:25],v140,768
M_ a[84:87],v[72:75],v[116:119],a[84:87]
T_ v[26:27],v140,1792
M_ a[88:91],v[72:75],v[120:123],a[88:91]
T_ v[28:29],v141,768
M_ a[92:95],v[72:75],v[124:127],a[92:95]
T_ v[30:31],v141,1792
M_ a[96:99],v[76:79],v[96:99],a[96:99]
s_add_u32 s55,s21,0x3c0000
M_ a[100:103],v[76:79],v[100:103],a[100:103]
s_add_u32 m0,s22,0x18000
M_ a[104:107],v[76:79],v[104:107],a[104:107]
buffer_load_dwordx4 v132,s[8:11],s55 offen lds
M_ a[108:111],v[76:79],v[108:111],a[108:111]
M_ a[112:115],v[76:79],v[112:115],a[112:115]
M_ a[116:119],v[76:79],v[116:119],a[116:119]
M_ a[120:123],v[76:79],v[120:123],a[120:123]
M_ a[124:127],v[76:79],v[124:127],a[124:127]
buffer_load_dwordx4 v133,s[8:11],s55 offen offset:1024 lds
M_ a[128:131],v[80:83],v[96:99],a[128:131]
M_ a[132:135],v[80:83],v[100:103],a[132:135]
buffer_load_dwordx4 v[240:243],v248,s[64:67],s62 offen
M_ a[136:139],v[80:83],v[104:107],a[136:139]
M_ a[140:143],v[80:83],v[108:111],a[140:143]
M_ a[144:147],v[80:83],v[112:115],a[144:147]
buffer_load_dwordx4 v134,s[8:11],s55 offen offset:2048 lds
M_ a[148:151],v[80:83],v[116:119],a[148:151]
M_ a[152:155],v[80:83],v[120:123],a[152:155]
M_ a[156:159],v[80:83],v[124:127],a[156:159]
M_ a[160:163],v[84:87],v[96:99],a[160:163]
M_ a[164:167],v[84:87],v[100:103],a[164:167]
buffer_load_dwordx4 v135,s[8:11],s55 offen offset:3072 lds
M_ a[168:171],v[84:87],v[104:107],a[168:171]
M_ a[172:175],v[84:87],v[108:111],a[172:175]
M_ a[176:179],v[84:87],v[112:115],a[176:179]
M_ a[180:183],v[84:87],v[116:119],a[180:183]
s_add_u32 s55,s20,0x3c0
M_ a[184:187],v[84:87],v[120:123],a[184:187]
s_add_u32 m0,s23,0x18000
M_ a[188:191],v[84:87],v[124:127],a[188:191]
buffer_load_dwordx4 v128,s[4:7],s55 offen lds
M_ a[192:195],v[88:91],v[96:99],a[192:195]
M_ a[196:199],v[88:91],v[100:103],a[196:199]
M_ a[200:203],v[88:91],v[104:107],a[200:203]
M_ a[204:207],v[88:91],v[108:111],a[204:207]
M_ a[208:211],v[88:91],v[112:115],a[208:211]
buffer_load_dwordx4 v129,s[4:7],s55 offen offset:1024 lds
M_ a[212:215],v[88:91],v[116:119],a[212:215]
M_ a[216:219],v[88:91],v[120:123],a[216:219]
M_ a[220:223],v[88:91],v[124:127],a[220:223]
M_ a[224:227],v[92:95],v[96:99],a[224:227]
M_ a[228:231],v[92:95],v[100:103],a[228:231]
buffer_load_dwordx4 v130,s[4:7],s55 offen offset:2048 lds
M_ a[232:235],v[92:95],v[104:107],a[232:235]
M_ a[236:239],v[92:95],v[108:111],a[236:239]
M_ a[240:243],v[92:95],v[112:115],a[240:243]
M_ a[244:247],v[92:95],v[116:119],a[244:247]
M_ a[248:251],v[92:95],v[120:123],a[248:251]
buffer_load_dwordx4 v131,s[4:7],s55 offen offset:3072 lds
M_ a[252:255],v[92:95],v[124:127],a[252:255]
s_waitcnt vmcnt(18)
s_waitcnt lgkmcnt(0)
s_barrier
M_ a[0:3],v[0:3],v[32:35],a[0:3]
T_ v[64:65],v140,32768
M_ a[4:7],v[0:3],v[36:39],a[4:7]
T_ v[66:67],v140,33792
M_ a[8:11],v[0:3],v[40:43],a[8:11]
R_ v[96:99],v136,32768
M_ a[12:15],v[0:3],v[44:47],a[12:15]
R_ v[100:103],v136,33792
M_ a[16:19],v[0:3],v[48:51],a[16:19]
R_ v[104:107],v136,34816
s_add_u32 s62,s62,0x100000
M_ a[20:23],v[0:3],v[52:55],a[20:23]
R_ v[108:111],v136,35840
M_ a[24:27],v[0:3],v[56:59],a[24:27]
R_ v[112:115],v136,36864
M_ a[28:31],v[0:3],v[60:63],a[28:31]
R_ v[116:119],v136,37888
M_ a[32:35],v[4:7],v[32:35],a[32:35]
R_ v[120:123],v136,38912
M_ a[36:39],v[4:7],v[36:39],a[36:39]
R_ v[124:127],v136,39936
M_ a[40:43],v[4:7],v[40:43],a[40:43]
T_ v[68:69],v141,32768
M_ a[44:47],v[4:7],v[44:47],a[44:47]
T_ v[70:71],v141,33792
M_ a[48:51],v[4:7],v[48:51],a[48:51]
T_ v[72:73],v140,33024
buffer_load_dwordx4 v[240:243],v248,s[64:67],s62 offen
M_ a[52:55],v[4:7],v[52:55],a[52:55]
T_ v[74:75],v140,34048
M_ a[56:59],v[4:7],v[56:59],a[56:59]
T_ v[76:77],v141,33024
M_ a[60:63],v[4:7],v[60:63],a[60:63]
T_ v[78:79],v141,34048
M_ a[64:67],v[8:11],v[32:35],a[64:67]
T_ v[80:81],v140,33280
M_ a[68:71],v[8:11],v[36:39],a[68:71]
T_ v[82:83],v140,34304
M_ a[72:75],v[8:11],v[40:43],a[72:75]
T_ v[84:85],v141,33280
M_ a[76:79],v[8:11],v[44:47],a[76:79]
T_ v[86:87],v141,34304
M_ a[80:83],v[8:11],v[48:51],a[80:83]
T_ v[88:89],v140,33536
v_mbcnt_lo_u32_b32 v228,-1,0
M_ a[84:87],v[8:11],v[52:55],a[84:87]
T_ v[90:91],v140,34560
M_ a[88:91],v[8:11],v[56:59],a[88:91]
T_ v[92:93],v141,33536
M_ a[92:95],v[8:11],v[60:63],a[92:95]
T_ v[94:95],v141,34560
M_ a[96:99],v[12:15],v[32:35],a[96:99]
M_ a[100:103],v[12:15],v[36:39],a[100:103]
M_ a[104:107],v[12:15],v[40:43],a[104:107]
M_ a[108:111],v[12:15],v[44:47],a[108:111]
M_ a[112:115],v[12:15],v[48:51],a[112:115]
v_mbcnt_hi_u32_b32 v228,-1,v228
M_ a[116:119],v[12:15],v[52:55],a[116:119]
M_ a[120:123],v[12:15],v[56:59],a[120:123]
M_ a[124:127],v[12:15],v[60:63],a[124:127]
M_ a[128:131],v[16:19],v[32:35],a[128:131]
M_ a[132:135],v[16:19],v[36:39],a[132:135]
M_ a[136:139],v[16:19],v[40:43],a[136:139]
M_ a[140:143],v[16:19],v[44:47],a[140:143]
M_ a[144:147],v[16:19],v[48:51],a[144:147]
M_ a[148:151],v[16:19],v[52:55],a[148:151]
v_lshlrev_b32 v228,4,v228
M_ a[152:155],v[16:19],v[56:59],a[152:155]
M_ a[156:159],v[16:19],v[60:63],a[156:159]
M_ a[160:163],v[20:23],v[32:35],a[160:163]
M_ a[164:167],v[20:23],v[36:39],a[164:167]
M_ a[168:171],v[20:23],v[40:43],a[168:171]
M_ a[172:175],v[20:23],v[44:47],a[172:175]
M_ a[176:179],v[20:23],v[48:51],a[176:179]
M_ a[180:183],v[20:23],v[52:55],a[180:183]
v_add_u32 v228,0x6400000,v228
M_ a[184:187],v[20:23],v[56:59],a[184:187]
M_ a[188:191],v[20:23],v[60:63],a[188:191]
M_ a[192:195],v[24:27],v[32:35],a[192:195]
M_ a[196:199],v[24:27],v[36:39],a[196:199]
M_ a[200:203],v[24:27],v[40:43],a[200:203]
M_ a[204:207],v[24:27],v[44:47],a[204:207]
M_ a[208:211],v[24:27],v[48:51],a[208:211]
M_ a[212:215],v[24:27],v[52:55],a[212:215]
global_load_dwordx4 v[224:227],v228,s[88:89] sc1
M_ a[216:219],v[24:27],v[56:59],a[216:219]
M_ a[220:223],v[24:27],v[60:63],a[220:223]
M_ a[224:227],v[28:31],v[32:35],a[224:227]
M_ a[228:231],v[28:31],v[36:39],a[228:231]
M_ a[232:235],v[28:31],v[40:43],a[232:235]
M_ a[236:239],v[28:31],v[44:47],a[236:239]
M_ a[240:243],v[28:31],v[48:51],a[240:243]
M_ a[244:247],v[28:31],v[52:55],a[244:247]
M_ a[248:251],v[28:31],v[56:59],a[248:251]
M_ a[252:255],v[28:31],v[60:63],a[252:255]
s_waitcnt vmcnt(11)
s_waitcnt lgkmcnt(0)
s_barrier
M_ a[0:3],v[64:67],v[96:99],a[0:3]
ds_read_b64_tr_b16 v[0:1],v142
M_ a[4:7],v[64:67],v[100:103],a[4:7]
T_ v[2:3],v142,1024
M_ a[8:11],v[64:67],v[104:107],a[8:11]
ds_read_b128 v[32:35],v137
M_ a[12:15],v[64:67],v[108:111],a[12:15]
R_ v[36:39],v137,1024
M_ a[16:19],v[64:67],v[112:115],a[16:19]
R_ v[40:43],v137,2048
s_add_u32 s62,s62,0x100000
M_ a[20:23],v[64:67],v[116:119],a[20:23]
R_ v[44:47],v137,3072
M_ a[24:27],v[64:67],v[120:123],a[24:27]
R_ v[48:51],v137,4096
M_ a[28:31],v[64:67],v[124:127],a[28:31]
R_ v[52:55],v137,5120
M_ a[32:35],v[68:71],v[96:99],a[32:35]
R_ v[56:59],v137,6144
M_ a[36:39],v[68:71],v[100:103],a[36:39]
R_ v[60:63],v137,7168
M_ a[40:43],v[68:71],v[104:107],a[40:43]
ds_read_b64_tr_b16 v[4:5],v143
)A"
R"A(M_ a[44:47],v[68:71],v[108:111],a[44:47]
T_ v[6:7],v143,1024
M_ a[48:51],v[68:71],v[112:115],a[48:51]
T_ v[8:9],v142,256
M_ a[52:55],v[68:71],v[116:119],a[52:55]
T_ v[10:11],v142,1280
M_ a[56:59],v[68:71],v[120:123],a[56:59]
T_ v[12:13],v143,256
M_ a[60:63],v[68:71],v[124:127],a[60:63]
T_ v[14:15],v143,1280
M_ a[64:67],v[72:75],v[96:99],a[64:67]
T_ v[16:17],v142,512
M_ a[68:71],v[72:75],v[100:103],a[68:71]
T_ v[18:19],v142,1536
M_ a[72:75],v[72:75],v[104:107],a[72:75]
T_ v[20:21],v143,512
M_ a[76:79],v[72:75],v[108:111],a[76:79]
T_ v[22:23],v143,1536
M_ a[80:83],v[72:75],v[112:115],a[80:83]
T_ v[24:25],v142,768
M_ a[84:87],v[72:75],v[116:119],a[84:87]
T_ v[26:27],v142,1792
M_ a[88:91],v[72:75],v[120:123],a[88:91]
T_ v[28:29],v143,768
M_ a[92:95],v[72:75],v[124:127],a[92:95]
T_ v[30:31],v143,1792
M_ a[96:99],v[76:79],v[96:99],a[96:99]
M_ a[100:103],v[76:79],v[100:103],a[100:103]
M_ a[104:107],v[76:79],v[104:107],a[104:107]
M_ a[108:111],v[76:79],v[108:111],a[108:111]
M_ a[112:115],v[76:79],v[112:115],a[112:115]
M_ a[116:119],v[76:79],v[116:119],a[116:119]
M_ a[120:123],v[76:79],v[120:123],a[120:123]
M_ a[124:127],v[76:79],v[124:127],a[124:127]
M_ a[128:131],v[80:83],v[96:99],a[128:131]
M_ a[132:135],v[80:83],v[100:103],a[132:135]
buffer_load_dwordx4 v[240:243],v248,s[64:67],s62 offen
M_ a[136:139],v[80:83],v[104:107],a[136:139]
M_ a[140:143],v[80:83],v[108:111],a[140:143]
M_ a[144:147],v[80:83],v[112:115],a[144:147]
M_ a[148:151],v[80:83],v[116:119],a[148:151]
M_ a[152:155],v[80:83],v[120:123],a[152:155]
M_ a[156:159],v[80:83],v[124:127],a[156:159]
M_ a[160:163],v[84:87],v[96:99],a[160:163]
M_ a[164:167],v[84:87],v[100:103],a[164:167]
M_ a[168:171],v[84:87],v[104:107],a[168:171]
M_ a[172:175],v[84:87],v[108:111],a[172:175]
M_ a[176:179],v[84:87],v[112:115],a[176:179]
M_ a[180:183],v[84:87],v[116:119],a[180:183]
M_ a[184:187],v[84:87],v[120:123],a[184:187]
M_ a[188:191],v[84:87],v[124:127],a[188:191]
M_ a[192:195],v[88:91],v[96:99],a[192:195]
M_ a[196:199],v[88:91],v[100:103],a[196:199]
M_ a[200:203],v[88:91],v[104:107],a[200:203]
M_ a[204:207],v[88:91],v[108:111],a[204:207]
M_ a[208:211],v[88:91],v[112:115],a[208:211]
M_ a[212:215],v[88:91],v[116:119],a[212:215]
M_ a[216:219],v[88:91],v[120:123],a[216:219]
M_ a[220:223],v[88:91],v[124:127],a[220:223]
M_ a[224:227],v[92:95],v[96:99],a[224:227]
M_ a[228:231],v[92:95],v[100:103],a[228:231]
M_ a[232:235],v[92:95],v[104:107],a[232:235]
M_ a[236:239],v[92:95],v[108:111],a[236:239]
M_ a[240:243],v[92:95],v[112:115],a[240:243]
M_ a[244:247],v[92:95],v[116:119],a[244:247]
M_ a[248:251],v[92:95],v[120:123],a[248:251]
M_ a[252:255],v[92:95],v[124:127],a[252:255]
s_waitcnt vmcnt(1)
v_xor_b32 v224,s86,v224
v_xor_b32 v225,s86,v225
v_xor_b32 v226,s86,v226
v_xor_b32 v227,s86,v227
v_or3_b32 v224,v224,v225,v226
v_or_b32 v224,v224,v227
v_cmp_ne_u32 vcc,0,v224
s_cbranch_vccz LQD5x21_%=
s_mov_b32 s59,0x30d40
LQL5x21_%=:
s_sleep 2
global_load_dwordx4 v[224:227],v228,s[88:89] sc1
s_waitcnt vmcnt(0)
v_xor_b32 v224,s86,v224
v_xor_b32 v225,s86,v225
v_xor_b32 v226,s86,v226
v_xor_b32 v227,s86,v227
v_or3_b32 v224,v224,v225,v226
v_or_b32 v224,v224,v227
v_cmp_ne_u32 vcc,0,v224
s_cbranch_vccz LQD5x21_%=
s_sub_u32 s59,s59,1
s_cmp_lg_u32 s59,0
s_cbranch_scc1 LQL5x21_%=
LQD5x21_%=:
s_waitcnt vmcnt(3)
s_waitcnt lgkmcnt(0)
s_barrier
M_ a[0:3],v[0:3],v[32:35],a[0:3]
T_ v[64:65],v142,32768
M_ a[4:7],v[0:3],v[36:39],a[4:7]
T_ v[66:67],v142,33792
M_ a[8:11],v[0:3],v[40:43],a[8:11]
R_ v[96:99],v137,32768
M_ a[12:15],v[0:3],v[44:47],a[12:15]
R_ v[100:103],v137,33792
M_ a[16:19],v[0:3],v[48:51],a[16:19]
R_ v[104:107],v137,34816
M_ a[20:23],v[0:3],v[52:55],a[20:23]
R_ v[108:111],v137,35840
M_ a[24:27],v[0:3],v[56:59],a[24:27]
R_ v[112:115],v137,36864
M_ a[28:31],v[0:3],v[60:63],a[28:31]
R_ v[116:119],v137,37888
M_ a[32:35],v[4:7],v[32:35],a[32:35]
R_ v[120:123],v137,38912
M_ a[36:39],v[4:7],v[36:39],a[36:39]
R_ v[124:127],v137,39936
M_ a[40:43],v[4:7],v[40:43],a[40:43]
T_ v[68:69],v143,32768
M_ a[44:47],v[4:7],v[44:47],a[44:47]
T_ v[70:71],v143,33792
M_ a[48:51],v[4:7],v[48:51],a[48:51]
T_ v[72:73],v142,33024
M_ a[52:55],v[4:7],v[52:55],a[52:55]
T_ v[74:75],v142,34048
M_ a[56:59],v[4:7],v[56:59],a[56:59]
T_ v[76:77],v143,33024
M_ a[60:63],v[4:7],v[60:63],a[60:63]
T_ v[78:79],v143,34048
M_ a[64:67],v[8:11],v[32:35],a[64:67]
T_ v[80:81],v142,33280
M_ a[68:71],v[8:11],v[36:39],a[68:71]
T_ v[82:83],v142,34304
M_ a[72:75],v[8:11],v[40:43],a[72:75]
T_ v[84:85],v143,33280
M_ a[76:79],v[8:11],v[44:47],a[76:79]
T_ v[86:87],v143,34304
M_ a[80:83],v[8:11],v[48:51],a[80:83]
T_ v[88:89],v142,33536
M_ a[84:87],v[8:11],v[52:55],a[84:87]
T_ v[90:91],v142,34560
M_ a[88:91],v[8:11],v[56:59],a[88:91]
T_ v[92:93],v143,33536
M_ a[92:95],v[8:11],v[60:63],a[92:95]
T_ v[94:95],v143,34560
M_ a[96:99],v[12:15],v[32:35],a[96:99]
s_add_u32 s44,s37,0x0
M_ a[100:103],v[12:15],v[36:39],a[100:103]
s_add_u32 m0,s39,0x0
M_ a[104:107],v[12:15],v[40:43],a[104:107]
buffer_load_dwordx4 v192,s[24:27],s44 offen lds
M_ a[108:111],v[12:15],v[44:47],a[108:111]
M_ a[112:115],v[12:15],v[48:51],a[112:115]
M_ a[116:119],v[12:15],v[52:55],a[116:119]
M_ a[120:123],v[12:15],v[56:59],a[120:123]
M_ a[124:127],v[12:15],v[60:63],a[124:127]
buffer_load_dwordx4 v193,s[24:27],s44 offen offset:1024 lds
M_ a[128:131],v[16:19],v[32:35],a[128:131]
M_ a[132:135],v[16:19],v[36:39],a[132:135]
M_ a[136:139],v[16:19],v[40:43],a[136:139]
M_ a[140:143],v[16:19],v[44:47],a[140:143]
M_ a[144:147],v[16:19],v[48:51],a[144:147]
buffer_load_dwordx4 v194,s[24:27],s44 offen offset:2048 lds
M_ a[148:151],v[16:19],v[52:55],a[148:151]
M_ a[152:155],v[16:19],v[56:59],a[152:155]
M_ a[156:159],v[16:19],v[60:63],a[156:159]
M_ a[160:163],v[20:23],v[32:35],a[160:163]
M_ a[164:167],v[20:23],v[36:39],a[164:167]
buffer_load_dwordx4 v195,s[24:27],s44 offen offset:3072 lds
M_ a[168:171],v[20:23],v[40:43],a[168:171]
M_ a[172:175],v[20:23],v[44:47],a[172:175]
M_ a[176:179],v[20:23],v[48:51],a[176:179]
M_ a[180:183],v[20:23],v[52:55],a[180:183]
s_add_u32 s44,s38,0x0
M_ a[184:187],v[20:23],v[56:59],a[184:187]
s_add_u32 m0,s40,0x0
M_ a[188:191],v[20:23],v[60:63],a[188:191]
buffer_load_dwordx4 v208,s[28:31],s44 offen sc1 lds
M_ a[192:195],v[24:27],v[32:35],a[192:195]
M_ a[196:199],v[24:27],v[36:39],a[196:199]
M_ a[200:203],v[24:27],v[40:43],a[200:203]
M_ a[204:207],v[24:27],v[44:47],a[204:207]
M_ a[208:211],v[24:27],v[48:51],a[208:211]
buffer_load_dwordx4 v208,s[28:31],s44 offen offset:1024 sc1 lds
M_ a[212:215],v[24:27],v[52:55],a[212:215]
M_ a[216:219],v[24:27],v[56:59],a[216:219]
M_ a[220:223],v[24:27],v[60:63],a[220:223]
M_ a[224:227],v[28:31],v[32:35],a[224:227]
M_ a[228:231],v[28:31],v[36:39],a[228:231]
buffer_load_dwordx4 v208,s[28:31],s44 offen offset:2048 sc1 lds
M_ a[232:235],v[28:31],v[40:43],a[232:235]
M_ a[236:239],v[28:31],v[44:47],a[236:239]
M_ a[240:243],v[28:31],v[48:51],a[240:243]
M_ a[244:247],v[28:31],v[52:55],a[244:247]
M_ a[248:251],v[28:31],v[56:59],a[248:251]
buffer_load_dwordx4 v208,s[28:31],s44 offen offset:3072 sc1 lds
M_ a[252:255],v[28:31],v[60:63],a[252:255]
s_waitcnt lgkmcnt(0)
M_ a[0:3],v[64:67],v[96:99],a[0:3]
M_ a[4:7],v[64:67],v[100:103],a[4:7]
M_ a[8:11],v[64:67],v[104:107],a[8:11]
M_ a[12:15],v[64:67],v[108:111],a[12:15]
M_ a[16:19],v[64:67],v[112:115],a[16:19]
M_ a[20:23],v[64:67],v[116:119],a[20:23]
M_ a[24:27],v[64:67],v[120:123],a[24:27]
M_ a[28:31],v[64:67],v[124:127],a[28:31]
M_ a[32:35],v[68:71],v[96:99],a[32:35]
M_ a[36:39],v[68:71],v[100:103],a[36:39]
M_ a[40:43],v[68:71],v[104:107],a[40:43]
M_ a[44:47],v[68:71],v[108:111],a[44:47]
M_ a[48:51],v[68:71],v[112:115],a[48:51]
M_ a[52:55],v[68:71],v[116:119],a[52:55]
M_ a[56:59],v[68:71],v[120:123],a[56:59]
M_ a[60:63],v[68:71],v[124:127],a[60:63]
M_ a[64:67],v[72:75],v[96:99],a[64:67]
M_ a[68:71],v[72:75],v[100:103],a[68:71]
M_ a[72:75],v[72:75],v[104:107],a[72:75]
M_ a[76:79],v[72:75],v[108:111],a[76:79]
M_ a[80:83],v[72:75],v[112:115],a[80:83]
M_ a[84:87],v[72:75],v[116:119],a[84:87]
M_ a[88:91],v[72:75],v[120:123],a[88:91]
M_ a[92:95],v[72:75],v[124:127],a[92:95]
M_ a[96:99],v[76:79],v[96:99],a[96:99]
s_add_u32 s44,s37,0x80
M_ a[100:103],v[76:79],v[100:103],a[100:103]
s_add_u32 m0,s39,0x8000
M_ a[104:107],v[76:79],v[104:107],a[104:107]
buffer_load_dwordx4 v192,s[24:27],s44 offen lds
M_ a[108:111],v[76:79],v[108:111],a[108:111]
M_ a[112:115],v[76:79],v[112:115],a[112:115]
M_ a[116:119],v[76:79],v[116:119],a[116:119]
M_ a[120:123],v[76:79],v[120:123],a[120:123]
M_ a[124:127],v[76:79],v[124:127],a[124:127]
buffer_load_dwordx4 v193,s[24:27],s44 offen offset:1024 lds
M_ a[128:131],v[80:83],v[96:99],a[128:131]
M_ a[132:135],v[80:83],v[100:103],a[132:135]
M_ a[136:139],v[80:83],v[104:107],a[136:139]
M_ a[140:143],v[80:83],v[108:111],a[140:143]
M_ a[144:147],v[80:83],v[112:115],a[144:147]
buffer_load_dwordx4 v194,s[24:27],s44 offen offset:2048 lds
M_ a[148:151],v[80:83],v[116:119],a[148:151]
M_ a[152:155],v[80:83],v[120:123],a[152:155]
M_ a[156:159],v[80:83],v[124:127],a[156:159]
M_ a[160:163],v[84:87],v[96:99],a[160:163]
M_ a[164:167],v[84:87],v[100:103],a[164:167]
buffer_load_dwordx4 v195,s[24:27],s44 offen offset:3072 lds
M_ a[168:171],v[84:87],v[104:107],a[168:171]
M_ a[172:175],v[84:87],v[108:111],a[172:175]
M_ a[176:179],v[84:87],v[112:115],a[176:179]
M_ a[180:183],v[84:87],v[116:119],a[180:183]
s_add_u32 s44,s38,0x2000
M_ a[184:187],v[84:87],v[120:123],a[184:187]
s_add_u32 m0,s40,0x8000
M_ a[188:191],v[84:87],v[124:127],a[188:191]
buffer_load_dwordx4 v208,s[28:31],s44 offen sc1 lds
M_ a[192:195],v[88:91],v[96:99],a[192:195]
M_ a[196:199],v[88:91],v[100:103],a[196:199]
M_ a[200:203],v[88:91],v[104:107],a[200:203]
M_ a[204:207],v[88:91],v[108:111],a[204:207]
M_ a[208:211],v[88:91],v[112:115],a[208:211]
buffer_load_dwordx4 v208,s[28:31],s44 offen offset:1024 sc1 lds
M_ a[212:215],v[88:91],v[116:119],a[212:215]
M_ a[216:219],v[88:91],v[120:123],a[216:219]
M_ a[220:223],v[88:91],v[124:127],a[220:223]
M_ a[224:227],v[92:95],v[96:99],a[224:227]
M_ a[228:231],v[92:95],v[100:103],a[228:231]
buffer_load_dwordx4 v208,s[28:31],s44 offen offset:2048 sc1 lds
M_ a[232:235],v[92:95],v[104:107],a[232:235]
M_ a[236:239],v[92:95],v[108:111],a[236:239]
M_ a[240:243],v[92:95],v[112:115],a[240:243]
M_ a[244:247],v[92:95],v[116:119],a[244:247]
M_ a[248:251],v[92:95],v[120:123],a[248:251]
buffer_load_dwordx4 v208,s[28:31],s44 offen offset:3072 sc1 lds
M_ a[252:255],v[92:95],v[124:127],a[252:255]
s_waitcnt vmcnt(8)
s_barrier
ds_read_b128 v[32:35],v200
R_ v[36:39],v200,4096
ds_read_b128 v[0:3],v196
ds_read_b128 v[4:7],v197
R_ v[8:11],v196,2048
R_ v[12:15],v197,2048
R_ v[16:19],v196,4096
R_ v[20:23],v197,4096
R_ v[24:27],v196,6144
R_ v[28:31],v197,6144
R_ v[40:43],v200,1024
R_ v[44:47],v200,5120
R_ v[48:51],v200,2048
R_ v[52:55],v200,6144
R_ v[56:59],v200,3072
R_ v[60:63],v200,7168
s_waitcnt vmcnt(0)
s_waitcnt lgkmcnt(0)
s_barrier
M_ v[128:131],v[32:35],v[0:3],0
s_add_u32 s44,s37,0x100
s_mov_b32 s4,s68
s_mov_b32 s5,s69
s_and_b32 s5,s5,0xffff
M_ v[128:131],v[36:39],v[4:7],v[128:131]
s_add_u32 m0,s39,0x10000
R_ v[96:99],v200,32768
s_mov_b32 s6,0x280000
s_mov_b32 s7,0x20000
M_ v[132:135],v[32:35],v[8:11],0
buffer_load_dwordx4 v192,s[24:27],s44 offen lds
s_mov_b32 s8,s70
s_mov_b32 s9,s71
M_ v[132:135],v[36:39],v[12:15],v[132:135]
R_ v[100:103],v200,36864
s_and_b32 s9,s9,0xffff
s_mov_b32 s10,0x500000
s_mov_b32 s11,0x20000
M_ v[136:139],v[32:35],v[16:19],0
buffer_load_dwordx4 v193,s[24:27],s44 offen offset:1024 lds
v_mbcnt_lo_u32_b32 v210,-1,0
v_mbcnt_hi_u32_b32 v210,-1,v210
M_ v[136:139],v[36:39],v[20:23],v[136:139]
R_ v[64:67],v196,32768
v_lshlrev_b32 v208,4,v210
v_and_b32 v211,15,v210
M_ v[140:143],v[32:35],v[24:27],0
buffer_load_dwordx4 v194,s[24:27],s44 offen offset:2048 lds
v_lshrrev_b32 v212,4,v210
s_and_b32 s54,s2,7
s_lshr_b32 s55,s2,3
M_ v[140:143],v[36:39],v[28:31],v[140:143]
R_ v[68:71],v197,32768
s_and_b32 s19,s55,7
s_lshr_b32 s55,s55,3
M_ v[144:147],v[40:43],v[0:3],0
buffer_load_dwordx4 v195,s[24:27],s44 offen offset:3072 lds
s_lshl_b32 s55,s55,3
s_add_u32 s18,s55,s54
M_ v[144:147],v[44:47],v[4:7],v[144:147]
R_ v[72:75],v196,34816
s_add_u32 s44,s38,0x4000
s_add_u32 m0,s40,0x10000
s_nop 0
buffer_load_dwordx4 v208,s[28:31],s44 offen sc1 lds
s_lshr_b32 s73,s3,1
s_lshl_b32 s75,s3,12
s_and_b32 s56,s3,1
M_ v[148:151],v[40:43],v[8:11],0
s_lshl_b32 s56,s56,2
s_sub_u32 s54,15,s19
M_ v[148:151],v[44:47],v[12:15],v[148:151]
R_ v[76:79],v197,34816
buffer_load_dwordx4 v208,s[28:31],s44 offen offset:1024 sc1 lds
s_lshl_b32 s54,s54,3
s_add_u32 s70,s54,s56
M_ v[152:155],v[40:43],v[16:19],0
s_lshl_b32 s54,s19,3
s_add_u32 s71,s54,s56
s_cmp_eq_u32 s73,0
s_cselect_b32 s72,s70,s71
M_ v[152:155],v[44:47],v[20:23],v[152:155]
R_ v[80:83],v196,36864
buffer_load_dwordx4 v208,s[28:31],s44 offen offset:2048 sc1 lds
s_mul_i32 s68,s72,0x5000
s_mul_i32 s69,s3,0x20000
M_ v[156:159],v[40:43],v[24:27],0
s_lshl_b32 s54,s18,8
s_add_u32 s69,s69,s54
M_ v[156:159],v[44:47],v[28:31],v[156:159]
R_ v[84:87],v197,36864
buffer_load_dwordx4 v208,s[28:31],s44 offen offset:3072 sc1 lds
v_lshrrev_b32 v213,3,v210
v_mul_u32_u24 v213,0x500,v213
M_ v[160:163],v[48:51],v[0:3],0
s_add_u32 s44,s37,0x180
v_and_b32 v215,7,v210
v_add_u32 v214,0,v212
v_xor_b32 v214,v214,v215
M_ v[160:163],v[52:55],v[4:7],v[160:163]
R_ v[88:91],v196,38912
s_add_u32 m0,s39,0x18000
s_nop 0
buffer_load_dwordx4 v192,s[24:27],s44 offen lds
v_lshl_add_u32 v214,v214,4,v213
v_add_u32 v224,0x0,v214
M_ v[164:167],v[48:51],v[8:11],0
v_add_u32 v226,0x4800,v214
v_add_u32 v214,4,v212
M_ v[164:167],v[52:55],v[12:15],v[164:167]
R_ v[92:95],v197,38912
buffer_load_dwordx4 v193,s[24:27],s44 offen offset:1024 lds
v_xor_b32 v214,v214,v215
v_lshl_add_u32 v214,v214,4,v213
v_add_u32 v225,0x2400,v214
M_ v[168:171],v[48:51],v[16:19],0
v_add_u32 v227,0x6c00,v214
v_lshrrev_b32 v213,2,v210
M_ v[168:171],v[52:55],v[20:23],v[168:171]
R_ v[104:107],v200,33792
buffer_load_dwordx4 v194,s[24:27],s44 offen offset:2048 lds
v_and_b32 v213,3,v213
v_lshlrev_b32 v213,13,v213
M_ v[172:175],v[48:51],v[24:27],0
v_and_b32 v215,3,v210
v_xor_b32 v214,0,v215
)A"
R"A(v_lshl_add_u32 v214,v212,2,v214
M_ v[172:175],v[52:55],v[28:31],v[172:175]
R_ v[108:111],v200,37888
buffer_load_dwordx4 v195,s[24:27],s44 offen offset:3072 lds
v_lshl_add_u32 v214,v214,4,v213
v_add_u32 v228,0x0,v214
M_ v[176:179],v[56:59],v[0:3],0
s_add_u32 s44,s38,0x6000
s_add_u32 m0,s40,0x18000
s_nop 0
buffer_load_dwordx4 v208,s[28:31],s44 offen sc1 lds
v_add_u32 v229,0x7c00,v214
v_xor_b32 v214,2,v215
M_ v[176:179],v[60:63],v[4:7],v[176:179]
R_ v[112:115],v200,34816
v_lshl_add_u32 v214,v212,2,v214
v_lshl_add_u32 v214,v214,4,v213
v_add_u32 v230,0xf800,v214
M_ v[180:183],v[56:59],v[8:11],0
buffer_load_dwordx4 v208,s[28:31],s44 offen offset:1024 sc1 lds
v_add_u32 v231,0x17400,v214
s_lshl_b32 s52,s18,20
M_ v[180:183],v[60:63],v[12:15],v[180:183]
R_ v[116:119],v200,38912
s_lshl_b32 s53,s19,17
s_add_u32 s52,s52,s53
M_ v[184:187],v[56:59],v[16:19],0
buffer_load_dwordx4 v208,s[28:31],s44 offen offset:2048 sc1 lds
s_and_b32 s53,s3,1
s_lshl_b32 s53,s53,16
s_add_u32 s52,s52,s53
M_ v[184:187],v[60:63],v[20:23],v[184:187]
R_ v[120:123],v200,35840
s_add_u32 s52,s52,0x0
s_add_u32 s92,s88,s52
s_addc_u32 s93,s89,0
M_ v[188:191],v[56:59],v[24:27],0
buffer_load_dwordx4 v208,s[28:31],s44 offen offset:3072 sc1 lds
s_add_u32 s94,s92,32768
s_addc_u32 s95,s93,0
AR_ v210,a0
M_ v[188:191],v[60:63],v[28:31],v[188:191]
R_ v[124:127],v200,39936
AR_ v211,a1
AR_ v212,a2
s_waitcnt vmcnt(8)
s_waitcnt lgkmcnt(0)
s_barrier
M_ v[128:131],v[96:99],v[64:67],v[128:131]
s_add_u32 s44,s37,0x200
AR_ v213,a3
AR_ v214,a32
AR_ v215,a33
M_ v[128:131],v[100:103],v[68:71],v[128:131]
s_add_u32 m0,s39,0x0
ds_read_b128 v[32:35],v201
AR_ v216,a34
AR_ v217,a35
M_ v[132:135],v[96:99],v[72:75],v[132:135]
buffer_load_dwordx4 v192,s[24:27],s44 offen lds
C_ v204,v210,v211
C_ v205,v212,v213
M_ v[132:135],v[100:103],v[76:79],v[132:135]
R_ v[36:39],v201,4096
C_ v206,v214,v215
C_ v207,v216,v217
AW_ a0,v204
M_ v[136:139],v[96:99],v[80:83],v[136:139]
AW_ a1,v205
AW_ a2,v206
M_ v[136:139],v[100:103],v[84:87],v[136:139]
ds_read_b128 v[0:3],v198
AW_ a3,v207
AR_ v244,a64
M_ v[140:143],v[96:99],v[88:91],v[140:143]
buffer_load_dwordx4 v193,s[24:27],s44 offen offset:1024 lds
AR_ v245,a65
AR_ v246,a66
AR_ v247,a67
M_ v[140:143],v[100:103],v[92:95],v[140:143]
ds_read_b128 v[4:7],v199
AR_ v248,a96
AR_ v249,a97
M_ v[144:147],v[104:107],v[64:67],v[144:147]
AR_ v202,a98
AR_ v203,a99
M_ v[144:147],v[108:111],v[68:71],v[144:147]
R_ v[8:11],v198,2048
C_ v218,v244,v245
C_ v219,v246,v247
C_ v220,v248,v249
M_ v[148:151],v[104:107],v[72:75],v[148:151]
buffer_load_dwordx4 v194,s[24:27],s44 offen offset:2048 lds
C_ v221,v202,v203
AW_ a64,v218
M_ v[148:151],v[108:111],v[76:79],v[148:151]
R_ v[12:15],v199,2048
AW_ a65,v219
AW_ a66,v220
M_ v[152:155],v[104:107],v[80:83],v[152:155]
AW_ a67,v221
AR_ v210,a128
AR_ v211,a129
M_ v[152:155],v[108:111],v[84:87],v[152:155]
R_ v[16:19],v198,4096
AR_ v212,a130
AR_ v213,a131
M_ v[156:159],v[104:107],v[88:91],v[156:159]
buffer_load_dwordx4 v195,s[24:27],s44 offen offset:3072 lds
AR_ v214,a160
AR_ v215,a161
M_ v[156:159],v[108:111],v[92:95],v[156:159]
R_ v[20:23],v199,4096
AR_ v216,a162
AR_ v217,a163
M_ v[160:163],v[112:115],v[64:67],v[160:163]
s_add_u32 s44,s38,0x8000
C_ v204,v210,v211
C_ v205,v212,v213
C_ v206,v214,v215
M_ v[160:163],v[116:119],v[68:71],v[160:163]
s_add_u32 m0,s40,0x0
R_ v[24:27],v198,6144
C_ v207,v216,v217
AW_ a128,v204
M_ v[164:167],v[112:115],v[72:75],v[164:167]
buffer_load_dwordx4 v208,s[28:31],s44 offen sc1 lds
AW_ a129,v205
AW_ a130,v206
M_ v[164:167],v[116:119],v[76:79],v[164:167]
R_ v[28:31],v199,6144
AW_ a131,v207
AR_ v244,a192
AR_ v245,a193
M_ v[168:171],v[112:115],v[80:83],v[168:171]
AR_ v246,a194
AR_ v247,a195
M_ v[168:171],v[116:119],v[84:87],v[168:171]
R_ v[40:43],v201,1024
AR_ v248,a224
AR_ v249,a225
M_ v[172:175],v[112:115],v[88:91],v[172:175]
buffer_load_dwordx4 v208,s[28:31],s44 offen offset:1024 sc1 lds
AR_ v202,a226
AR_ v203,a227
C_ v218,v244,v245
M_ v[172:175],v[116:119],v[92:95],v[172:175]
R_ v[44:47],v201,5120
C_ v219,v246,v247
C_ v220,v248,v249
M_ v[176:179],v[120:123],v[64:67],v[176:179]
C_ v221,v202,v203
AW_ a192,v218
M_ v[176:179],v[124:127],v[68:71],v[176:179]
R_ v[48:51],v201,2048
AW_ a193,v219
AW_ a194,v220
AW_ a195,v221
M_ v[180:183],v[120:123],v[72:75],v[180:183]
buffer_load_dwordx4 v208,s[28:31],s44 offen offset:2048 sc1 lds
AR_ v210,a4
AR_ v211,a5
M_ v[180:183],v[124:127],v[76:79],v[180:183]
R_ v[52:55],v201,6144
AR_ v212,a6
AR_ v213,a7
M_ v[184:187],v[120:123],v[80:83],v[184:187]
AR_ v214,a36
AR_ v215,a37
AR_ v216,a38
M_ v[184:187],v[124:127],v[84:87],v[184:187]
R_ v[56:59],v201,3072
AR_ v217,a39
C_ v204,v210,v211
M_ v[188:191],v[120:123],v[88:91],v[188:191]
buffer_load_dwordx4 v208,s[28:31],s44 offen offset:3072 sc1 lds
C_ v205,v212,v213
C_ v206,v214,v215
M_ v[188:191],v[124:127],v[92:95],v[188:191]
R_ v[60:63],v201,7168
C_ v207,v216,v217
AW_ a4,v204
s_waitcnt vmcnt(0)
s_waitcnt lgkmcnt(0)
s_barrier
M_ v[128:131],v[32:35],v[0:3],v[128:131]
s_add_u32 s44,s37,0x280
AW_ a5,v205
AW_ a6,v206
AW_ a7,v207
M_ v[128:131],v[36:39],v[4:7],v[128:131]
s_add_u32 m0,s39,0x8000
R_ v[96:99],v201,32768
AR_ v244,a68
AR_ v245,a69
M_ v[132:135],v[32:35],v[8:11],v[132:135]
buffer_load_dwordx4 v192,s[24:27],s44 offen lds
AR_ v246,a70
AR_ v247,a71
M_ v[132:135],v[36:39],v[12:15],v[132:135]
R_ v[100:103],v201,36864
AR_ v248,a100
AR_ v249,a101
AR_ v202,a102
M_ v[136:139],v[32:35],v[16:19],v[136:139]
AR_ v203,a103
C_ v218,v244,v245
M_ v[136:139],v[36:39],v[20:23],v[136:139]
R_ v[64:67],v198,32768
C_ v219,v246,v247
C_ v220,v248,v249
M_ v[140:143],v[32:35],v[24:27],v[140:143]
buffer_load_dwordx4 v193,s[24:27],s44 offen offset:1024 lds
C_ v221,v202,v203
AW_ a68,v218
AW_ a69,v219
M_ v[140:143],v[36:39],v[28:31],v[140:143]
R_ v[68:71],v199,32768
AW_ a70,v220
AW_ a71,v221
M_ v[144:147],v[40:43],v[0:3],v[144:147]
AR_ v210,a132
AR_ v211,a133
M_ v[144:147],v[44:47],v[4:7],v[144:147]
R_ v[72:75],v198,34816
AR_ v212,a134
AR_ v213,a135
AR_ v214,a164
M_ v[148:151],v[40:43],v[8:11],v[148:151]
buffer_load_dwordx4 v194,s[24:27],s44 offen offset:2048 lds
AR_ v215,a165
AR_ v216,a166
M_ v[148:151],v[44:47],v[12:15],v[148:151]
R_ v[76:79],v199,34816
AR_ v217,a167
C_ v204,v210,v211
M_ v[152:155],v[40:43],v[16:19],v[152:155]
C_ v205,v212,v213
C_ v206,v214,v215
C_ v207,v216,v217
M_ v[152:155],v[44:47],v[20:23],v[152:155]
R_ v[80:83],v198,36864
AW_ a132,v204
AW_ a133,v205
M_ v[156:159],v[40:43],v[24:27],v[156:159]
buffer_load_dwordx4 v195,s[24:27],s44 offen offset:3072 lds
AW_ a134,v206
AW_ a135,v207
M_ v[156:159],v[44:47],v[28:31],v[156:159]
R_ v[84:87],v199,36864
AR_ v244,a196
AR_ v245,a197
M_ v[160:163],v[48:51],v[0:3],v[160:163]
s_add_u32 s44,s38,0xa000
AR_ v246,a198
AR_ v247,a199
AR_ v248,a228
M_ v[160:163],v[52:55],v[4:7],v[160:163]
s_add_u32 m0,s40,0x8000
R_ v[88:91],v198,38912
AR_ v249,a229
AR_ v202,a230
M_ v[164:167],v[48:51],v[8:11],v[164:167]
buffer_load_dwordx4 v208,s[28:31],s44 offen sc1 lds
AR_ v203,a231
C_ v218,v244,v245
M_ v[164:167],v[52:55],v[12:15],v[164:167]
R_ v[92:95],v199,38912
C_ v219,v246,v247
C_ v220,v248,v249
C_ v221,v202,v203
M_ v[168:171],v[48:51],v[16:19],v[168:171]
AW_ a196,v218
AW_ a197,v219
M_ v[168:171],v[52:55],v[20:23],v[168:171]
R_ v[104:107],v201,33792
AW_ a198,v220
AW_ a199,v221
M_ v[172:175],v[48:51],v[24:27],v[172:175]
buffer_load_dwordx4 v208,s[28:31],s44 offen offset:1024 sc1 lds
AR_ v210,a8
AR_ v211,a9
AR_ v212,a10
M_ v[172:175],v[52:55],v[28:31],v[172:175]
R_ v[108:111],v201,37888
AR_ v213,a11
AR_ v214,a40
M_ v[176:179],v[56:59],v[0:3],v[176:179]
AR_ v215,a41
AR_ v216,a42
M_ v[176:179],v[60:63],v[4:7],v[176:179]
R_ v[112:115],v201,34816
AR_ v217,a43
C_ v204,v210,v211
C_ v205,v212,v213
M_ v[180:183],v[56:59],v[8:11],v[180:183]
buffer_load_dwordx4 v208,s[28:31],s44 offen offset:2048 sc1 lds
C_ v206,v214,v215
C_ v207,v216,v217
M_ v[180:183],v[60:63],v[12:15],v[180:183]
R_ v[116:119],v201,38912
AW_ a8,v204
AW_ a9,v205
M_ v[184:187],v[56:59],v[16:19],v[184:187]
AW_ a10,v206
AW_ a11,v207
AR_ v244,a72
M_ v[184:187],v[60:63],v[20:23],v[184:187]
R_ v[120:123],v201,35840
AR_ v245,a73
AR_ v246,a74
M_ v[188:191],v[56:59],v[24:27],v[188:191]
buffer_load_dwordx4 v208,s[28:31],s44 offen offset:3072 sc1 lds
AR_ v247,a75
AR_ v248,a104
M_ v[188:191],v[60:63],v[28:31],v[188:191]
R_ v[124:127],v201,39936
AR_ v249,a105
AR_ v202,a106
s_waitcnt lgkmcnt(2)
M_ v[128:131],v[96:99],v[64:67],v[128:131]
s_add_u32 s44,s37,0x300
AR_ v203,a107
C_ v218,v244,v245
C_ v219,v246,v247
M_ v[128:131],v[100:103],v[68:71],v[128:131]
s_add_u32 m0,s39,0x10000
ds_read_b128 v[32:35],v200
C_ v220,v248,v249
C_ v221,v202,v203
M_ v[132:135],v[96:99],v[72:75],v[132:135]
buffer_load_dwordx4 v192,s[24:27],s44 offen lds
AW_ a72,v218
AW_ a73,v219
M_ v[132:135],v[100:103],v[76:79],v[132:135]
R_ v[36:39],v200,4096
AW_ a74,v220
AW_ a75,v221
AR_ v210,a136
M_ v[136:139],v[96:99],v[80:83],v[136:139]
AR_ v211,a137
AR_ v212,a138
M_ v[136:139],v[100:103],v[84:87],v[136:139]
ds_read_b128 v[0:3],v196
AR_ v213,a139
AR_ v214,a168
M_ v[140:143],v[96:99],v[88:91],v[140:143]
buffer_load_dwordx4 v193,s[24:27],s44 offen offset:1024 lds
AR_ v215,a169
AR_ v216,a170
AR_ v217,a171
M_ v[140:143],v[100:103],v[92:95],v[140:143]
ds_read_b128 v[4:7],v197
C_ v204,v210,v211
C_ v205,v212,v213
M_ v[144:147],v[104:107],v[64:67],v[144:147]
C_ v206,v214,v215
C_ v207,v216,v217
M_ v[144:147],v[108:111],v[68:71],v[144:147]
R_ v[8:11],v196,2048
AW_ a136,v204
AW_ a137,v205
AW_ a138,v206
M_ v[148:151],v[104:107],v[72:75],v[148:151]
buffer_load_dwordx4 v194,s[24:27],s44 offen offset:2048 lds
AW_ a139,v207
AR_ v244,a200
M_ v[148:151],v[108:111],v[76:79],v[148:151]
R_ v[12:15],v197,2048
AR_ v245,a201
AR_ v246,a202
M_ v[152:155],v[104:107],v[80:83],v[152:155]
AR_ v247,a203
AR_ v248,a232
AR_ v249,a233
M_ v[152:155],v[108:111],v[84:87],v[152:155]
R_ v[16:19],v196,4096
AR_ v202,a234
AR_ v203,a235
M_ v[156:159],v[104:107],v[88:91],v[156:159]
buffer_load_dwordx4 v195,s[24:27],s44 offen offset:3072 lds
C_ v218,v244,v245
C_ v219,v246,v247
M_ v[156:159],v[108:111],v[92:95],v[156:159]
R_ v[20:23],v197,4096
C_ v220,v248,v249
C_ v221,v202,v203
M_ v[160:163],v[112:115],v[64:67],v[160:163]
s_add_u32 s44,s38,0xc000
AW_ a200,v218
AW_ a201,v219
AW_ a202,v220
M_ v[160:163],v[116:119],v[68:71],v[160:163]
s_add_u32 m0,s40,0x10000
R_ v[24:27],v196,6144
AW_ a203,v221
AR_ v210,a12
M_ v[164:167],v[112:115],v[72:75],v[164:167]
buffer_load_dwordx4 v208,s[28:31],s44 offen sc1 lds
AR_ v211,a13
AR_ v212,a14
M_ v[164:167],v[116:119],v[76:79],v[164:167]
R_ v[28:31],v197,6144
AR_ v213,a15
AR_ v214,a44
AR_ v215,a45
M_ v[168:171],v[112:115],v[80:83],v[168:171]
AR_ v216,a46
AR_ v217,a47
M_ v[168:171],v[116:119],v[84:87],v[168:171]
R_ v[40:43],v200,1024
C_ v204,v210,v211
C_ v205,v212,v213
M_ v[172:175],v[112:115],v[88:91],v[172:175]
buffer_load_dwordx4 v208,s[28:31],s44 offen offset:1024 sc1 lds
C_ v206,v214,v215
C_ v207,v216,v217
AW_ a12,v204
M_ v[172:175],v[116:119],v[92:95],v[172:175]
R_ v[44:47],v200,5120
AW_ a13,v205
AW_ a14,v206
s_waitcnt lgkmcnt(2)
M_ v[176:179],v[120:123],v[64:67],v[176:179]
AW_ a15,v207
AR_ v244,a76
M_ v[176:179],v[124:127],v[68:71],v[176:179]
R_ v[48:51],v200,2048
AR_ v245,a77
AR_ v246,a78
AR_ v247,a79
M_ v[180:183],v[120:123],v[72:75],v[180:183]
buffer_load_dwordx4 v208,s[28:31],s44 offen offset:2048 sc1 lds
AR_ v248,a108
AR_ v249,a109
M_ v[180:183],v[124:127],v[76:79],v[180:183]
R_ v[52:55],v200,6144
AR_ v202,a110
AR_ v203,a111
M_ v[184:187],v[120:123],v[80:83],v[184:187]
C_ v218,v244,v245
C_ v219,v246,v247
C_ v220,v248,v249
M_ v[184:187],v[124:127],v[84:87],v[184:187]
R_ v[56:59],v200,3072
C_ v221,v202,v203
AW_ a76,v218
M_ v[188:191],v[120:123],v[88:91],v[188:191]
buffer_load_dwordx4 v208,s[28:31],s44 offen offset:3072 sc1 lds
AW_ a77,v219
AW_ a78,v220
M_ v[188:191],v[124:127],v[92:95],v[188:191]
R_ v[60:63],v200,7168
AW_ a79,v221
AR_ v210,a140
s_waitcnt vmcnt(0)
s_waitcnt lgkmcnt(0)
s_barrier
M_ v[128:131],v[32:35],v[0:3],v[128:131]
s_add_u32 s44,s37,0x380
AR_ v211,a141
AR_ v212,a142
AR_ v213,a143
M_ v[128:131],v[36:39],v[4:7],v[128:131]
s_add_u32 m0,s39,0x18000
R_ v[96:99],v200,32768
AR_ v214,a172
AR_ v215,a173
M_ v[132:135],v[32:35],v[8:11],v[132:135]
buffer_load_dwordx4 v192,s[24:27],s44 offen lds
AR_ v216,a174
AR_ v217,a175
M_ v[132:135],v[36:39],v[12:15],v[132:135]
R_ v[100:103],v200,36864
C_ v204,v210,v211
C_ v205,v212,v213
C_ v206,v214,v215
M_ v[136:139],v[32:35],v[16:19],v[136:139]
C_ v207,v216,v217
AW_ a140,v204
M_ v[136:139],v[36:39],v[20:23],v[136:139]
R_ v[64:67],v196,32768
AW_ a141,v205
AW_ a142,v206
M_ v[140:143],v[32:35],v[24:27],v[140:143]
buffer_load_dwordx4 v193,s[24:27],s44 offen offset:1024 lds
AW_ a143,v207
AR_ v244,a204
AR_ v245,a205
M_ v[140:143],v[36:39],v[28:31],v[140:143]
R_ v[68:71],v197,32768
AR_ v246,a206
AR_ v247,a207
M_ v[144:147],v[40:43],v[0:3],v[144:147]
AR_ v248,a236
AR_ v249,a237
M_ v[144:147],v[44:47],v[4:7],v[144:147]
R_ v[72:75],v196,34816
AR_ v202,a238
AR_ v203,a239
C_ v218,v244,v245
M_ v[148:151],v[40:43],v[8:11],v[148:151]
buffer_load_dwordx4 v194,s[24:27],s44 offen offset:2048 lds
C_ v219,v246,v247
C_ v220,v248,v249
M_ v[148:151],v[44:47],v[12:15],v[148:151]
R_ v[76:79],v197,34816
C_ v221,v202,v203
AW_ a204,v218
M_ v[152:155],v[40:43],v[16:19],v[152:155]
AW_ a205,v219
AW_ a206,v220
AW_ a207,v221
M_ v[152:155],v[44:47],v[20:23],v[152:155]
R_ v[80:83],v196,36864
AR_ v210,a16
AR_ v211,a17
M_ v[156:159],v[40:43],v[24:27],v[156:159]
buffer_load_dwordx4 v195,s[24:27],s44 offen offset:3072 lds
AR_ v212,a18
AR_ v213,a19
M_ v[156:159],v[44:47],v[28:31],v[156:159]
R_ v[84:87],v197,36864
AR_ v214,a48
AR_ v215,a49
M_ v[160:163],v[48:51],v[0:3],v[160:163]
s_add_u32 s44,s38,0xe000
AR_ v216,a50
AR_ v217,a51
C_ v204,v210,v211
M_ v[160:163],v[52:55],v[4:7],v[160:163]
s_add_u32 m0,s40,0x18000
R_ v[88:91],v196,38912
C_ v205,v212,v213
C_ v206,v214,v215
M_ v[164:167],v[48:51],v[8:11],v[164:167]
buffer_load_dwordx4 v208,s[28:31],s44 offen sc1 lds
C_ v207,v216,v217
AW_ a16,v204
M_ v[164:167],v[52:55],v[12:15],v[164:167]
R_ v[92:95],v197,38912
AW_ a17,v205
AW_ a18,v206
AW_ a19,v207
)A"
R"A(M_ v[168:171],v[48:51],v[16:19],v[168:171]
AR_ v244,a80
AR_ v245,a81
M_ v[168:171],v[52:55],v[20:23],v[168:171]
R_ v[104:107],v200,33792
AR_ v246,a82
AR_ v247,a83
M_ v[172:175],v[48:51],v[24:27],v[172:175]
buffer_load_dwordx4 v208,s[28:31],s44 offen offset:1024 sc1 lds
AR_ v248,a112
AR_ v249,a113
AR_ v202,a114
M_ v[172:175],v[52:55],v[28:31],v[172:175]
R_ v[108:111],v200,37888
AR_ v203,a115
C_ v218,v244,v245
M_ v[176:179],v[56:59],v[0:3],v[176:179]
C_ v219,v246,v247
C_ v220,v248,v249
M_ v[176:179],v[60:63],v[4:7],v[176:179]
R_ v[112:115],v200,34816
C_ v221,v202,v203
AW_ a80,v218
AW_ a81,v219
M_ v[180:183],v[56:59],v[8:11],v[180:183]
buffer_load_dwordx4 v208,s[28:31],s44 offen offset:2048 sc1 lds
AW_ a82,v220
AW_ a83,v221
M_ v[180:183],v[60:63],v[12:15],v[180:183]
R_ v[116:119],v200,38912
AR_ v210,a144
AR_ v211,a145
M_ v[184:187],v[56:59],v[16:19],v[184:187]
AR_ v212,a146
AR_ v213,a147
AR_ v214,a176
M_ v[184:187],v[60:63],v[20:23],v[184:187]
R_ v[120:123],v200,35840
AR_ v215,a177
AR_ v216,a178
M_ v[188:191],v[56:59],v[24:27],v[188:191]
buffer_load_dwordx4 v208,s[28:31],s44 offen offset:3072 sc1 lds
AR_ v217,a179
C_ v204,v210,v211
M_ v[188:191],v[60:63],v[28:31],v[188:191]
R_ v[124:127],v200,39936
C_ v205,v212,v213
C_ v206,v214,v215
s_waitcnt lgkmcnt(2)
M_ v[128:131],v[96:99],v[64:67],v[128:131]
s_add_u32 s44,s37,0x400
C_ v207,v216,v217
AW_ a144,v204
AW_ a145,v205
M_ v[128:131],v[100:103],v[68:71],v[128:131]
s_add_u32 m0,s39,0x0
ds_read_b128 v[32:35],v201
AW_ a146,v206
AW_ a147,v207
M_ v[132:135],v[96:99],v[72:75],v[132:135]
buffer_load_dwordx4 v192,s[24:27],s44 offen lds
AR_ v244,a208
AR_ v245,a209
M_ v[132:135],v[100:103],v[76:79],v[132:135]
R_ v[36:39],v201,4096
AR_ v246,a210
AR_ v247,a211
AR_ v248,a240
M_ v[136:139],v[96:99],v[80:83],v[136:139]
AR_ v249,a241
AR_ v202,a242
M_ v[136:139],v[100:103],v[84:87],v[136:139]
ds_read_b128 v[0:3],v198
AR_ v203,a243
C_ v218,v244,v245
M_ v[140:143],v[96:99],v[88:91],v[140:143]
buffer_load_dwordx4 v193,s[24:27],s44 offen offset:1024 lds
C_ v219,v246,v247
C_ v220,v248,v249
C_ v221,v202,v203
M_ v[140:143],v[100:103],v[92:95],v[140:143]
ds_read_b128 v[4:7],v199
AW_ a208,v218
AW_ a209,v219
M_ v[144:147],v[104:107],v[64:67],v[144:147]
AW_ a210,v220
AW_ a211,v221
M_ v[144:147],v[108:111],v[68:71],v[144:147]
R_ v[8:11],v198,2048
AR_ v210,a20
AR_ v211,a21
AR_ v212,a22
M_ v[148:151],v[104:107],v[72:75],v[148:151]
buffer_load_dwordx4 v194,s[24:27],s44 offen offset:2048 lds
AR_ v213,a23
AR_ v214,a52
M_ v[148:151],v[108:111],v[76:79],v[148:151]
R_ v[12:15],v199,2048
AR_ v215,a53
AR_ v216,a54
M_ v[152:155],v[104:107],v[80:83],v[152:155]
AR_ v217,a55
C_ v204,v210,v211
C_ v205,v212,v213
M_ v[152:155],v[108:111],v[84:87],v[152:155]
R_ v[16:19],v198,4096
C_ v206,v214,v215
C_ v207,v216,v217
M_ v[156:159],v[104:107],v[88:91],v[156:159]
buffer_load_dwordx4 v195,s[24:27],s44 offen offset:3072 lds
AW_ a20,v204
AW_ a21,v205
M_ v[156:159],v[108:111],v[92:95],v[156:159]
R_ v[20:23],v199,4096
AW_ a22,v206
AW_ a23,v207
M_ v[160:163],v[112:115],v[64:67],v[160:163]
s_add_u32 s44,s38,0x10000
AR_ v244,a84
AR_ v245,a85
AR_ v246,a86
M_ v[160:163],v[116:119],v[68:71],v[160:163]
s_add_u32 m0,s40,0x0
R_ v[24:27],v198,6144
AR_ v247,a87
AR_ v248,a116
M_ v[164:167],v[112:115],v[72:75],v[164:167]
buffer_load_dwordx4 v208,s[28:31],s44 offen sc1 lds
AR_ v249,a117
AR_ v202,a118
M_ v[164:167],v[116:119],v[76:79],v[164:167]
R_ v[28:31],v199,6144
AR_ v203,a119
C_ v218,v244,v245
C_ v219,v246,v247
M_ v[168:171],v[112:115],v[80:83],v[168:171]
C_ v220,v248,v249
C_ v221,v202,v203
M_ v[168:171],v[116:119],v[84:87],v[168:171]
R_ v[40:43],v201,1024
AW_ a84,v218
AW_ a85,v219
M_ v[172:175],v[112:115],v[88:91],v[172:175]
buffer_load_dwordx4 v208,s[28:31],s44 offen offset:1024 sc1 lds
AW_ a86,v220
AW_ a87,v221
AR_ v210,a148
M_ v[172:175],v[116:119],v[92:95],v[172:175]
R_ v[44:47],v201,5120
AR_ v211,a149
AR_ v212,a150
s_waitcnt lgkmcnt(2)
M_ v[176:179],v[120:123],v[64:67],v[176:179]
AR_ v213,a151
AR_ v214,a180
M_ v[176:179],v[124:127],v[68:71],v[176:179]
R_ v[48:51],v201,2048
AR_ v215,a181
AR_ v216,a182
AR_ v217,a183
M_ v[180:183],v[120:123],v[72:75],v[180:183]
buffer_load_dwordx4 v208,s[28:31],s44 offen offset:2048 sc1 lds
C_ v204,v210,v211
C_ v205,v212,v213
M_ v[180:183],v[124:127],v[76:79],v[180:183]
R_ v[52:55],v201,6144
C_ v206,v214,v215
C_ v207,v216,v217
M_ v[184:187],v[120:123],v[80:83],v[184:187]
AW_ a148,v204
AW_ a149,v205
AW_ a150,v206
M_ v[184:187],v[124:127],v[84:87],v[184:187]
R_ v[56:59],v201,3072
AW_ a151,v207
AR_ v244,a212
M_ v[188:191],v[120:123],v[88:91],v[188:191]
buffer_load_dwordx4 v208,s[28:31],s44 offen offset:3072 sc1 lds
AR_ v245,a213
AR_ v246,a214
M_ v[188:191],v[124:127],v[92:95],v[188:191]
R_ v[60:63],v201,7168
AR_ v247,a215
AR_ v248,a244
s_waitcnt vmcnt(0)
s_waitcnt lgkmcnt(0)
s_barrier
M_ v[128:131],v[32:35],v[0:3],v[128:131]
s_add_u32 s44,s37,0x480
AR_ v249,a245
AR_ v202,a246
AR_ v203,a247
M_ v[128:131],v[36:39],v[4:7],v[128:131]
s_add_u32 m0,s39,0x8000
R_ v[96:99],v201,32768
C_ v218,v244,v245
C_ v219,v246,v247
M_ v[132:135],v[32:35],v[8:11],v[132:135]
buffer_load_dwordx4 v192,s[24:27],s44 offen lds
C_ v220,v248,v249
C_ v221,v202,v203
M_ v[132:135],v[36:39],v[12:15],v[132:135]
R_ v[100:103],v201,36864
AW_ a212,v218
AW_ a213,v219
AW_ a214,v220
M_ v[136:139],v[32:35],v[16:19],v[136:139]
AW_ a215,v221
AR_ v210,a24
M_ v[136:139],v[36:39],v[20:23],v[136:139]
R_ v[64:67],v198,32768
AR_ v211,a25
AR_ v212,a26
M_ v[140:143],v[32:35],v[24:27],v[140:143]
buffer_load_dwordx4 v193,s[24:27],s44 offen offset:1024 lds
AR_ v213,a27
AR_ v214,a56
AR_ v215,a57
M_ v[140:143],v[36:39],v[28:31],v[140:143]
R_ v[68:71],v199,32768
AR_ v216,a58
AR_ v217,a59
M_ v[144:147],v[40:43],v[0:3],v[144:147]
C_ v204,v210,v211
C_ v205,v212,v213
M_ v[144:147],v[44:47],v[4:7],v[144:147]
R_ v[72:75],v198,34816
C_ v206,v214,v215
C_ v207,v216,v217
AW_ a24,v204
M_ v[148:151],v[40:43],v[8:11],v[148:151]
buffer_load_dwordx4 v194,s[24:27],s44 offen offset:2048 lds
AW_ a25,v205
AW_ a26,v206
M_ v[148:151],v[44:47],v[12:15],v[148:151]
R_ v[76:79],v199,34816
AW_ a27,v207
AR_ v244,a88
M_ v[152:155],v[40:43],v[16:19],v[152:155]
AR_ v245,a89
AR_ v246,a90
AR_ v247,a91
M_ v[152:155],v[44:47],v[20:23],v[152:155]
R_ v[80:83],v198,36864
AR_ v248,a120
AR_ v249,a121
M_ v[156:159],v[40:43],v[24:27],v[156:159]
buffer_load_dwordx4 v195,s[24:27],s44 offen offset:3072 lds
AR_ v202,a122
AR_ v203,a123
M_ v[156:159],v[44:47],v[28:31],v[156:159]
R_ v[84:87],v199,36864
C_ v218,v244,v245
C_ v219,v246,v247
M_ v[160:163],v[48:51],v[0:3],v[160:163]
s_add_u32 s44,s38,0x12000
C_ v220,v248,v249
C_ v221,v202,v203
AW_ a88,v218
M_ v[160:163],v[52:55],v[4:7],v[160:163]
s_add_u32 m0,s40,0x8000
R_ v[88:91],v198,38912
AW_ a89,v219
AW_ a90,v220
M_ v[164:167],v[48:51],v[8:11],v[164:167]
buffer_load_dwordx4 v208,s[28:31],s44 offen sc1 lds
AW_ a91,v221
AR_ v210,a152
M_ v[164:167],v[52:55],v[12:15],v[164:167]
R_ v[92:95],v199,38912
AR_ v211,a153
AR_ v212,a154
AR_ v213,a155
M_ v[168:171],v[48:51],v[16:19],v[168:171]
AR_ v214,a184
AR_ v215,a185
M_ v[168:171],v[52:55],v[20:23],v[168:171]
R_ v[104:107],v201,33792
AR_ v216,a186
AR_ v217,a187
M_ v[172:175],v[48:51],v[24:27],v[172:175]
buffer_load_dwordx4 v208,s[28:31],s44 offen offset:1024 sc1 lds
C_ v204,v210,v211
C_ v205,v212,v213
C_ v206,v214,v215
M_ v[172:175],v[52:55],v[28:31],v[172:175]
R_ v[108:111],v201,37888
C_ v207,v216,v217
AW_ a152,v204
M_ v[176:179],v[56:59],v[0:3],v[176:179]
AW_ a153,v205
AW_ a154,v206
M_ v[176:179],v[60:63],v[4:7],v[176:179]
R_ v[112:115],v201,34816
AW_ a155,v207
AR_ v244,a216
AR_ v245,a217
M_ v[180:183],v[56:59],v[8:11],v[180:183]
buffer_load_dwordx4 v208,s[28:31],s44 offen offset:2048 sc1 lds
AR_ v246,a218
AR_ v247,a219
M_ v[180:183],v[60:63],v[12:15],v[180:183]
R_ v[116:119],v201,38912
AR_ v248,a248
AR_ v249,a249
M_ v[184:187],v[56:59],v[16:19],v[184:187]
AR_ v202,a250
AR_ v203,a251
C_ v218,v244,v245
M_ v[184:187],v[60:63],v[20:23],v[184:187]
R_ v[120:123],v201,35840
C_ v219,v246,v247
C_ v220,v248,v249
M_ v[188:191],v[56:59],v[24:27],v[188:191]
buffer_load_dwordx4 v208,s[28:31],s44 offen offset:3072 sc1 lds
C_ v221,v202,v203
AW_ a216,v218
M_ v[188:191],v[60:63],v[28:31],v[188:191]
R_ v[124:127],v201,39936
AW_ a217,v219
AW_ a218,v220
s_waitcnt lgkmcnt(2)
M_ v[128:131],v[96:99],v[64:67],v[128:131]
AW_ a219,v221
AR_ v210,a28
AR_ v211,a29
M_ v[128:131],v[100:103],v[68:71],v[128:131]
ds_read_b128 v[32:35],v200
AR_ v212,a30
AR_ v213,a31
M_ v[132:135],v[96:99],v[72:75],v[132:135]
AR_ v214,a60
AR_ v215,a61
M_ v[132:135],v[100:103],v[76:79],v[132:135]
R_ v[36:39],v200,4096
AR_ v216,a62
AR_ v217,a63
M_ v[136:139],v[96:99],v[80:83],v[136:139]
C_ v204,v210,v211
C_ v205,v212,v213
M_ v[136:139],v[100:103],v[84:87],v[136:139]
ds_read_b128 v[0:3],v196
C_ v206,v214,v215
C_ v207,v216,v217
AW_ a28,v204
M_ v[140:143],v[96:99],v[88:91],v[140:143]
AW_ a29,v205
AW_ a30,v206
M_ v[140:143],v[100:103],v[92:95],v[140:143]
ds_read_b128 v[4:7],v197
AW_ a31,v207
AR_ v244,a92
M_ v[144:147],v[104:107],v[64:67],v[144:147]
AR_ v245,a93
AR_ v246,a94
M_ v[144:147],v[108:111],v[68:71],v[144:147]
R_ v[8:11],v196,2048
AR_ v247,a95
AR_ v248,a124
M_ v[148:151],v[104:107],v[72:75],v[148:151]
AR_ v249,a125
AR_ v202,a126
AR_ v203,a127
M_ v[148:151],v[108:111],v[76:79],v[148:151]
R_ v[12:15],v197,2048
C_ v218,v244,v245
C_ v219,v246,v247
M_ v[152:155],v[104:107],v[80:83],v[152:155]
C_ v220,v248,v249
C_ v221,v202,v203
M_ v[152:155],v[108:111],v[84:87],v[152:155]
R_ v[16:19],v196,4096
AW_ a92,v218
AW_ a93,v219
M_ v[156:159],v[104:107],v[88:91],v[156:159]
AW_ a94,v220
AW_ a95,v221
M_ v[156:159],v[108:111],v[92:95],v[156:159]
R_ v[20:23],v197,4096
AR_ v210,a156
AR_ v211,a157
M_ v[160:163],v[112:115],v[64:67],v[160:163]
AR_ v212,a158
AR_ v213,a159
AR_ v214,a188
M_ v[160:163],v[116:119],v[68:71],v[160:163]
R_ v[24:27],v196,6144
AR_ v215,a189
AR_ v216,a190
M_ v[164:167],v[112:115],v[72:75],v[164:167]
AR_ v217,a191
C_ v204,v210,v211
M_ v[164:167],v[116:119],v[76:79],v[164:167]
R_ v[28:31],v197,6144
C_ v205,v212,v213
C_ v206,v214,v215
M_ v[168:171],v[112:115],v[80:83],v[168:171]
C_ v207,v216,v217
AW_ a156,v204
M_ v[168:171],v[116:119],v[84:87],v[168:171]
R_ v[40:43],v200,1024
AW_ a157,v205
AW_ a158,v206
AW_ a159,v207
M_ v[172:175],v[112:115],v[88:91],v[172:175]
AR_ v244,a220
AR_ v245,a221
M_ v[172:175],v[116:119],v[92:95],v[172:175]
R_ v[44:47],v200,5120
AR_ v246,a222
AR_ v247,a223
s_waitcnt lgkmcnt(2)
M_ v[176:179],v[120:123],v[64:67],v[176:179]
AR_ v248,a252
AR_ v249,a253
M_ v[176:179],v[124:127],v[68:71],v[176:179]
R_ v[48:51],v200,2048
AR_ v202,a254
AR_ v203,a255
M_ v[180:183],v[120:123],v[72:75],v[180:183]
C_ v218,v244,v245
C_ v219,v246,v247
C_ v220,v248,v249
M_ v[180:183],v[124:127],v[76:79],v[180:183]
R_ v[52:55],v200,6144
C_ v221,v202,v203
AW_ a220,v218
M_ v[184:187],v[120:123],v[80:83],v[184:187]
AW_ a221,v219
AW_ a222,v220
M_ v[184:187],v[124:127],v[84:87],v[184:187]
R_ v[56:59],v200,3072
AW_ a223,v221
s_mov_b32 s28,0x0
M_ v[188:191],v[120:123],v[88:91],v[188:191]
s_mov_b32 s29,0x1000
s_mov_b32 s30,0x2000
M_ v[188:191],v[124:127],v[92:95],v[188:191]
R_ v[60:63],v200,7168
s_mov_b32 s31,0x3000
s_mov_b32 s33,1024
s_waitcnt vmcnt(0)
s_waitcnt lgkmcnt(0)
s_barrier
M_ v[128:131],v[32:35],v[0:3],v[128:131]
s_add_u32 s55,s68,0x0
M_ v[128:131],v[36:39],v[4:7],v[128:131]
s_lshl_b32 s56,s3,13
R_ v[96:99],v200,32768
M_ v[132:135],v[32:35],v[8:11],v[132:135]
s_add_u32 m0,s56,0x10000
s_nop 0
buffer_load_dwordx4 v224,s[4:7],s55 offen lds
M_ v[132:135],v[36:39],v[12:15],v[132:135]
R_ v[100:103],v200,36864
M_ v[136:139],v[32:35],v[16:19],v[136:139]
M_ v[136:139],v[36:39],v[20:23],v[136:139]
R_ v[64:67],v196,32768
buffer_load_dwordx4 v225,s[4:7],s55 offen offset:1024 lds
M_ v[140:143],v[32:35],v[24:27],v[140:143]
M_ v[140:143],v[36:39],v[28:31],v[140:143]
R_ v[68:71],v197,32768
buffer_load_dwordx4 v226,s[4:7],s55 offen offset:2048 lds
M_ v[144:147],v[40:43],v[0:3],v[144:147]
M_ v[144:147],v[44:47],v[4:7],v[144:147]
R_ v[72:75],v196,34816
M_ v[148:151],v[40:43],v[8:11],v[148:151]
buffer_load_dwordx4 v227,s[4:7],s55 offen offset:3072 lds
M_ v[148:151],v[44:47],v[12:15],v[148:151]
s_add_u32 s55,s68,0xa000
R_ v[76:79],v197,34816
M_ v[152:155],v[40:43],v[16:19],v[152:155]
s_lshl_b32 s56,s3,13
s_add_u32 m0,s56,0x11000
s_nop 0
buffer_load_dwordx4 v224,s[4:7],s55 offen lds
M_ v[152:155],v[44:47],v[20:23],v[152:155]
R_ v[80:83],v196,36864
M_ v[156:159],v[40:43],v[24:27],v[156:159]
M_ v[156:159],v[44:47],v[28:31],v[156:159]
R_ v[84:87],v197,36864
buffer_load_dwordx4 v225,s[4:7],s55 offen offset:1024 lds
M_ v[160:163],v[48:51],v[0:3],v[160:163]
M_ v[160:163],v[52:55],v[4:7],v[160:163]
R_ v[88:91],v196,38912
buffer_load_dwordx4 v226,s[4:7],s55 offen offset:2048 lds
M_ v[164:167],v[48:51],v[8:11],v[164:167]
M_ v[164:167],v[52:55],v[12:15],v[164:167]
R_ v[92:95],v197,38912
M_ v[168:171],v[48:51],v[16:19],v[168:171]
buffer_load_dwordx4 v227,s[4:7],s55 offen offset:3072 lds
M_ v[168:171],v[52:55],v[20:23],v[168:171]
s_add_u32 s55,s69,0x0
R_ v[104:107],v200,33792
M_ v[172:175],v[48:51],v[24:27],v[172:175]
s_add_u32 m0,s75,0x18000
s_nop 0
buffer_load_dwordx4 v228,s[8:11],s55 offen lds
M_ v[172:175],v[52:55],v[28:31],v[172:175]
R_ v[108:111],v200,37888
M_ v[176:179],v[56:59],v[0:3],v[176:179]
M_ v[176:179],v[60:63],v[4:7],v[176:179]
R_ v[112:115],v200,34816
buffer_load_dwordx4 v229,s[8:11],s55 offen offset:1024 lds
M_ v[180:183],v[56:59],v[8:11],v[180:183]
M_ v[180:183],v[60:63],v[12:15],v[180:183]
R_ v[116:119],v200,38912
buffer_load_dwordx4 v230,s[8:11],s55 offen offset:2048 lds
M_ v[184:187],v[56:59],v[16:19],v[184:187]
M_ v[184:187],v[60:63],v[20:23],v[184:187]
R_ v[120:123],v200,35840
M_ v[188:191],v[56:59],v[24:27],v[188:191]
buffer_load_dwordx4 v231,s[8:11],s55 offen offset:3072 lds
M_ v[188:191],v[60:63],v[28:31],v[188:191]
R_ v[124:127],v200,39936
s_waitcnt lgkmcnt(4)
M_ v[128:131],v[96:99],v[64:67],v[128:131]
s_add_u32 s55,s68,0x80
M_ v[128:131],v[100:103],v[68:71],v[128:131]
s_lshl_b32 s56,s3,13
M_ v[132:135],v[96:99],v[72:75],v[132:135]
s_add_u32 m0,s56,0x1c000
s_nop 0
)A"
R"A(buffer_load_dwordx4 v224,s[4:7],s55 offen lds
M_ v[132:135],v[100:103],v[76:79],v[132:135]
M_ v[136:139],v[96:99],v[80:83],v[136:139]
M_ v[136:139],v[100:103],v[84:87],v[136:139]
buffer_load_dwordx4 v225,s[4:7],s55 offen offset:1024 lds
M_ v[140:143],v[96:99],v[88:91],v[140:143]
M_ v[140:143],v[100:103],v[92:95],v[140:143]
buffer_load_dwordx4 v226,s[4:7],s55 offen offset:2048 lds
M_ v[144:147],v[104:107],v[64:67],v[144:147]
M_ v[144:147],v[108:111],v[68:71],v[144:147]
M_ v[148:151],v[104:107],v[72:75],v[148:151]
buffer_load_dwordx4 v227,s[4:7],s55 offen offset:3072 lds
M_ v[148:151],v[108:111],v[76:79],v[148:151]
s_add_u32 s55,s68,0xa080
M_ v[152:155],v[104:107],v[80:83],v[152:155]
s_lshl_b32 s56,s3,13
s_add_u32 m0,s56,0x1d000
s_nop 0
buffer_load_dwordx4 v224,s[4:7],s55 offen lds
M_ v[152:155],v[108:111],v[84:87],v[152:155]
M_ v[156:159],v[104:107],v[88:91],v[156:159]
M_ v[156:159],v[108:111],v[92:95],v[156:159]
buffer_load_dwordx4 v225,s[4:7],s55 offen offset:1024 lds
s_waitcnt lgkmcnt(0)
M_ v[160:163],v[112:115],v[64:67],v[160:163]
M_ v[160:163],v[116:119],v[68:71],v[160:163]
buffer_load_dwordx4 v226,s[4:7],s55 offen offset:2048 lds
M_ v[164:167],v[112:115],v[72:75],v[164:167]
M_ v[164:167],v[116:119],v[76:79],v[164:167]
M_ v[168:171],v[112:115],v[80:83],v[168:171]
buffer_load_dwordx4 v227,s[4:7],s55 offen offset:3072 lds
M_ v[168:171],v[116:119],v[84:87],v[168:171]
s_add_u32 s55,s69,0x80000
M_ v[172:175],v[112:115],v[88:91],v[172:175]
s_add_u32 m0,s75,0x24000
s_nop 0
buffer_load_dwordx4 v228,s[8:11],s55 offen lds
M_ v[172:175],v[116:119],v[92:95],v[172:175]
M_ v[176:179],v[120:123],v[64:67],v[176:179]
M_ v[176:179],v[124:127],v[68:71],v[176:179]
buffer_load_dwordx4 v229,s[8:11],s55 offen offset:1024 lds
M_ v[180:183],v[120:123],v[72:75],v[180:183]
M_ v[180:183],v[124:127],v[76:79],v[180:183]
buffer_load_dwordx4 v230,s[8:11],s55 offen offset:2048 lds
M_ v[184:187],v[120:123],v[80:83],v[184:187]
M_ v[184:187],v[124:127],v[84:87],v[184:187]
M_ v[188:191],v[120:123],v[88:91],v[188:191]
buffer_load_dwordx4 v231,s[8:11],s55 offen offset:3072 lds
M_ v[188:191],v[124:127],v[92:95],v[188:191]
s_branch LKV_E6_%=
LKV_V6_%=:
ds_read_b64_tr_b16 v[0:1],v140
T_ v[2:3],v140,1024
ds_read_b128 v[32:35],v136
R_ v[36:39],v136,1024
R_ v[40:43],v136,2048
R_ v[44:47],v136,3072
R_ v[48:51],v136,4096
R_ v[52:55],v136,5120
R_ v[56:59],v136,6144
R_ v[60:63],v136,7168
ds_read_b64_tr_b16 v[4:5],v141
T_ v[6:7],v141,1024
T_ v[8:9],v140,256
T_ v[10:11],v140,1280
T_ v[12:13],v141,256
T_ v[14:15],v141,1280
T_ v[16:17],v140,512
T_ v[18:19],v140,1536
T_ v[20:21],v141,512
T_ v[22:23],v141,1536
T_ v[24:25],v140,768
T_ v[26:27],v140,1792
T_ v[28:29],v141,768
T_ v[30:31],v141,1792
s_waitcnt vmcnt(16)
s_waitcnt lgkmcnt(0)
s_barrier
M_ a[0:3],v[32:35],v[0:3],0
T_ v[64:65],v140,32768
M_ a[4:7],v[36:39],v[0:3],0
T_ v[66:67],v140,33792
M_ a[8:11],v[40:43],v[0:3],0
R_ v[96:99],v136,32768
M_ a[12:15],v[44:47],v[0:3],0
R_ v[100:103],v136,33792
M_ a[16:19],v[48:51],v[0:3],0
R_ v[104:107],v136,34816
v_mbcnt_lo_u32_b32 v248,-1,0
M_ a[20:23],v[52:55],v[0:3],0
R_ v[108:111],v136,35840
M_ a[24:27],v[56:59],v[0:3],0
R_ v[112:115],v136,36864
M_ a[28:31],v[60:63],v[0:3],0
R_ v[116:119],v136,37888
M_ a[32:35],v[32:35],v[4:7],0
R_ v[120:123],v136,38912
v_mbcnt_hi_u32_b32 v248,-1,v248
M_ a[36:39],v[36:39],v[4:7],0
R_ v[124:127],v136,39936
M_ a[40:43],v[40:43],v[4:7],0
T_ v[68:69],v141,32768
M_ a[44:47],v[44:47],v[4:7],0
T_ v[70:71],v141,33792
M_ a[48:51],v[48:51],v[4:7],0
T_ v[72:73],v140,33024
v_lshlrev_b32 v248,4,v248
M_ a[52:55],v[52:55],v[4:7],0
T_ v[74:75],v140,34048
M_ a[56:59],v[56:59],v[4:7],0
T_ v[76:77],v141,33024
M_ a[60:63],v[60:63],v[4:7],0
T_ v[78:79],v141,34048
M_ a[64:67],v[32:35],v[8:11],0
T_ v[80:81],v140,33280
s_mov_b32 s60,s72
M_ a[68:71],v[36:39],v[8:11],0
T_ v[82:83],v140,34304
M_ a[72:75],v[40:43],v[8:11],0
T_ v[84:85],v141,33280
M_ a[76:79],v[44:47],v[8:11],0
T_ v[86:87],v141,34304
M_ a[80:83],v[48:51],v[8:11],0
T_ v[88:89],v140,33536
s_mov_b32 s61,s73
M_ a[84:87],v[52:55],v[8:11],0
T_ v[90:91],v140,34560
M_ a[88:91],v[56:59],v[8:11],0
T_ v[92:93],v141,33536
M_ a[92:95],v[60:63],v[8:11],0
T_ v[94:95],v141,34560
M_ a[96:99],v[32:35],v[12:15],0
s_add_u32 s55,s21,0x100000
s_mov_b32 s64,s60
M_ a[100:103],v[36:39],v[12:15],0
s_add_u32 m0,s22,0x0
M_ a[104:107],v[40:43],v[12:15],0
buffer_load_dwordx4 v132,s[8:11],s55 offen lds
M_ a[108:111],v[44:47],v[12:15],0
M_ a[112:115],v[48:51],v[12:15],0
s_mov_b32 s65,s61
M_ a[116:119],v[52:55],v[12:15],0
M_ a[120:123],v[56:59],v[12:15],0
M_ a[124:127],v[60:63],v[12:15],0
buffer_load_dwordx4 v133,s[8:11],s55 offen offset:1024 lds
M_ a[128:131],v[32:35],v[16:19],0
M_ a[132:135],v[36:39],v[16:19],0
s_and_b32 s65,s65,0xffff
M_ a[136:139],v[40:43],v[16:19],0
M_ a[140:143],v[44:47],v[16:19],0
M_ a[144:147],v[48:51],v[16:19],0
buffer_load_dwordx4 v134,s[8:11],s55 offen offset:2048 lds
M_ a[148:151],v[52:55],v[16:19],0
s_mov_b32 s66,0x280000
M_ a[152:155],v[56:59],v[16:19],0
M_ a[156:159],v[60:63],v[16:19],0
M_ a[160:163],v[32:35],v[20:23],0
M_ a[164:167],v[36:39],v[20:23],0
buffer_load_dwordx4 v135,s[8:11],s55 offen offset:3072 lds
s_mov_b32 s67,0x20000
M_ a[168:171],v[40:43],v[20:23],0
M_ a[172:175],v[44:47],v[20:23],0
M_ a[176:179],v[48:51],v[20:23],0
M_ a[180:183],v[52:55],v[20:23],0
s_add_u32 s55,s20,0x100
s_lshl_b32 s62,s2,2
M_ a[184:187],v[56:59],v[20:23],0
s_add_u32 m0,s23,0x0
M_ a[188:191],v[60:63],v[20:23],0
buffer_load_dwordx4 v128,s[4:7],s55 offen lds
M_ a[192:195],v[32:35],v[24:27],0
M_ a[196:199],v[36:39],v[24:27],0
s_add_u32 s62,s62,s3
M_ a[200:203],v[40:43],v[24:27],0
M_ a[204:207],v[44:47],v[24:27],0
M_ a[208:211],v[48:51],v[24:27],0
buffer_load_dwordx4 v129,s[4:7],s55 offen offset:1024 lds
M_ a[212:215],v[52:55],v[24:27],0
s_lshl_b32 s62,s62,10
M_ a[216:219],v[56:59],v[24:27],0
M_ a[220:223],v[60:63],v[24:27],0
M_ a[224:227],v[32:35],v[28:31],0
M_ a[228:231],v[36:39],v[28:31],0
buffer_load_dwordx4 v130,s[4:7],s55 offen offset:2048 lds
buffer_load_dwordx4 v[240:243],v248,s[64:67],s62 offen
M_ a[232:235],v[40:43],v[28:31],0
M_ a[236:239],v[44:47],v[28:31],0
M_ a[240:243],v[48:51],v[28:31],0
M_ a[244:247],v[52:55],v[28:31],0
M_ a[248:251],v[56:59],v[28:31],0
buffer_load_dwordx4 v131,s[4:7],s55 offen offset:3072 lds
M_ a[252:255],v[60:63],v[28:31],0
s_waitcnt vmcnt(16)
s_waitcnt lgkmcnt(0)
s_barrier
M_ a[0:3],v[96:99],v[64:67],a[0:3]
ds_read_b64_tr_b16 v[0:1],v142
M_ a[4:7],v[100:103],v[64:67],a[4:7]
T_ v[2:3],v142,1024
M_ a[8:11],v[104:107],v[64:67],a[8:11]
ds_read_b128 v[32:35],v137
M_ a[12:15],v[108:111],v[64:67],a[12:15]
R_ v[36:39],v137,1024
M_ a[16:19],v[112:115],v[64:67],a[16:19]
R_ v[40:43],v137,2048
v_mbcnt_lo_u32_b32 v161,-1,0
M_ a[20:23],v[116:119],v[64:67],a[20:23]
R_ v[44:47],v137,3072
M_ a[24:27],v[120:123],v[64:67],a[24:27]
R_ v[48:51],v137,4096
M_ a[28:31],v[124:127],v[64:67],a[28:31]
R_ v[52:55],v137,5120
v_mbcnt_hi_u32_b32 v161,-1,v161
M_ a[32:35],v[96:99],v[68:71],a[32:35]
R_ v[56:59],v137,6144
M_ a[36:39],v[100:103],v[68:71],a[36:39]
R_ v[60:63],v137,7168
M_ a[40:43],v[104:107],v[68:71],a[40:43]
ds_read_b64_tr_b16 v[4:5],v143
M_ a[44:47],v[108:111],v[68:71],a[44:47]
T_ v[6:7],v143,1024
v_lshlrev_b32 v160,4,v161
M_ a[48:51],v[112:115],v[68:71],a[48:51]
T_ v[8:9],v142,256
M_ a[52:55],v[116:119],v[68:71],a[52:55]
T_ v[10:11],v142,1280
M_ a[56:59],v[120:123],v[68:71],a[56:59]
T_ v[12:13],v143,256
M_ a[60:63],v[124:127],v[68:71],a[60:63]
T_ v[14:15],v143,1280
s_and_b32 s25,s25,0xffff
M_ a[64:67],v[96:99],v[72:75],a[64:67]
T_ v[16:17],v142,512
M_ a[68:71],v[100:103],v[72:75],a[68:71]
T_ v[18:19],v142,1536
M_ a[72:75],v[104:107],v[72:75],a[72:75]
T_ v[20:21],v143,512
M_ a[76:79],v[108:111],v[72:75],a[76:79]
T_ v[22:23],v143,1536
s_mov_b32 s26,0x258000
M_ a[80:83],v[112:115],v[72:75],a[80:83]
T_ v[24:25],v142,768
M_ a[84:87],v[116:119],v[72:75],a[84:87]
T_ v[26:27],v142,1792
M_ a[88:91],v[120:123],v[72:75],a[88:91]
T_ v[28:29],v143,768
M_ a[92:95],v[124:127],v[72:75],a[92:95]
T_ v[30:31],v143,1792
s_mov_b32 s27,0x20000
M_ a[96:99],v[96:99],v[76:79],a[96:99]
s_add_u32 s55,s21,0x140000
M_ a[100:103],v[100:103],v[76:79],a[100:103]
s_add_u32 m0,s22,0x8000
M_ a[104:107],v[104:107],v[76:79],a[104:107]
buffer_load_dwordx4 v132,s[8:11],s55 offen lds
M_ a[108:111],v[108:111],v[76:79],a[108:111]
s_and_b32 s29,s29,0xffff
M_ a[112:115],v[112:115],v[76:79],a[112:115]
M_ a[116:119],v[116:119],v[76:79],a[116:119]
M_ a[120:123],v[120:123],v[76:79],a[120:123]
M_ a[124:127],v[124:127],v[76:79],a[124:127]
buffer_load_dwordx4 v133,s[8:11],s55 offen offset:1024 lds
s_mov_b32 s30,0x6400
M_ a[128:131],v[96:99],v[80:83],a[128:131]
M_ a[132:135],v[100:103],v[80:83],a[132:135]
M_ a[136:139],v[104:107],v[80:83],a[136:139]
s_mov_b32 s31,0x20000
M_ a[140:143],v[108:111],v[80:83],a[140:143]
M_ a[144:147],v[112:115],v[80:83],a[144:147]
buffer_load_dwordx4 v134,s[8:11],s55 offen offset:2048 lds
M_ a[148:151],v[116:119],v[80:83],a[148:151]
M_ a[152:155],v[120:123],v[80:83],a[152:155]
s_and_b32 s37,s37,0xffff
M_ a[156:159],v[124:127],v[80:83],a[156:159]
M_ a[160:163],v[96:99],v[84:87],a[160:163]
M_ a[164:167],v[100:103],v[84:87],a[164:167]
buffer_load_dwordx4 v135,s[8:11],s55 offen offset:3072 lds
M_ a[168:171],v[104:107],v[84:87],a[168:171]
s_mov_b32 s38,0xc8000
M_ a[172:175],v[108:111],v[84:87],a[172:175]
M_ a[176:179],v[112:115],v[84:87],a[176:179]
M_ a[180:183],v[116:119],v[84:87],a[180:183]
s_add_u32 s55,s20,0x140
M_ a[184:187],v[120:123],v[84:87],a[184:187]
s_add_u32 m0,s23,0x8000
s_mov_b32 s39,0x20000
M_ a[188:191],v[124:127],v[84:87],a[188:191]
buffer_load_dwordx4 v128,s[4:7],s55 offen lds
M_ a[192:195],v[96:99],v[88:91],a[192:195]
M_ a[196:199],v[100:103],v[88:91],a[196:199]
M_ a[200:203],v[104:107],v[88:91],a[200:203]
s_lshl_b32 s40,s2,2
M_ a[204:207],v[108:111],v[88:91],a[204:207]
M_ a[208:211],v[112:115],v[88:91],a[208:211]
buffer_load_dwordx4 v129,s[4:7],s55 offen offset:1024 lds
M_ a[212:215],v[116:119],v[88:91],a[212:215]
M_ a[216:219],v[120:123],v[88:91],a[216:219]
s_add_u32 s40,s40,s3
M_ a[220:223],v[124:127],v[88:91],a[220:223]
M_ a[224:227],v[96:99],v[92:95],a[224:227]
M_ a[228:231],v[100:103],v[92:95],a[228:231]
buffer_load_dwordx4 v130,s[4:7],s55 offen offset:2048 lds
M_ a[232:235],v[104:107],v[92:95],a[232:235]
s_cmp_ge_u32 s40,640
s_cbranch_scc1 LS2x24_%=
s_mul_i32 s41,s40,0xcccd
s_lshr_b32 s41,s41,20
s_mul_i32 s42,s41,20
s_sub_u32 s42,s40,s42
v_and_b32 v162,15,v161
v_lshlrev_b32 v163,3,v162
v_mov_b32 v164,0
v_cmp_le_u32 vcc,5,v162
v_mov_b32 v165,0x9d8
s_nop 0
v_cndmask_b32 v164,v164,v165,vcc
v_cmp_le_u32 vcc,8,v162
v_mov_b32 v165,0x4c0
s_nop 0
v_cndmask_b32 v164,v164,v165,vcc
v_cmp_le_u32 vcc,13,v162
v_mov_b32 v165,0x9b0
s_nop 0
v_cndmask_b32 v164,v164,v165,vcc
v_cmp_le_u32 vcc,15,v162
v_mov_b32 v165,0x7000000
s_nop 0
v_cndmask_b32 v164,v164,v165,vcc
v_add_u32 v163,v163,v164
v_lshrrev_b32 v164,4,v161
v_mul_u32_u24 v164,0xf00,v164
v_add_u32 v163,v163,v164
s_mul_i32 s43,s42,122880
s_mul_i32 s41,s41,40
s_add_u32 s43,s43,s41
buffer_load_dwordx2 v[170:171],v163,s[24:27],s43 offen
s_add_u32 s43,s43,0x3c00
buffer_load_dwordx2 v[172:173],v163,s[24:27],s43 offen
s_add_u32 s43,s43,0x3c00
buffer_load_dwordx2 v[174:175],v163,s[24:27],s43 offen
s_add_u32 s43,s43,0x3c00
buffer_load_dwordx2 v[176:177],v163,s[24:27],s43 offen
s_add_u32 s43,s43,0x3c00
buffer_load_dwordx2 v[178:179],v163,s[24:27],s43 offen
s_add_u32 s43,s43,0x3c00
buffer_load_dwordx2 v[180:181],v163,s[24:27],s43 offen
s_add_u32 s43,s43,0x3c00
buffer_load_dwordx2 v[182:183],v163,s[24:27],s43 offen
s_add_u32 s43,s43,0x3c00
buffer_load_dwordx2 v[184:185],v163,s[24:27],s43 offen
s_branch LT2x24_%=
LS2x24_%=:
s_cmp_ge_u32 s40,672
s_cbranch_scc1 LU2x24_%=
v_and_b32 v162,15,v161
v_lshrrev_b32 v163,4,v161
v_lshrrev_b32 v164,2,v162
v_mul_u32_u24 v164,5,v164
v_and_b32 v165,3,v162
v_add_u32 v166,v164,v165
v_add_u32 v167,4,v164
v_cmp_eq_u32 vcc,0,v165
v_mov_b32 v165,0x1000000
s_nop 0
v_cndmask_b32 v167,v165,v167,vcc
s_sub_u32 s41,s40,640
s_mul_i32 s41,s41,800
v_lshl_add_u32 v162,v163,2,0
v_mul_u32_u24 v162,20,v162
v_add_u32 v162,v162,v166
v_lshlrev_b32 v162,1,v162
buffer_load_ushort v170,v162,s[28:31],s41 offen
v_lshl_add_u32 v162,v163,2,1
v_mul_u32_u24 v162,20,v162
v_add_u32 v162,v162,v166
v_lshlrev_b32 v162,1,v162
buffer_load_ushort v171,v162,s[28:31],s41 offen
v_lshl_add_u32 v162,v163,2,2
v_mul_u32_u24 v162,20,v162
v_add_u32 v162,v162,v166
v_lshlrev_b32 v162,1,v162
buffer_load_ushort v172,v162,s[28:31],s41 offen
v_lshl_add_u32 v162,v163,2,3
v_mul_u32_u24 v162,20,v162
v_add_u32 v162,v162,v166
v_lshlrev_b32 v162,1,v162
buffer_load_ushort v173,v162,s[28:31],s41 offen
v_cmp_eq_u32 vcc,0,v163
v_mov_b32 v162,0x100000
v_mov_b32 v164,16
s_nop 0
v_cndmask_b32 v162,v162,v164,vcc
v_mul_u32_u24 v162,20,v162
v_add_u32 v162,v162,v166
v_lshlrev_b32 v162,1,v162
buffer_load_ushort v174,v162,s[28:31],s41 offen
v_cmp_eq_u32 vcc,0,v163
v_mov_b32 v162,0x100000
v_mov_b32 v164,17
s_nop 0
v_cndmask_b32 v162,v162,v164,vcc
v_mul_u32_u24 v162,20,v162
v_add_u32 v162,v162,v166
v_lshlrev_b32 v162,1,v162
buffer_load_ushort v175,v162,s[28:31],s41 offen
v_cmp_eq_u32 vcc,0,v163
v_mov_b32 v162,0x100000
v_mov_b32 v164,18
s_nop 0
v_cndmask_b32 v162,v162,v164,vcc
v_mul_u32_u24 v162,20,v162
v_add_u32 v162,v162,v166
v_lshlrev_b32 v162,1,v162
buffer_load_ushort v176,v162,s[28:31],s41 offen
v_cmp_eq_u32 vcc,0,v163
v_mov_b32 v162,0x100000
v_mov_b32 v164,19
s_nop 0
v_cndmask_b32 v162,v162,v164,vcc
v_mul_u32_u24 v162,20,v162
v_add_u32 v162,v162,v166
v_lshlrev_b32 v162,1,v162
buffer_load_ushort v177,v162,s[28:31],s41 offen
v_lshl_add_u32 v162,v163,2,0
v_mul_u32_u24 v162,20,v162
v_add_u32 v162,v162,v167
v_lshlrev_b32 v162,1,v162
buffer_load_ushort v178,v162,s[28:31],s41 offen
v_lshl_add_u32 v162,v163,2,1
v_mul_u32_u24 v162,20,v162
v_add_u32 v162,v162,v167
v_lshlrev_b32 v162,1,v162
buffer_load_ushort v179,v162,s[28:31],s41 offen
v_lshl_add_u32 v162,v163,2,2
v_mul_u32_u24 v162,20,v162
v_add_u32 v162,v162,v167
v_lshlrev_b32 v162,1,v162
buffer_load_ushort v180,v162,s[28:31],s41 offen
v_lshl_add_u32 v162,v163,2,3
)A"
R"A(v_mul_u32_u24 v162,20,v162
v_add_u32 v162,v162,v167
v_lshlrev_b32 v162,1,v162
buffer_load_ushort v181,v162,s[28:31],s41 offen
v_cmp_eq_u32 vcc,0,v163
v_mov_b32 v162,0x100000
v_mov_b32 v164,16
s_nop 0
v_cndmask_b32 v162,v162,v164,vcc
v_mul_u32_u24 v162,20,v162
v_add_u32 v162,v162,v167
v_lshlrev_b32 v162,1,v162
buffer_load_ushort v182,v162,s[28:31],s41 offen
v_cmp_eq_u32 vcc,0,v163
v_mov_b32 v162,0x100000
v_mov_b32 v164,17
s_nop 0
v_cndmask_b32 v162,v162,v164,vcc
v_mul_u32_u24 v162,20,v162
v_add_u32 v162,v162,v167
v_lshlrev_b32 v162,1,v162
buffer_load_ushort v183,v162,s[28:31],s41 offen
v_cmp_eq_u32 vcc,0,v163
v_mov_b32 v162,0x100000
v_mov_b32 v164,18
s_nop 0
v_cndmask_b32 v162,v162,v164,vcc
v_mul_u32_u24 v162,20,v162
v_add_u32 v162,v162,v167
v_lshlrev_b32 v162,1,v162
buffer_load_ushort v184,v162,s[28:31],s41 offen
v_cmp_eq_u32 vcc,0,v163
v_mov_b32 v162,0x100000
v_mov_b32 v164,19
s_nop 0
v_cndmask_b32 v162,v162,v164,vcc
v_mul_u32_u24 v162,20,v162
v_add_u32 v162,v162,v167
v_lshlrev_b32 v162,1,v162
buffer_load_ushort v185,v162,s[28:31],s41 offen
s_branch LT2x24_%=
LU2x24_%=:
s_cmp_ge_u32 s40,832
s_cbranch_scc1 LT2x24_%=
s_sub_u32 s43,s40,672
s_lshr_b32 s41,s43,3
s_lshr_b32 s42,s43,1
s_and_b32 s42,s42,3
s_and_b32 s43,s43,1
s_mul_i32 s41,s41,40960
s_mul_i32 s42,s42,320
s_add_u32 s41,s41,s42
s_lshl_b32 s43,s43,7
s_add_u32 s41,s41,s43
v_add_u32 v163,0,v161
v_mul_u32_u24 v164,0xaaab,v163
v_lshrrev_b32 v164,19,v164
v_mul_u32_u24 v165,12,v164
v_sub_u32 v165,v163,v165
v_mul_u32_u24 v164,0x500,v164
v_lshl_add_u32 v164,v165,4,v164
buffer_load_dwordx4 v[170:173],v164,s[36:39],s41 offen
v_add_u32 v163,64,v161
v_mul_u32_u24 v164,0xaaab,v163
v_lshrrev_b32 v164,19,v164
v_mul_u32_u24 v165,12,v164
v_sub_u32 v165,v163,v165
v_mul_u32_u24 v164,0x500,v164
v_lshl_add_u32 v164,v165,4,v164
buffer_load_dwordx4 v[174:177],v164,s[36:39],s41 offen
v_add_u32 v163,128,v161
v_mul_u32_u24 v164,0xaaab,v163
v_lshrrev_b32 v164,19,v164
v_mul_u32_u24 v165,12,v164
v_sub_u32 v165,v163,v165
v_mul_u32_u24 v164,0x500,v164
v_lshl_add_u32 v164,v165,4,v164
buffer_load_dwordx4 v[178:181],v164,s[36:39],s41 offen
v_add_u32 v163,192,v161
v_mul_u32_u24 v164,0xaaab,v163
v_lshrrev_b32 v164,19,v164
v_mul_u32_u24 v165,12,v164
v_sub_u32 v165,v163,v165
v_mul_u32_u24 v164,0x500,v164
v_lshl_add_u32 v164,v165,4,v164
buffer_load_dwordx4 v[182:185],v164,s[36:39],s41 offen
v_add_u32 v163,256,v161
v_mul_u32_u24 v164,0xaaab,v163
v_lshrrev_b32 v164,19,v164
v_mul_u32_u24 v165,12,v164
v_sub_u32 v165,v163,v165
v_mul_u32_u24 v164,0x500,v164
v_lshl_add_u32 v164,v165,4,v164
buffer_load_dwordx4 v[186:189],v164,s[36:39],s41 offen
v_add_u32 v163,320,v161
v_mul_u32_u24 v164,0xaaab,v163
v_lshrrev_b32 v164,19,v164
v_mul_u32_u24 v165,12,v164
v_sub_u32 v165,v163,v165
v_mul_u32_u24 v164,0x500,v164
v_lshl_add_u32 v164,v165,4,v164
buffer_load_dwordx4 v[190:193],v164,s[36:39],s41 offen
LT2x24_%=:
M_ a[236:239],v[108:111],v[92:95],a[236:239]
M_ a[240:243],v[112:115],v[92:95],a[240:243]
M_ a[244:247],v[116:119],v[92:95],a[244:247]
M_ a[248:251],v[120:123],v[92:95],a[248:251]
buffer_load_dwordx4 v131,s[4:7],s55 offen offset:3072 lds
M_ a[252:255],v[124:127],v[92:95],a[252:255]
s_waitcnt vmcnt(16)
s_waitcnt lgkmcnt(0)
s_barrier
M_ a[0:3],v[32:35],v[0:3],a[0:3]
T_ v[64:65],v142,32768
M_ a[4:7],v[36:39],v[0:3],a[4:7]
T_ v[66:67],v142,33792
M_ a[8:11],v[40:43],v[0:3],a[8:11]
R_ v[96:99],v137,32768
M_ a[12:15],v[44:47],v[0:3],a[12:15]
R_ v[100:103],v137,33792
M_ a[16:19],v[48:51],v[0:3],a[16:19]
R_ v[104:107],v137,34816
s_add_u32 s62,s62,0x100000
M_ a[20:23],v[52:55],v[0:3],a[20:23]
R_ v[108:111],v137,35840
M_ a[24:27],v[56:59],v[0:3],a[24:27]
R_ v[112:115],v137,36864
M_ a[28:31],v[60:63],v[0:3],a[28:31]
R_ v[116:119],v137,37888
M_ a[32:35],v[32:35],v[4:7],a[32:35]
R_ v[120:123],v137,38912
M_ a[36:39],v[36:39],v[4:7],a[36:39]
R_ v[124:127],v137,39936
M_ a[40:43],v[40:43],v[4:7],a[40:43]
T_ v[68:69],v143,32768
M_ a[44:47],v[44:47],v[4:7],a[44:47]
T_ v[70:71],v143,33792
M_ a[48:51],v[48:51],v[4:7],a[48:51]
T_ v[72:73],v142,33024
M_ a[52:55],v[52:55],v[4:7],a[52:55]
T_ v[74:75],v142,34048
M_ a[56:59],v[56:59],v[4:7],a[56:59]
T_ v[76:77],v143,33024
M_ a[60:63],v[60:63],v[4:7],a[60:63]
T_ v[78:79],v143,34048
M_ a[64:67],v[32:35],v[8:11],a[64:67]
T_ v[80:81],v142,33280
M_ a[68:71],v[36:39],v[8:11],a[68:71]
T_ v[82:83],v142,34304
M_ a[72:75],v[40:43],v[8:11],a[72:75]
T_ v[84:85],v143,33280
M_ a[76:79],v[44:47],v[8:11],a[76:79]
T_ v[86:87],v143,34304
M_ a[80:83],v[48:51],v[8:11],a[80:83]
T_ v[88:89],v142,33536
M_ a[84:87],v[52:55],v[8:11],a[84:87]
T_ v[90:91],v142,34560
M_ a[88:91],v[56:59],v[8:11],a[88:91]
T_ v[92:93],v143,33536
M_ a[92:95],v[60:63],v[8:11],a[92:95]
T_ v[94:95],v143,34560
M_ a[96:99],v[32:35],v[12:15],a[96:99]
s_add_u32 s55,s21,0x180000
M_ a[100:103],v[36:39],v[12:15],a[100:103]
s_add_u32 m0,s22,0x10000
M_ a[104:107],v[40:43],v[12:15],a[104:107]
buffer_load_dwordx4 v132,s[8:11],s55 offen lds
M_ a[108:111],v[44:47],v[12:15],a[108:111]
M_ a[112:115],v[48:51],v[12:15],a[112:115]
M_ a[116:119],v[52:55],v[12:15],a[116:119]
M_ a[120:123],v[56:59],v[12:15],a[120:123]
M_ a[124:127],v[60:63],v[12:15],a[124:127]
buffer_load_dwordx4 v133,s[8:11],s55 offen offset:1024 lds
M_ a[128:131],v[32:35],v[16:19],a[128:131]
M_ a[132:135],v[36:39],v[16:19],a[132:135]
buffer_load_dwordx4 v[240:243],v248,s[64:67],s62 offen
M_ a[136:139],v[40:43],v[16:19],a[136:139]
M_ a[140:143],v[44:47],v[16:19],a[140:143]
M_ a[144:147],v[48:51],v[16:19],a[144:147]
buffer_load_dwordx4 v134,s[8:11],s55 offen offset:2048 lds
M_ a[148:151],v[52:55],v[16:19],a[148:151]
M_ a[152:155],v[56:59],v[16:19],a[152:155]
M_ a[156:159],v[60:63],v[16:19],a[156:159]
M_ a[160:163],v[32:35],v[20:23],a[160:163]
M_ a[164:167],v[36:39],v[20:23],a[164:167]
buffer_load_dwordx4 v135,s[8:11],s55 offen offset:3072 lds
M_ a[168:171],v[40:43],v[20:23],a[168:171]
M_ a[172:175],v[44:47],v[20:23],a[172:175]
M_ a[176:179],v[48:51],v[20:23],a[176:179]
M_ a[180:183],v[52:55],v[20:23],a[180:183]
s_add_u32 s55,s20,0x180
M_ a[184:187],v[56:59],v[20:23],a[184:187]
s_add_u32 m0,s23,0x10000
M_ a[188:191],v[60:63],v[20:23],a[188:191]
buffer_load_dwordx4 v128,s[4:7],s55 offen lds
M_ a[192:195],v[32:35],v[24:27],a[192:195]
M_ a[196:199],v[36:39],v[24:27],a[196:199]
M_ a[200:203],v[40:43],v[24:27],a[200:203]
M_ a[204:207],v[44:47],v[24:27],a[204:207]
M_ a[208:211],v[48:51],v[24:27],a[208:211]
buffer_load_dwordx4 v129,s[4:7],s55 offen offset:1024 lds
M_ a[212:215],v[52:55],v[24:27],a[212:215]
M_ a[216:219],v[56:59],v[24:27],a[216:219]
M_ a[220:223],v[60:63],v[24:27],a[220:223]
M_ a[224:227],v[32:35],v[28:31],a[224:227]
M_ a[228:231],v[36:39],v[28:31],a[228:231]
buffer_load_dwordx4 v130,s[4:7],s55 offen offset:2048 lds
M_ a[232:235],v[40:43],v[28:31],a[232:235]
M_ a[236:239],v[44:47],v[28:31],a[236:239]
M_ a[240:243],v[48:51],v[28:31],a[240:243]
M_ a[244:247],v[52:55],v[28:31],a[244:247]
M_ a[248:251],v[56:59],v[28:31],a[248:251]
buffer_load_dwordx4 v131,s[4:7],s55 offen offset:3072 lds
M_ a[252:255],v[60:63],v[28:31],a[252:255]
s_waitcnt vmcnt(17)
s_waitcnt lgkmcnt(0)
s_barrier
M_ a[0:3],v[96:99],v[64:67],a[0:3]
ds_read_b64_tr_b16 v[0:1],v140
M_ a[4:7],v[100:103],v[64:67],a[4:7]
T_ v[2:3],v140,1024
M_ a[8:11],v[104:107],v[64:67],a[8:11]
ds_read_b128 v[32:35],v136
M_ a[12:15],v[108:111],v[64:67],a[12:15]
R_ v[36:39],v136,1024
M_ a[16:19],v[112:115],v[64:67],a[16:19]
R_ v[40:43],v136,2048
s_add_u32 s62,s62,0x100000
M_ a[20:23],v[116:119],v[64:67],a[20:23]
R_ v[44:47],v136,3072
M_ a[24:27],v[120:123],v[64:67],a[24:27]
R_ v[48:51],v136,4096
M_ a[28:31],v[124:127],v[64:67],a[28:31]
R_ v[52:55],v136,5120
M_ a[32:35],v[96:99],v[68:71],a[32:35]
R_ v[56:59],v136,6144
M_ a[36:39],v[100:103],v[68:71],a[36:39]
R_ v[60:63],v136,7168
M_ a[40:43],v[104:107],v[68:71],a[40:43]
ds_read_b64_tr_b16 v[4:5],v141
M_ a[44:47],v[108:111],v[68:71],a[44:47]
T_ v[6:7],v141,1024
M_ a[48:51],v[112:115],v[68:71],a[48:51]
T_ v[8:9],v140,256
M_ a[52:55],v[116:119],v[68:71],a[52:55]
T_ v[10:11],v140,1280
M_ a[56:59],v[120:123],v[68:71],a[56:59]
T_ v[12:13],v141,256
M_ a[60:63],v[124:127],v[68:71],a[60:63]
T_ v[14:15],v141,1280
M_ a[64:67],v[96:99],v[72:75],a[64:67]
T_ v[16:17],v140,512
M_ a[68:71],v[100:103],v[72:75],a[68:71]
T_ v[18:19],v140,1536
M_ a[72:75],v[104:107],v[72:75],a[72:75]
T_ v[20:21],v141,512
M_ a[76:79],v[108:111],v[72:75],a[76:79]
T_ v[22:23],v141,1536
M_ a[80:83],v[112:115],v[72:75],a[80:83]
T_ v[24:25],v140,768
M_ a[84:87],v[116:119],v[72:75],a[84:87]
T_ v[26:27],v140,1792
M_ a[88:91],v[120:123],v[72:75],a[88:91]
T_ v[28:29],v141,768
M_ a[92:95],v[124:127],v[72:75],a[92:95]
T_ v[30:31],v141,1792
M_ a[96:99],v[96:99],v[76:79],a[96:99]
s_add_u32 s55,s21,0x1c0000
M_ a[100:103],v[100:103],v[76:79],a[100:103]
s_add_u32 m0,s22,0x18000
M_ a[104:107],v[104:107],v[76:79],a[104:107]
buffer_load_dwordx4 v132,s[8:11],s55 offen lds
M_ a[108:111],v[108:111],v[76:79],a[108:111]
M_ a[112:115],v[112:115],v[76:79],a[112:115]
M_ a[116:119],v[116:119],v[76:79],a[116:119]
M_ a[120:123],v[120:123],v[76:79],a[120:123]
M_ a[124:127],v[124:127],v[76:79],a[124:127]
buffer_load_dwordx4 v133,s[8:11],s55 offen offset:1024 lds
M_ a[128:131],v[96:99],v[80:83],a[128:131]
M_ a[132:135],v[100:103],v[80:83],a[132:135]
buffer_load_dwordx4 v[240:243],v248,s[64:67],s62 offen
M_ a[136:139],v[104:107],v[80:83],a[136:139]
M_ a[140:143],v[108:111],v[80:83],a[140:143]
M_ a[144:147],v[112:115],v[80:83],a[144:147]
buffer_load_dwordx4 v134,s[8:11],s55 offen offset:2048 lds
M_ a[148:151],v[116:119],v[80:83],a[148:151]
M_ a[152:155],v[120:123],v[80:83],a[152:155]
M_ a[156:159],v[124:127],v[80:83],a[156:159]
M_ a[160:163],v[96:99],v[84:87],a[160:163]
M_ a[164:167],v[100:103],v[84:87],a[164:167]
buffer_load_dwordx4 v135,s[8:11],s55 offen offset:3072 lds
M_ a[168:171],v[104:107],v[84:87],a[168:171]
M_ a[172:175],v[108:111],v[84:87],a[172:175]
M_ a[176:179],v[112:115],v[84:87],a[176:179]
M_ a[180:183],v[116:119],v[84:87],a[180:183]
s_add_u32 s55,s20,0x1c0
M_ a[184:187],v[120:123],v[84:87],a[184:187]
s_add_u32 m0,s23,0x18000
M_ a[188:191],v[124:127],v[84:87],a[188:191]
buffer_load_dwordx4 v128,s[4:7],s55 offen lds
M_ a[192:195],v[96:99],v[88:91],a[192:195]
M_ a[196:199],v[100:103],v[88:91],a[196:199]
M_ a[200:203],v[104:107],v[88:91],a[200:203]
M_ a[204:207],v[108:111],v[88:91],a[204:207]
M_ a[208:211],v[112:115],v[88:91],a[208:211]
buffer_load_dwordx4 v129,s[4:7],s55 offen offset:1024 lds
M_ a[212:215],v[116:119],v[88:91],a[212:215]
M_ a[216:219],v[120:123],v[88:91],a[216:219]
M_ a[220:223],v[124:127],v[88:91],a[220:223]
M_ a[224:227],v[96:99],v[92:95],a[224:227]
M_ a[228:231],v[100:103],v[92:95],a[228:231]
buffer_load_dwordx4 v130,s[4:7],s55 offen offset:2048 lds
M_ a[232:235],v[104:107],v[92:95],a[232:235]
M_ a[236:239],v[108:111],v[92:95],a[236:239]
M_ a[240:243],v[112:115],v[92:95],a[240:243]
M_ a[244:247],v[116:119],v[92:95],a[244:247]
M_ a[248:251],v[120:123],v[92:95],a[248:251]
buffer_load_dwordx4 v131,s[4:7],s55 offen offset:3072 lds
M_ a[252:255],v[124:127],v[92:95],a[252:255]
s_waitcnt vmcnt(18)
s_waitcnt lgkmcnt(0)
s_barrier
M_ a[0:3],v[32:35],v[0:3],a[0:3]
T_ v[64:65],v140,32768
M_ a[4:7],v[36:39],v[0:3],a[4:7]
T_ v[66:67],v140,33792
M_ a[8:11],v[40:43],v[0:3],a[8:11]
R_ v[96:99],v136,32768
M_ a[12:15],v[44:47],v[0:3],a[12:15]
R_ v[100:103],v136,33792
M_ a[16:19],v[48:51],v[0:3],a[16:19]
R_ v[104:107],v136,34816
v_mbcnt_lo_u32_b32 v248,-1,0
M_ a[20:23],v[52:55],v[0:3],a[20:23]
R_ v[108:111],v136,35840
M_ a[24:27],v[56:59],v[0:3],a[24:27]
R_ v[112:115],v136,36864
M_ a[28:31],v[60:63],v[0:3],a[28:31]
R_ v[116:119],v136,37888
M_ a[32:35],v[32:35],v[4:7],a[32:35]
R_ v[120:123],v136,38912
v_mbcnt_hi_u32_b32 v248,-1,v248
M_ a[36:39],v[36:39],v[4:7],a[36:39]
R_ v[124:127],v136,39936
M_ a[40:43],v[40:43],v[4:7],a[40:43]
T_ v[68:69],v141,32768
M_ a[44:47],v[44:47],v[4:7],a[44:47]
T_ v[70:71],v141,33792
M_ a[48:51],v[48:51],v[4:7],a[48:51]
T_ v[72:73],v140,33024
v_lshlrev_b32 v248,4,v248
M_ a[52:55],v[52:55],v[4:7],a[52:55]
T_ v[74:75],v140,34048
M_ a[56:59],v[56:59],v[4:7],a[56:59]
T_ v[76:77],v141,33024
M_ a[60:63],v[60:63],v[4:7],a[60:63]
T_ v[78:79],v141,34048
M_ a[64:67],v[32:35],v[8:11],a[64:67]
T_ v[80:81],v140,33280
s_mov_b32 s60,s68
M_ a[68:71],v[36:39],v[8:11],a[68:71]
T_ v[82:83],v140,34304
M_ a[72:75],v[40:43],v[8:11],a[72:75]
T_ v[84:85],v141,33280
M_ a[76:79],v[44:47],v[8:11],a[76:79]
T_ v[86:87],v141,34304
M_ a[80:83],v[48:51],v[8:11],a[80:83]
T_ v[88:89],v140,33536
s_mov_b32 s61,s69
M_ a[84:87],v[52:55],v[8:11],a[84:87]
T_ v[90:91],v140,34560
M_ a[88:91],v[56:59],v[8:11],a[88:91]
T_ v[92:93],v141,33536
M_ a[92:95],v[60:63],v[8:11],a[92:95]
T_ v[94:95],v141,34560
M_ a[96:99],v[32:35],v[12:15],a[96:99]
s_add_u32 s55,s21,0x200000
s_mov_b32 s64,s60
M_ a[100:103],v[36:39],v[12:15],a[100:103]
s_add_u32 m0,s22,0x0
M_ a[104:107],v[40:43],v[12:15],a[104:107]
buffer_load_dwordx4 v132,s[8:11],s55 offen lds
M_ a[108:111],v[44:47],v[12:15],a[108:111]
M_ a[112:115],v[48:51],v[12:15],a[112:115]
s_mov_b32 s65,s61
M_ a[116:119],v[52:55],v[12:15],a[116:119]
M_ a[120:123],v[56:59],v[12:15],a[120:123]
M_ a[124:127],v[60:63],v[12:15],a[124:127]
buffer_load_dwordx4 v133,s[8:11],s55 offen offset:1024 lds
M_ a[128:131],v[32:35],v[16:19],a[128:131]
M_ a[132:135],v[36:39],v[16:19],a[132:135]
s_and_b32 s65,s65,0xffff
M_ a[136:139],v[40:43],v[16:19],a[136:139]
M_ a[140:143],v[44:47],v[16:19],a[140:143]
M_ a[144:147],v[48:51],v[16:19],a[144:147]
buffer_load_dwordx4 v134,s[8:11],s55 offen offset:2048 lds
M_ a[148:151],v[52:55],v[16:19],a[148:151]
s_mov_b32 s66,0x280000
M_ a[152:155],v[56:59],v[16:19],a[152:155]
M_ a[156:159],v[60:63],v[16:19],a[156:159]
M_ a[160:163],v[32:35],v[20:23],a[160:163]
M_ a[164:167],v[36:39],v[20:23],a[164:167]
buffer_load_dwordx4 v135,s[8:11],s55 offen offset:3072 lds
s_mov_b32 s67,0x20000
M_ a[168:171],v[40:43],v[20:23],a[168:171]
M_ a[172:175],v[44:47],v[20:23],a[172:175]
M_ a[176:179],v[48:51],v[20:23],a[176:179]
M_ a[180:183],v[52:55],v[20:23],a[180:183]
s_add_u32 s55,s20,0x200
s_lshl_b32 s62,s2,2
M_ a[184:187],v[56:59],v[20:23],a[184:187]
)A"
R"A(s_add_u32 m0,s23,0x0
M_ a[188:191],v[60:63],v[20:23],a[188:191]
buffer_load_dwordx4 v128,s[4:7],s55 offen lds
M_ a[192:195],v[32:35],v[24:27],a[192:195]
M_ a[196:199],v[36:39],v[24:27],a[196:199]
s_add_u32 s62,s62,s3
M_ a[200:203],v[40:43],v[24:27],a[200:203]
M_ a[204:207],v[44:47],v[24:27],a[204:207]
M_ a[208:211],v[48:51],v[24:27],a[208:211]
buffer_load_dwordx4 v129,s[4:7],s55 offen offset:1024 lds
M_ a[212:215],v[52:55],v[24:27],a[212:215]
s_lshl_b32 s62,s62,10
M_ a[216:219],v[56:59],v[24:27],a[216:219]
M_ a[220:223],v[60:63],v[24:27],a[220:223]
M_ a[224:227],v[32:35],v[28:31],a[224:227]
M_ a[228:231],v[36:39],v[28:31],a[228:231]
buffer_load_dwordx4 v130,s[4:7],s55 offen offset:2048 lds
buffer_load_dwordx4 v[240:243],v248,s[64:67],s62 offen
M_ a[232:235],v[40:43],v[28:31],a[232:235]
M_ a[236:239],v[44:47],v[28:31],a[236:239]
M_ a[240:243],v[48:51],v[28:31],a[240:243]
M_ a[244:247],v[52:55],v[28:31],a[244:247]
M_ a[248:251],v[56:59],v[28:31],a[248:251]
buffer_load_dwordx4 v131,s[4:7],s55 offen offset:3072 lds
M_ a[252:255],v[60:63],v[28:31],a[252:255]
s_waitcnt vmcnt(18)
s_waitcnt lgkmcnt(0)
s_barrier
M_ a[0:3],v[96:99],v[64:67],a[0:3]
ds_read_b64_tr_b16 v[0:1],v142
M_ a[4:7],v[100:103],v[64:67],a[4:7]
T_ v[2:3],v142,1024
M_ a[8:11],v[104:107],v[64:67],a[8:11]
ds_read_b128 v[32:35],v137
M_ a[12:15],v[108:111],v[64:67],a[12:15]
R_ v[36:39],v137,1024
M_ a[16:19],v[112:115],v[64:67],a[16:19]
R_ v[40:43],v137,2048
s_cmp_ge_u32 s40,640
s_cbranch_scc1 LS3x25_%=
s_mul_i32 s42,s3,8192
s_add_u32 s42,s42,0x20000
v_lshlrev_b32 v164,3,v161
v_add_u32 v164,s42,v164
ds_write_b64 v164,v[170:171]
ds_write_b64 v164,v[172:173] offset:512
ds_write_b64 v164,v[174:175] offset:1024
ds_write_b64 v164,v[176:177] offset:1536
ds_write_b64 v164,v[178:179] offset:2048
ds_write_b64 v164,v[180:181] offset:2560
ds_write_b64 v164,v[182:183] offset:3072
ds_write_b64 v164,v[184:185] offset:3584
v_lshrrev_b32 v162,4,v161
v_lshlrev_b32 v162,10,v162
v_lshrrev_b32 v165,2,v161
v_and_b32 v165,3,v165
v_lshl_add_u32 v162,v165,7,v162
v_and_b32 v165,3,v161
v_lshl_add_u32 v162,v165,3,v162
v_add_u32 v162,s42,v162
s_waitcnt lgkmcnt(0)
ds_read_b64_tr_b16 v[194:195],v162
T_ v[196:197],v162,512
T_ v[198:199],v162,32
T_ v[200:201],v162,544
T_ v[202:203],v162,64
T_ v[204:205],v162,576
T_ v[206:207],v162,96
T_ v[208:209],v162,608
s_lshl_b32 s41,s40,12
s_add_u32 s41,s41,0x3000000
s_add_u32 s44,s88,s41
s_addc_u32 s45,s89,0
s_waitcnt lgkmcnt(6)
global_store_dwordx4 v160,v[194:197],s[44:45] sc1
s_waitcnt lgkmcnt(4)
global_store_dwordx4 v160,v[198:201],s[44:45] offset:1024 sc1
s_waitcnt lgkmcnt(2)
global_store_dwordx4 v160,v[202:205],s[44:45] offset:2048 sc1
s_waitcnt lgkmcnt(0)
global_store_dwordx4 v160,v[206:209],s[44:45] offset:3072 sc1
s_branch LT3x25_%=
LS3x25_%=:
s_cmp_ge_u32 s40,672
s_cbranch_scc1 LU3x25_%=
v_lshl_or_b32 v194,v171,16,v170
v_lshl_or_b32 v195,v173,16,v172
v_lshl_or_b32 v196,v175,16,v174
v_lshl_or_b32 v197,v177,16,v176
v_lshl_or_b32 v198,v179,16,v178
v_lshl_or_b32 v199,v181,16,v180
v_lshl_or_b32 v200,v183,16,v182
v_lshl_or_b32 v201,v185,16,v184
s_sub_u32 s41,s40,640
s_lshl_b32 s41,s41,11
s_add_u32 s41,s41,0x3300000
s_add_u32 s44,s88,s41
s_addc_u32 s45,s89,0
global_store_dwordx4 v160,v[194:197],s[44:45] sc1
global_store_dwordx4 v160,v[198:201],s[44:45] offset:1024 sc1
s_branch LT3x25_%=
LU3x25_%=:
s_cmp_ge_u32 s40,832
s_cbranch_scc1 LT3x25_%=
s_mul_i32 s42,s3,8192
s_add_u32 s42,s42,0x20000
v_add_u32 v162,s42,v160
ds_write_b128 v162,v[170:173]
ds_write_b128 v162,v[174:177] offset:1024
ds_write_b128 v162,v[178:181] offset:2048
ds_write_b128 v162,v[182:185] offset:3072
ds_write_b128 v162,v[186:189] offset:4096
ds_write_b128 v162,v[190:193] offset:5120
v_lshrrev_b32 v162,4,v161
v_lshlrev_b32 v162,3,v162
v_lshrrev_b32 v165,2,v161
v_and_b32 v165,3,v165
v_add_u32 v162,v162,v165
v_mul_u32_u24 v162,0xc0,v162
v_and_b32 v165,3,v161
v_lshl_add_u32 v162,v165,4,v162
v_add_u32 v162,s42,v162
s_sub_u32 s43,s40,672
s_lshr_b32 s41,s43,3
s_lshr_b32 s42,s43,1
s_and_b32 s42,s42,3
s_and_b32 s43,s43,1
s_lshl_b32 s44,s43,3
v_add_u32 v162,s44,v162
s_mul_i32 s44,s43,48
v_add_u32 v163,s44,v162
s_waitcnt lgkmcnt(0)
ds_read_b64_tr_b16 v[194:195],v162
T_ v[196:197],v162,768
T_ v[198:199],v163,8
T_ v[200:201],v163,776
T_ v[202:203],v162,64
T_ v[204:205],v162,832
T_ v[206:207],v163,72
T_ v[208:209],v163,840
T_ v[210:211],v162,128
T_ v[212:213],v162,896
s_mul_i32 s42,s42,20
s_add_u32 s41,s41,s42
s_mul_i32 s41,s41,10
s_mul_i32 s43,s43,5
s_add_u32 s41,s41,s43
s_lshl_b32 s41,s41,10
s_add_u32 s41,s41,0x3a00000
s_add_u32 s44,s88,s41
s_addc_u32 s45,s89,0
v_add_u32 v164,4096,v160
s_waitcnt lgkmcnt(8)
global_store_dwordx4 v160,v[194:197],s[44:45] sc1
s_waitcnt lgkmcnt(6)
global_store_dwordx4 v160,v[198:201],s[44:45] offset:1024 sc1
s_waitcnt lgkmcnt(4)
global_store_dwordx4 v160,v[202:205],s[44:45] offset:2048 sc1
s_waitcnt lgkmcnt(2)
global_store_dwordx4 v160,v[206:209],s[44:45] offset:3072 sc1
s_waitcnt lgkmcnt(0)
global_store_dwordx4 v164,v[210:213],s[44:45] sc1
LT3x25_%=:
M_ a[20:23],v[116:119],v[64:67],a[20:23]
R_ v[44:47],v137,3072
M_ a[24:27],v[120:123],v[64:67],a[24:27]
R_ v[48:51],v137,4096
M_ a[28:31],v[124:127],v[64:67],a[28:31]
R_ v[52:55],v137,5120
M_ a[32:35],v[96:99],v[68:71],a[32:35]
R_ v[56:59],v137,6144
M_ a[36:39],v[100:103],v[68:71],a[36:39]
R_ v[60:63],v137,7168
M_ a[40:43],v[104:107],v[68:71],a[40:43]
ds_read_b64_tr_b16 v[4:5],v143
M_ a[44:47],v[108:111],v[68:71],a[44:47]
T_ v[6:7],v143,1024
M_ a[48:51],v[112:115],v[68:71],a[48:51]
T_ v[8:9],v142,256
M_ a[52:55],v[116:119],v[68:71],a[52:55]
T_ v[10:11],v142,1280
M_ a[56:59],v[120:123],v[68:71],a[56:59]
T_ v[12:13],v143,256
M_ a[60:63],v[124:127],v[68:71],a[60:63]
T_ v[14:15],v143,1280
M_ a[64:67],v[96:99],v[72:75],a[64:67]
T_ v[16:17],v142,512
M_ a[68:71],v[100:103],v[72:75],a[68:71]
T_ v[18:19],v142,1536
M_ a[72:75],v[104:107],v[72:75],a[72:75]
T_ v[20:21],v143,512
M_ a[76:79],v[108:111],v[72:75],a[76:79]
T_ v[22:23],v143,1536
M_ a[80:83],v[112:115],v[72:75],a[80:83]
T_ v[24:25],v142,768
M_ a[84:87],v[116:119],v[72:75],a[84:87]
T_ v[26:27],v142,1792
M_ a[88:91],v[120:123],v[72:75],a[88:91]
T_ v[28:29],v143,768
M_ a[92:95],v[124:127],v[72:75],a[92:95]
T_ v[30:31],v143,1792
M_ a[96:99],v[96:99],v[76:79],a[96:99]
s_add_u32 s55,s21,0x240000
M_ a[100:103],v[100:103],v[76:79],a[100:103]
s_add_u32 m0,s22,0x8000
M_ a[104:107],v[104:107],v[76:79],a[104:107]
buffer_load_dwordx4 v132,s[8:11],s55 offen lds
M_ a[108:111],v[108:111],v[76:79],a[108:111]
M_ a[112:115],v[112:115],v[76:79],a[112:115]
M_ a[116:119],v[116:119],v[76:79],a[116:119]
M_ a[120:123],v[120:123],v[76:79],a[120:123]
M_ a[124:127],v[124:127],v[76:79],a[124:127]
buffer_load_dwordx4 v133,s[8:11],s55 offen offset:1024 lds
M_ a[128:131],v[96:99],v[80:83],a[128:131]
M_ a[132:135],v[100:103],v[80:83],a[132:135]
M_ a[136:139],v[104:107],v[80:83],a[136:139]
M_ a[140:143],v[108:111],v[80:83],a[140:143]
M_ a[144:147],v[112:115],v[80:83],a[144:147]
buffer_load_dwordx4 v134,s[8:11],s55 offen offset:2048 lds
M_ a[148:151],v[116:119],v[80:83],a[148:151]
M_ a[152:155],v[120:123],v[80:83],a[152:155]
M_ a[156:159],v[124:127],v[80:83],a[156:159]
M_ a[160:163],v[96:99],v[84:87],a[160:163]
M_ a[164:167],v[100:103],v[84:87],a[164:167]
buffer_load_dwordx4 v135,s[8:11],s55 offen offset:3072 lds
M_ a[168:171],v[104:107],v[84:87],a[168:171]
M_ a[172:175],v[108:111],v[84:87],a[172:175]
M_ a[176:179],v[112:115],v[84:87],a[176:179]
M_ a[180:183],v[116:119],v[84:87],a[180:183]
s_add_u32 s55,s20,0x240
M_ a[184:187],v[120:123],v[84:87],a[184:187]
s_add_u32 m0,s23,0x8000
M_ a[188:191],v[124:127],v[84:87],a[188:191]
buffer_load_dwordx4 v128,s[4:7],s55 offen lds
M_ a[192:195],v[96:99],v[88:91],a[192:195]
M_ a[196:199],v[100:103],v[88:91],a[196:199]
M_ a[200:203],v[104:107],v[88:91],a[200:203]
M_ a[204:207],v[108:111],v[88:91],a[204:207]
M_ a[208:211],v[112:115],v[88:91],a[208:211]
buffer_load_dwordx4 v129,s[4:7],s55 offen offset:1024 lds
M_ a[212:215],v[116:119],v[88:91],a[212:215]
M_ a[216:219],v[120:123],v[88:91],a[216:219]
M_ a[220:223],v[124:127],v[88:91],a[220:223]
M_ a[224:227],v[96:99],v[92:95],a[224:227]
M_ a[228:231],v[100:103],v[92:95],a[228:231]
buffer_load_dwordx4 v130,s[4:7],s55 offen offset:2048 lds
M_ a[232:235],v[104:107],v[92:95],a[232:235]
M_ a[236:239],v[108:111],v[92:95],a[236:239]
M_ a[240:243],v[112:115],v[92:95],a[240:243]
M_ a[244:247],v[116:119],v[92:95],a[244:247]
M_ a[248:251],v[120:123],v[92:95],a[248:251]
buffer_load_dwordx4 v131,s[4:7],s55 offen offset:3072 lds
M_ a[252:255],v[124:127],v[92:95],a[252:255]
s_waitcnt vmcnt(17)
s_waitcnt lgkmcnt(0)
s_barrier
M_ a[0:3],v[32:35],v[0:3],a[0:3]
T_ v[64:65],v142,32768
M_ a[4:7],v[36:39],v[0:3],a[4:7]
T_ v[66:67],v142,33792
M_ a[8:11],v[40:43],v[0:3],a[8:11]
R_ v[96:99],v137,32768
M_ a[12:15],v[44:47],v[0:3],a[12:15]
R_ v[100:103],v137,33792
M_ a[16:19],v[48:51],v[0:3],a[16:19]
R_ v[104:107],v137,34816
s_add_u32 s62,s62,0x100000
M_ a[20:23],v[52:55],v[0:3],a[20:23]
R_ v[108:111],v137,35840
M_ a[24:27],v[56:59],v[0:3],a[24:27]
R_ v[112:115],v137,36864
M_ a[28:31],v[60:63],v[0:3],a[28:31]
R_ v[116:119],v137,37888
M_ a[32:35],v[32:35],v[4:7],a[32:35]
R_ v[120:123],v137,38912
M_ a[36:39],v[36:39],v[4:7],a[36:39]
R_ v[124:127],v137,39936
M_ a[40:43],v[40:43],v[4:7],a[40:43]
T_ v[68:69],v143,32768
M_ a[44:47],v[44:47],v[4:7],a[44:47]
T_ v[70:71],v143,33792
M_ a[48:51],v[48:51],v[4:7],a[48:51]
T_ v[72:73],v142,33024
M_ a[52:55],v[52:55],v[4:7],a[52:55]
T_ v[74:75],v142,34048
M_ a[56:59],v[56:59],v[4:7],a[56:59]
T_ v[76:77],v143,33024
M_ a[60:63],v[60:63],v[4:7],a[60:63]
T_ v[78:79],v143,34048
M_ a[64:67],v[32:35],v[8:11],a[64:67]
T_ v[80:81],v142,33280
M_ a[68:71],v[36:39],v[8:11],a[68:71]
T_ v[82:83],v142,34304
M_ a[72:75],v[40:43],v[8:11],a[72:75]
T_ v[84:85],v143,33280
M_ a[76:79],v[44:47],v[8:11],a[76:79]
T_ v[86:87],v143,34304
M_ a[80:83],v[48:51],v[8:11],a[80:83]
T_ v[88:89],v142,33536
M_ a[84:87],v[52:55],v[8:11],a[84:87]
T_ v[90:91],v142,34560
M_ a[88:91],v[56:59],v[8:11],a[88:91]
T_ v[92:93],v143,33536
M_ a[92:95],v[60:63],v[8:11],a[92:95]
T_ v[94:95],v143,34560
M_ a[96:99],v[32:35],v[12:15],a[96:99]
s_add_u32 s55,s21,0x280000
M_ a[100:103],v[36:39],v[12:15],a[100:103]
s_add_u32 m0,s22,0x10000
M_ a[104:107],v[40:43],v[12:15],a[104:107]
buffer_load_dwordx4 v132,s[8:11],s55 offen lds
M_ a[108:111],v[44:47],v[12:15],a[108:111]
M_ a[112:115],v[48:51],v[12:15],a[112:115]
M_ a[116:119],v[52:55],v[12:15],a[116:119]
M_ a[120:123],v[56:59],v[12:15],a[120:123]
M_ a[124:127],v[60:63],v[12:15],a[124:127]
buffer_load_dwordx4 v133,s[8:11],s55 offen offset:1024 lds
M_ a[128:131],v[32:35],v[16:19],a[128:131]
M_ a[132:135],v[36:39],v[16:19],a[132:135]
buffer_load_dwordx4 v[240:243],v248,s[64:67],s62 offen
M_ a[136:139],v[40:43],v[16:19],a[136:139]
M_ a[140:143],v[44:47],v[16:19],a[140:143]
M_ a[144:147],v[48:51],v[16:19],a[144:147]
buffer_load_dwordx4 v134,s[8:11],s55 offen offset:2048 lds
M_ a[148:151],v[52:55],v[16:19],a[148:151]
M_ a[152:155],v[56:59],v[16:19],a[152:155]
M_ a[156:159],v[60:63],v[16:19],a[156:159]
M_ a[160:163],v[32:35],v[20:23],a[160:163]
M_ a[164:167],v[36:39],v[20:23],a[164:167]
buffer_load_dwordx4 v135,s[8:11],s55 offen offset:3072 lds
M_ a[168:171],v[40:43],v[20:23],a[168:171]
M_ a[172:175],v[44:47],v[20:23],a[172:175]
M_ a[176:179],v[48:51],v[20:23],a[176:179]
M_ a[180:183],v[52:55],v[20:23],a[180:183]
s_add_u32 s55,s20,0x280
M_ a[184:187],v[56:59],v[20:23],a[184:187]
s_add_u32 m0,s23,0x10000
M_ a[188:191],v[60:63],v[20:23],a[188:191]
buffer_load_dwordx4 v128,s[4:7],s55 offen lds
M_ a[192:195],v[32:35],v[24:27],a[192:195]
M_ a[196:199],v[36:39],v[24:27],a[196:199]
M_ a[200:203],v[40:43],v[24:27],a[200:203]
M_ a[204:207],v[44:47],v[24:27],a[204:207]
M_ a[208:211],v[48:51],v[24:27],a[208:211]
buffer_load_dwordx4 v129,s[4:7],s55 offen offset:1024 lds
M_ a[212:215],v[52:55],v[24:27],a[212:215]
M_ a[216:219],v[56:59],v[24:27],a[216:219]
M_ a[220:223],v[60:63],v[24:27],a[220:223]
M_ a[224:227],v[32:35],v[28:31],a[224:227]
M_ a[228:231],v[36:39],v[28:31],a[228:231]
buffer_load_dwordx4 v130,s[4:7],s55 offen offset:2048 lds
M_ a[232:235],v[40:43],v[28:31],a[232:235]
M_ a[236:239],v[44:47],v[28:31],a[236:239]
M_ a[240:243],v[48:51],v[28:31],a[240:243]
M_ a[244:247],v[52:55],v[28:31],a[244:247]
M_ a[248:251],v[56:59],v[28:31],a[248:251]
buffer_load_dwordx4 v131,s[4:7],s55 offen offset:3072 lds
M_ a[252:255],v[60:63],v[28:31],a[252:255]
s_waitcnt vmcnt(17)
s_waitcnt lgkmcnt(0)
s_barrier
M_ a[0:3],v[96:99],v[64:67],a[0:3]
ds_read_b64_tr_b16 v[0:1],v140
M_ a[4:7],v[100:103],v[64:67],a[4:7]
T_ v[2:3],v140,1024
M_ a[8:11],v[104:107],v[64:67],a[8:11]
ds_read_b128 v[32:35],v136
M_ a[12:15],v[108:111],v[64:67],a[12:15]
R_ v[36:39],v136,1024
M_ a[16:19],v[112:115],v[64:67],a[16:19]
R_ v[40:43],v136,2048
s_add_u32 s62,s62,0x100000
M_ a[20:23],v[116:119],v[64:67],a[20:23]
R_ v[44:47],v136,3072
M_ a[24:27],v[120:123],v[64:67],a[24:27]
R_ v[48:51],v136,4096
M_ a[28:31],v[124:127],v[64:67],a[28:31]
R_ v[52:55],v136,5120
M_ a[32:35],v[96:99],v[68:71],a[32:35]
R_ v[56:59],v136,6144
M_ a[36:39],v[100:103],v[68:71],a[36:39]
R_ v[60:63],v136,7168
M_ a[40:43],v[104:107],v[68:71],a[40:43]
ds_read_b64_tr_b16 v[4:5],v141
M_ a[44:47],v[108:111],v[68:71],a[44:47]
T_ v[6:7],v141,1024
M_ a[48:51],v[112:115],v[68:71],a[48:51]
T_ v[8:9],v140,256
M_ a[52:55],v[116:119],v[68:71],a[52:55]
T_ v[10:11],v140,1280
M_ a[56:59],v[120:123],v[68:71],a[56:59]
T_ v[12:13],v141,256
M_ a[60:63],v[124:127],v[68:71],a[60:63]
T_ v[14:15],v141,1280
M_ a[64:67],v[96:99],v[72:75],a[64:67]
T_ v[16:17],v140,512
M_ a[68:71],v[100:103],v[72:75],a[68:71]
T_ v[18:19],v140,1536
M_ a[72:75],v[104:107],v[72:75],a[72:75]
T_ v[20:21],v141,512
M_ a[76:79],v[108:111],v[72:75],a[76:79]
T_ v[22:23],v141,1536
M_ a[80:83],v[112:115],v[72:75],a[80:83]
T_ v[24:25],v140,768
M_ a[84:87],v[116:119],v[72:75],a[84:87]
T_ v[26:27],v140,1792
M_ a[88:91],v[120:123],v[72:75],a[88:91]
T_ v[28:29],v141,768
M_ a[92:95],v[124:127],v[72:75],a[92:95]
T_ v[30:31],v141,1792
M_ a[96:99],v[96:99],v[76:79],a[96:99]
)A"
R"A(s_add_u32 s55,s21,0x2c0000
M_ a[100:103],v[100:103],v[76:79],a[100:103]
s_add_u32 m0,s22,0x18000
M_ a[104:107],v[104:107],v[76:79],a[104:107]
buffer_load_dwordx4 v132,s[8:11],s55 offen lds
M_ a[108:111],v[108:111],v[76:79],a[108:111]
M_ a[112:115],v[112:115],v[76:79],a[112:115]
M_ a[116:119],v[116:119],v[76:79],a[116:119]
M_ a[120:123],v[120:123],v[76:79],a[120:123]
M_ a[124:127],v[124:127],v[76:79],a[124:127]
buffer_load_dwordx4 v133,s[8:11],s55 offen offset:1024 lds
M_ a[128:131],v[96:99],v[80:83],a[128:131]
M_ a[132:135],v[100:103],v[80:83],a[132:135]
buffer_load_dwordx4 v[240:243],v248,s[64:67],s62 offen
M_ a[136:139],v[104:107],v[80:83],a[136:139]
M_ a[140:143],v[108:111],v[80:83],a[140:143]
M_ a[144:147],v[112:115],v[80:83],a[144:147]
buffer_load_dwordx4 v134,s[8:11],s55 offen offset:2048 lds
M_ a[148:151],v[116:119],v[80:83],a[148:151]
M_ a[152:155],v[120:123],v[80:83],a[152:155]
M_ a[156:159],v[124:127],v[80:83],a[156:159]
M_ a[160:163],v[96:99],v[84:87],a[160:163]
M_ a[164:167],v[100:103],v[84:87],a[164:167]
buffer_load_dwordx4 v135,s[8:11],s55 offen offset:3072 lds
M_ a[168:171],v[104:107],v[84:87],a[168:171]
M_ a[172:175],v[108:111],v[84:87],a[172:175]
M_ a[176:179],v[112:115],v[84:87],a[176:179]
M_ a[180:183],v[116:119],v[84:87],a[180:183]
s_add_u32 s55,s20,0x2c0
M_ a[184:187],v[120:123],v[84:87],a[184:187]
s_add_u32 m0,s23,0x18000
M_ a[188:191],v[124:127],v[84:87],a[188:191]
buffer_load_dwordx4 v128,s[4:7],s55 offen lds
M_ a[192:195],v[96:99],v[88:91],a[192:195]
M_ a[196:199],v[100:103],v[88:91],a[196:199]
M_ a[200:203],v[104:107],v[88:91],a[200:203]
M_ a[204:207],v[108:111],v[88:91],a[204:207]
M_ a[208:211],v[112:115],v[88:91],a[208:211]
buffer_load_dwordx4 v129,s[4:7],s55 offen offset:1024 lds
M_ a[212:215],v[116:119],v[88:91],a[212:215]
M_ a[216:219],v[120:123],v[88:91],a[216:219]
M_ a[220:223],v[124:127],v[88:91],a[220:223]
M_ a[224:227],v[96:99],v[92:95],a[224:227]
M_ a[228:231],v[100:103],v[92:95],a[228:231]
buffer_load_dwordx4 v130,s[4:7],s55 offen offset:2048 lds
M_ a[232:235],v[104:107],v[92:95],a[232:235]
M_ a[236:239],v[108:111],v[92:95],a[236:239]
M_ a[240:243],v[112:115],v[92:95],a[240:243]
M_ a[244:247],v[116:119],v[92:95],a[244:247]
M_ a[248:251],v[120:123],v[92:95],a[248:251]
buffer_load_dwordx4 v131,s[4:7],s55 offen offset:3072 lds
M_ a[252:255],v[124:127],v[92:95],a[252:255]
s_waitcnt vmcnt(18)
s_waitcnt lgkmcnt(0)
s_barrier
M_ a[0:3],v[32:35],v[0:3],a[0:3]
T_ v[64:65],v140,32768
M_ a[4:7],v[36:39],v[0:3],a[4:7]
T_ v[66:67],v140,33792
M_ a[8:11],v[40:43],v[0:3],a[8:11]
R_ v[96:99],v136,32768
M_ a[12:15],v[44:47],v[0:3],a[12:15]
R_ v[100:103],v136,33792
M_ a[16:19],v[48:51],v[0:3],a[16:19]
R_ v[104:107],v136,34816
v_mbcnt_lo_u32_b32 v248,-1,0
M_ a[20:23],v[52:55],v[0:3],a[20:23]
R_ v[108:111],v136,35840
M_ a[24:27],v[56:59],v[0:3],a[24:27]
R_ v[112:115],v136,36864
M_ a[28:31],v[60:63],v[0:3],a[28:31]
R_ v[116:119],v136,37888
M_ a[32:35],v[32:35],v[4:7],a[32:35]
R_ v[120:123],v136,38912
v_mbcnt_hi_u32_b32 v248,-1,v248
M_ a[36:39],v[36:39],v[4:7],a[36:39]
R_ v[124:127],v136,39936
M_ a[40:43],v[40:43],v[4:7],a[40:43]
T_ v[68:69],v141,32768
M_ a[44:47],v[44:47],v[4:7],a[44:47]
T_ v[70:71],v141,33792
M_ a[48:51],v[48:51],v[4:7],a[48:51]
T_ v[72:73],v140,33024
v_lshlrev_b32 v248,4,v248
M_ a[52:55],v[52:55],v[4:7],a[52:55]
T_ v[74:75],v140,34048
M_ a[56:59],v[56:59],v[4:7],a[56:59]
T_ v[76:77],v141,33024
M_ a[60:63],v[60:63],v[4:7],a[60:63]
T_ v[78:79],v141,34048
M_ a[64:67],v[32:35],v[8:11],a[64:67]
T_ v[80:81],v140,33280
s_mov_b32 s60,s70
M_ a[68:71],v[36:39],v[8:11],a[68:71]
T_ v[82:83],v140,34304
M_ a[72:75],v[40:43],v[8:11],a[72:75]
T_ v[84:85],v141,33280
M_ a[76:79],v[44:47],v[8:11],a[76:79]
T_ v[86:87],v141,34304
M_ a[80:83],v[48:51],v[8:11],a[80:83]
T_ v[88:89],v140,33536
s_mov_b32 s61,s71
M_ a[84:87],v[52:55],v[8:11],a[84:87]
T_ v[90:91],v140,34560
M_ a[88:91],v[56:59],v[8:11],a[88:91]
T_ v[92:93],v141,33536
M_ a[92:95],v[60:63],v[8:11],a[92:95]
T_ v[94:95],v141,34560
M_ a[96:99],v[32:35],v[12:15],a[96:99]
s_add_u32 s55,s21,0x300000
s_mov_b32 s64,s60
M_ a[100:103],v[36:39],v[12:15],a[100:103]
s_add_u32 m0,s22,0x0
M_ a[104:107],v[40:43],v[12:15],a[104:107]
buffer_load_dwordx4 v132,s[8:11],s55 offen lds
M_ a[108:111],v[44:47],v[12:15],a[108:111]
M_ a[112:115],v[48:51],v[12:15],a[112:115]
s_mov_b32 s65,s61
M_ a[116:119],v[52:55],v[12:15],a[116:119]
M_ a[120:123],v[56:59],v[12:15],a[120:123]
M_ a[124:127],v[60:63],v[12:15],a[124:127]
buffer_load_dwordx4 v133,s[8:11],s55 offen offset:1024 lds
M_ a[128:131],v[32:35],v[16:19],a[128:131]
M_ a[132:135],v[36:39],v[16:19],a[132:135]
s_and_b32 s65,s65,0xffff
M_ a[136:139],v[40:43],v[16:19],a[136:139]
M_ a[140:143],v[44:47],v[16:19],a[140:143]
M_ a[144:147],v[48:51],v[16:19],a[144:147]
buffer_load_dwordx4 v134,s[8:11],s55 offen offset:2048 lds
M_ a[148:151],v[52:55],v[16:19],a[148:151]
s_mov_b32 s66,0x500000
M_ a[152:155],v[56:59],v[16:19],a[152:155]
M_ a[156:159],v[60:63],v[16:19],a[156:159]
M_ a[160:163],v[32:35],v[20:23],a[160:163]
M_ a[164:167],v[36:39],v[20:23],a[164:167]
buffer_load_dwordx4 v135,s[8:11],s55 offen offset:3072 lds
s_mov_b32 s67,0x20000
M_ a[168:171],v[40:43],v[20:23],a[168:171]
M_ a[172:175],v[44:47],v[20:23],a[172:175]
M_ a[176:179],v[48:51],v[20:23],a[176:179]
M_ a[180:183],v[52:55],v[20:23],a[180:183]
s_add_u32 s55,s20,0x300
s_lshl_b32 s62,s2,2
M_ a[184:187],v[56:59],v[20:23],a[184:187]
s_add_u32 m0,s23,0x0
M_ a[188:191],v[60:63],v[20:23],a[188:191]
buffer_load_dwordx4 v128,s[4:7],s55 offen lds
M_ a[192:195],v[32:35],v[24:27],a[192:195]
M_ a[196:199],v[36:39],v[24:27],a[196:199]
s_add_u32 s62,s62,s3
M_ a[200:203],v[40:43],v[24:27],a[200:203]
M_ a[204:207],v[44:47],v[24:27],a[204:207]
M_ a[208:211],v[48:51],v[24:27],a[208:211]
buffer_load_dwordx4 v129,s[4:7],s55 offen offset:1024 lds
M_ a[212:215],v[52:55],v[24:27],a[212:215]
s_lshl_b32 s62,s62,10
M_ a[216:219],v[56:59],v[24:27],a[216:219]
M_ a[220:223],v[60:63],v[24:27],a[220:223]
M_ a[224:227],v[32:35],v[28:31],a[224:227]
M_ a[228:231],v[36:39],v[28:31],a[228:231]
buffer_load_dwordx4 v130,s[4:7],s55 offen offset:2048 lds
buffer_load_dwordx4 v[240:243],v248,s[64:67],s62 offen
M_ a[232:235],v[40:43],v[28:31],a[232:235]
M_ a[236:239],v[44:47],v[28:31],a[236:239]
M_ a[240:243],v[48:51],v[28:31],a[240:243]
M_ a[244:247],v[52:55],v[28:31],a[244:247]
M_ a[248:251],v[56:59],v[28:31],a[248:251]
buffer_load_dwordx4 v131,s[4:7],s55 offen offset:3072 lds
M_ a[252:255],v[60:63],v[28:31],a[252:255]
s_waitcnt vmcnt(18)
s_waitcnt lgkmcnt(0)
s_barrier
M_ a[0:3],v[96:99],v[64:67],a[0:3]
ds_read_b64_tr_b16 v[0:1],v142
M_ a[4:7],v[100:103],v[64:67],a[4:7]
T_ v[2:3],v142,1024
M_ a[8:11],v[104:107],v[64:67],a[8:11]
ds_read_b128 v[32:35],v137
M_ a[12:15],v[108:111],v[64:67],a[12:15]
R_ v[36:39],v137,1024
M_ a[16:19],v[112:115],v[64:67],a[16:19]
R_ v[40:43],v137,2048
s_cmp_lg_u32 s3,0
s_cbranch_scc1 LPB4x26_%=
s_lshl_b32 s54,s2,2
s_add_u32 s54,s54,0x6400000
v_mov_b32 v162,s54
v_mov_b32 v163,s86
s_mov_b64 exec,1
global_store_dword v162,v163,s[88:89] sc1
s_mov_b64 exec,-1
LPB4x26_%=:
M_ a[20:23],v[116:119],v[64:67],a[20:23]
R_ v[44:47],v137,3072
M_ a[24:27],v[120:123],v[64:67],a[24:27]
R_ v[48:51],v137,4096
M_ a[28:31],v[124:127],v[64:67],a[28:31]
R_ v[52:55],v137,5120
M_ a[32:35],v[96:99],v[68:71],a[32:35]
R_ v[56:59],v137,6144
M_ a[36:39],v[100:103],v[68:71],a[36:39]
R_ v[60:63],v137,7168
M_ a[40:43],v[104:107],v[68:71],a[40:43]
ds_read_b64_tr_b16 v[4:5],v143
M_ a[44:47],v[108:111],v[68:71],a[44:47]
T_ v[6:7],v143,1024
M_ a[48:51],v[112:115],v[68:71],a[48:51]
T_ v[8:9],v142,256
M_ a[52:55],v[116:119],v[68:71],a[52:55]
T_ v[10:11],v142,1280
M_ a[56:59],v[120:123],v[68:71],a[56:59]
T_ v[12:13],v143,256
M_ a[60:63],v[124:127],v[68:71],a[60:63]
T_ v[14:15],v143,1280
M_ a[64:67],v[96:99],v[72:75],a[64:67]
T_ v[16:17],v142,512
M_ a[68:71],v[100:103],v[72:75],a[68:71]
T_ v[18:19],v142,1536
M_ a[72:75],v[104:107],v[72:75],a[72:75]
T_ v[20:21],v143,512
M_ a[76:79],v[108:111],v[72:75],a[76:79]
T_ v[22:23],v143,1536
M_ a[80:83],v[112:115],v[72:75],a[80:83]
T_ v[24:25],v142,768
M_ a[84:87],v[116:119],v[72:75],a[84:87]
T_ v[26:27],v142,1792
M_ a[88:91],v[120:123],v[72:75],a[88:91]
T_ v[28:29],v143,768
M_ a[92:95],v[124:127],v[72:75],a[92:95]
T_ v[30:31],v143,1792
M_ a[96:99],v[96:99],v[76:79],a[96:99]
s_add_u32 s55,s21,0x340000
M_ a[100:103],v[100:103],v[76:79],a[100:103]
s_add_u32 m0,s22,0x8000
M_ a[104:107],v[104:107],v[76:79],a[104:107]
buffer_load_dwordx4 v132,s[8:11],s55 offen lds
M_ a[108:111],v[108:111],v[76:79],a[108:111]
M_ a[112:115],v[112:115],v[76:79],a[112:115]
M_ a[116:119],v[116:119],v[76:79],a[116:119]
M_ a[120:123],v[120:123],v[76:79],a[120:123]
M_ a[124:127],v[124:127],v[76:79],a[124:127]
buffer_load_dwordx4 v133,s[8:11],s55 offen offset:1024 lds
M_ a[128:131],v[96:99],v[80:83],a[128:131]
M_ a[132:135],v[100:103],v[80:83],a[132:135]
M_ a[136:139],v[104:107],v[80:83],a[136:139]
M_ a[140:143],v[108:111],v[80:83],a[140:143]
M_ a[144:147],v[112:115],v[80:83],a[144:147]
buffer_load_dwordx4 v134,s[8:11],s55 offen offset:2048 lds
M_ a[148:151],v[116:119],v[80:83],a[148:151]
M_ a[152:155],v[120:123],v[80:83],a[152:155]
M_ a[156:159],v[124:127],v[80:83],a[156:159]
M_ a[160:163],v[96:99],v[84:87],a[160:163]
M_ a[164:167],v[100:103],v[84:87],a[164:167]
buffer_load_dwordx4 v135,s[8:11],s55 offen offset:3072 lds
M_ a[168:171],v[104:107],v[84:87],a[168:171]
M_ a[172:175],v[108:111],v[84:87],a[172:175]
M_ a[176:179],v[112:115],v[84:87],a[176:179]
M_ a[180:183],v[116:119],v[84:87],a[180:183]
s_add_u32 s55,s20,0x340
M_ a[184:187],v[120:123],v[84:87],a[184:187]
s_add_u32 m0,s23,0x8000
M_ a[188:191],v[124:127],v[84:87],a[188:191]
buffer_load_dwordx4 v128,s[4:7],s55 offen lds
M_ a[192:195],v[96:99],v[88:91],a[192:195]
M_ a[196:199],v[100:103],v[88:91],a[196:199]
M_ a[200:203],v[104:107],v[88:91],a[200:203]
M_ a[204:207],v[108:111],v[88:91],a[204:207]
M_ a[208:211],v[112:115],v[88:91],a[208:211]
buffer_load_dwordx4 v129,s[4:7],s55 offen offset:1024 lds
M_ a[212:215],v[116:119],v[88:91],a[212:215]
M_ a[216:219],v[120:123],v[88:91],a[216:219]
M_ a[220:223],v[124:127],v[88:91],a[220:223]
M_ a[224:227],v[96:99],v[92:95],a[224:227]
M_ a[228:231],v[100:103],v[92:95],a[228:231]
buffer_load_dwordx4 v130,s[4:7],s55 offen offset:2048 lds
M_ a[232:235],v[104:107],v[92:95],a[232:235]
M_ a[236:239],v[108:111],v[92:95],a[236:239]
M_ a[240:243],v[112:115],v[92:95],a[240:243]
M_ a[244:247],v[116:119],v[92:95],a[244:247]
M_ a[248:251],v[120:123],v[92:95],a[248:251]
buffer_load_dwordx4 v131,s[4:7],s55 offen offset:3072 lds
M_ a[252:255],v[124:127],v[92:95],a[252:255]
s_waitcnt vmcnt(17)
s_waitcnt lgkmcnt(0)
s_barrier
M_ a[0:3],v[32:35],v[0:3],a[0:3]
T_ v[64:65],v142,32768
M_ a[4:7],v[36:39],v[0:3],a[4:7]
T_ v[66:67],v142,33792
M_ a[8:11],v[40:43],v[0:3],a[8:11]
R_ v[96:99],v137,32768
M_ a[12:15],v[44:47],v[0:3],a[12:15]
R_ v[100:103],v137,33792
M_ a[16:19],v[48:51],v[0:3],a[16:19]
R_ v[104:107],v137,34816
s_add_u32 s62,s62,0x100000
buffer_load_dwordx4 v[240:243],v248,s[64:67],s62 offen
M_ a[20:23],v[52:55],v[0:3],a[20:23]
R_ v[108:111],v137,35840
v_mbcnt_lo_u32_b32 v210,-1,0
M_ a[24:27],v[56:59],v[0:3],a[24:27]
R_ v[112:115],v137,36864
v_mbcnt_hi_u32_b32 v210,-1,v210
M_ a[28:31],v[60:63],v[0:3],a[28:31]
R_ v[116:119],v137,37888
v_lshlrev_b32 v208,4,v210
s_and_b32 s44,s2,7
M_ a[32:35],v[32:35],v[4:7],a[32:35]
R_ v[120:123],v137,38912
s_lshr_b32 s37,s2,3
M_ a[36:39],v[36:39],v[4:7],a[36:39]
R_ v[124:127],v137,39936
s_lshr_b32 s38,s44,1
M_ a[40:43],v[40:43],v[4:7],a[40:43]
T_ v[68:69],v143,32768
s_lshl_b32 s38,s38,2
M_ a[44:47],v[44:47],v[4:7],a[44:47]
T_ v[70:71],v143,33792
s_and_b32 s32,s37,3
s_add_u32 s32,s32,s38
M_ a[48:51],v[48:51],v[4:7],a[48:51]
T_ v[72:73],v142,33024
s_and_b32 s44,s44,1
M_ a[52:55],v[52:55],v[4:7],a[52:55]
T_ v[74:75],v142,34048
s_lshl_b32 s44,s44,3
M_ a[56:59],v[56:59],v[4:7],a[56:59]
T_ v[76:77],v143,33024
s_lshr_b32 s33,s37,2
s_add_u32 s33,s33,s44
M_ a[60:63],v[60:63],v[4:7],a[60:63]
T_ v[78:79],v143,34048
s_lshr_b32 s34,s3,1
M_ a[64:67],v[32:35],v[8:11],a[64:67]
T_ v[80:81],v142,33280
s_and_b32 s35,s3,1
M_ a[68:71],v[36:39],v[8:11],a[68:71]
T_ v[82:83],v142,34304
s_lshl_b32 s36,s33,1
M_ a[72:75],v[40:43],v[8:11],a[72:75]
T_ v[84:85],v143,33280
s_add_u32 s36,s36,s35
v_and_b32 v211,15,v210
M_ a[76:79],v[44:47],v[8:11],a[76:79]
T_ v[86:87],v143,34304
v_lshrrev_b32 v212,4,v210
M_ a[80:83],v[48:51],v[8:11],a[80:83]
T_ v[88:89],v142,33536
s_lshl_b32 s44,s32,2
M_ a[84:87],v[52:55],v[8:11],a[84:87]
T_ v[90:91],v142,34560
s_add_u32 s44,s44,s3
M_ a[88:91],v[56:59],v[8:11],a[88:91]
T_ v[92:93],v143,33536
s_mul_i32 s37,s44,0xa000
s_lshl_b32 s39,s3,12
M_ a[92:95],v[60:63],v[8:11],a[92:95]
T_ v[94:95],v143,34560
v_lshrrev_b32 v213,3,v210
M_ a[96:99],v[32:35],v[12:15],a[96:99]
s_add_u32 s55,s21,0x380000
v_mul_u32_u24 v213,0x500,v213
M_ a[100:103],v[36:39],v[12:15],a[100:103]
s_add_u32 m0,s22,0x10000
v_and_b32 v215,7,v210
v_add_u32 v214,0,v212
M_ a[104:107],v[40:43],v[12:15],a[104:107]
buffer_load_dwordx4 v132,s[8:11],s55 offen lds
v_xor_b32 v214,v214,v215
M_ a[108:111],v[44:47],v[12:15],a[108:111]
v_lshl_add_u32 v214,v214,4,v213
M_ a[112:115],v[48:51],v[12:15],a[112:115]
v_add_u32 v192,0x0,v214
M_ a[116:119],v[52:55],v[12:15],a[116:119]
v_add_u32 v194,0x4800,v214
v_add_u32 v214,4,v212
M_ a[120:123],v[56:59],v[12:15],a[120:123]
v_xor_b32 v214,v214,v215
M_ a[124:127],v[60:63],v[12:15],a[124:127]
buffer_load_dwordx4 v133,s[8:11],s55 offen offset:1024 lds
v_lshl_add_u32 v214,v214,4,v213
M_ a[128:131],v[32:35],v[16:19],a[128:131]
v_add_u32 v193,0x2400,v214
M_ a[132:135],v[36:39],v[16:19],a[132:135]
v_add_u32 v195,0x6c00,v214
s_lshl_b32 s44,s33,1
M_ a[136:139],v[40:43],v[16:19],a[136:139]
s_add_u32 s44,s44,s34
M_ a[140:143],v[44:47],v[16:19],a[140:143]
s_mul_i32 s44,s44,20
M_ a[144:147],v[48:51],v[16:19],a[144:147]
buffer_load_dwordx4 v134,s[8:11],s55 offen offset:2048 lds
s_add_u32 s44,s44,s35
s_lshl_b32 s38,s44,12
M_ a[148:151],v[52:55],v[16:19],a[148:151]
s_lshl_b32 s40,s3,12
M_ a[152:155],v[56:59],v[16:19],a[152:155]
)A"
R"A(s_add_u32 s40,s40,16384
M_ a[156:159],v[60:63],v[16:19],a[156:159]
s_lshl_b32 s44,s34,13
M_ a[160:163],v[32:35],v[20:23],a[160:163]
v_add_u32 v213,0,v212
v_lshrrev_b32 v214,1,v211
M_ a[164:167],v[36:39],v[20:23],a[164:167]
buffer_load_dwordx4 v135,s[8:11],s55 offen offset:3072 lds
v_xor_b32 v213,v213,v214
M_ a[168:171],v[40:43],v[20:23],a[168:171]
v_lshlrev_b32 v213,4,v213
M_ a[172:175],v[44:47],v[20:23],a[172:175]
v_lshl_add_u32 v213,v211,7,v213
v_add_u32 v196,s44,v213
M_ a[176:179],v[48:51],v[20:23],a[176:179]
v_add_u32 v198,0x10000,v196
M_ a[180:183],v[52:55],v[20:23],a[180:183]
s_add_u32 s55,s20,0x380
v_add_u32 v213,4,v212
M_ a[184:187],v[56:59],v[20:23],a[184:187]
s_add_u32 m0,s23,0x10000
v_lshrrev_b32 v214,1,v211
M_ a[188:191],v[60:63],v[20:23],a[188:191]
buffer_load_dwordx4 v128,s[4:7],s55 offen lds
v_xor_b32 v213,v213,v214
v_lshlrev_b32 v213,4,v213
M_ a[192:195],v[32:35],v[24:27],a[192:195]
v_lshl_add_u32 v213,v211,7,v213
M_ a[196:199],v[36:39],v[24:27],a[196:199]
v_add_u32 v197,s44,v213
M_ a[200:203],v[40:43],v[24:27],a[200:203]
v_add_u32 v199,0x10000,v197
M_ a[204:207],v[44:47],v[24:27],a[204:207]
s_lshl_b32 s44,s35,13
s_add_u32 s44,s44,16384
M_ a[208:211],v[48:51],v[24:27],a[208:211]
buffer_load_dwordx4 v129,s[4:7],s55 offen offset:1024 lds
v_add_u32 v200,s44,v208
M_ a[212:215],v[52:55],v[24:27],a[212:215]
v_add_u32 v201,0x10000,v200
M_ a[216:219],v[56:59],v[24:27],a[216:219]
s_mov_b32 s41,0x3fb8aa3b
s_mov_b32 s24,s72
M_ a[220:223],v[60:63],v[24:27],a[220:223]
s_mov_b32 s25,s73
M_ a[224:227],v[32:35],v[28:31],a[224:227]
s_and_b32 s25,s25,0xffff
M_ a[228:231],v[36:39],v[28:31],a[228:231]
buffer_load_dwordx4 v130,s[4:7],s55 offen offset:2048 lds
s_mov_b32 s26,0x280000
M_ a[232:235],v[40:43],v[28:31],a[232:235]
s_mov_b32 s27,0x20000
s_add_u32 s28,s88,0x3000000
s_addc_u32 s29,s89,0
M_ a[236:239],v[44:47],v[28:31],a[236:239]
s_and_b32 s29,s29,0xffff
M_ a[240:243],v[48:51],v[28:31],a[240:243]
s_mov_b32 s30,0x280000
M_ a[244:247],v[52:55],v[28:31],a[244:247]
s_mov_b32 s31,0x20000
M_ a[248:251],v[56:59],v[28:31],a[248:251]
buffer_load_dwordx4 v131,s[4:7],s55 offen offset:3072 lds
M_ a[252:255],v[60:63],v[28:31],a[252:255]
s_waitcnt vmcnt(17)
s_waitcnt lgkmcnt(0)
s_barrier
M_ a[0:3],v[96:99],v[64:67],a[0:3]
ds_read_b64_tr_b16 v[0:1],v140
M_ a[4:7],v[100:103],v[64:67],a[4:7]
T_ v[2:3],v140,1024
M_ a[8:11],v[104:107],v[64:67],a[8:11]
ds_read_b128 v[32:35],v136
M_ a[12:15],v[108:111],v[64:67],a[12:15]
R_ v[36:39],v136,1024
M_ a[16:19],v[112:115],v[64:67],a[16:19]
R_ v[40:43],v136,2048
s_add_u32 s62,s62,0x100000
M_ a[20:23],v[116:119],v[64:67],a[20:23]
R_ v[44:47],v136,3072
M_ a[24:27],v[120:123],v[64:67],a[24:27]
R_ v[48:51],v136,4096
M_ a[28:31],v[124:127],v[64:67],a[28:31]
R_ v[52:55],v136,5120
M_ a[32:35],v[96:99],v[68:71],a[32:35]
R_ v[56:59],v136,6144
M_ a[36:39],v[100:103],v[68:71],a[36:39]
R_ v[60:63],v136,7168
M_ a[40:43],v[104:107],v[68:71],a[40:43]
ds_read_b64_tr_b16 v[4:5],v141
M_ a[44:47],v[108:111],v[68:71],a[44:47]
T_ v[6:7],v141,1024
M_ a[48:51],v[112:115],v[68:71],a[48:51]
T_ v[8:9],v140,256
M_ a[52:55],v[116:119],v[68:71],a[52:55]
T_ v[10:11],v140,1280
M_ a[56:59],v[120:123],v[68:71],a[56:59]
T_ v[12:13],v141,256
M_ a[60:63],v[124:127],v[68:71],a[60:63]
T_ v[14:15],v141,1280
M_ a[64:67],v[96:99],v[72:75],a[64:67]
T_ v[16:17],v140,512
M_ a[68:71],v[100:103],v[72:75],a[68:71]
T_ v[18:19],v140,1536
M_ a[72:75],v[104:107],v[72:75],a[72:75]
T_ v[20:21],v141,512
M_ a[76:79],v[108:111],v[72:75],a[76:79]
T_ v[22:23],v141,1536
M_ a[80:83],v[112:115],v[72:75],a[80:83]
T_ v[24:25],v140,768
M_ a[84:87],v[116:119],v[72:75],a[84:87]
T_ v[26:27],v140,1792
M_ a[88:91],v[120:123],v[72:75],a[88:91]
T_ v[28:29],v141,768
M_ a[92:95],v[124:127],v[72:75],a[92:95]
T_ v[30:31],v141,1792
M_ a[96:99],v[96:99],v[76:79],a[96:99]
s_add_u32 s55,s21,0x3c0000
M_ a[100:103],v[100:103],v[76:79],a[100:103]
s_add_u32 m0,s22,0x18000
M_ a[104:107],v[104:107],v[76:79],a[104:107]
buffer_load_dwordx4 v132,s[8:11],s55 offen lds
M_ a[108:111],v[108:111],v[76:79],a[108:111]
M_ a[112:115],v[112:115],v[76:79],a[112:115]
M_ a[116:119],v[116:119],v[76:79],a[116:119]
M_ a[120:123],v[120:123],v[76:79],a[120:123]
M_ a[124:127],v[124:127],v[76:79],a[124:127]
buffer_load_dwordx4 v133,s[8:11],s55 offen offset:1024 lds
M_ a[128:131],v[96:99],v[80:83],a[128:131]
M_ a[132:135],v[100:103],v[80:83],a[132:135]
buffer_load_dwordx4 v[240:243],v248,s[64:67],s62 offen
M_ a[136:139],v[104:107],v[80:83],a[136:139]
M_ a[140:143],v[108:111],v[80:83],a[140:143]
M_ a[144:147],v[112:115],v[80:83],a[144:147]
buffer_load_dwordx4 v134,s[8:11],s55 offen offset:2048 lds
M_ a[148:151],v[116:119],v[80:83],a[148:151]
M_ a[152:155],v[120:123],v[80:83],a[152:155]
M_ a[156:159],v[124:127],v[80:83],a[156:159]
M_ a[160:163],v[96:99],v[84:87],a[160:163]
M_ a[164:167],v[100:103],v[84:87],a[164:167]
buffer_load_dwordx4 v135,s[8:11],s55 offen offset:3072 lds
M_ a[168:171],v[104:107],v[84:87],a[168:171]
M_ a[172:175],v[108:111],v[84:87],a[172:175]
M_ a[176:179],v[112:115],v[84:87],a[176:179]
M_ a[180:183],v[116:119],v[84:87],a[180:183]
s_add_u32 s55,s20,0x3c0
M_ a[184:187],v[120:123],v[84:87],a[184:187]
s_add_u32 m0,s23,0x18000
M_ a[188:191],v[124:127],v[84:87],a[188:191]
buffer_load_dwordx4 v128,s[4:7],s55 offen lds
M_ a[192:195],v[96:99],v[88:91],a[192:195]
M_ a[196:199],v[100:103],v[88:91],a[196:199]
M_ a[200:203],v[104:107],v[88:91],a[200:203]
M_ a[204:207],v[108:111],v[88:91],a[204:207]
M_ a[208:211],v[112:115],v[88:91],a[208:211]
buffer_load_dwordx4 v129,s[4:7],s55 offen offset:1024 lds
M_ a[212:215],v[116:119],v[88:91],a[212:215]
M_ a[216:219],v[120:123],v[88:91],a[216:219]
M_ a[220:223],v[124:127],v[88:91],a[220:223]
M_ a[224:227],v[96:99],v[92:95],a[224:227]
M_ a[228:231],v[100:103],v[92:95],a[228:231]
buffer_load_dwordx4 v130,s[4:7],s55 offen offset:2048 lds
M_ a[232:235],v[104:107],v[92:95],a[232:235]
M_ a[236:239],v[108:111],v[92:95],a[236:239]
M_ a[240:243],v[112:115],v[92:95],a[240:243]
M_ a[244:247],v[116:119],v[92:95],a[244:247]
M_ a[248:251],v[120:123],v[92:95],a[248:251]
buffer_load_dwordx4 v131,s[4:7],s55 offen offset:3072 lds
M_ a[252:255],v[124:127],v[92:95],a[252:255]
s_waitcnt vmcnt(18)
s_waitcnt lgkmcnt(0)
s_barrier
M_ a[0:3],v[32:35],v[0:3],a[0:3]
T_ v[64:65],v140,32768
M_ a[4:7],v[36:39],v[0:3],a[4:7]
T_ v[66:67],v140,33792
M_ a[8:11],v[40:43],v[0:3],a[8:11]
R_ v[96:99],v136,32768
M_ a[12:15],v[44:47],v[0:3],a[12:15]
R_ v[100:103],v136,33792
M_ a[16:19],v[48:51],v[0:3],a[16:19]
R_ v[104:107],v136,34816
s_add_u32 s62,s62,0x100000
M_ a[20:23],v[52:55],v[0:3],a[20:23]
R_ v[108:111],v136,35840
M_ a[24:27],v[56:59],v[0:3],a[24:27]
R_ v[112:115],v136,36864
M_ a[28:31],v[60:63],v[0:3],a[28:31]
R_ v[116:119],v136,37888
M_ a[32:35],v[32:35],v[4:7],a[32:35]
R_ v[120:123],v136,38912
M_ a[36:39],v[36:39],v[4:7],a[36:39]
R_ v[124:127],v136,39936
M_ a[40:43],v[40:43],v[4:7],a[40:43]
T_ v[68:69],v141,32768
M_ a[44:47],v[44:47],v[4:7],a[44:47]
T_ v[70:71],v141,33792
M_ a[48:51],v[48:51],v[4:7],a[48:51]
T_ v[72:73],v140,33024
buffer_load_dwordx4 v[240:243],v248,s[64:67],s62 offen
M_ a[52:55],v[52:55],v[4:7],a[52:55]
T_ v[74:75],v140,34048
M_ a[56:59],v[56:59],v[4:7],a[56:59]
T_ v[76:77],v141,33024
M_ a[60:63],v[60:63],v[4:7],a[60:63]
T_ v[78:79],v141,34048
M_ a[64:67],v[32:35],v[8:11],a[64:67]
T_ v[80:81],v140,33280
M_ a[68:71],v[36:39],v[8:11],a[68:71]
T_ v[82:83],v140,34304
M_ a[72:75],v[40:43],v[8:11],a[72:75]
T_ v[84:85],v141,33280
M_ a[76:79],v[44:47],v[8:11],a[76:79]
T_ v[86:87],v141,34304
M_ a[80:83],v[48:51],v[8:11],a[80:83]
T_ v[88:89],v140,33536
v_mbcnt_lo_u32_b32 v228,-1,0
M_ a[84:87],v[52:55],v[8:11],a[84:87]
T_ v[90:91],v140,34560
M_ a[88:91],v[56:59],v[8:11],a[88:91]
T_ v[92:93],v141,33536
M_ a[92:95],v[60:63],v[8:11],a[92:95]
T_ v[94:95],v141,34560
M_ a[96:99],v[32:35],v[12:15],a[96:99]
M_ a[100:103],v[36:39],v[12:15],a[100:103]
M_ a[104:107],v[40:43],v[12:15],a[104:107]
M_ a[108:111],v[44:47],v[12:15],a[108:111]
M_ a[112:115],v[48:51],v[12:15],a[112:115]
v_mbcnt_hi_u32_b32 v228,-1,v228
M_ a[116:119],v[52:55],v[12:15],a[116:119]
M_ a[120:123],v[56:59],v[12:15],a[120:123]
M_ a[124:127],v[60:63],v[12:15],a[124:127]
M_ a[128:131],v[32:35],v[16:19],a[128:131]
M_ a[132:135],v[36:39],v[16:19],a[132:135]
M_ a[136:139],v[40:43],v[16:19],a[136:139]
M_ a[140:143],v[44:47],v[16:19],a[140:143]
M_ a[144:147],v[48:51],v[16:19],a[144:147]
M_ a[148:151],v[52:55],v[16:19],a[148:151]
v_lshlrev_b32 v228,4,v228
M_ a[152:155],v[56:59],v[16:19],a[152:155]
M_ a[156:159],v[60:63],v[16:19],a[156:159]
M_ a[160:163],v[32:35],v[20:23],a[160:163]
M_ a[164:167],v[36:39],v[20:23],a[164:167]
M_ a[168:171],v[40:43],v[20:23],a[168:171]
M_ a[172:175],v[44:47],v[20:23],a[172:175]
M_ a[176:179],v[48:51],v[20:23],a[176:179]
M_ a[180:183],v[52:55],v[20:23],a[180:183]
v_add_u32 v228,0x6400000,v228
M_ a[184:187],v[56:59],v[20:23],a[184:187]
M_ a[188:191],v[60:63],v[20:23],a[188:191]
M_ a[192:195],v[32:35],v[24:27],a[192:195]
M_ a[196:199],v[36:39],v[24:27],a[196:199]
M_ a[200:203],v[40:43],v[24:27],a[200:203]
M_ a[204:207],v[44:47],v[24:27],a[204:207]
M_ a[208:211],v[48:51],v[24:27],a[208:211]
M_ a[212:215],v[52:55],v[24:27],a[212:215]
global_load_dwordx4 v[224:227],v228,s[88:89] sc1
M_ a[216:219],v[56:59],v[24:27],a[216:219]
M_ a[220:223],v[60:63],v[24:27],a[220:223]
M_ a[224:227],v[32:35],v[28:31],a[224:227]
M_ a[228:231],v[36:39],v[28:31],a[228:231]
M_ a[232:235],v[40:43],v[28:31],a[232:235]
M_ a[236:239],v[44:47],v[28:31],a[236:239]
M_ a[240:243],v[48:51],v[28:31],a[240:243]
M_ a[244:247],v[52:55],v[28:31],a[244:247]
M_ a[248:251],v[56:59],v[28:31],a[248:251]
M_ a[252:255],v[60:63],v[28:31],a[252:255]
s_waitcnt vmcnt(11)
s_waitcnt lgkmcnt(0)
s_barrier
M_ a[0:3],v[96:99],v[64:67],a[0:3]
ds_read_b64_tr_b16 v[0:1],v142
M_ a[4:7],v[100:103],v[64:67],a[4:7]
T_ v[2:3],v142,1024
M_ a[8:11],v[104:107],v[64:67],a[8:11]
ds_read_b128 v[32:35],v137
M_ a[12:15],v[108:111],v[64:67],a[12:15]
R_ v[36:39],v137,1024
M_ a[16:19],v[112:115],v[64:67],a[16:19]
R_ v[40:43],v137,2048
s_add_u32 s62,s62,0x100000
M_ a[20:23],v[116:119],v[64:67],a[20:23]
R_ v[44:47],v137,3072
M_ a[24:27],v[120:123],v[64:67],a[24:27]
R_ v[48:51],v137,4096
M_ a[28:31],v[124:127],v[64:67],a[28:31]
R_ v[52:55],v137,5120
M_ a[32:35],v[96:99],v[68:71],a[32:35]
R_ v[56:59],v137,6144
M_ a[36:39],v[100:103],v[68:71],a[36:39]
R_ v[60:63],v137,7168
M_ a[40:43],v[104:107],v[68:71],a[40:43]
ds_read_b64_tr_b16 v[4:5],v143
M_ a[44:47],v[108:111],v[68:71],a[44:47]
T_ v[6:7],v143,1024
M_ a[48:51],v[112:115],v[68:71],a[48:51]
T_ v[8:9],v142,256
M_ a[52:55],v[116:119],v[68:71],a[52:55]
T_ v[10:11],v142,1280
M_ a[56:59],v[120:123],v[68:71],a[56:59]
T_ v[12:13],v143,256
M_ a[60:63],v[124:127],v[68:71],a[60:63]
T_ v[14:15],v143,1280
M_ a[64:67],v[96:99],v[72:75],a[64:67]
T_ v[16:17],v142,512
M_ a[68:71],v[100:103],v[72:75],a[68:71]
T_ v[18:19],v142,1536
M_ a[72:75],v[104:107],v[72:75],a[72:75]
T_ v[20:21],v143,512
M_ a[76:79],v[108:111],v[72:75],a[76:79]
T_ v[22:23],v143,1536
M_ a[80:83],v[112:115],v[72:75],a[80:83]
T_ v[24:25],v142,768
M_ a[84:87],v[116:119],v[72:75],a[84:87]
T_ v[26:27],v142,1792
M_ a[88:91],v[120:123],v[72:75],a[88:91]
T_ v[28:29],v143,768
M_ a[92:95],v[124:127],v[72:75],a[92:95]
T_ v[30:31],v143,1792
M_ a[96:99],v[96:99],v[76:79],a[96:99]
M_ a[100:103],v[100:103],v[76:79],a[100:103]
M_ a[104:107],v[104:107],v[76:79],a[104:107]
M_ a[108:111],v[108:111],v[76:79],a[108:111]
M_ a[112:115],v[112:115],v[76:79],a[112:115]
M_ a[116:119],v[116:119],v[76:79],a[116:119]
M_ a[120:123],v[120:123],v[76:79],a[120:123]
M_ a[124:127],v[124:127],v[76:79],a[124:127]
M_ a[128:131],v[96:99],v[80:83],a[128:131]
M_ a[132:135],v[100:103],v[80:83],a[132:135]
buffer_load_dwordx4 v[240:243],v248,s[64:67],s62 offen
M_ a[136:139],v[104:107],v[80:83],a[136:139]
M_ a[140:143],v[108:111],v[80:83],a[140:143]
M_ a[144:147],v[112:115],v[80:83],a[144:147]
M_ a[148:151],v[116:119],v[80:83],a[148:151]
M_ a[152:155],v[120:123],v[80:83],a[152:155]
M_ a[156:159],v[124:127],v[80:83],a[156:159]
M_ a[160:163],v[96:99],v[84:87],a[160:163]
M_ a[164:167],v[100:103],v[84:87],a[164:167]
M_ a[168:171],v[104:107],v[84:87],a[168:171]
M_ a[172:175],v[108:111],v[84:87],a[172:175]
M_ a[176:179],v[112:115],v[84:87],a[176:179]
M_ a[180:183],v[116:119],v[84:87],a[180:183]
M_ a[184:187],v[120:123],v[84:87],a[184:187]
M_ a[188:191],v[124:127],v[84:87],a[188:191]
M_ a[192:195],v[96:99],v[88:91],a[192:195]
M_ a[196:199],v[100:103],v[88:91],a[196:199]
M_ a[200:203],v[104:107],v[88:91],a[200:203]
M_ a[204:207],v[108:111],v[88:91],a[204:207]
M_ a[208:211],v[112:115],v[88:91],a[208:211]
M_ a[212:215],v[116:119],v[88:91],a[212:215]
M_ a[216:219],v[120:123],v[88:91],a[216:219]
M_ a[220:223],v[124:127],v[88:91],a[220:223]
M_ a[224:227],v[96:99],v[92:95],a[224:227]
M_ a[228:231],v[100:103],v[92:95],a[228:231]
M_ a[232:235],v[104:107],v[92:95],a[232:235]
M_ a[236:239],v[108:111],v[92:95],a[236:239]
M_ a[240:243],v[112:115],v[92:95],a[240:243]
M_ a[244:247],v[116:119],v[92:95],a[244:247]
M_ a[248:251],v[120:123],v[92:95],a[248:251]
M_ a[252:255],v[124:127],v[92:95],a[252:255]
s_waitcnt vmcnt(1)
v_xor_b32 v224,s86,v224
v_xor_b32 v225,s86,v225
v_xor_b32 v226,s86,v226
v_xor_b32 v227,s86,v227
v_or3_b32 v224,v224,v225,v226
v_or_b32 v224,v224,v227
v_cmp_ne_u32 vcc,0,v224
s_cbranch_vccz LQD5x38_%=
s_mov_b32 s59,0x30d40
LQL5x38_%=:
s_sleep 2
global_load_dwordx4 v[224:227],v228,s[88:89] sc1
s_waitcnt vmcnt(0)
v_xor_b32 v224,s86,v224
v_xor_b32 v225,s86,v225
v_xor_b32 v226,s86,v226
v_xor_b32 v227,s86,v227
v_or3_b32 v224,v224,v225,v226
v_or_b32 v224,v224,v227
v_cmp_ne_u32 vcc,0,v224
s_cbranch_vccz LQD5x38_%=
s_sub_u32 s59,s59,1
s_cmp_lg_u32 s59,0
s_cbranch_scc1 LQL5x38_%=
LQD5x38_%=:
s_waitcnt vmcnt(3)
s_waitcnt lgkmcnt(0)
s_barrier
M_ a[0:3],v[32:35],v[0:3],a[0:3]
T_ v[64:65],v142,32768
M_ a[4:7],v[36:39],v[0:3],a[4:7]
T_ v[66:67],v142,33792
M_ a[8:11],v[40:43],v[0:3],a[8:11]
R_ v[96:99],v137,32768
M_ a[12:15],v[44:47],v[0:3],a[12:15]
R_ v[100:103],v137,33792
M_ a[16:19],v[48:51],v[0:3],a[16:19]
R_ v[104:107],v137,34816
)A"
R"A(M_ a[20:23],v[52:55],v[0:3],a[20:23]
R_ v[108:111],v137,35840
M_ a[24:27],v[56:59],v[0:3],a[24:27]
R_ v[112:115],v137,36864
M_ a[28:31],v[60:63],v[0:3],a[28:31]
R_ v[116:119],v137,37888
M_ a[32:35],v[32:35],v[4:7],a[32:35]
R_ v[120:123],v137,38912
M_ a[36:39],v[36:39],v[4:7],a[36:39]
R_ v[124:127],v137,39936
M_ a[40:43],v[40:43],v[4:7],a[40:43]
T_ v[68:69],v143,32768
M_ a[44:47],v[44:47],v[4:7],a[44:47]
T_ v[70:71],v143,33792
M_ a[48:51],v[48:51],v[4:7],a[48:51]
T_ v[72:73],v142,33024
M_ a[52:55],v[52:55],v[4:7],a[52:55]
T_ v[74:75],v142,34048
M_ a[56:59],v[56:59],v[4:7],a[56:59]
T_ v[76:77],v143,33024
M_ a[60:63],v[60:63],v[4:7],a[60:63]
T_ v[78:79],v143,34048
M_ a[64:67],v[32:35],v[8:11],a[64:67]
T_ v[80:81],v142,33280
M_ a[68:71],v[36:39],v[8:11],a[68:71]
T_ v[82:83],v142,34304
M_ a[72:75],v[40:43],v[8:11],a[72:75]
T_ v[84:85],v143,33280
M_ a[76:79],v[44:47],v[8:11],a[76:79]
T_ v[86:87],v143,34304
M_ a[80:83],v[48:51],v[8:11],a[80:83]
T_ v[88:89],v142,33536
M_ a[84:87],v[52:55],v[8:11],a[84:87]
T_ v[90:91],v142,34560
M_ a[88:91],v[56:59],v[8:11],a[88:91]
T_ v[92:93],v143,33536
M_ a[92:95],v[60:63],v[8:11],a[92:95]
T_ v[94:95],v143,34560
M_ a[96:99],v[32:35],v[12:15],a[96:99]
s_add_u32 s44,s37,0x0
M_ a[100:103],v[36:39],v[12:15],a[100:103]
s_add_u32 m0,s39,0x0
M_ a[104:107],v[40:43],v[12:15],a[104:107]
buffer_load_dwordx4 v192,s[24:27],s44 offen lds
M_ a[108:111],v[44:47],v[12:15],a[108:111]
M_ a[112:115],v[48:51],v[12:15],a[112:115]
M_ a[116:119],v[52:55],v[12:15],a[116:119]
M_ a[120:123],v[56:59],v[12:15],a[120:123]
M_ a[124:127],v[60:63],v[12:15],a[124:127]
buffer_load_dwordx4 v193,s[24:27],s44 offen offset:1024 lds
M_ a[128:131],v[32:35],v[16:19],a[128:131]
M_ a[132:135],v[36:39],v[16:19],a[132:135]
M_ a[136:139],v[40:43],v[16:19],a[136:139]
M_ a[140:143],v[44:47],v[16:19],a[140:143]
M_ a[144:147],v[48:51],v[16:19],a[144:147]
buffer_load_dwordx4 v194,s[24:27],s44 offen offset:2048 lds
M_ a[148:151],v[52:55],v[16:19],a[148:151]
M_ a[152:155],v[56:59],v[16:19],a[152:155]
M_ a[156:159],v[60:63],v[16:19],a[156:159]
M_ a[160:163],v[32:35],v[20:23],a[160:163]
M_ a[164:167],v[36:39],v[20:23],a[164:167]
buffer_load_dwordx4 v195,s[24:27],s44 offen offset:3072 lds
M_ a[168:171],v[40:43],v[20:23],a[168:171]
M_ a[172:175],v[44:47],v[20:23],a[172:175]
M_ a[176:179],v[48:51],v[20:23],a[176:179]
M_ a[180:183],v[52:55],v[20:23],a[180:183]
s_add_u32 s44,s38,0x0
M_ a[184:187],v[56:59],v[20:23],a[184:187]
s_add_u32 m0,s40,0x0
M_ a[188:191],v[60:63],v[20:23],a[188:191]
buffer_load_dwordx4 v208,s[28:31],s44 offen sc1 lds
M_ a[192:195],v[32:35],v[24:27],a[192:195]
M_ a[196:199],v[36:39],v[24:27],a[196:199]
M_ a[200:203],v[40:43],v[24:27],a[200:203]
M_ a[204:207],v[44:47],v[24:27],a[204:207]
M_ a[208:211],v[48:51],v[24:27],a[208:211]
buffer_load_dwordx4 v208,s[28:31],s44 offen offset:1024 sc1 lds
M_ a[212:215],v[52:55],v[24:27],a[212:215]
M_ a[216:219],v[56:59],v[24:27],a[216:219]
M_ a[220:223],v[60:63],v[24:27],a[220:223]
M_ a[224:227],v[32:35],v[28:31],a[224:227]
M_ a[228:231],v[36:39],v[28:31],a[228:231]
buffer_load_dwordx4 v208,s[28:31],s44 offen offset:2048 sc1 lds
M_ a[232:235],v[40:43],v[28:31],a[232:235]
M_ a[236:239],v[44:47],v[28:31],a[236:239]
M_ a[240:243],v[48:51],v[28:31],a[240:243]
M_ a[244:247],v[52:55],v[28:31],a[244:247]
M_ a[248:251],v[56:59],v[28:31],a[248:251]
buffer_load_dwordx4 v208,s[28:31],s44 offen offset:3072 sc1 lds
M_ a[252:255],v[60:63],v[28:31],a[252:255]
s_waitcnt lgkmcnt(0)
M_ a[0:3],v[96:99],v[64:67],a[0:3]
M_ a[4:7],v[100:103],v[64:67],a[4:7]
M_ a[8:11],v[104:107],v[64:67],a[8:11]
M_ a[12:15],v[108:111],v[64:67],a[12:15]
M_ a[16:19],v[112:115],v[64:67],a[16:19]
M_ a[20:23],v[116:119],v[64:67],a[20:23]
M_ a[24:27],v[120:123],v[64:67],a[24:27]
M_ a[28:31],v[124:127],v[64:67],a[28:31]
M_ a[32:35],v[96:99],v[68:71],a[32:35]
M_ a[36:39],v[100:103],v[68:71],a[36:39]
M_ a[40:43],v[104:107],v[68:71],a[40:43]
M_ a[44:47],v[108:111],v[68:71],a[44:47]
M_ a[48:51],v[112:115],v[68:71],a[48:51]
M_ a[52:55],v[116:119],v[68:71],a[52:55]
M_ a[56:59],v[120:123],v[68:71],a[56:59]
M_ a[60:63],v[124:127],v[68:71],a[60:63]
M_ a[64:67],v[96:99],v[72:75],a[64:67]
M_ a[68:71],v[100:103],v[72:75],a[68:71]
M_ a[72:75],v[104:107],v[72:75],a[72:75]
M_ a[76:79],v[108:111],v[72:75],a[76:79]
M_ a[80:83],v[112:115],v[72:75],a[80:83]
M_ a[84:87],v[116:119],v[72:75],a[84:87]
M_ a[88:91],v[120:123],v[72:75],a[88:91]
M_ a[92:95],v[124:127],v[72:75],a[92:95]
M_ a[96:99],v[96:99],v[76:79],a[96:99]
s_add_u32 s44,s37,0x80
M_ a[100:103],v[100:103],v[76:79],a[100:103]
s_add_u32 m0,s39,0x8000
M_ a[104:107],v[104:107],v[76:79],a[104:107]
buffer_load_dwordx4 v192,s[24:27],s44 offen lds
M_ a[108:111],v[108:111],v[76:79],a[108:111]
M_ a[112:115],v[112:115],v[76:79],a[112:115]
M_ a[116:119],v[116:119],v[76:79],a[116:119]
M_ a[120:123],v[120:123],v[76:79],a[120:123]
M_ a[124:127],v[124:127],v[76:79],a[124:127]
buffer_load_dwordx4 v193,s[24:27],s44 offen offset:1024 lds
M_ a[128:131],v[96:99],v[80:83],a[128:131]
M_ a[132:135],v[100:103],v[80:83],a[132:135]
M_ a[136:139],v[104:107],v[80:83],a[136:139]
M_ a[140:143],v[108:111],v[80:83],a[140:143]
M_ a[144:147],v[112:115],v[80:83],a[144:147]
buffer_load_dwordx4 v194,s[24:27],s44 offen offset:2048 lds
M_ a[148:151],v[116:119],v[80:83],a[148:151]
M_ a[152:155],v[120:123],v[80:83],a[152:155]
M_ a[156:159],v[124:127],v[80:83],a[156:159]
M_ a[160:163],v[96:99],v[84:87],a[160:163]
M_ a[164:167],v[100:103],v[84:87],a[164:167]
buffer_load_dwordx4 v195,s[24:27],s44 offen offset:3072 lds
M_ a[168:171],v[104:107],v[84:87],a[168:171]
M_ a[172:175],v[108:111],v[84:87],a[172:175]
M_ a[176:179],v[112:115],v[84:87],a[176:179]
M_ a[180:183],v[116:119],v[84:87],a[180:183]
s_add_u32 s44,s38,0x2000
M_ a[184:187],v[120:123],v[84:87],a[184:187]
s_add_u32 m0,s40,0x8000
M_ a[188:191],v[124:127],v[84:87],a[188:191]
buffer_load_dwordx4 v208,s[28:31],s44 offen sc1 lds
M_ a[192:195],v[96:99],v[88:91],a[192:195]
M_ a[196:199],v[100:103],v[88:91],a[196:199]
M_ a[200:203],v[104:107],v[88:91],a[200:203]
M_ a[204:207],v[108:111],v[88:91],a[204:207]
M_ a[208:211],v[112:115],v[88:91],a[208:211]
buffer_load_dwordx4 v208,s[28:31],s44 offen offset:1024 sc1 lds
M_ a[212:215],v[116:119],v[88:91],a[212:215]
M_ a[216:219],v[120:123],v[88:91],a[216:219]
M_ a[220:223],v[124:127],v[88:91],a[220:223]
M_ a[224:227],v[96:99],v[92:95],a[224:227]
M_ a[228:231],v[100:103],v[92:95],a[228:231]
buffer_load_dwordx4 v208,s[28:31],s44 offen offset:2048 sc1 lds
M_ a[232:235],v[104:107],v[92:95],a[232:235]
M_ a[236:239],v[108:111],v[92:95],a[236:239]
M_ a[240:243],v[112:115],v[92:95],a[240:243]
M_ a[244:247],v[116:119],v[92:95],a[244:247]
M_ a[248:251],v[120:123],v[92:95],a[248:251]
buffer_load_dwordx4 v208,s[28:31],s44 offen offset:3072 sc1 lds
M_ a[252:255],v[124:127],v[92:95],a[252:255]
s_waitcnt vmcnt(8)
s_barrier
ds_read_b128 v[32:35],v200
R_ v[36:39],v200,4096
ds_read_b128 v[0:3],v196
ds_read_b128 v[4:7],v197
R_ v[8:11],v196,2048
R_ v[12:15],v197,2048
R_ v[16:19],v196,4096
R_ v[20:23],v197,4096
R_ v[24:27],v196,6144
R_ v[28:31],v197,6144
R_ v[40:43],v200,1024
R_ v[44:47],v200,5120
R_ v[48:51],v200,2048
R_ v[52:55],v200,6144
R_ v[56:59],v200,3072
R_ v[60:63],v200,7168
s_waitcnt vmcnt(0)
s_waitcnt lgkmcnt(0)
s_barrier
M_ v[128:131],v[32:35],v[0:3],0
s_add_u32 s44,s37,0x100
s_mov_b32 s4,s68
s_mov_b32 s5,s69
s_and_b32 s5,s5,0xffff
M_ v[128:131],v[36:39],v[4:7],v[128:131]
s_add_u32 m0,s39,0x10000
R_ v[96:99],v200,32768
s_mov_b32 s6,0x280000
s_mov_b32 s7,0x20000
M_ v[132:135],v[32:35],v[8:11],0
buffer_load_dwordx4 v192,s[24:27],s44 offen lds
s_mov_b32 s8,s70
s_mov_b32 s9,s71
M_ v[132:135],v[36:39],v[12:15],v[132:135]
R_ v[100:103],v200,36864
s_and_b32 s9,s9,0xffff
s_mov_b32 s10,0x500000
s_mov_b32 s11,0x20000
M_ v[136:139],v[32:35],v[16:19],0
buffer_load_dwordx4 v193,s[24:27],s44 offen offset:1024 lds
v_mbcnt_lo_u32_b32 v210,-1,0
v_mbcnt_hi_u32_b32 v210,-1,v210
M_ v[136:139],v[36:39],v[20:23],v[136:139]
R_ v[64:67],v196,32768
v_lshlrev_b32 v208,4,v210
v_and_b32 v211,15,v210
M_ v[140:143],v[32:35],v[24:27],0
buffer_load_dwordx4 v194,s[24:27],s44 offen offset:2048 lds
v_lshrrev_b32 v212,4,v210
s_and_b32 s54,s2,7
s_lshr_b32 s55,s2,3
M_ v[140:143],v[36:39],v[28:31],v[140:143]
R_ v[68:71],v197,32768
s_and_b32 s19,s55,7
s_lshr_b32 s55,s55,3
M_ v[144:147],v[40:43],v[0:3],0
buffer_load_dwordx4 v195,s[24:27],s44 offen offset:3072 lds
s_lshl_b32 s55,s55,3
s_add_u32 s18,s55,s54
M_ v[144:147],v[44:47],v[4:7],v[144:147]
R_ v[72:75],v196,34816
s_add_u32 s44,s38,0x4000
s_add_u32 m0,s40,0x10000
s_nop 0
buffer_load_dwordx4 v208,s[28:31],s44 offen sc1 lds
s_lshr_b32 s73,s3,1
s_lshl_b32 s75,s3,12
s_and_b32 s56,s3,1
M_ v[148:151],v[40:43],v[8:11],0
s_lshl_b32 s56,s56,2
s_sub_u32 s54,15,s19
M_ v[148:151],v[44:47],v[12:15],v[148:151]
R_ v[76:79],v197,34816
buffer_load_dwordx4 v208,s[28:31],s44 offen offset:1024 sc1 lds
s_lshl_b32 s54,s54,3
s_add_u32 s70,s54,s56
M_ v[152:155],v[40:43],v[16:19],0
s_lshl_b32 s54,s19,3
s_add_u32 s71,s54,s56
s_cmp_eq_u32 s73,0
s_cselect_b32 s72,s70,s71
M_ v[152:155],v[44:47],v[20:23],v[152:155]
R_ v[80:83],v196,36864
buffer_load_dwordx4 v208,s[28:31],s44 offen offset:2048 sc1 lds
s_mul_i32 s68,s72,0x5000
s_mul_i32 s69,s3,0x20000
M_ v[156:159],v[40:43],v[24:27],0
s_lshl_b32 s54,s18,8
s_add_u32 s69,s69,s54
M_ v[156:159],v[44:47],v[28:31],v[156:159]
R_ v[84:87],v197,36864
buffer_load_dwordx4 v208,s[28:31],s44 offen offset:3072 sc1 lds
v_lshrrev_b32 v213,3,v210
v_mul_u32_u24 v213,0x500,v213
M_ v[160:163],v[48:51],v[0:3],0
s_add_u32 s44,s37,0x180
v_and_b32 v215,7,v210
v_add_u32 v214,0,v212
v_xor_b32 v214,v214,v215
M_ v[160:163],v[52:55],v[4:7],v[160:163]
R_ v[88:91],v196,38912
s_add_u32 m0,s39,0x18000
s_nop 0
buffer_load_dwordx4 v192,s[24:27],s44 offen lds
v_lshl_add_u32 v214,v214,4,v213
v_add_u32 v224,0x0,v214
M_ v[164:167],v[48:51],v[8:11],0
v_add_u32 v226,0x4800,v214
v_add_u32 v214,4,v212
M_ v[164:167],v[52:55],v[12:15],v[164:167]
R_ v[92:95],v197,38912
buffer_load_dwordx4 v193,s[24:27],s44 offen offset:1024 lds
v_xor_b32 v214,v214,v215
v_lshl_add_u32 v214,v214,4,v213
v_add_u32 v225,0x2400,v214
M_ v[168:171],v[48:51],v[16:19],0
v_add_u32 v227,0x6c00,v214
v_lshrrev_b32 v213,2,v210
M_ v[168:171],v[52:55],v[20:23],v[168:171]
R_ v[104:107],v200,33792
buffer_load_dwordx4 v194,s[24:27],s44 offen offset:2048 lds
v_and_b32 v213,3,v213
v_lshlrev_b32 v213,13,v213
M_ v[172:175],v[48:51],v[24:27],0
v_and_b32 v215,3,v210
v_xor_b32 v214,0,v215
v_lshl_add_u32 v214,v212,2,v214
M_ v[172:175],v[52:55],v[28:31],v[172:175]
R_ v[108:111],v200,37888
buffer_load_dwordx4 v195,s[24:27],s44 offen offset:3072 lds
v_lshl_add_u32 v214,v214,4,v213
v_add_u32 v228,0x0,v214
M_ v[176:179],v[56:59],v[0:3],0
s_add_u32 s44,s38,0x6000
s_add_u32 m0,s40,0x18000
s_nop 0
buffer_load_dwordx4 v208,s[28:31],s44 offen sc1 lds
v_add_u32 v229,0x7c00,v214
v_xor_b32 v214,2,v215
M_ v[176:179],v[60:63],v[4:7],v[176:179]
R_ v[112:115],v200,34816
v_lshl_add_u32 v214,v212,2,v214
v_lshl_add_u32 v214,v214,4,v213
v_add_u32 v230,0xf800,v214
M_ v[180:183],v[56:59],v[8:11],0
buffer_load_dwordx4 v208,s[28:31],s44 offen offset:1024 sc1 lds
v_add_u32 v231,0x17400,v214
s_lshl_b32 s52,s18,20
M_ v[180:183],v[60:63],v[12:15],v[180:183]
R_ v[116:119],v200,38912
s_lshl_b32 s53,s19,17
s_add_u32 s52,s52,s53
M_ v[184:187],v[56:59],v[16:19],0
buffer_load_dwordx4 v208,s[28:31],s44 offen offset:2048 sc1 lds
s_and_b32 s53,s3,1
s_lshl_b32 s53,s53,16
s_add_u32 s52,s52,s53
M_ v[184:187],v[60:63],v[20:23],v[184:187]
R_ v[120:123],v200,35840
s_add_u32 s52,s52,0x4000
s_add_u32 s92,s88,s52
s_addc_u32 s93,s89,0
M_ v[188:191],v[56:59],v[24:27],0
buffer_load_dwordx4 v208,s[28:31],s44 offen offset:3072 sc1 lds
s_add_u32 s94,s92,32768
s_addc_u32 s95,s93,0
AR_ v210,a0
M_ v[188:191],v[60:63],v[28:31],v[188:191]
R_ v[124:127],v200,39936
AR_ v211,a1
AR_ v212,a2
s_waitcnt vmcnt(8)
s_waitcnt lgkmcnt(0)
s_barrier
M_ v[128:131],v[96:99],v[64:67],v[128:131]
s_add_u32 s44,s37,0x200
AR_ v213,a3
AR_ v214,a4
AR_ v215,a5
M_ v[128:131],v[100:103],v[68:71],v[128:131]
s_add_u32 m0,s39,0x0
ds_read_b128 v[32:35],v201
AR_ v216,a6
AR_ v217,a7
M_ v[132:135],v[96:99],v[72:75],v[132:135]
buffer_load_dwordx4 v192,s[24:27],s44 offen lds
C_ v204,v210,v211
C_ v205,v212,v213
M_ v[132:135],v[100:103],v[76:79],v[132:135]
R_ v[36:39],v201,4096
C_ v206,v214,v215
C_ v207,v216,v217
AW_ a0,v204
M_ v[136:139],v[96:99],v[80:83],v[136:139]
AW_ a1,v205
AW_ a2,v206
M_ v[136:139],v[100:103],v[84:87],v[136:139]
ds_read_b128 v[0:3],v198
AW_ a3,v207
AR_ v244,a8
M_ v[140:143],v[96:99],v[88:91],v[140:143]
buffer_load_dwordx4 v193,s[24:27],s44 offen offset:1024 lds
AR_ v245,a9
AR_ v246,a10
AR_ v247,a11
M_ v[140:143],v[100:103],v[92:95],v[140:143]
ds_read_b128 v[4:7],v199
AR_ v248,a12
AR_ v249,a13
M_ v[144:147],v[104:107],v[64:67],v[144:147]
AR_ v202,a14
AR_ v203,a15
M_ v[144:147],v[108:111],v[68:71],v[144:147]
R_ v[8:11],v198,2048
C_ v218,v244,v245
C_ v219,v246,v247
C_ v220,v248,v249
M_ v[148:151],v[104:107],v[72:75],v[148:151]
buffer_load_dwordx4 v194,s[24:27],s44 offen offset:2048 lds
C_ v221,v202,v203
AW_ a8,v218
M_ v[148:151],v[108:111],v[76:79],v[148:151]
R_ v[12:15],v199,2048
AW_ a9,v219
AW_ a10,v220
M_ v[152:155],v[104:107],v[80:83],v[152:155]
AW_ a11,v221
AR_ v210,a16
AR_ v211,a17
M_ v[152:155],v[108:111],v[84:87],v[152:155]
R_ v[16:19],v198,4096
AR_ v212,a18
AR_ v213,a19
M_ v[156:159],v[104:107],v[88:91],v[156:159]
buffer_load_dwordx4 v195,s[24:27],s44 offen offset:3072 lds
AR_ v214,a20
AR_ v215,a21
M_ v[156:159],v[108:111],v[92:95],v[156:159]
R_ v[20:23],v199,4096
AR_ v216,a22
AR_ v217,a23
M_ v[160:163],v[112:115],v[64:67],v[160:163]
s_add_u32 s44,s38,0x8000
C_ v204,v210,v211
C_ v205,v212,v213
C_ v206,v214,v215
M_ v[160:163],v[116:119],v[68:71],v[160:163]
s_add_u32 m0,s40,0x0
R_ v[24:27],v198,6144
C_ v207,v216,v217
AW_ a16,v204
M_ v[164:167],v[112:115],v[72:75],v[164:167]
buffer_load_dwordx4 v208,s[28:31],s44 offen sc1 lds
AW_ a17,v205
AW_ a18,v206
M_ v[164:167],v[116:119],v[76:79],v[164:167]
R_ v[28:31],v199,6144
AW_ a19,v207
AR_ v244,a24
AR_ v245,a25
)A"
R"A(M_ v[168:171],v[112:115],v[80:83],v[168:171]
AR_ v246,a26
AR_ v247,a27
M_ v[168:171],v[116:119],v[84:87],v[168:171]
R_ v[40:43],v201,1024
AR_ v248,a28
AR_ v249,a29
M_ v[172:175],v[112:115],v[88:91],v[172:175]
buffer_load_dwordx4 v208,s[28:31],s44 offen offset:1024 sc1 lds
AR_ v202,a30
AR_ v203,a31
C_ v218,v244,v245
M_ v[172:175],v[116:119],v[92:95],v[172:175]
R_ v[44:47],v201,5120
C_ v219,v246,v247
C_ v220,v248,v249
M_ v[176:179],v[120:123],v[64:67],v[176:179]
C_ v221,v202,v203
AW_ a24,v218
M_ v[176:179],v[124:127],v[68:71],v[176:179]
R_ v[48:51],v201,2048
AW_ a25,v219
AW_ a26,v220
AW_ a27,v221
M_ v[180:183],v[120:123],v[72:75],v[180:183]
buffer_load_dwordx4 v208,s[28:31],s44 offen offset:2048 sc1 lds
AR_ v210,a32
AR_ v211,a33
M_ v[180:183],v[124:127],v[76:79],v[180:183]
R_ v[52:55],v201,6144
AR_ v212,a34
AR_ v213,a35
M_ v[184:187],v[120:123],v[80:83],v[184:187]
AR_ v214,a36
AR_ v215,a37
AR_ v216,a38
M_ v[184:187],v[124:127],v[84:87],v[184:187]
R_ v[56:59],v201,3072
AR_ v217,a39
C_ v204,v210,v211
M_ v[188:191],v[120:123],v[88:91],v[188:191]
buffer_load_dwordx4 v208,s[28:31],s44 offen offset:3072 sc1 lds
C_ v205,v212,v213
C_ v206,v214,v215
M_ v[188:191],v[124:127],v[92:95],v[188:191]
R_ v[60:63],v201,7168
C_ v207,v216,v217
AW_ a4,v204
s_waitcnt vmcnt(0)
s_waitcnt lgkmcnt(0)
s_barrier
M_ v[128:131],v[32:35],v[0:3],v[128:131]
s_add_u32 s44,s37,0x280
AW_ a5,v205
AW_ a6,v206
AW_ a7,v207
M_ v[128:131],v[36:39],v[4:7],v[128:131]
s_add_u32 m0,s39,0x8000
R_ v[96:99],v201,32768
AR_ v244,a40
AR_ v245,a41
M_ v[132:135],v[32:35],v[8:11],v[132:135]
buffer_load_dwordx4 v192,s[24:27],s44 offen lds
AR_ v246,a42
AR_ v247,a43
M_ v[132:135],v[36:39],v[12:15],v[132:135]
R_ v[100:103],v201,36864
AR_ v248,a44
AR_ v249,a45
AR_ v202,a46
M_ v[136:139],v[32:35],v[16:19],v[136:139]
AR_ v203,a47
C_ v218,v244,v245
M_ v[136:139],v[36:39],v[20:23],v[136:139]
R_ v[64:67],v198,32768
C_ v219,v246,v247
C_ v220,v248,v249
M_ v[140:143],v[32:35],v[24:27],v[140:143]
buffer_load_dwordx4 v193,s[24:27],s44 offen offset:1024 lds
C_ v221,v202,v203
AW_ a12,v218
AW_ a13,v219
M_ v[140:143],v[36:39],v[28:31],v[140:143]
R_ v[68:71],v199,32768
AW_ a14,v220
AW_ a15,v221
M_ v[144:147],v[40:43],v[0:3],v[144:147]
AR_ v210,a48
AR_ v211,a49
M_ v[144:147],v[44:47],v[4:7],v[144:147]
R_ v[72:75],v198,34816
AR_ v212,a50
AR_ v213,a51
AR_ v214,a52
M_ v[148:151],v[40:43],v[8:11],v[148:151]
buffer_load_dwordx4 v194,s[24:27],s44 offen offset:2048 lds
AR_ v215,a53
AR_ v216,a54
M_ v[148:151],v[44:47],v[12:15],v[148:151]
R_ v[76:79],v199,34816
AR_ v217,a55
C_ v204,v210,v211
M_ v[152:155],v[40:43],v[16:19],v[152:155]
C_ v205,v212,v213
C_ v206,v214,v215
C_ v207,v216,v217
M_ v[152:155],v[44:47],v[20:23],v[152:155]
R_ v[80:83],v198,36864
AW_ a20,v204
AW_ a21,v205
M_ v[156:159],v[40:43],v[24:27],v[156:159]
buffer_load_dwordx4 v195,s[24:27],s44 offen offset:3072 lds
AW_ a22,v206
AW_ a23,v207
M_ v[156:159],v[44:47],v[28:31],v[156:159]
R_ v[84:87],v199,36864
AR_ v244,a56
AR_ v245,a57
M_ v[160:163],v[48:51],v[0:3],v[160:163]
s_add_u32 s44,s38,0xa000
AR_ v246,a58
AR_ v247,a59
AR_ v248,a60
M_ v[160:163],v[52:55],v[4:7],v[160:163]
s_add_u32 m0,s40,0x8000
R_ v[88:91],v198,38912
AR_ v249,a61
AR_ v202,a62
M_ v[164:167],v[48:51],v[8:11],v[164:167]
buffer_load_dwordx4 v208,s[28:31],s44 offen sc1 lds
AR_ v203,a63
C_ v218,v244,v245
M_ v[164:167],v[52:55],v[12:15],v[164:167]
R_ v[92:95],v199,38912
C_ v219,v246,v247
C_ v220,v248,v249
C_ v221,v202,v203
M_ v[168:171],v[48:51],v[16:19],v[168:171]
AW_ a28,v218
AW_ a29,v219
M_ v[168:171],v[52:55],v[20:23],v[168:171]
R_ v[104:107],v201,33792
AW_ a30,v220
AW_ a31,v221
M_ v[172:175],v[48:51],v[24:27],v[172:175]
buffer_load_dwordx4 v208,s[28:31],s44 offen offset:1024 sc1 lds
AR_ v210,a64
AR_ v211,a65
AR_ v212,a66
M_ v[172:175],v[52:55],v[28:31],v[172:175]
R_ v[108:111],v201,37888
AR_ v213,a67
AR_ v214,a68
M_ v[176:179],v[56:59],v[0:3],v[176:179]
AR_ v215,a69
AR_ v216,a70
M_ v[176:179],v[60:63],v[4:7],v[176:179]
R_ v[112:115],v201,34816
AR_ v217,a71
C_ v204,v210,v211
C_ v205,v212,v213
M_ v[180:183],v[56:59],v[8:11],v[180:183]
buffer_load_dwordx4 v208,s[28:31],s44 offen offset:2048 sc1 lds
C_ v206,v214,v215
C_ v207,v216,v217
M_ v[180:183],v[60:63],v[12:15],v[180:183]
R_ v[116:119],v201,38912
AW_ a64,v204
AW_ a65,v205
M_ v[184:187],v[56:59],v[16:19],v[184:187]
AW_ a66,v206
AW_ a67,v207
AR_ v244,a72
M_ v[184:187],v[60:63],v[20:23],v[184:187]
R_ v[120:123],v201,35840
AR_ v245,a73
AR_ v246,a74
M_ v[188:191],v[56:59],v[24:27],v[188:191]
buffer_load_dwordx4 v208,s[28:31],s44 offen offset:3072 sc1 lds
AR_ v247,a75
AR_ v248,a76
M_ v[188:191],v[60:63],v[28:31],v[188:191]
R_ v[124:127],v201,39936
AR_ v249,a77
AR_ v202,a78
s_waitcnt lgkmcnt(2)
M_ v[128:131],v[96:99],v[64:67],v[128:131]
s_add_u32 s44,s37,0x300
AR_ v203,a79
C_ v218,v244,v245
C_ v219,v246,v247
M_ v[128:131],v[100:103],v[68:71],v[128:131]
s_add_u32 m0,s39,0x10000
ds_read_b128 v[32:35],v200
C_ v220,v248,v249
C_ v221,v202,v203
M_ v[132:135],v[96:99],v[72:75],v[132:135]
buffer_load_dwordx4 v192,s[24:27],s44 offen lds
AW_ a72,v218
AW_ a73,v219
M_ v[132:135],v[100:103],v[76:79],v[132:135]
R_ v[36:39],v200,4096
AW_ a74,v220
AW_ a75,v221
AR_ v210,a80
M_ v[136:139],v[96:99],v[80:83],v[136:139]
AR_ v211,a81
AR_ v212,a82
M_ v[136:139],v[100:103],v[84:87],v[136:139]
ds_read_b128 v[0:3],v196
AR_ v213,a83
AR_ v214,a84
M_ v[140:143],v[96:99],v[88:91],v[140:143]
buffer_load_dwordx4 v193,s[24:27],s44 offen offset:1024 lds
AR_ v215,a85
AR_ v216,a86
AR_ v217,a87
M_ v[140:143],v[100:103],v[92:95],v[140:143]
ds_read_b128 v[4:7],v197
C_ v204,v210,v211
C_ v205,v212,v213
M_ v[144:147],v[104:107],v[64:67],v[144:147]
C_ v206,v214,v215
C_ v207,v216,v217
M_ v[144:147],v[108:111],v[68:71],v[144:147]
R_ v[8:11],v196,2048
AW_ a80,v204
AW_ a81,v205
AW_ a82,v206
M_ v[148:151],v[104:107],v[72:75],v[148:151]
buffer_load_dwordx4 v194,s[24:27],s44 offen offset:2048 lds
AW_ a83,v207
AR_ v244,a88
M_ v[148:151],v[108:111],v[76:79],v[148:151]
R_ v[12:15],v197,2048
AR_ v245,a89
AR_ v246,a90
M_ v[152:155],v[104:107],v[80:83],v[152:155]
AR_ v247,a91
AR_ v248,a92
AR_ v249,a93
M_ v[152:155],v[108:111],v[84:87],v[152:155]
R_ v[16:19],v196,4096
AR_ v202,a94
AR_ v203,a95
M_ v[156:159],v[104:107],v[88:91],v[156:159]
buffer_load_dwordx4 v195,s[24:27],s44 offen offset:3072 lds
C_ v218,v244,v245
C_ v219,v246,v247
M_ v[156:159],v[108:111],v[92:95],v[156:159]
R_ v[20:23],v197,4096
C_ v220,v248,v249
C_ v221,v202,v203
M_ v[160:163],v[112:115],v[64:67],v[160:163]
s_add_u32 s44,s38,0xc000
AW_ a88,v218
AW_ a89,v219
AW_ a90,v220
M_ v[160:163],v[116:119],v[68:71],v[160:163]
s_add_u32 m0,s40,0x10000
R_ v[24:27],v196,6144
AW_ a91,v221
AR_ v210,a96
M_ v[164:167],v[112:115],v[72:75],v[164:167]
buffer_load_dwordx4 v208,s[28:31],s44 offen sc1 lds
AR_ v211,a97
AR_ v212,a98
M_ v[164:167],v[116:119],v[76:79],v[164:167]
R_ v[28:31],v197,6144
AR_ v213,a99
AR_ v214,a100
AR_ v215,a101
M_ v[168:171],v[112:115],v[80:83],v[168:171]
AR_ v216,a102
AR_ v217,a103
M_ v[168:171],v[116:119],v[84:87],v[168:171]
R_ v[40:43],v200,1024
C_ v204,v210,v211
C_ v205,v212,v213
M_ v[172:175],v[112:115],v[88:91],v[172:175]
buffer_load_dwordx4 v208,s[28:31],s44 offen offset:1024 sc1 lds
C_ v206,v214,v215
C_ v207,v216,v217
AW_ a68,v204
M_ v[172:175],v[116:119],v[92:95],v[172:175]
R_ v[44:47],v200,5120
AW_ a69,v205
AW_ a70,v206
s_waitcnt lgkmcnt(2)
M_ v[176:179],v[120:123],v[64:67],v[176:179]
AW_ a71,v207
AR_ v244,a104
M_ v[176:179],v[124:127],v[68:71],v[176:179]
R_ v[48:51],v200,2048
AR_ v245,a105
AR_ v246,a106
AR_ v247,a107
M_ v[180:183],v[120:123],v[72:75],v[180:183]
buffer_load_dwordx4 v208,s[28:31],s44 offen offset:2048 sc1 lds
AR_ v248,a108
AR_ v249,a109
M_ v[180:183],v[124:127],v[76:79],v[180:183]
R_ v[52:55],v200,6144
AR_ v202,a110
AR_ v203,a111
M_ v[184:187],v[120:123],v[80:83],v[184:187]
C_ v218,v244,v245
C_ v219,v246,v247
C_ v220,v248,v249
M_ v[184:187],v[124:127],v[84:87],v[184:187]
R_ v[56:59],v200,3072
C_ v221,v202,v203
AW_ a76,v218
M_ v[188:191],v[120:123],v[88:91],v[188:191]
buffer_load_dwordx4 v208,s[28:31],s44 offen offset:3072 sc1 lds
AW_ a77,v219
AW_ a78,v220
M_ v[188:191],v[124:127],v[92:95],v[188:191]
R_ v[60:63],v200,7168
AW_ a79,v221
AR_ v210,a112
s_waitcnt vmcnt(0)
s_waitcnt lgkmcnt(0)
s_barrier
M_ v[128:131],v[32:35],v[0:3],v[128:131]
s_add_u32 s44,s37,0x380
AR_ v211,a113
AR_ v212,a114
AR_ v213,a115
M_ v[128:131],v[36:39],v[4:7],v[128:131]
s_add_u32 m0,s39,0x18000
R_ v[96:99],v200,32768
AR_ v214,a116
AR_ v215,a117
M_ v[132:135],v[32:35],v[8:11],v[132:135]
buffer_load_dwordx4 v192,s[24:27],s44 offen lds
AR_ v216,a118
AR_ v217,a119
M_ v[132:135],v[36:39],v[12:15],v[132:135]
R_ v[100:103],v200,36864
C_ v204,v210,v211
C_ v205,v212,v213
C_ v206,v214,v215
M_ v[136:139],v[32:35],v[16:19],v[136:139]
C_ v207,v216,v217
AW_ a84,v204
M_ v[136:139],v[36:39],v[20:23],v[136:139]
R_ v[64:67],v196,32768
AW_ a85,v205
AW_ a86,v206
M_ v[140:143],v[32:35],v[24:27],v[140:143]
buffer_load_dwordx4 v193,s[24:27],s44 offen offset:1024 lds
AW_ a87,v207
AR_ v244,a120
AR_ v245,a121
M_ v[140:143],v[36:39],v[28:31],v[140:143]
R_ v[68:71],v197,32768
AR_ v246,a122
AR_ v247,a123
M_ v[144:147],v[40:43],v[0:3],v[144:147]
AR_ v248,a124
AR_ v249,a125
M_ v[144:147],v[44:47],v[4:7],v[144:147]
R_ v[72:75],v196,34816
AR_ v202,a126
AR_ v203,a127
C_ v218,v244,v245
M_ v[148:151],v[40:43],v[8:11],v[148:151]
buffer_load_dwordx4 v194,s[24:27],s44 offen offset:2048 lds
C_ v219,v246,v247
C_ v220,v248,v249
M_ v[148:151],v[44:47],v[12:15],v[148:151]
R_ v[76:79],v197,34816
C_ v221,v202,v203
AW_ a92,v218
M_ v[152:155],v[40:43],v[16:19],v[152:155]
AW_ a93,v219
AW_ a94,v220
AW_ a95,v221
M_ v[152:155],v[44:47],v[20:23],v[152:155]
R_ v[80:83],v196,36864
AR_ v210,a128
AR_ v211,a129
M_ v[156:159],v[40:43],v[24:27],v[156:159]
buffer_load_dwordx4 v195,s[24:27],s44 offen offset:3072 lds
AR_ v212,a130
AR_ v213,a131
M_ v[156:159],v[44:47],v[28:31],v[156:159]
R_ v[84:87],v197,36864
AR_ v214,a132
AR_ v215,a133
M_ v[160:163],v[48:51],v[0:3],v[160:163]
s_add_u32 s44,s38,0xe000
AR_ v216,a134
AR_ v217,a135
C_ v204,v210,v211
M_ v[160:163],v[52:55],v[4:7],v[160:163]
s_add_u32 m0,s40,0x18000
R_ v[88:91],v196,38912
C_ v205,v212,v213
C_ v206,v214,v215
M_ v[164:167],v[48:51],v[8:11],v[164:167]
buffer_load_dwordx4 v208,s[28:31],s44 offen sc1 lds
C_ v207,v216,v217
AW_ a128,v204
M_ v[164:167],v[52:55],v[12:15],v[164:167]
R_ v[92:95],v197,38912
AW_ a129,v205
AW_ a130,v206
AW_ a131,v207
M_ v[168:171],v[48:51],v[16:19],v[168:171]
AR_ v244,a136
AR_ v245,a137
M_ v[168:171],v[52:55],v[20:23],v[168:171]
R_ v[104:107],v200,33792
AR_ v246,a138
AR_ v247,a139
M_ v[172:175],v[48:51],v[24:27],v[172:175]
buffer_load_dwordx4 v208,s[28:31],s44 offen offset:1024 sc1 lds
AR_ v248,a140
AR_ v249,a141
AR_ v202,a142
M_ v[172:175],v[52:55],v[28:31],v[172:175]
R_ v[108:111],v200,37888
AR_ v203,a143
C_ v218,v244,v245
M_ v[176:179],v[56:59],v[0:3],v[176:179]
C_ v219,v246,v247
C_ v220,v248,v249
M_ v[176:179],v[60:63],v[4:7],v[176:179]
R_ v[112:115],v200,34816
C_ v221,v202,v203
AW_ a136,v218
AW_ a137,v219
M_ v[180:183],v[56:59],v[8:11],v[180:183]
buffer_load_dwordx4 v208,s[28:31],s44 offen offset:2048 sc1 lds
AW_ a138,v220
AW_ a139,v221
M_ v[180:183],v[60:63],v[12:15],v[180:183]
R_ v[116:119],v200,38912
AR_ v210,a144
AR_ v211,a145
M_ v[184:187],v[56:59],v[16:19],v[184:187]
AR_ v212,a146
AR_ v213,a147
AR_ v214,a148
M_ v[184:187],v[60:63],v[20:23],v[184:187]
R_ v[120:123],v200,35840
AR_ v215,a149
AR_ v216,a150
M_ v[188:191],v[56:59],v[24:27],v[188:191]
buffer_load_dwordx4 v208,s[28:31],s44 offen offset:3072 sc1 lds
AR_ v217,a151
C_ v204,v210,v211
M_ v[188:191],v[60:63],v[28:31],v[188:191]
R_ v[124:127],v200,39936
C_ v205,v212,v213
C_ v206,v214,v215
s_waitcnt lgkmcnt(2)
M_ v[128:131],v[96:99],v[64:67],v[128:131]
s_add_u32 s44,s37,0x400
C_ v207,v216,v217
AW_ a144,v204
AW_ a145,v205
M_ v[128:131],v[100:103],v[68:71],v[128:131]
s_add_u32 m0,s39,0x0
ds_read_b128 v[32:35],v201
AW_ a146,v206
AW_ a147,v207
M_ v[132:135],v[96:99],v[72:75],v[132:135]
buffer_load_dwordx4 v192,s[24:27],s44 offen lds
AR_ v244,a152
AR_ v245,a153
M_ v[132:135],v[100:103],v[76:79],v[132:135]
R_ v[36:39],v201,4096
AR_ v246,a154
AR_ v247,a155
AR_ v248,a156
M_ v[136:139],v[96:99],v[80:83],v[136:139]
AR_ v249,a157
AR_ v202,a158
M_ v[136:139],v[100:103],v[84:87],v[136:139]
ds_read_b128 v[0:3],v198
AR_ v203,a159
C_ v218,v244,v245
M_ v[140:143],v[96:99],v[88:91],v[140:143]
buffer_load_dwordx4 v193,s[24:27],s44 offen offset:1024 lds
C_ v219,v246,v247
C_ v220,v248,v249
C_ v221,v202,v203
M_ v[140:143],v[100:103],v[92:95],v[140:143]
ds_read_b128 v[4:7],v199
AW_ a152,v218
AW_ a153,v219
M_ v[144:147],v[104:107],v[64:67],v[144:147]
AW_ a154,v220
AW_ a155,v221
M_ v[144:147],v[108:111],v[68:71],v[144:147]
R_ v[8:11],v198,2048
AR_ v210,a160
AR_ v211,a161
AR_ v212,a162
M_ v[148:151],v[104:107],v[72:75],v[148:151]
buffer_load_dwordx4 v194,s[24:27],s44 offen offset:2048 lds
AR_ v213,a163
AR_ v214,a164
M_ v[148:151],v[108:111],v[76:79],v[148:151]
R_ v[12:15],v199,2048
AR_ v215,a165
AR_ v216,a166
M_ v[152:155],v[104:107],v[80:83],v[152:155]
AR_ v217,a167
C_ v204,v210,v211
C_ v205,v212,v213
M_ v[152:155],v[108:111],v[84:87],v[152:155]
R_ v[16:19],v198,4096
C_ v206,v214,v215
C_ v207,v216,v217
M_ v[156:159],v[104:107],v[88:91],v[156:159]
buffer_load_dwordx4 v195,s[24:27],s44 offen offset:3072 lds
AW_ a132,v204
AW_ a133,v205
M_ v[156:159],v[108:111],v[92:95],v[156:159]
R_ v[20:23],v199,4096
AW_ a134,v206
AW_ a135,v207
M_ v[160:163],v[112:115],v[64:67],v[160:163]
s_add_u32 s44,s38,0x10000
AR_ v244,a168
AR_ v245,a169
AR_ v246,a170
M_ v[160:163],v[116:119],v[68:71],v[160:163]
s_add_u32 m0,s40,0x0
R_ v[24:27],v198,6144
AR_ v247,a171
AR_ v248,a172
M_ v[164:167],v[112:115],v[72:75],v[164:167]
buffer_load_dwordx4 v208,s[28:31],s44 offen sc1 lds
AR_ v249,a173
AR_ v202,a174
M_ v[164:167],v[116:119],v[76:79],v[164:167]
R_ v[28:31],v199,6144
AR_ v203,a175
C_ v218,v244,v245
C_ v219,v246,v247
M_ v[168:171],v[112:115],v[80:83],v[168:171]
C_ v220,v248,v249
C_ v221,v202,v203
M_ v[168:171],v[116:119],v[84:87],v[168:171]
R_ v[40:43],v201,1024
AW_ a140,v218
AW_ a141,v219
)A"
R"A(M_ v[172:175],v[112:115],v[88:91],v[172:175]
buffer_load_dwordx4 v208,s[28:31],s44 offen offset:1024 sc1 lds
AW_ a142,v220
AW_ a143,v221
AR_ v210,a176
M_ v[172:175],v[116:119],v[92:95],v[172:175]
R_ v[44:47],v201,5120
AR_ v211,a177
AR_ v212,a178
s_waitcnt lgkmcnt(2)
M_ v[176:179],v[120:123],v[64:67],v[176:179]
AR_ v213,a179
AR_ v214,a180
M_ v[176:179],v[124:127],v[68:71],v[176:179]
R_ v[48:51],v201,2048
AR_ v215,a181
AR_ v216,a182
AR_ v217,a183
M_ v[180:183],v[120:123],v[72:75],v[180:183]
buffer_load_dwordx4 v208,s[28:31],s44 offen offset:2048 sc1 lds
C_ v204,v210,v211
C_ v205,v212,v213
M_ v[180:183],v[124:127],v[76:79],v[180:183]
R_ v[52:55],v201,6144
C_ v206,v214,v215
C_ v207,v216,v217
M_ v[184:187],v[120:123],v[80:83],v[184:187]
AW_ a148,v204
AW_ a149,v205
AW_ a150,v206
M_ v[184:187],v[124:127],v[84:87],v[184:187]
R_ v[56:59],v201,3072
AW_ a151,v207
AR_ v244,a184
M_ v[188:191],v[120:123],v[88:91],v[188:191]
buffer_load_dwordx4 v208,s[28:31],s44 offen offset:3072 sc1 lds
AR_ v245,a185
AR_ v246,a186
M_ v[188:191],v[124:127],v[92:95],v[188:191]
R_ v[60:63],v201,7168
AR_ v247,a187
AR_ v248,a188
s_waitcnt vmcnt(0)
s_waitcnt lgkmcnt(0)
s_barrier
M_ v[128:131],v[32:35],v[0:3],v[128:131]
s_add_u32 s44,s37,0x480
AR_ v249,a189
AR_ v202,a190
AR_ v203,a191
M_ v[128:131],v[36:39],v[4:7],v[128:131]
s_add_u32 m0,s39,0x8000
R_ v[96:99],v201,32768
C_ v218,v244,v245
C_ v219,v246,v247
M_ v[132:135],v[32:35],v[8:11],v[132:135]
buffer_load_dwordx4 v192,s[24:27],s44 offen lds
C_ v220,v248,v249
C_ v221,v202,v203
M_ v[132:135],v[36:39],v[12:15],v[132:135]
R_ v[100:103],v201,36864
AW_ a156,v218
AW_ a157,v219
AW_ a158,v220
M_ v[136:139],v[32:35],v[16:19],v[136:139]
AW_ a159,v221
AR_ v210,a192
M_ v[136:139],v[36:39],v[20:23],v[136:139]
R_ v[64:67],v198,32768
AR_ v211,a193
AR_ v212,a194
M_ v[140:143],v[32:35],v[24:27],v[140:143]
buffer_load_dwordx4 v193,s[24:27],s44 offen offset:1024 lds
AR_ v213,a195
AR_ v214,a196
AR_ v215,a197
M_ v[140:143],v[36:39],v[28:31],v[140:143]
R_ v[68:71],v199,32768
AR_ v216,a198
AR_ v217,a199
M_ v[144:147],v[40:43],v[0:3],v[144:147]
C_ v204,v210,v211
C_ v205,v212,v213
M_ v[144:147],v[44:47],v[4:7],v[144:147]
R_ v[72:75],v198,34816
C_ v206,v214,v215
C_ v207,v216,v217
AW_ a192,v204
M_ v[148:151],v[40:43],v[8:11],v[148:151]
buffer_load_dwordx4 v194,s[24:27],s44 offen offset:2048 lds
AW_ a193,v205
AW_ a194,v206
M_ v[148:151],v[44:47],v[12:15],v[148:151]
R_ v[76:79],v199,34816
AW_ a195,v207
AR_ v244,a200
M_ v[152:155],v[40:43],v[16:19],v[152:155]
AR_ v245,a201
AR_ v246,a202
AR_ v247,a203
M_ v[152:155],v[44:47],v[20:23],v[152:155]
R_ v[80:83],v198,36864
AR_ v248,a204
AR_ v249,a205
M_ v[156:159],v[40:43],v[24:27],v[156:159]
buffer_load_dwordx4 v195,s[24:27],s44 offen offset:3072 lds
AR_ v202,a206
AR_ v203,a207
M_ v[156:159],v[44:47],v[28:31],v[156:159]
R_ v[84:87],v199,36864
C_ v218,v244,v245
C_ v219,v246,v247
M_ v[160:163],v[48:51],v[0:3],v[160:163]
s_add_u32 s44,s38,0x12000
C_ v220,v248,v249
C_ v221,v202,v203
AW_ a200,v218
M_ v[160:163],v[52:55],v[4:7],v[160:163]
s_add_u32 m0,s40,0x8000
R_ v[88:91],v198,38912
AW_ a201,v219
AW_ a202,v220
M_ v[164:167],v[48:51],v[8:11],v[164:167]
buffer_load_dwordx4 v208,s[28:31],s44 offen sc1 lds
AW_ a203,v221
AR_ v210,a208
M_ v[164:167],v[52:55],v[12:15],v[164:167]
R_ v[92:95],v199,38912
AR_ v211,a209
AR_ v212,a210
AR_ v213,a211
M_ v[168:171],v[48:51],v[16:19],v[168:171]
AR_ v214,a212
AR_ v215,a213
M_ v[168:171],v[52:55],v[20:23],v[168:171]
R_ v[104:107],v201,33792
AR_ v216,a214
AR_ v217,a215
M_ v[172:175],v[48:51],v[24:27],v[172:175]
buffer_load_dwordx4 v208,s[28:31],s44 offen offset:1024 sc1 lds
C_ v204,v210,v211
C_ v205,v212,v213
C_ v206,v214,v215
M_ v[172:175],v[52:55],v[28:31],v[172:175]
R_ v[108:111],v201,37888
C_ v207,v216,v217
AW_ a208,v204
M_ v[176:179],v[56:59],v[0:3],v[176:179]
AW_ a209,v205
AW_ a210,v206
M_ v[176:179],v[60:63],v[4:7],v[176:179]
R_ v[112:115],v201,34816
AW_ a211,v207
AR_ v244,a216
AR_ v245,a217
M_ v[180:183],v[56:59],v[8:11],v[180:183]
buffer_load_dwordx4 v208,s[28:31],s44 offen offset:2048 sc1 lds
AR_ v246,a218
AR_ v247,a219
M_ v[180:183],v[60:63],v[12:15],v[180:183]
R_ v[116:119],v201,38912
AR_ v248,a220
AR_ v249,a221
M_ v[184:187],v[56:59],v[16:19],v[184:187]
AR_ v202,a222
AR_ v203,a223
C_ v218,v244,v245
M_ v[184:187],v[60:63],v[20:23],v[184:187]
R_ v[120:123],v201,35840
C_ v219,v246,v247
C_ v220,v248,v249
M_ v[188:191],v[56:59],v[24:27],v[188:191]
buffer_load_dwordx4 v208,s[28:31],s44 offen offset:3072 sc1 lds
C_ v221,v202,v203
AW_ a216,v218
M_ v[188:191],v[60:63],v[28:31],v[188:191]
R_ v[124:127],v201,39936
AW_ a217,v219
AW_ a218,v220
s_waitcnt lgkmcnt(2)
M_ v[128:131],v[96:99],v[64:67],v[128:131]
AW_ a219,v221
AR_ v210,a224
AR_ v211,a225
M_ v[128:131],v[100:103],v[68:71],v[128:131]
ds_read_b128 v[32:35],v200
AR_ v212,a226
AR_ v213,a227
M_ v[132:135],v[96:99],v[72:75],v[132:135]
AR_ v214,a228
AR_ v215,a229
M_ v[132:135],v[100:103],v[76:79],v[132:135]
R_ v[36:39],v200,4096
AR_ v216,a230
AR_ v217,a231
M_ v[136:139],v[96:99],v[80:83],v[136:139]
C_ v204,v210,v211
C_ v205,v212,v213
M_ v[136:139],v[100:103],v[84:87],v[136:139]
ds_read_b128 v[0:3],v196
C_ v206,v214,v215
C_ v207,v216,v217
AW_ a196,v204
M_ v[140:143],v[96:99],v[88:91],v[140:143]
AW_ a197,v205
AW_ a198,v206
M_ v[140:143],v[100:103],v[92:95],v[140:143]
ds_read_b128 v[4:7],v197
AW_ a199,v207
AR_ v244,a232
M_ v[144:147],v[104:107],v[64:67],v[144:147]
AR_ v245,a233
AR_ v246,a234
M_ v[144:147],v[108:111],v[68:71],v[144:147]
R_ v[8:11],v196,2048
AR_ v247,a235
AR_ v248,a236
M_ v[148:151],v[104:107],v[72:75],v[148:151]
AR_ v249,a237
AR_ v202,a238
AR_ v203,a239
M_ v[148:151],v[108:111],v[76:79],v[148:151]
R_ v[12:15],v197,2048
C_ v218,v244,v245
C_ v219,v246,v247
M_ v[152:155],v[104:107],v[80:83],v[152:155]
C_ v220,v248,v249
C_ v221,v202,v203
M_ v[152:155],v[108:111],v[84:87],v[152:155]
R_ v[16:19],v196,4096
AW_ a204,v218
AW_ a205,v219
M_ v[156:159],v[104:107],v[88:91],v[156:159]
AW_ a206,v220
AW_ a207,v221
M_ v[156:159],v[108:111],v[92:95],v[156:159]
R_ v[20:23],v197,4096
AR_ v210,a240
AR_ v211,a241
M_ v[160:163],v[112:115],v[64:67],v[160:163]
AR_ v212,a242
AR_ v213,a243
AR_ v214,a244
M_ v[160:163],v[116:119],v[68:71],v[160:163]
R_ v[24:27],v196,6144
AR_ v215,a245
AR_ v216,a246
M_ v[164:167],v[112:115],v[72:75],v[164:167]
AR_ v217,a247
C_ v204,v210,v211
M_ v[164:167],v[116:119],v[76:79],v[164:167]
R_ v[28:31],v197,6144
C_ v205,v212,v213
C_ v206,v214,v215
M_ v[168:171],v[112:115],v[80:83],v[168:171]
C_ v207,v216,v217
AW_ a212,v204
M_ v[168:171],v[116:119],v[84:87],v[168:171]
R_ v[40:43],v200,1024
AW_ a213,v205
AW_ a214,v206
AW_ a215,v207
M_ v[172:175],v[112:115],v[88:91],v[172:175]
AR_ v244,a248
AR_ v245,a249
M_ v[172:175],v[116:119],v[92:95],v[172:175]
R_ v[44:47],v200,5120
AR_ v246,a250
AR_ v247,a251
s_waitcnt lgkmcnt(2)
M_ v[176:179],v[120:123],v[64:67],v[176:179]
AR_ v248,a252
AR_ v249,a253
M_ v[176:179],v[124:127],v[68:71],v[176:179]
R_ v[48:51],v200,2048
AR_ v202,a254
AR_ v203,a255
M_ v[180:183],v[120:123],v[72:75],v[180:183]
C_ v218,v244,v245
C_ v219,v246,v247
C_ v220,v248,v249
M_ v[180:183],v[124:127],v[76:79],v[180:183]
R_ v[52:55],v200,6144
C_ v221,v202,v203
AW_ a220,v218
M_ v[184:187],v[120:123],v[80:83],v[184:187]
AW_ a221,v219
AW_ a222,v220
M_ v[184:187],v[124:127],v[84:87],v[184:187]
R_ v[56:59],v200,3072
AW_ a223,v221
s_mov_b32 s28,0x0
M_ v[188:191],v[120:123],v[88:91],v[188:191]
s_mov_b32 s29,0x400
s_mov_b32 s30,0x2000
M_ v[188:191],v[124:127],v[92:95],v[188:191]
R_ v[60:63],v200,7168
s_mov_b32 s31,0x2400
s_mov_b32 s33,2048
s_waitcnt vmcnt(0)
s_waitcnt lgkmcnt(0)
s_barrier
M_ v[128:131],v[32:35],v[0:3],v[128:131]
s_add_u32 s55,s68,0x0
M_ v[128:131],v[36:39],v[4:7],v[128:131]
s_lshl_b32 s56,s3,13
R_ v[96:99],v200,32768
M_ v[132:135],v[32:35],v[8:11],v[132:135]
s_add_u32 m0,s56,0x10000
s_nop 0
buffer_load_dwordx4 v224,s[4:7],s55 offen lds
M_ v[132:135],v[36:39],v[12:15],v[132:135]
R_ v[100:103],v200,36864
M_ v[136:139],v[32:35],v[16:19],v[136:139]
M_ v[136:139],v[36:39],v[20:23],v[136:139]
R_ v[64:67],v196,32768
buffer_load_dwordx4 v225,s[4:7],s55 offen offset:1024 lds
M_ v[140:143],v[32:35],v[24:27],v[140:143]
M_ v[140:143],v[36:39],v[28:31],v[140:143]
R_ v[68:71],v197,32768
buffer_load_dwordx4 v226,s[4:7],s55 offen offset:2048 lds
M_ v[144:147],v[40:43],v[0:3],v[144:147]
M_ v[144:147],v[44:47],v[4:7],v[144:147]
R_ v[72:75],v196,34816
M_ v[148:151],v[40:43],v[8:11],v[148:151]
buffer_load_dwordx4 v227,s[4:7],s55 offen offset:3072 lds
M_ v[148:151],v[44:47],v[12:15],v[148:151]
s_add_u32 s55,s68,0xa000
R_ v[76:79],v197,34816
M_ v[152:155],v[40:43],v[16:19],v[152:155]
s_lshl_b32 s56,s3,13
s_add_u32 m0,s56,0x11000
s_nop 0
buffer_load_dwordx4 v224,s[4:7],s55 offen lds
M_ v[152:155],v[44:47],v[20:23],v[152:155]
R_ v[80:83],v196,36864
M_ v[156:159],v[40:43],v[24:27],v[156:159]
M_ v[156:159],v[44:47],v[28:31],v[156:159]
R_ v[84:87],v197,36864
buffer_load_dwordx4 v225,s[4:7],s55 offen offset:1024 lds
M_ v[160:163],v[48:51],v[0:3],v[160:163]
M_ v[160:163],v[52:55],v[4:7],v[160:163]
R_ v[88:91],v196,38912
buffer_load_dwordx4 v226,s[4:7],s55 offen offset:2048 lds
M_ v[164:167],v[48:51],v[8:11],v[164:167]
M_ v[164:167],v[52:55],v[12:15],v[164:167]
R_ v[92:95],v197,38912
M_ v[168:171],v[48:51],v[16:19],v[168:171]
buffer_load_dwordx4 v227,s[4:7],s55 offen offset:3072 lds
M_ v[168:171],v[52:55],v[20:23],v[168:171]
s_add_u32 s55,s69,0x0
R_ v[104:107],v200,33792
M_ v[172:175],v[48:51],v[24:27],v[172:175]
s_add_u32 m0,s75,0x18000
s_nop 0
buffer_load_dwordx4 v228,s[8:11],s55 offen lds
M_ v[172:175],v[52:55],v[28:31],v[172:175]
R_ v[108:111],v200,37888
M_ v[176:179],v[56:59],v[0:3],v[176:179]
M_ v[176:179],v[60:63],v[4:7],v[176:179]
R_ v[112:115],v200,34816
buffer_load_dwordx4 v229,s[8:11],s55 offen offset:1024 lds
M_ v[180:183],v[56:59],v[8:11],v[180:183]
M_ v[180:183],v[60:63],v[12:15],v[180:183]
R_ v[116:119],v200,38912
buffer_load_dwordx4 v230,s[8:11],s55 offen offset:2048 lds
M_ v[184:187],v[56:59],v[16:19],v[184:187]
M_ v[184:187],v[60:63],v[20:23],v[184:187]
R_ v[120:123],v200,35840
M_ v[188:191],v[56:59],v[24:27],v[188:191]
buffer_load_dwordx4 v231,s[8:11],s55 offen offset:3072 lds
M_ v[188:191],v[60:63],v[28:31],v[188:191]
R_ v[124:127],v200,39936
s_waitcnt lgkmcnt(4)
M_ v[128:131],v[96:99],v[64:67],v[128:131]
s_add_u32 s55,s68,0x80
M_ v[128:131],v[100:103],v[68:71],v[128:131]
s_lshl_b32 s56,s3,13
M_ v[132:135],v[96:99],v[72:75],v[132:135]
s_add_u32 m0,s56,0x1c000
s_nop 0
buffer_load_dwordx4 v224,s[4:7],s55 offen lds
M_ v[132:135],v[100:103],v[76:79],v[132:135]
M_ v[136:139],v[96:99],v[80:83],v[136:139]
M_ v[136:139],v[100:103],v[84:87],v[136:139]
buffer_load_dwordx4 v225,s[4:7],s55 offen offset:1024 lds
M_ v[140:143],v[96:99],v[88:91],v[140:143]
M_ v[140:143],v[100:103],v[92:95],v[140:143]
buffer_load_dwordx4 v226,s[4:7],s55 offen offset:2048 lds
M_ v[144:147],v[104:107],v[64:67],v[144:147]
M_ v[144:147],v[108:111],v[68:71],v[144:147]
M_ v[148:151],v[104:107],v[72:75],v[148:151]
buffer_load_dwordx4 v227,s[4:7],s55 offen offset:3072 lds
M_ v[148:151],v[108:111],v[76:79],v[148:151]
s_add_u32 s55,s68,0xa080
M_ v[152:155],v[104:107],v[80:83],v[152:155]
s_lshl_b32 s56,s3,13
s_add_u32 m0,s56,0x1d000
s_nop 0
buffer_load_dwordx4 v224,s[4:7],s55 offen lds
M_ v[152:155],v[108:111],v[84:87],v[152:155]
M_ v[156:159],v[104:107],v[88:91],v[156:159]
M_ v[156:159],v[108:111],v[92:95],v[156:159]
buffer_load_dwordx4 v225,s[4:7],s55 offen offset:1024 lds
s_waitcnt lgkmcnt(0)
M_ v[160:163],v[112:115],v[64:67],v[160:163]
M_ v[160:163],v[116:119],v[68:71],v[160:163]
buffer_load_dwordx4 v226,s[4:7],s55 offen offset:2048 lds
M_ v[164:167],v[112:115],v[72:75],v[164:167]
M_ v[164:167],v[116:119],v[76:79],v[164:167]
M_ v[168:171],v[112:115],v[80:83],v[168:171]
buffer_load_dwordx4 v227,s[4:7],s55 offen offset:3072 lds
M_ v[168:171],v[116:119],v[84:87],v[168:171]
s_add_u32 s55,s69,0x80000
M_ v[172:175],v[112:115],v[88:91],v[172:175]
s_add_u32 m0,s75,0x24000
s_nop 0
buffer_load_dwordx4 v228,s[8:11],s55 offen lds
M_ v[172:175],v[116:119],v[92:95],v[172:175]
M_ v[176:179],v[120:123],v[64:67],v[176:179]
M_ v[176:179],v[124:127],v[68:71],v[176:179]
buffer_load_dwordx4 v229,s[8:11],s55 offen offset:1024 lds
M_ v[180:183],v[120:123],v[72:75],v[180:183]
M_ v[180:183],v[124:127],v[76:79],v[180:183]
buffer_load_dwordx4 v230,s[8:11],s55 offen offset:2048 lds
M_ v[184:187],v[120:123],v[80:83],v[184:187]
M_ v[184:187],v[124:127],v[84:87],v[184:187]
M_ v[188:191],v[120:123],v[88:91],v[188:191]
buffer_load_dwordx4 v231,s[8:11],s55 offen offset:3072 lds
M_ v[188:191],v[124:127],v[92:95],v[188:191]
LKV_E6_%=:
s_waitcnt lgkmcnt(0)
s_barrier
s_add_u32 s55,s68,0x100
s_lshl_b32 s56,s3,13
s_add_u32 m0,s56,0x0
s_nop 0
buffer_load_dwordx4 v224,s[4:7],s55 offen lds
buffer_load_dwordx4 v225,s[4:7],s55 offen offset:1024 lds
buffer_load_dwordx4 v226,s[4:7],s55 offen offset:2048 lds
buffer_load_dwordx4 v227,s[4:7],s55 offen offset:3072 lds
s_add_u32 s55,s68,0xa100
s_lshl_b32 s56,s3,13
s_add_u32 m0,s56,0x1000
s_nop 0
buffer_load_dwordx4 v224,s[4:7],s55 offen lds
buffer_load_dwordx4 v225,s[4:7],s55 offen offset:1024 lds
buffer_load_dwordx4 v226,s[4:7],s55 offen offset:2048 lds
buffer_load_dwordx4 v227,s[4:7],s55 offen offset:3072 lds
s_add_u32 s55,s69,0x100000
s_add_u32 m0,s75,0x8000
s_nop 0
buffer_load_dwordx4 v228,s[8:11],s55 offen lds
buffer_load_dwordx4 v229,s[8:11],s55 offen offset:1024 lds
buffer_load_dwordx4 v230,s[8:11],s55 offen offset:2048 lds
buffer_load_dwordx4 v231,s[8:11],s55 offen offset:3072 lds
s_nop 7
s_mov_b32 s60,s32
s_mov_b32 s61,s34
s_mov_b32 s62,s36
s_mov_b32 s63,s41
C_ v192,v128,v129
C_ v193,v130,v131
C_ v194,v144,v145
C_ v195,v146,v147
C_ v196,v160,v161
C_ v197,v162,v163
C_ v198,v176,v177
C_ v199,v178,v179
C_ v200,v132,v133
C_ v201,v134,v135
C_ v202,v148,v149
C_ v203,v150,v151
C_ v204,v164,v165
C_ v205,v166,v167
C_ v206,v180,v181
C_ v207,v182,v183
C_ v210,v136,v137
C_ v211,v138,v139
C_ v212,v152,v153
v_add_u32 v222,s28,v208
C_ v213,v154,v155
C_ v214,v168,v169
C_ v215,v170,v171
C_ v216,v184,v185
C_ v217,v186,v187
C_ v218,v140,v141
C_ v219,v142,v143
)A"
R"A(C_ v220,v156,v157
C_ v221,v158,v159
C_ v244,v172,v173
C_ v245,v174,v175
C_ v246,v188,v189
C_ v247,v190,v191
v_mov_b32 v128,v192
v_mov_b32 v129,v193
v_mov_b32 v130,v194
v_mov_b32 v131,v195
v_mov_b32 v132,v196
v_mov_b32 v133,v197
v_mov_b32 v134,v198
v_mov_b32 v135,v199
v_mov_b32 v136,v200
v_mov_b32 v137,v201
v_add_u32 v223,s29,v208
v_mov_b32 v138,v202
v_mov_b32 v139,v203
v_mov_b32 v140,v204
v_mov_b32 v141,v205
v_mov_b32 v142,v206
v_mov_b32 v143,v207
v_mov_b32 v144,v210
v_mov_b32 v145,v211
v_mov_b32 v146,v212
v_mov_b32 v147,v213
v_mov_b32 v148,v214
v_mov_b32 v149,v215
v_mov_b32 v150,v216
v_mov_b32 v151,v217
v_mov_b32 v152,v218
v_mov_b32 v153,v219
v_mov_b32 v154,v220
v_mov_b32 v155,v221
v_mov_b32 v156,v244
v_mov_b32 v157,v245
v_mov_b32 v158,v246
v_mov_b32 v159,v247
v_mbcnt_lo_u32_b32 v160,-1,0
v_mbcnt_hi_u32_b32 v160,-1,v160
v_add_u32 v248,s30,v208
v_and_b32 v161,15,v160
v_lshrrev_b32 v162,4,v160
v_mov_b32 v165,0
s_lshl_b32 s44,s36,11
s_add_u32 s44,s44,0x3300000
s_add_u32 s42,s88,s44
s_addc_u32 s43,s89,0
v_lshlrev_b32 v163,4,v160
global_load_dwordx4 a[32:35],v163,s[42:43] sc1
global_load_dwordx4 a[36:39],v163,s[42:43] offset:1024 sc1
v_and_b32 v163,1,v161
v_lshlrev_b32 v163,4,v163
v_mov_b32 v164,0x3f80
v_lshlrev_b32 v164,v163,v164
v_and_b32 v163,2,v161
v_lshrrev_b32 v167,2,v161
v_cmp_eq_u32 s[46:47],v162,v167
s_nop 0
v_cmp_eq_u32 vcc,0,v163
s_nop 0
s_and_b64 s[48:49],s[46:47],vcc
s_andn2_b64 s[50:51],s[46:47],vcc
v_cndmask_b32 v166,v165,v164,s[48:49]
AW_ a52,v166
v_add_u32 v249,s31,v208
v_cndmask_b32 v166,v165,v164,s[50:51]
AW_ a53,v166
AW_ a54,v165
AW_ a55,v165
v_cmp_eq_u32 s[46:47],v162,v167
s_nop 0
v_cmp_eq_u32 vcc,0,v163
s_nop 0
s_and_b64 s[48:49],s[46:47],vcc
s_andn2_b64 s[50:51],s[46:47],vcc
v_cndmask_b32 v166,v165,v164,s[48:49]
AW_ a58,v166
v_cndmask_b32 v166,v165,v164,s[50:51]
AW_ a59,v166
AW_ a56,v165
AW_ a57,v165
v_add_u32 v166,1,v167
v_cmp_eq_u32 s[46:47],v162,v166
s_nop 0
v_cmp_eq_u32 vcc,0,v163
s_nop 0
s_and_b64 s[48:49],s[46:47],vcc
s_andn2_b64 s[50:51],s[46:47],vcc
v_cndmask_b32 v166,v165,v164,s[48:49]
global_store_dwordx4 v222,a[0:3],s[92:93] sc1
AW_ a42,v166
v_cndmask_b32 v166,v165,v164,s[50:51]
AW_ a43,v166
AW_ a40,v165
AW_ a41,v165
v_cmp_eq_u32 s[46:47],1,v162
v_cmp_eq_u32 s[52:53],3,v167
s_nop 0
s_and_b64 s[46:47],s[46:47],s[52:53]
v_cmp_eq_u32 vcc,0,v163
s_nop 0
s_and_b64 s[48:49],s[46:47],vcc
s_andn2_b64 s[50:51],s[46:47],vcc
v_cndmask_b32 v166,v165,v164,s[48:49]
AW_ a46,v166
v_cndmask_b32 v166,v165,v164,s[50:51]
AW_ a47,v166
AW_ a44,v165
AW_ a45,v165
v_cmp_eq_u32 s[46:47],2,v162
v_cmp_eq_u32 s[52:53],0,v167
s_nop 0
s_and_b64 s[46:47],s[46:47],s[52:53]
global_store_dwordx4 v223,a[4:7],s[92:93] sc1
v_cmp_eq_u32 vcc,0,v163
s_nop 0
s_and_b64 s[48:49],s[46:47],vcc
s_andn2_b64 s[50:51],s[46:47],vcc
v_cndmask_b32 v166,v165,v164,s[48:49]
AW_ a50,v166
v_cndmask_b32 v166,v165,v164,s[50:51]
AW_ a51,v166
AW_ a48,v165
AW_ a49,v165
v_cmp_eq_u32 vcc,4,v161
v_mov_b32 v166,0x3f800000
s_nop 0
v_cndmask_b32 v218,v165,v166,vcc
v_cndmask_b32 v219,v165,v166,vcc
v_cndmask_b32 v220,v165,v166,vcc
v_cndmask_b32 v221,v165,v166,vcc
s_waitcnt vmcnt(3)
M_ v[184:187],a[32:35],v[128:131],0
s_waitcnt vmcnt(2)
M_ v[188:191],a[36:39],v[128:131],0
M_ v[192:195],v[128:131],a[40:43],0
M_ v[192:195],v[132:135],a[44:47],v[192:195]
M_ v[196:199],v[132:135],a[48:51],v[218:221]
s_nop 6
C_ v160,v192,v193
C_ v161,v194,v195
global_store_dwordx4 v248,a[8:11],s[92:93] sc1
C_ v164,v196,v197
C_ v165,v198,v199
v_max3_f32 v197,v184,v185,v186
v_min3_f32 v198,v184,v185,v186
v_max3_f32 v197,v197,v187,v188
v_min3_f32 v198,v198,v187,v188
v_mov_b32 v199,v197
v_mov_b32 v202,v198
s_nop 0
v_permlane32_swap_b32 v197,v199
v_permlane32_swap_b32 v198,v202
v_max_f32 v197,v197,v199
v_min_f32 v198,v198,v202
v_mov_b32 v199,v197
v_mov_b32 v202,v198
s_nop 0
v_permlane16_swap_b32 v197,v199
v_permlane16_swap_b32 v198,v202
v_max_f32 v197,v197,v199
v_min_f32 v198,v198,v202
v_mul_f32 v197,s63,v197
v_mul_f32 v198,s63,v198
v_fma_f32 v192,-v184,s63,v198
v_fma_f32 v184,v184,s63,-v197
v_fma_f32 v193,-v185,s63,v198
global_store_dwordx4 v249,a[12:15],s[92:93] sc1
v_fma_f32 v185,v185,s63,-v197
v_fma_f32 v194,-v186,s63,v198
v_fma_f32 v186,v186,s63,-v197
v_fma_f32 v195,-v187,s63,v198
v_fma_f32 v187,v187,s63,-v197
v_fma_f32 v196,-v188,s63,v198
v_fma_f32 v188,v188,s63,-v197
E_ v184,v184
E_ v192,v192
E_ v185,v185
E_ v193,v193
E_ v186,v186
E_ v194,v194
E_ v187,v187
E_ v195,v195
E_ v188,v188
E_ v196,v196
v_add_f32 v200,v184,v185
v_add_f32 v201,v192,v193
v_add_f32 v200,v200,v186
v_add_f32 v201,v201,v194
v_add_f32 v200,v200,v187
v_add_f32 v201,v201,v195
v_add_f32 v200,v200,v188
global_store_dwordx4 v222,a[16:19],s[94:95] sc1
v_add_f32 v201,v201,v196
v_mov_b32 v199,v200
v_mov_b32 v202,v201
s_nop 0
v_permlane32_swap_b32 v200,v199
v_permlane32_swap_b32 v201,v202
v_add_f32 v200,v200,v199
v_add_f32 v201,v201,v202
v_mov_b32 v199,v200
v_mov_b32 v202,v201
s_nop 0
v_permlane16_swap_b32 v200,v199
v_permlane16_swap_b32 v201,v202
v_add_f32 v200,v200,v199
v_add_f32 v201,v201,v202
v_rcp_f32 v200,v200
v_rcp_f32 v201,v201
v_mul_f32 v184,v184,v200
v_mul_f32 v192,v192,v201
v_mul_f32 v185,v185,v200
v_mul_f32 v193,v193,v201
v_mul_f32 v186,v186,v200
v_mul_f32 v194,v194,v201
v_mul_f32 v187,v187,v200
v_mul_f32 v195,v195,v201
global_store_dwordx4 v223,a[20:23],s[94:95] sc1
v_mul_f32 v188,v188,v200
v_mul_f32 v196,v196,v201
C_ v204,v184,v185
C_ v205,v186,v187
C_ v206,v188,0
v_mov_b32 v207,0
C_ v210,v192,v193
C_ v211,v194,v195
C_ v212,v196,0
v_mov_b32 v213,0
AW_ a160,v204
AW_ a161,v205
AW_ a162,v206
AW_ a163,v207
AW_ a164,v210
AW_ a165,v211
AW_ a166,v212
AW_ a167,v213
M_ v[184:187],a[32:35],v[132:135],0
M_ v[188:191],a[36:39],v[132:135],0
s_nop 6
v_max3_f32 v197,v184,v185,v186
v_min3_f32 v198,v184,v185,v186
v_max3_f32 v197,v197,v187,v188
v_min3_f32 v198,v198,v187,v188
global_store_dwordx4 v248,a[24:27],s[94:95] sc1
v_mov_b32 v199,v197
v_mov_b32 v202,v198
s_nop 0
v_permlane32_swap_b32 v197,v199
v_permlane32_swap_b32 v198,v202
v_max_f32 v197,v197,v199
v_min_f32 v198,v198,v202
v_mov_b32 v199,v197
v_mov_b32 v202,v198
s_nop 0
v_permlane16_swap_b32 v197,v199
v_permlane16_swap_b32 v198,v202
v_max_f32 v197,v197,v199
v_min_f32 v198,v198,v202
v_mul_f32 v197,s63,v197
v_mul_f32 v198,s63,v198
v_fma_f32 v192,-v184,s63,v198
v_fma_f32 v184,v184,s63,-v197
v_fma_f32 v193,-v185,s63,v198
v_fma_f32 v185,v185,s63,-v197
v_fma_f32 v194,-v186,s63,v198
v_fma_f32 v186,v186,s63,-v197
v_fma_f32 v195,-v187,s63,v198
v_fma_f32 v187,v187,s63,-v197
v_fma_f32 v196,-v188,s63,v198
global_store_dwordx4 v249,a[28:31],s[94:95] sc1
v_fma_f32 v188,v188,s63,-v197
E_ v184,v184
E_ v192,v192
E_ v185,v185
E_ v193,v193
E_ v186,v186
E_ v194,v194
E_ v187,v187
E_ v195,v195
E_ v188,v188
E_ v196,v196
v_add_f32 v200,v184,v185
v_add_f32 v201,v192,v193
v_add_f32 v200,v200,v186
v_add_f32 v201,v201,v194
v_add_f32 v200,v200,v187
v_add_f32 v201,v201,v195
v_add_f32 v200,v200,v188
v_add_f32 v201,v201,v196
v_mov_b32 v199,v200
v_mov_b32 v202,v201
s_nop 0
v_permlane32_swap_b32 v200,v199
v_permlane32_swap_b32 v201,v202
v_add_f32 v200,v200,v199
s_add_u32 s92,s92,s33
s_addc_u32 s93,s93,0
v_add_f32 v201,v201,v202
v_mov_b32 v199,v200
v_mov_b32 v202,v201
s_nop 0
v_permlane16_swap_b32 v200,v199
v_permlane16_swap_b32 v201,v202
v_add_f32 v200,v200,v199
v_add_f32 v201,v201,v202
v_rcp_f32 v200,v200
v_rcp_f32 v201,v201
v_mul_f32 v184,v184,v200
v_mul_f32 v192,v192,v201
v_mul_f32 v185,v185,v200
v_mul_f32 v193,v193,v201
v_mul_f32 v186,v186,v200
v_mul_f32 v194,v194,v201
v_mul_f32 v187,v187,v200
v_mul_f32 v195,v195,v201
v_mul_f32 v188,v188,v200
v_mul_f32 v196,v196,v201
C_ v204,v184,v185
C_ v205,v186,v187
C_ v206,v188,0
v_mov_b32 v207,0
s_add_u32 s94,s94,s33
s_addc_u32 s95,s95,0
C_ v210,v192,v193
C_ v211,v194,v195
C_ v212,v196,0
v_mov_b32 v213,0
M_ v[184:187],v[204:207],a[52:55],0
M_ v[188:191],v[204:207],a[56:59],0
M_ v[192:195],v[210:213],a[52:55],0
M_ v[196:199],v[210:213],a[56:59],0
s_nop 4
C_ v168,v184,v185
C_ v169,v186,v187
C_ v172,v188,v189
C_ v173,v190,v191
C_ v176,v192,v193
C_ v177,v194,v195
C_ v180,v196,v197
C_ v181,v198,v199
M_ v[184:187],a[32:35],v[136:139],0
M_ v[188:191],a[36:39],v[136:139],0
M_ v[192:195],v[136:139],a[40:43],0
M_ v[192:195],v[140:143],a[44:47],v[192:195]
M_ v[196:199],v[140:143],a[48:51],v[218:221]
s_nop 6
C_ v162,v192,v193
C_ v163,v194,v195
C_ v166,v196,v197
global_store_dwordx4 v222,a[64:67],s[92:93] sc1
C_ v167,v198,v199
v_max3_f32 v197,v184,v185,v186
v_min3_f32 v198,v184,v185,v186
v_max3_f32 v197,v197,v187,v188
v_min3_f32 v198,v198,v187,v188
v_mov_b32 v199,v197
v_mov_b32 v202,v198
s_nop 0
v_permlane32_swap_b32 v197,v199
v_permlane32_swap_b32 v198,v202
v_max_f32 v197,v197,v199
v_min_f32 v198,v198,v202
v_mov_b32 v199,v197
v_mov_b32 v202,v198
s_nop 0
v_permlane16_swap_b32 v197,v199
v_permlane16_swap_b32 v198,v202
v_max_f32 v197,v197,v199
v_min_f32 v198,v198,v202
v_mul_f32 v197,s63,v197
v_mul_f32 v198,s63,v198
v_fma_f32 v192,-v184,s63,v198
v_fma_f32 v184,v184,s63,-v197
v_fma_f32 v193,-v185,s63,v198
v_fma_f32 v185,v185,s63,-v197
global_store_dwordx4 v223,a[68:71],s[92:93] sc1
v_fma_f32 v194,-v186,s63,v198
v_fma_f32 v186,v186,s63,-v197
v_fma_f32 v195,-v187,s63,v198
v_fma_f32 v187,v187,s63,-v197
v_fma_f32 v196,-v188,s63,v198
v_fma_f32 v188,v188,s63,-v197
E_ v184,v184
E_ v192,v192
E_ v185,v185
E_ v193,v193
E_ v186,v186
E_ v194,v194
E_ v187,v187
E_ v195,v195
E_ v188,v188
E_ v196,v196
v_add_f32 v200,v184,v185
v_add_f32 v201,v192,v193
v_add_f32 v200,v200,v186
v_add_f32 v201,v201,v194
v_add_f32 v200,v200,v187
v_add_f32 v201,v201,v195
v_add_f32 v200,v200,v188
v_add_f32 v201,v201,v196
global_store_dwordx4 v248,a[72:75],s[92:93] sc1
v_mov_b32 v199,v200
v_mov_b32 v202,v201
s_nop 0
v_permlane32_swap_b32 v200,v199
v_permlane32_swap_b32 v201,v202
v_add_f32 v200,v200,v199
v_add_f32 v201,v201,v202
v_mov_b32 v199,v200
v_mov_b32 v202,v201
s_nop 0
v_permlane16_swap_b32 v200,v199
v_permlane16_swap_b32 v201,v202
v_add_f32 v200,v200,v199
v_add_f32 v201,v201,v202
v_rcp_f32 v200,v200
v_rcp_f32 v201,v201
v_mul_f32 v184,v184,v200
v_mul_f32 v192,v192,v201
v_mul_f32 v185,v185,v200
v_mul_f32 v193,v193,v201
v_mul_f32 v186,v186,v200
v_mul_f32 v194,v194,v201
v_mul_f32 v187,v187,v200
v_mul_f32 v195,v195,v201
v_mul_f32 v188,v188,v200
global_store_dwordx4 v249,a[76:79],s[92:93] sc1
v_mul_f32 v196,v196,v201
C_ v204,v184,v185
C_ v205,v186,v187
C_ v206,v188,0
v_mov_b32 v207,0
C_ v210,v192,v193
C_ v211,v194,v195
C_ v212,v196,0
v_mov_b32 v213,0
AW_ a168,v204
AW_ a169,v205
AW_ a170,v206
AW_ a171,v207
AW_ a172,v210
AW_ a173,v211
AW_ a174,v212
AW_ a175,v213
M_ v[184:187],a[32:35],v[140:143],0
M_ v[188:191],a[36:39],v[140:143],0
s_nop 6
v_max3_f32 v197,v184,v185,v186
v_min3_f32 v198,v184,v185,v186
v_max3_f32 v197,v197,v187,v188
v_min3_f32 v198,v198,v187,v188
v_mov_b32 v199,v197
global_store_dwordx4 v222,a[80:83],s[94:95] sc1
v_mov_b32 v202,v198
v_permlane32_swap_b32 v197,v199
s_nop 0
v_permlane32_swap_b32 v198,v202
v_max_f32 v197,v197,v199
v_min_f32 v198,v198,v202
v_mov_b32 v199,v197
v_mov_b32 v202,v198
s_nop 0
v_permlane16_swap_b32 v197,v199
v_permlane16_swap_b32 v198,v202
v_max_f32 v197,v197,v199
v_min_f32 v198,v198,v202
v_mul_f32 v197,s63,v197
v_mul_f32 v198,s63,v198
v_fma_f32 v192,-v184,s63,v198
v_fma_f32 v184,v184,s63,-v197
v_fma_f32 v193,-v185,s63,v198
v_fma_f32 v185,v185,s63,-v197
v_fma_f32 v194,-v186,s63,v198
v_fma_f32 v186,v186,s63,-v197
v_fma_f32 v195,-v187,s63,v198
v_fma_f32 v187,v187,s63,-v197
v_fma_f32 v196,-v188,s63,v198
v_fma_f32 v188,v188,s63,-v197
global_store_dwordx4 v223,a[84:87],s[94:95] sc1
E_ v184,v184
E_ v192,v192
E_ v185,v185
E_ v193,v193
E_ v186,v186
E_ v194,v194
E_ v187,v187
E_ v195,v195
E_ v188,v188
E_ v196,v196
v_add_f32 v200,v184,v185
v_add_f32 v201,v192,v193
v_add_f32 v200,v200,v186
v_add_f32 v201,v201,v194
v_add_f32 v200,v200,v187
v_add_f32 v201,v201,v195
v_add_f32 v200,v200,v188
v_add_f32 v201,v201,v196
v_mov_b32 v199,v200
v_mov_b32 v202,v201
s_nop 0
v_permlane32_swap_b32 v200,v199
v_permlane32_swap_b32 v201,v202
v_add_f32 v200,v200,v199
global_store_dwordx4 v248,a[88:91],s[94:95] sc1
v_add_f32 v201,v201,v202
v_mov_b32 v199,v200
v_mov_b32 v202,v201
s_nop 0
v_permlane16_swap_b32 v200,v199
v_permlane16_swap_b32 v201,v202
v_add_f32 v200,v200,v199
v_add_f32 v201,v201,v202
v_rcp_f32 v200,v200
v_rcp_f32 v201,v201
v_mul_f32 v184,v184,v200
v_mul_f32 v192,v192,v201
v_mul_f32 v185,v185,v200
v_mul_f32 v193,v193,v201
v_mul_f32 v186,v186,v200
v_mul_f32 v194,v194,v201
v_mul_f32 v187,v187,v200
v_mul_f32 v195,v195,v201
v_mul_f32 v188,v188,v200
v_mul_f32 v196,v196,v201
C_ v204,v184,v185
C_ v205,v186,v187
C_ v206,v188,0
v_mov_b32 v207,0
C_ v210,v192,v193
global_store_dwordx4 v249,a[92:95],s[94:95] sc1
C_ v211,v194,v195
C_ v212,v196,0
v_mov_b32 v213,0
M_ v[184:187],v[204:207],a[52:55],0
M_ v[188:191],v[204:207],a[56:59],0
M_ v[192:195],v[210:213],a[52:55],0
M_ v[196:199],v[210:213],a[56:59],0
s_nop 4
C_ v170,v184,v185
C_ v171,v186,v187
C_ v174,v188,v189
C_ v175,v190,v191
C_ v178,v192,v193
C_ v179,v194,v195
C_ v182,v196,v197
C_ v183,v198,v199
M_ a[0:3],v[168:171],v[160:163],0
M_ a[4:7],v[168:171],v[164:167],0
M_ a[8:11],v[172:175],v[160:163],0
M_ a[12:15],v[172:175],v[164:167],0
M_ a[16:19],v[176:179],v[160:163],0
M_ a[20:23],v[176:179],v[164:167],0
M_ a[24:27],v[180:183],v[160:163],0
M_ a[28:31],v[180:183],v[164:167],0
s_add_u32 s92,s92,s33
s_addc_u32 s93,s93,0
M_ v[184:187],a[32:35],v[144:147],0
M_ v[188:191],a[36:39],v[144:147],0
M_ v[192:195],v[144:147],a[40:43],0
M_ v[192:195],v[148:151],a[44:47],v[192:195]
M_ v[196:199],v[148:151],a[48:51],v[218:221]
s_nop 6
C_ v160,v192,v193
C_ v161,v194,v195
C_ v164,v196,v197
C_ v165,v198,v199
v_max3_f32 v197,v184,v185,v186
v_min3_f32 v198,v184,v185,v186
v_max3_f32 v197,v197,v187,v188
v_min3_f32 v198,v198,v187,v188
v_mov_b32 v199,v197
v_mov_b32 v202,v198
s_nop 0
v_permlane32_swap_b32 v197,v199
v_permlane32_swap_b32 v198,v202
v_max_f32 v197,v197,v199
v_min_f32 v198,v198,v202
v_mov_b32 v199,v197
v_mov_b32 v202,v198
s_nop 0
v_permlane16_swap_b32 v197,v199
v_permlane16_swap_b32 v198,v202
v_max_f32 v197,v197,v199
s_add_u32 s94,s94,s33
s_addc_u32 s95,s95,0
)A"
R"A(v_min_f32 v198,v198,v202
v_mul_f32 v197,s63,v197
v_mul_f32 v198,s63,v198
v_fma_f32 v192,-v184,s63,v198
v_fma_f32 v184,v184,s63,-v197
v_fma_f32 v193,-v185,s63,v198
v_fma_f32 v185,v185,s63,-v197
v_fma_f32 v194,-v186,s63,v198
v_fma_f32 v186,v186,s63,-v197
v_fma_f32 v195,-v187,s63,v198
v_fma_f32 v187,v187,s63,-v197
v_fma_f32 v196,-v188,s63,v198
v_fma_f32 v188,v188,s63,-v197
E_ v184,v184
E_ v192,v192
E_ v185,v185
E_ v193,v193
E_ v186,v186
E_ v194,v194
E_ v187,v187
E_ v195,v195
E_ v188,v188
E_ v196,v196
global_store_dwordx4 v222,a[128:131],s[92:93] sc1
v_add_f32 v200,v184,v185
v_add_f32 v201,v192,v193
v_add_f32 v200,v200,v186
v_add_f32 v201,v201,v194
v_add_f32 v200,v200,v187
v_add_f32 v201,v201,v195
v_add_f32 v200,v200,v188
v_add_f32 v201,v201,v196
v_mov_b32 v199,v200
v_mov_b32 v202,v201
s_nop 0
v_permlane32_swap_b32 v200,v199
v_permlane32_swap_b32 v201,v202
v_add_f32 v200,v200,v199
v_add_f32 v201,v201,v202
v_mov_b32 v199,v200
v_mov_b32 v202,v201
s_nop 0
v_permlane16_swap_b32 v200,v199
v_permlane16_swap_b32 v201,v202
v_add_f32 v200,v200,v199
v_add_f32 v201,v201,v202
v_rcp_f32 v200,v200
v_rcp_f32 v201,v201
v_mul_f32 v184,v184,v200
v_mul_f32 v192,v192,v201
global_store_dwordx4 v223,a[132:135],s[92:93] sc1
v_mul_f32 v185,v185,v200
v_mul_f32 v193,v193,v201
v_mul_f32 v186,v186,v200
v_mul_f32 v194,v194,v201
v_mul_f32 v187,v187,v200
v_mul_f32 v195,v195,v201
v_mul_f32 v188,v188,v200
v_mul_f32 v196,v196,v201
C_ v204,v184,v185
C_ v205,v186,v187
C_ v206,v188,0
v_mov_b32 v207,0
C_ v210,v192,v193
C_ v211,v194,v195
C_ v212,v196,0
v_mov_b32 v213,0
AW_ a176,v204
AW_ a177,v205
AW_ a178,v206
AW_ a179,v207
AW_ a180,v210
AW_ a181,v211
AW_ a182,v212
global_store_dwordx4 v248,a[136:139],s[92:93] sc1
AW_ a183,v213
M_ v[184:187],a[32:35],v[148:151],0
M_ v[188:191],a[36:39],v[148:151],0
s_nop 6
v_max3_f32 v197,v184,v185,v186
v_min3_f32 v198,v184,v185,v186
v_max3_f32 v197,v197,v187,v188
v_min3_f32 v198,v198,v187,v188
v_mov_b32 v199,v197
v_mov_b32 v202,v198
s_nop 0
v_permlane32_swap_b32 v197,v199
v_permlane32_swap_b32 v198,v202
v_max_f32 v197,v197,v199
v_min_f32 v198,v198,v202
v_mov_b32 v199,v197
v_mov_b32 v202,v198
s_nop 0
v_permlane16_swap_b32 v197,v199
v_permlane16_swap_b32 v198,v202
v_max_f32 v197,v197,v199
v_min_f32 v198,v198,v202
v_mul_f32 v197,s63,v197
v_mul_f32 v198,s63,v198
v_fma_f32 v192,-v184,s63,v198
v_fma_f32 v184,v184,s63,-v197
v_fma_f32 v193,-v185,s63,v198
global_store_dwordx4 v249,a[140:143],s[92:93] sc1
v_fma_f32 v185,v185,s63,-v197
v_fma_f32 v194,-v186,s63,v198
v_fma_f32 v186,v186,s63,-v197
v_fma_f32 v195,-v187,s63,v198
v_fma_f32 v187,v187,s63,-v197
v_fma_f32 v196,-v188,s63,v198
v_fma_f32 v188,v188,s63,-v197
E_ v184,v184
E_ v192,v192
E_ v185,v185
E_ v193,v193
E_ v186,v186
E_ v194,v194
E_ v187,v187
E_ v195,v195
E_ v188,v188
E_ v196,v196
v_add_f32 v200,v184,v185
v_add_f32 v201,v192,v193
v_add_f32 v200,v200,v186
v_add_f32 v201,v201,v194
v_add_f32 v200,v200,v187
v_add_f32 v201,v201,v195
global_store_dwordx4 v222,a[144:147],s[94:95] sc1
v_add_f32 v200,v200,v188
v_add_f32 v201,v201,v196
v_mov_b32 v199,v200
v_mov_b32 v202,v201
s_nop 0
v_permlane32_swap_b32 v200,v199
v_permlane32_swap_b32 v201,v202
v_add_f32 v200,v200,v199
v_add_f32 v201,v201,v202
v_mov_b32 v199,v200
v_mov_b32 v202,v201
s_nop 0
v_permlane16_swap_b32 v200,v199
v_permlane16_swap_b32 v201,v202
v_add_f32 v200,v200,v199
v_add_f32 v201,v201,v202
v_rcp_f32 v200,v200
v_rcp_f32 v201,v201
v_mul_f32 v184,v184,v200
v_mul_f32 v192,v192,v201
v_mul_f32 v185,v185,v200
v_mul_f32 v193,v193,v201
v_mul_f32 v186,v186,v200
v_mul_f32 v194,v194,v201
v_mul_f32 v187,v187,v200
v_mul_f32 v195,v195,v201
global_store_dwordx4 v223,a[148:151],s[94:95] sc1
v_mul_f32 v188,v188,v200
v_mul_f32 v196,v196,v201
C_ v204,v184,v185
C_ v205,v186,v187
C_ v206,v188,0
v_mov_b32 v207,0
C_ v210,v192,v193
C_ v211,v194,v195
C_ v212,v196,0
v_mov_b32 v213,0
M_ v[184:187],v[204:207],a[52:55],0
M_ v[188:191],v[204:207],a[56:59],0
M_ v[192:195],v[210:213],a[52:55],0
M_ v[196:199],v[210:213],a[56:59],0
s_nop 4
C_ v168,v184,v185
C_ v169,v186,v187
C_ v172,v188,v189
C_ v173,v190,v191
C_ v176,v192,v193
C_ v177,v194,v195
C_ v180,v196,v197
C_ v181,v198,v199
M_ v[184:187],a[32:35],v[152:155],0
global_store_dwordx4 v248,a[152:155],s[94:95] sc1
M_ v[188:191],a[36:39],v[152:155],0
M_ v[192:195],v[152:155],a[40:43],0
M_ v[192:195],v[156:159],a[44:47],v[192:195]
M_ v[196:199],v[156:159],a[48:51],v[218:221]
s_nop 6
C_ v162,v192,v193
C_ v163,v194,v195
C_ v166,v196,v197
C_ v167,v198,v199
v_max3_f32 v197,v184,v185,v186
v_min3_f32 v198,v184,v185,v186
v_max3_f32 v197,v197,v187,v188
v_min3_f32 v198,v198,v187,v188
v_mov_b32 v199,v197
v_mov_b32 v202,v198
s_nop 0
v_permlane32_swap_b32 v197,v199
v_permlane32_swap_b32 v198,v202
v_max_f32 v197,v197,v199
v_min_f32 v198,v198,v202
v_mov_b32 v199,v197
v_mov_b32 v202,v198
s_nop 0
v_permlane16_swap_b32 v197,v199
v_permlane16_swap_b32 v198,v202
v_max_f32 v197,v197,v199
v_min_f32 v198,v198,v202
global_store_dwordx4 v249,a[156:159],s[94:95] sc1
v_mul_f32 v197,s63,v197
v_mul_f32 v198,s63,v198
v_fma_f32 v192,-v184,s63,v198
v_fma_f32 v184,v184,s63,-v197
v_fma_f32 v193,-v185,s63,v198
v_fma_f32 v185,v185,s63,-v197
v_fma_f32 v194,-v186,s63,v198
v_fma_f32 v186,v186,s63,-v197
v_fma_f32 v195,-v187,s63,v198
v_fma_f32 v187,v187,s63,-v197
v_fma_f32 v196,-v188,s63,v198
v_fma_f32 v188,v188,s63,-v197
E_ v184,v184
E_ v192,v192
E_ v185,v185
E_ v193,v193
E_ v186,v186
E_ v194,v194
E_ v187,v187
E_ v195,v195
E_ v188,v188
E_ v196,v196
v_add_f32 v200,v184,v185
s_add_u32 s92,s92,s33
s_addc_u32 s93,s93,0
v_add_f32 v201,v192,v193
v_add_f32 v200,v200,v186
v_add_f32 v201,v201,v194
v_add_f32 v200,v200,v187
v_add_f32 v201,v201,v195
v_add_f32 v200,v200,v188
v_add_f32 v201,v201,v196
v_mov_b32 v199,v200
v_mov_b32 v202,v201
s_nop 0
v_permlane32_swap_b32 v200,v199
v_permlane32_swap_b32 v201,v202
v_add_f32 v200,v200,v199
v_add_f32 v201,v201,v202
v_mov_b32 v199,v200
v_mov_b32 v202,v201
s_nop 0
v_permlane16_swap_b32 v200,v199
v_permlane16_swap_b32 v201,v202
v_add_f32 v200,v200,v199
v_add_f32 v201,v201,v202
v_rcp_f32 v200,v200
v_rcp_f32 v201,v201
v_mul_f32 v184,v184,v200
v_mul_f32 v192,v192,v201
v_mul_f32 v185,v185,v200
s_add_u32 s94,s94,s33
s_addc_u32 s95,s95,0
v_mul_f32 v193,v193,v201
v_mul_f32 v186,v186,v200
v_mul_f32 v194,v194,v201
v_mul_f32 v187,v187,v200
v_mul_f32 v195,v195,v201
v_mul_f32 v188,v188,v200
v_mul_f32 v196,v196,v201
C_ v204,v184,v185
C_ v205,v186,v187
C_ v206,v188,0
v_mov_b32 v207,0
C_ v210,v192,v193
C_ v211,v194,v195
C_ v212,v196,0
v_mov_b32 v213,0
AW_ a184,v204
AW_ a185,v205
AW_ a186,v206
AW_ a187,v207
AW_ a188,v210
AW_ a189,v211
AW_ a190,v212
AW_ a191,v213
global_store_dwordx4 v222,a[192:195],s[92:93] sc1
M_ v[184:187],a[32:35],v[156:159],0
M_ v[188:191],a[36:39],v[156:159],0
s_nop 6
v_max3_f32 v197,v184,v185,v186
v_min3_f32 v198,v184,v185,v186
v_max3_f32 v197,v197,v187,v188
v_min3_f32 v198,v198,v187,v188
v_mov_b32 v199,v197
v_mov_b32 v202,v198
s_nop 0
v_permlane32_swap_b32 v197,v199
v_permlane32_swap_b32 v198,v202
v_max_f32 v197,v197,v199
v_min_f32 v198,v198,v202
v_mov_b32 v199,v197
v_mov_b32 v202,v198
s_nop 0
v_permlane16_swap_b32 v197,v199
v_permlane16_swap_b32 v198,v202
v_max_f32 v197,v197,v199
v_min_f32 v198,v198,v202
v_mul_f32 v197,s63,v197
v_mul_f32 v198,s63,v198
v_fma_f32 v192,-v184,s63,v198
v_fma_f32 v184,v184,s63,-v197
v_fma_f32 v193,-v185,s63,v198
v_fma_f32 v185,v185,s63,-v197
global_store_dwordx4 v223,a[196:199],s[92:93] sc1
v_fma_f32 v194,-v186,s63,v198
v_fma_f32 v186,v186,s63,-v197
v_fma_f32 v195,-v187,s63,v198
v_fma_f32 v187,v187,s63,-v197
v_fma_f32 v196,-v188,s63,v198
v_fma_f32 v188,v188,s63,-v197
E_ v184,v184
E_ v192,v192
E_ v185,v185
E_ v193,v193
E_ v186,v186
E_ v194,v194
E_ v187,v187
E_ v195,v195
E_ v188,v188
E_ v196,v196
v_add_f32 v200,v184,v185
v_add_f32 v201,v192,v193
v_add_f32 v200,v200,v186
v_add_f32 v201,v201,v194
v_add_f32 v200,v200,v187
v_add_f32 v201,v201,v195
v_add_f32 v200,v200,v188
global_store_dwordx4 v248,a[200:203],s[92:93] sc1
v_add_f32 v201,v201,v196
v_mov_b32 v199,v200
v_mov_b32 v202,v201
s_nop 0
v_permlane32_swap_b32 v200,v199
v_permlane32_swap_b32 v201,v202
v_add_f32 v200,v200,v199
v_add_f32 v201,v201,v202
v_mov_b32 v199,v200
v_mov_b32 v202,v201
s_nop 0
v_permlane16_swap_b32 v200,v199
v_permlane16_swap_b32 v201,v202
v_add_f32 v200,v200,v199
v_add_f32 v201,v201,v202
v_rcp_f32 v200,v200
v_rcp_f32 v201,v201
v_mul_f32 v184,v184,v200
v_mul_f32 v192,v192,v201
v_mul_f32 v185,v185,v200
v_mul_f32 v193,v193,v201
v_mul_f32 v186,v186,v200
v_mul_f32 v194,v194,v201
v_mul_f32 v187,v187,v200
v_mul_f32 v195,v195,v201
v_mul_f32 v188,v188,v200
global_store_dwordx4 v249,a[204:207],s[92:93] sc1
v_mul_f32 v196,v196,v201
C_ v204,v184,v185
C_ v205,v186,v187
C_ v206,v188,0
v_mov_b32 v207,0
C_ v210,v192,v193
C_ v211,v194,v195
C_ v212,v196,0
v_mov_b32 v213,0
M_ v[184:187],v[204:207],a[52:55],0
M_ v[188:191],v[204:207],a[56:59],0
M_ v[192:195],v[210:213],a[52:55],0
M_ v[196:199],v[210:213],a[56:59],0
s_nop 4
C_ v170,v184,v185
C_ v171,v186,v187
C_ v174,v188,v189
C_ v175,v190,v191
C_ v178,v192,v193
C_ v179,v194,v195
C_ v182,v196,v197
C_ v183,v198,v199
M_ a[0:3],v[168:171],v[160:163],a[0:3]
M_ a[4:7],v[168:171],v[164:167],a[4:7]
global_store_dwordx4 v222,a[208:211],s[94:95] sc1
M_ a[8:11],v[172:175],v[160:163],a[8:11]
M_ a[12:15],v[172:175],v[164:167],a[12:15]
M_ a[16:19],v[176:179],v[160:163],a[16:19]
M_ a[20:23],v[176:179],v[164:167],a[20:23]
M_ a[24:27],v[180:183],v[160:163],a[24:27]
M_ a[28:31],v[180:183],v[164:167],a[28:31]
v_mbcnt_lo_u32_b32 v210,-1,0
v_mbcnt_hi_u32_b32 v210,-1,v210
v_and_b32 v211,15,v210
v_lshrrev_b32 v212,4,v210
s_add_u32 s20,s19,1
s_lshl_b32 s20,s20,1
s_lshl_b32 s54,s18,20
s_add_u32 s12,s88,s54
s_addc_u32 s13,s89,0
s_and_b32 s13,s13,0xffff
s_mov_b32 s14,0x100000
s_mov_b32 s15,0x20000
s_add_u32 s16,s88,0x2000000
s_addc_u32 s17,s89,0
s_lshl_b32 s54,s3,13
v_add_u32 v213,0,v212
v_lshrrev_b32 v214,1,v211
v_xor_b32 v213,v213,v214
v_lshlrev_b32 v213,4,v213
v_lshl_add_u32 v213,v211,7,v213
global_store_dwordx4 v223,a[212:215],s[94:95] sc1
v_add_u32 v213,s54,v213
v_add_u32 v232,0x10000,v213
v_add_u32 v234,0x1c000,v213
v_add_u32 v236,0x0,v213
v_add_u32 v213,4,v212
v_lshrrev_b32 v214,1,v211
v_xor_b32 v213,v213,v214
v_lshlrev_b32 v213,4,v213
v_lshl_add_u32 v213,v211,7,v213
v_add_u32 v213,s54,v213
v_add_u32 v233,0x10000,v213
v_add_u32 v235,0x1c000,v213
v_add_u32 v237,0x0,v213
v_lshlrev_b32 v213,11,v212
v_lshrrev_b32 v214,2,v211
v_lshl_add_u32 v213,v214,6,v213
v_and_b32 v214,3,v210
v_lshl_add_u32 v213,v214,3,v213
v_add_u32 v213,0x8000,v213
v_and_b32 v214,1,v212
v_xor_b32 v214,0,v214
v_lshl_add_u32 v214,v214,5,v213
v_add_u32 v238,0x10000,v214
global_store_dwordx4 v248,a[216:219],s[94:95] sc1
v_add_u32 v240,0x1c000,v214
v_add_u32 v242,0x0,v214
v_and_b32 v214,1,v212
v_xor_b32 v214,1,v214
v_lshl_add_u32 v214,v214,5,v213
v_add_u32 v239,0x10000,v214
v_add_u32 v241,0x1c000,v214
v_add_u32 v243,0x0,v214
v_lshl_add_u32 v213,v212,2,0
v_cmp_le_u32 s[46:47],v213,v211
v_lshl_add_u32 v213,v212,2,1
v_cmp_le_u32 s[48:49],v213,v211
v_lshl_add_u32 v213,v212,2,2
v_cmp_le_u32 s[50:51],v213,v211
v_lshl_add_u32 v213,v212,2,3
v_cmp_le_u32 s[52:53],v213,v211
v_mov_b32 v209,0xf149f2ca
s_mov_b32 s27,0x67800000
s_getpc_b64 s[36:37]
LPC0_%=:
s_add_u32 s36,s36,LSLOW0_%=-LPC0_%=
s_addc_u32 s37,s37,0
s_getpc_b64 s[38:39]
LPC1_%=:
s_add_u32 s38,s38,LSLOW1_%=-LPC1_%=
s_addc_u32 s39,s39,0
s_getpc_b64 s[40:41]
LPC2_%=:
s_add_u32 s40,s40,LSLOW2_%=-LPC2_%=
s_addc_u32 s41,s41,0
s_getpc_b64 s[42:43]
LPC3_%=:
s_add_u32 s42,s42,LSLOW3_%=-LPC3_%=
s_addc_u32 s43,s43,0
s_cmp_lg_u32 s3,0
s_cbranch_scc1 LPWW1_%=
s_cmp_lg_u32 s19,0
s_cbranch_scc1 LPW0_0_%=
s_mov_b32 s22,0x11111111
s_mov_b32 s23,0x21111111
s_mov_b32 s79,0xf4
s_branch LPWE_%=
LPW0_0_%=:
s_cmp_lg_u32 s19,1
s_cbranch_scc1 LPW0_1_%=
s_mov_b32 s22,0x11111100
s_mov_b32 s23,0x21111111
s_mov_b32 s79,0xf4
s_branch LPWE_%=
LPW0_1_%=:
s_cmp_lg_u32 s19,2
s_cbranch_scc1 LPW0_2_%=
s_mov_b32 s22,0x11110000
s_mov_b32 s23,0x21111111
s_mov_b32 s79,0xf4
s_branch LPWE_%=
LPW0_2_%=:
s_cmp_lg_u32 s19,3
s_cbranch_scc1 LPW0_3_%=
s_mov_b32 s22,0x11000000
s_mov_b32 s23,0x21111111
s_mov_b32 s79,0xf4
s_branch LPWE_%=
LPW0_3_%=:
s_cmp_lg_u32 s19,4
s_cbranch_scc1 LPW0_4_%=
s_mov_b32 s22,0x0
s_mov_b32 s23,0x21111111
s_mov_b32 s79,0xf4
s_branch LPWE_%=
LPW0_4_%=:
s_cmp_lg_u32 s19,5
s_cbranch_scc1 LPW0_5_%=
s_mov_b32 s22,0x0
s_mov_b32 s23,0x21111100
s_mov_b32 s79,0xf4
s_branch LPWE_%=
LPW0_5_%=:
s_cmp_lg_u32 s19,6
s_cbranch_scc1 LPW0_6_%=
s_mov_b32 s22,0x0
s_mov_b32 s23,0x21110000
s_mov_b32 s79,0xf4
s_branch LPWE_%=
LPW0_6_%=:
s_mov_b32 s22,0x0
s_mov_b32 s23,0x21000000
s_mov_b32 s79,0xf4
s_branch LPWE_%=
LPWW1_%=:
s_cmp_lg_u32 s3,1
s_cbranch_scc1 LPWW2_%=
s_cmp_lg_u32 s19,0
s_cbranch_scc1 LPW1_0_%=
s_mov_b32 s22,0x11111111
s_mov_b32 s23,0x21111111
s_mov_b32 s79,0xf2
s_branch LPWE_%=
LPW1_0_%=:
s_cmp_lg_u32 s19,1
s_cbranch_scc1 LPW1_1_%=
s_mov_b32 s22,0x11111100
s_mov_b32 s23,0x21111111
s_mov_b32 s79,0xf2
s_branch LPWE_%=
LPW1_1_%=:
s_cmp_lg_u32 s19,2
s_cbranch_scc1 LPW1_2_%=
s_mov_b32 s22,0x11110000
s_mov_b32 s23,0x21111111
s_mov_b32 s79,0xf2
s_branch LPWE_%=
LPW1_2_%=:
s_cmp_lg_u32 s19,3
s_cbranch_scc1 LPW1_3_%=
s_mov_b32 s22,0x11000000
s_mov_b32 s23,0x21111111
s_mov_b32 s79,0xf2
s_branch LPWE_%=
LPW1_3_%=:
s_cmp_lg_u32 s19,4
s_cbranch_scc1 LPW1_4_%=
s_mov_b32 s22,0x0
s_mov_b32 s23,0x21111111
s_mov_b32 s79,0xf2
s_branch LPWE_%=
LPW1_4_%=:
s_cmp_lg_u32 s19,5
s_cbranch_scc1 LPW1_5_%=
s_mov_b32 s22,0x0
s_mov_b32 s23,0x21111100
s_mov_b32 s79,0xf2
s_branch LPWE_%=
LPW1_5_%=:
s_cmp_lg_u32 s19,6
s_cbranch_scc1 LPW1_6_%=
s_mov_b32 s22,0x0
s_mov_b32 s23,0x21110000
s_mov_b32 s79,0xf2
s_branch LPWE_%=
LPW1_6_%=:
s_mov_b32 s22,0x0
s_mov_b32 s23,0x21000000
s_mov_b32 s79,0xf2
s_branch LPWE_%=
LPWW2_%=:
s_cmp_lg_u32 s3,2
s_cbranch_scc1 LPWW3_%=
s_cmp_lg_u32 s19,0
s_cbranch_scc1 LPW2_0_%=
s_mov_b32 s22,0x11111153
s_mov_b32 s23,0x21111111
s_mov_b32 s79,0xf7
s_branch LPWE_%=
LPW2_0_%=:
s_cmp_lg_u32 s19,1
s_cbranch_scc1 LPW2_1_%=
s_mov_b32 s22,0x11115300
s_mov_b32 s23,0x21111111
s_mov_b32 s79,0xf7
s_branch LPWE_%=
LPW2_1_%=:
s_cmp_lg_u32 s19,2
)A"
R"A(s_cbranch_scc1 LPW2_2_%=
s_mov_b32 s22,0x11530000
s_mov_b32 s23,0x21111111
s_mov_b32 s79,0xf7
s_branch LPWE_%=
LPW2_2_%=:
s_cmp_lg_u32 s19,3
s_cbranch_scc1 LPW2_3_%=
s_mov_b32 s22,0x53000000
s_mov_b32 s23,0x21111111
s_mov_b32 s79,0xf7
s_branch LPWE_%=
LPW2_3_%=:
s_cmp_lg_u32 s19,4
s_cbranch_scc1 LPW2_4_%=
s_mov_b32 s22,0x0
s_mov_b32 s23,0x21111153
s_mov_b32 s79,0xf7
s_branch LPWE_%=
LPW2_4_%=:
s_cmp_lg_u32 s19,5
s_cbranch_scc1 LPW2_5_%=
s_mov_b32 s22,0x0
s_mov_b32 s23,0x21115300
s_mov_b32 s79,0xf7
s_branch LPWE_%=
LPW2_5_%=:
s_cmp_lg_u32 s19,6
s_cbranch_scc1 LPW2_6_%=
s_mov_b32 s22,0x0
s_mov_b32 s23,0x21530000
s_mov_b32 s79,0xf7
s_branch LPWE_%=
LPW2_6_%=:
s_mov_b32 s22,0x0
s_mov_b32 s23,0x63000000
s_mov_b32 s79,0xf7
s_branch LPWE_%=
LPWW3_%=:
s_cmp_lg_u32 s19,0
s_cbranch_scc1 LPW3_0_%=
s_mov_b32 s22,0x11111831
s_mov_b32 s23,0x11111111
s_mov_b32 s79,0xf42
s_branch LPWE_%=
LPW3_0_%=:
s_cmp_lg_u32 s19,1
s_cbranch_scc1 LPW3_1_%=
s_mov_b32 s22,0x11183100
s_mov_b32 s23,0x11111111
s_mov_b32 s79,0xf42
s_branch LPWE_%=
LPW3_1_%=:
s_cmp_lg_u32 s19,2
s_cbranch_scc1 LPW3_2_%=
s_mov_b32 s22,0x18310000
s_mov_b32 s23,0x11111111
s_mov_b32 s79,0xf42
s_branch LPWE_%=
LPW3_2_%=:
s_cmp_lg_u32 s19,3
s_cbranch_scc1 LPW3_3_%=
s_mov_b32 s22,0x31000000
s_mov_b32 s23,0x11111118
s_mov_b32 s79,0xf42
s_branch LPWE_%=
LPW3_3_%=:
s_cmp_lg_u32 s19,4
s_cbranch_scc1 LPW3_4_%=
s_mov_b32 s22,0x0
s_mov_b32 s23,0x11111831
s_mov_b32 s79,0xf42
s_branch LPWE_%=
LPW3_4_%=:
s_cmp_lg_u32 s19,5
s_cbranch_scc1 LPW3_5_%=
s_mov_b32 s22,0x0
s_mov_b32 s23,0x11183100
s_mov_b32 s79,0xf42
s_branch LPWE_%=
LPW3_5_%=:
s_cmp_lg_u32 s19,6
s_cbranch_scc1 LPW3_6_%=
s_mov_b32 s22,0x0
s_mov_b32 s23,0x18310000
s_mov_b32 s79,0xf42
s_branch LPWE_%=
LPW3_6_%=:
s_mov_b32 s22,0x0
s_mov_b32 s23,0x41000000
s_mov_b32 s79,0xf48
s_branch LPWE_%=
LPWE_%=:
global_store_dwordx4 v249,a[220:223],s[94:95] sc1
s_lshl_b32 s66,s60,3
s_lshl_b32 s67,s61,2
s_add_u32 s66,s66,s67
s_lshl_b32 s66,s66,5
s_add_u32 s66,s66,s62
s_lshl_b32 s66,s66,11
s_add_u32 s66,s66,0x4000000
s_add_u32 s64,s88,s66
s_addc_u32 s65,s89,0
v_add_u32 v214,0x0,v208
global_store_dwordx4 v214,a[160:163],s[64:65] sc1
global_store_dwordx4 v214,a[164:167],s[64:65] offset:1024 sc1
v_add_u32 v214,0x10000,v208
global_store_dwordx4 v214,a[168:171],s[64:65] sc1
global_store_dwordx4 v214,a[172:175],s[64:65] offset:1024 sc1
v_add_u32 v214,0x20000,v208
global_store_dwordx4 v214,a[176:179],s[64:65] sc1
global_store_dwordx4 v214,a[180:183],s[64:65] offset:1024 sc1
v_add_u32 v214,0x30000,v208
global_store_dwordx4 v214,a[184:187],s[64:65] sc1
global_store_dwordx4 v214,a[188:191],s[64:65] offset:1024 sc1
s_lshl_b32 s66,s62,4
s_add_u32 s66,s66,s60
s_lshl_b32 s66,s66,1
s_add_u32 s66,s66,s61
s_mul_i32 s66,s66,5120
s_add_u32 s66,s66,0x4800000
s_add_u32 s64,s88,s66
s_addc_u32 s65,s89,0
v_lshrrev_b32 v215,2,v208
global_store_dwordx4 v208,a[0:3],s[64:65] sc1
global_store_dword v215,a8,s[64:65] offset:1024 sc1
global_store_dwordx4 v208,a[4:7],s[64:65] offset:1280 sc1
global_store_dword v215,a12,s[64:65] offset:2304 sc1
s_add_u32 s64,s64,2560
s_addc_u32 s65,s65,0
global_store_dwordx4 v208,a[16:19],s[64:65] sc1
global_store_dword v215,a24,s[64:65] offset:1024 sc1
global_store_dwordx4 v208,a[20:23],s[64:65] offset:1280 sc1
global_store_dword v215,a28,s[64:65] offset:2304 sc1
s_waitcnt vmcnt(62)
s_barrier
ds_read_b64_tr_b16 a[64:65],v238
T_ a[66:67],v238,1024
ds_read_b128 a[96:99],v232
R_ a[100:103],v232,2048
R_ a[104:107],v232,4096
R_ a[108:111],v232,6144
ds_read_b64_tr_b16 a[68:69],v239
T_ a[70:71],v239,1024
T_ a[72:73],v238,256
T_ a[74:75],v238,1280
T_ a[76:77],v239,256
T_ a[78:79],v239,1280
T_ a[80:81],v238,512
T_ a[82:83],v238,1536
T_ a[84:85],v239,512
T_ a[86:87],v239,1536
T_ a[88:89],v238,768
T_ a[90:91],v238,1792
T_ a[92:93],v239,768
T_ a[94:95],v239,1792
s_waitcnt lgkmcnt(13)
M_ v[0:3],a[64:67],a[96:99],0
T_ a[112:113],v238,8192
M_ v[32:35],a[64:67],a[100:103],0
T_ a[114:115],v238,9216
M_ v[64:67],a[64:67],a[104:107],0
ds_read_b128 a[144:147],v233
M_ v[96:99],a[64:67],a[108:111],0
R_ a[148:151],v233,2048
s_waitcnt lgkmcnt(9)
M_ v[4:7],a[68:71],a[96:99],0
R_ a[152:155],v233,4096
M_ v[36:39],a[68:71],a[100:103],0
R_ a[156:159],v233,6144
M_ v[68:71],a[68:71],a[104:107],0
T_ a[116:117],v239,8192
M_ v[100:103],a[68:71],a[108:111],0
T_ a[118:119],v239,9216
M_ v[8:11],a[72:75],a[96:99],0
T_ a[120:121],v238,8448
M_ v[40:43],a[72:75],a[100:103],0
T_ a[122:123],v238,9472
M_ v[72:75],a[72:75],a[104:107],0
T_ a[124:125],v239,8448
M_ v[104:107],a[72:75],a[108:111],0
T_ a[126:127],v239,9472
M_ v[12:15],a[76:79],a[96:99],0
T_ a[128:129],v238,8704
M_ v[44:47],a[76:79],a[100:103],0
T_ a[130:131],v238,9728
M_ v[76:79],a[76:79],a[104:107],0
T_ a[132:133],v239,8704
M_ v[108:111],a[76:79],a[108:111],0
T_ a[134:135],v239,9728
M_ v[16:19],a[80:83],a[96:99],0
T_ a[136:137],v238,8960
M_ v[48:51],a[80:83],a[100:103],0
T_ a[138:139],v238,9984
M_ v[80:83],a[80:83],a[104:107],0
T_ a[140:141],v239,8960
M_ v[112:115],a[80:83],a[108:111],0
T_ a[142:143],v239,9984
s_waitcnt lgkmcnt(7)
M_ v[20:23],a[84:87],a[96:99],0
M_ v[52:55],a[84:87],a[100:103],0
M_ v[84:87],a[84:87],a[104:107],0
M_ v[116:119],a[84:87],a[108:111],0
M_ v[24:27],a[88:91],a[96:99],0
M_ v[56:59],a[88:91],a[100:103],0
M_ v[88:91],a[88:91],a[104:107],0
M_ v[120:123],a[88:91],a[108:111],0
M_ v[28:31],a[92:95],a[96:99],0
M_ v[60:63],a[92:95],a[100:103],0
M_ v[92:95],a[92:95],a[104:107],0
M_ v[124:127],a[92:95],a[108:111],0
s_waitcnt lgkmcnt(0)
s_waitcnt vmcnt(62)
s_barrier
M_ v[0:3],a[112:115],a[144:147],v[0:3]
ds_read_b64_tr_b16 a[64:65],v240
M_ v[32:35],a[112:115],a[148:151],v[32:35]
T_ a[66:67],v240,1024
M_ v[64:67],a[112:115],a[152:155],v[64:67]
ds_read_b128 a[96:99],v234
M_ v[96:99],a[112:115],a[156:159],v[96:99]
R_ a[100:103],v234,2048
M_ v[4:7],a[116:119],a[144:147],v[4:7]
R_ a[104:107],v234,4096
M_ v[36:39],a[116:119],a[148:151],v[36:39]
s_add_u32 s55,s68,0x180
R_ a[108:111],v234,6144
M_ v[68:71],a[116:119],a[152:155],v[68:71]
s_lshl_b32 s56,s3,13
ds_read_b64_tr_b16 a[68:69],v241
M_ v[100:103],a[116:119],a[156:159],v[100:103]
s_add_u32 m0,s56,0x10000
T_ a[70:71],v241,1024
M_ v[8:11],a[120:123],a[144:147],v[8:11]
T_ a[72:73],v240,256
buffer_load_dwordx4 v224,s[4:7],s55 offen lds
M_ v[40:43],a[120:123],a[148:151],v[40:43]
T_ a[74:75],v240,1280
M_ v[72:75],a[120:123],a[152:155],v[72:75]
T_ a[76:77],v241,256
M_ v[104:107],a[120:123],a[156:159],v[104:107]
T_ a[78:79],v241,1280
M_ v[12:15],a[124:127],a[144:147],v[12:15]
T_ a[80:81],v240,512
M_ v[44:47],a[124:127],a[148:151],v[44:47]
T_ a[82:83],v240,1536
buffer_load_dwordx4 v225,s[4:7],s55 offen offset:1024 lds
M_ v[76:79],a[124:127],a[152:155],v[76:79]
T_ a[84:85],v241,512
M_ v[108:111],a[124:127],a[156:159],v[108:111]
T_ a[86:87],v241,1536
M_ v[16:19],a[128:131],a[144:147],v[16:19]
T_ a[88:89],v240,768
M_ v[48:51],a[128:131],a[148:151],v[48:51]
T_ a[90:91],v240,1792
buffer_load_dwordx4 v226,s[4:7],s55 offen offset:2048 lds
M_ v[80:83],a[128:131],a[152:155],v[80:83]
T_ a[92:93],v241,768
M_ v[112:115],a[128:131],a[156:159],v[112:115]
T_ a[94:95],v241,1792
M_ v[20:23],a[132:135],a[144:147],v[20:23]
M_ v[52:55],a[132:135],a[148:151],v[52:55]
M_ v[84:87],a[132:135],a[152:155],v[84:87]
buffer_load_dwordx4 v227,s[4:7],s55 offen offset:3072 lds
M_ v[116:119],a[132:135],a[156:159],v[116:119]
s_add_u32 s55,s68,0xa180
M_ v[24:27],a[136:139],a[144:147],v[24:27]
s_lshl_b32 s56,s3,13
M_ v[56:59],a[136:139],a[148:151],v[56:59]
s_add_u32 m0,s56,0x11000
M_ v[88:91],a[136:139],a[152:155],v[88:91]
buffer_load_dwordx4 v224,s[4:7],s55 offen lds
M_ v[120:123],a[136:139],a[156:159],v[120:123]
M_ v[28:31],a[140:143],a[144:147],v[28:31]
M_ v[60:63],a[140:143],a[148:151],v[60:63]
M_ v[92:95],a[140:143],a[152:155],v[92:95]
M_ v[124:127],a[140:143],a[156:159],v[124:127]
buffer_load_dwordx4 v225,s[4:7],s55 offen offset:1024 lds
s_waitcnt lgkmcnt(0)
M_ v[0:3],a[64:67],a[96:99],v[0:3]
T_ a[112:113],v240,8192
M_ v[32:35],a[64:67],a[100:103],v[32:35]
T_ a[114:115],v240,9216
M_ v[64:67],a[64:67],a[104:107],v[64:67]
ds_read_b128 a[144:147],v235
M_ v[96:99],a[64:67],a[108:111],v[96:99]
R_ a[148:151],v235,2048
M_ v[4:7],a[68:71],a[96:99],v[4:7]
R_ a[152:155],v235,4096
M_ v[36:39],a[68:71],a[100:103],v[36:39]
s_add_u32 s55,s68,0xa180
R_ a[156:159],v235,6144
M_ v[68:71],a[68:71],a[104:107],v[68:71]
s_lshl_b32 s56,s3,13
T_ a[116:117],v241,8192
M_ v[100:103],a[68:71],a[108:111],v[100:103]
s_add_u32 m0,s56,0x11000
T_ a[118:119],v241,9216
M_ v[8:11],a[72:75],a[96:99],v[8:11]
T_ a[120:121],v240,8448
buffer_load_dwordx4 v226,s[4:7],s55 offen offset:2048 lds
M_ v[40:43],a[72:75],a[100:103],v[40:43]
T_ a[122:123],v240,9472
M_ v[72:75],a[72:75],a[104:107],v[72:75]
T_ a[124:125],v241,8448
M_ v[104:107],a[72:75],a[108:111],v[104:107]
T_ a[126:127],v241,9472
M_ v[12:15],a[76:79],a[96:99],v[12:15]
T_ a[128:129],v240,8704
M_ v[44:47],a[76:79],a[100:103],v[44:47]
T_ a[130:131],v240,9728
buffer_load_dwordx4 v227,s[4:7],s55 offen offset:3072 lds
M_ v[76:79],a[76:79],a[104:107],v[76:79]
T_ a[132:133],v241,8704
M_ v[108:111],a[76:79],a[108:111],v[108:111]
s_add_u32 s55,s69,0x180000
T_ a[134:135],v241,9728
M_ v[16:19],a[80:83],a[96:99],v[16:19]
s_add_u32 m0,s75,0x18000
T_ a[136:137],v240,8960
M_ v[48:51],a[80:83],a[100:103],v[48:51]
T_ a[138:139],v240,9984
buffer_load_dwordx4 v228,s[8:11],s55 offen lds
M_ v[80:83],a[80:83],a[104:107],v[80:83]
T_ a[140:141],v241,8960
M_ v[112:115],a[80:83],a[108:111],v[112:115]
T_ a[142:143],v241,9984
M_ v[20:23],a[84:87],a[96:99],v[20:23]
M_ v[52:55],a[84:87],a[100:103],v[52:55]
M_ v[84:87],a[84:87],a[104:107],v[84:87]
buffer_load_dwordx4 v229,s[8:11],s55 offen offset:1024 lds
M_ v[116:119],a[84:87],a[108:111],v[116:119]
M_ v[24:27],a[88:91],a[96:99],v[24:27]
M_ v[56:59],a[88:91],a[100:103],v[56:59]
M_ v[88:91],a[88:91],a[104:107],v[88:91]
buffer_load_dwordx4 v230,s[8:11],s55 offen offset:2048 lds
M_ v[120:123],a[88:91],a[108:111],v[120:123]
M_ v[28:31],a[92:95],a[96:99],v[28:31]
M_ v[60:63],a[92:95],a[100:103],v[60:63]
M_ v[92:95],a[92:95],a[104:107],v[92:95]
M_ v[124:127],a[92:95],a[108:111],v[124:127]
buffer_load_dwordx4 v231,s[8:11],s55 offen offset:3072 lds
s_waitcnt lgkmcnt(0)
s_waitcnt vmcnt(62)
s_barrier
M_ v[0:3],a[112:115],a[144:147],v[0:3]
ds_read_b64_tr_b16 a[64:65],v242
M_ v[32:35],a[112:115],a[148:151],v[32:35]
T_ a[66:67],v242,1024
M_ v[64:67],a[112:115],a[152:155],v[64:67]
ds_read_b128 a[96:99],v236
M_ v[96:99],a[112:115],a[156:159],v[96:99]
R_ a[100:103],v236,2048
M_ v[4:7],a[116:119],a[144:147],v[4:7]
R_ a[104:107],v236,4096
M_ v[36:39],a[116:119],a[148:151],v[36:39]
s_add_u32 s55,s68,0x200
R_ a[108:111],v236,6144
M_ v[68:71],a[116:119],a[152:155],v[68:71]
s_lshl_b32 s56,s3,13
ds_read_b64_tr_b16 a[68:69],v243
M_ v[100:103],a[116:119],a[156:159],v[100:103]
s_add_u32 m0,s56,0x1c000
T_ a[70:71],v243,1024
M_ v[8:11],a[120:123],a[144:147],v[8:11]
T_ a[72:73],v242,256
buffer_load_dwordx4 v224,s[4:7],s55 offen lds
M_ v[40:43],a[120:123],a[148:151],v[40:43]
T_ a[74:75],v242,1280
M_ v[72:75],a[120:123],a[152:155],v[72:75]
T_ a[76:77],v243,256
M_ v[104:107],a[120:123],a[156:159],v[104:107]
T_ a[78:79],v243,1280
M_ v[12:15],a[124:127],a[144:147],v[12:15]
T_ a[80:81],v242,512
M_ v[44:47],a[124:127],a[148:151],v[44:47]
T_ a[82:83],v242,1536
buffer_load_dwordx4 v225,s[4:7],s55 offen offset:1024 lds
M_ v[76:79],a[124:127],a[152:155],v[76:79]
T_ a[84:85],v243,512
M_ v[108:111],a[124:127],a[156:159],v[108:111]
T_ a[86:87],v243,1536
M_ v[16:19],a[128:131],a[144:147],v[16:19]
T_ a[88:89],v242,768
M_ v[48:51],a[128:131],a[148:151],v[48:51]
T_ a[90:91],v242,1792
buffer_load_dwordx4 v226,s[4:7],s55 offen offset:2048 lds
M_ v[80:83],a[128:131],a[152:155],v[80:83]
T_ a[92:93],v243,768
M_ v[112:115],a[128:131],a[156:159],v[112:115]
T_ a[94:95],v243,1792
M_ v[20:23],a[132:135],a[144:147],v[20:23]
M_ v[52:55],a[132:135],a[148:151],v[52:55]
M_ v[84:87],a[132:135],a[152:155],v[84:87]
buffer_load_dwordx4 v227,s[4:7],s55 offen offset:3072 lds
M_ v[116:119],a[132:135],a[156:159],v[116:119]
s_add_u32 s55,s68,0xa200
M_ v[24:27],a[136:139],a[144:147],v[24:27]
s_lshl_b32 s56,s3,13
M_ v[56:59],a[136:139],a[148:151],v[56:59]
s_add_u32 m0,s56,0x1d000
M_ v[88:91],a[136:139],a[152:155],v[88:91]
buffer_load_dwordx4 v224,s[4:7],s55 offen lds
M_ v[120:123],a[136:139],a[156:159],v[120:123]
M_ v[28:31],a[140:143],a[144:147],v[28:31]
M_ v[60:63],a[140:143],a[148:151],v[60:63]
M_ v[92:95],a[140:143],a[152:155],v[92:95]
M_ v[124:127],a[140:143],a[156:159],v[124:127]
buffer_load_dwordx4 v225,s[4:7],s55 offen offset:1024 lds
s_waitcnt lgkmcnt(0)
M_ v[0:3],a[64:67],a[96:99],v[0:3]
T_ a[112:113],v242,8192
M_ v[32:35],a[64:67],a[100:103],v[32:35]
T_ a[114:115],v242,9216
M_ v[64:67],a[64:67],a[104:107],v[64:67]
ds_read_b128 a[144:147],v237
M_ v[96:99],a[64:67],a[108:111],v[96:99]
R_ a[148:151],v237,2048
M_ v[4:7],a[68:71],a[96:99],v[4:7]
R_ a[152:155],v237,4096
M_ v[36:39],a[68:71],a[100:103],v[36:39]
s_add_u32 s55,s68,0xa200
R_ a[156:159],v237,6144
M_ v[68:71],a[68:71],a[104:107],v[68:71]
s_lshl_b32 s56,s3,13
T_ a[116:117],v243,8192
M_ v[100:103],a[68:71],a[108:111],v[100:103]
s_add_u32 m0,s56,0x1d000
T_ a[118:119],v243,9216
M_ v[8:11],a[72:75],a[96:99],v[8:11]
T_ a[120:121],v242,8448
buffer_load_dwordx4 v226,s[4:7],s55 offen offset:2048 lds
M_ v[40:43],a[72:75],a[100:103],v[40:43]
T_ a[122:123],v242,9472
M_ v[72:75],a[72:75],a[104:107],v[72:75]
T_ a[124:125],v243,8448
M_ v[104:107],a[72:75],a[108:111],v[104:107]
T_ a[126:127],v243,9472
M_ v[12:15],a[76:79],a[96:99],v[12:15]
T_ a[128:129],v242,8704
M_ v[44:47],a[76:79],a[100:103],v[44:47]
T_ a[130:131],v242,9728
buffer_load_dwordx4 v227,s[4:7],s55 offen offset:3072 lds
M_ v[76:79],a[76:79],a[104:107],v[76:79]
T_ a[132:133],v243,8704
M_ v[108:111],a[76:79],a[108:111],v[108:111]
s_add_u32 s55,s69,0x200000
T_ a[134:135],v243,9728
M_ v[16:19],a[80:83],a[96:99],v[16:19]
s_add_u32 m0,s75,0x24000
T_ a[136:137],v242,8960
M_ v[48:51],a[80:83],a[100:103],v[48:51]
T_ a[138:139],v242,9984
buffer_load_dwordx4 v228,s[8:11],s55 offen lds
M_ v[80:83],a[80:83],a[104:107],v[80:83]
T_ a[140:141],v243,8960
)A"
R"A(M_ v[112:115],a[80:83],a[108:111],v[112:115]
T_ a[142:143],v243,9984
M_ v[20:23],a[84:87],a[96:99],v[20:23]
M_ v[52:55],a[84:87],a[100:103],v[52:55]
M_ v[84:87],a[84:87],a[104:107],v[84:87]
buffer_load_dwordx4 v229,s[8:11],s55 offen offset:1024 lds
M_ v[116:119],a[84:87],a[108:111],v[116:119]
M_ v[24:27],a[88:91],a[96:99],v[24:27]
M_ v[56:59],a[88:91],a[100:103],v[56:59]
M_ v[88:91],a[88:91],a[104:107],v[88:91]
buffer_load_dwordx4 v230,s[8:11],s55 offen offset:2048 lds
M_ v[120:123],a[88:91],a[108:111],v[120:123]
M_ v[28:31],a[92:95],a[96:99],v[28:31]
M_ v[60:63],a[92:95],a[100:103],v[60:63]
M_ v[92:95],a[92:95],a[104:107],v[92:95]
M_ v[124:127],a[92:95],a[108:111],v[124:127]
buffer_load_dwordx4 v231,s[8:11],s55 offen offset:3072 lds
s_waitcnt lgkmcnt(0)
s_waitcnt vmcnt(12)
s_barrier
s_and_b32 s90,s2,7
s_lshr_b32 s91,s2,3
s_lshr_b32 s94,s91,3
s_lshl_b32 s94,s94,3
s_add_u32 s90,s90,s94
s_lshl_b32 s90,s90,3
s_and_b32 s91,s91,7
s_add_u32 s90,s90,s91
s_cmp_lg_u32 s3,0
s_cbranch_scc1 LPB41_%=
s_lshl_b32 s54,s90,2
s_add_u32 s54,s54,0x6400400
v_mov_b32 v246,s54
v_mov_b32 v247,s86
s_mov_b64 exec,1
global_store_dword v246,v247,s[88:89] sc1
s_mov_b64 exec,-1
LPB41_%=:
s_cmp_lg_u32 s3,0
s_cbranch_scc1 LPB42_%=
s_lshl_b32 s54,s2,2
s_add_u32 s54,s54,0x6400800
v_mov_b32 v246,s54
v_mov_b32 v247,s86
s_mov_b64 exec,1
global_store_dword v246,v247,s[88:89] sc1
s_mov_b64 exec,-1
LPB42_%=:
M_ v[0:3],a[112:115],a[144:147],v[0:3]
ds_read_b64_tr_b16 a[64:65],v238
M_ v[32:35],a[112:115],a[148:151],v[32:35]
T_ a[66:67],v238,1024
M_ v[64:67],a[112:115],a[152:155],v[64:67]
ds_read_b128 a[96:99],v232
M_ v[96:99],a[112:115],a[156:159],v[96:99]
R_ a[100:103],v232,2048
M_ v[4:7],a[116:119],a[144:147],v[4:7]
R_ a[104:107],v232,4096
M_ v[36:39],a[116:119],a[148:151],v[36:39]
s_add_u32 s55,s68,0x280
R_ a[108:111],v232,6144
M_ v[68:71],a[116:119],a[152:155],v[68:71]
s_lshl_b32 s56,s3,13
ds_read_b64_tr_b16 a[68:69],v239
M_ v[100:103],a[116:119],a[156:159],v[100:103]
s_add_u32 m0,s56,0x0
T_ a[70:71],v239,1024
M_ v[8:11],a[120:123],a[144:147],v[8:11]
T_ a[72:73],v238,256
buffer_load_dwordx4 v224,s[4:7],s55 offen lds
M_ v[40:43],a[120:123],a[148:151],v[40:43]
T_ a[74:75],v238,1280
M_ v[72:75],a[120:123],a[152:155],v[72:75]
T_ a[76:77],v239,256
M_ v[104:107],a[120:123],a[156:159],v[104:107]
T_ a[78:79],v239,1280
M_ v[12:15],a[124:127],a[144:147],v[12:15]
T_ a[80:81],v238,512
M_ v[44:47],a[124:127],a[148:151],v[44:47]
T_ a[82:83],v238,1536
buffer_load_dwordx4 v225,s[4:7],s55 offen offset:1024 lds
M_ v[76:79],a[124:127],a[152:155],v[76:79]
T_ a[84:85],v239,512
M_ v[108:111],a[124:127],a[156:159],v[108:111]
T_ a[86:87],v239,1536
M_ v[16:19],a[128:131],a[144:147],v[16:19]
T_ a[88:89],v238,768
M_ v[48:51],a[128:131],a[148:151],v[48:51]
T_ a[90:91],v238,1792
buffer_load_dwordx4 v226,s[4:7],s55 offen offset:2048 lds
M_ v[80:83],a[128:131],a[152:155],v[80:83]
T_ a[92:93],v239,768
M_ v[112:115],a[128:131],a[156:159],v[112:115]
T_ a[94:95],v239,1792
M_ v[20:23],a[132:135],a[144:147],v[20:23]
M_ v[52:55],a[132:135],a[148:151],v[52:55]
M_ v[84:87],a[132:135],a[152:155],v[84:87]
buffer_load_dwordx4 v227,s[4:7],s55 offen offset:3072 lds
M_ v[116:119],a[132:135],a[156:159],v[116:119]
s_add_u32 s55,s68,0xa280
M_ v[24:27],a[136:139],a[144:147],v[24:27]
s_lshl_b32 s56,s3,13
M_ v[56:59],a[136:139],a[148:151],v[56:59]
s_add_u32 m0,s56,0x1000
M_ v[88:91],a[136:139],a[152:155],v[88:91]
buffer_load_dwordx4 v224,s[4:7],s55 offen lds
M_ v[120:123],a[136:139],a[156:159],v[120:123]
M_ v[28:31],a[140:143],a[144:147],v[28:31]
M_ v[60:63],a[140:143],a[148:151],v[60:63]
M_ v[92:95],a[140:143],a[152:155],v[92:95]
M_ v[124:127],a[140:143],a[156:159],v[124:127]
buffer_load_dwordx4 v225,s[4:7],s55 offen offset:1024 lds
s_waitcnt lgkmcnt(0)
M_ v[0:3],a[64:67],a[96:99],v[0:3]
T_ a[112:113],v238,8192
M_ v[32:35],a[64:67],a[100:103],v[32:35]
T_ a[114:115],v238,9216
M_ v[64:67],a[64:67],a[104:107],v[64:67]
ds_read_b128 a[144:147],v233
M_ v[96:99],a[64:67],a[108:111],v[96:99]
R_ a[148:151],v233,2048
M_ v[4:7],a[68:71],a[96:99],v[4:7]
R_ a[152:155],v233,4096
M_ v[36:39],a[68:71],a[100:103],v[36:39]
s_add_u32 s55,s68,0xa280
R_ a[156:159],v233,6144
M_ v[68:71],a[68:71],a[104:107],v[68:71]
s_lshl_b32 s56,s3,13
T_ a[116:117],v239,8192
M_ v[100:103],a[68:71],a[108:111],v[100:103]
s_add_u32 m0,s56,0x1000
T_ a[118:119],v239,9216
M_ v[8:11],a[72:75],a[96:99],v[8:11]
T_ a[120:121],v238,8448
buffer_load_dwordx4 v226,s[4:7],s55 offen offset:2048 lds
M_ v[40:43],a[72:75],a[100:103],v[40:43]
T_ a[122:123],v238,9472
M_ v[72:75],a[72:75],a[104:107],v[72:75]
T_ a[124:125],v239,8448
M_ v[104:107],a[72:75],a[108:111],v[104:107]
T_ a[126:127],v239,9472
M_ v[12:15],a[76:79],a[96:99],v[12:15]
T_ a[128:129],v238,8704
M_ v[44:47],a[76:79],a[100:103],v[44:47]
T_ a[130:131],v238,9728
buffer_load_dwordx4 v227,s[4:7],s55 offen offset:3072 lds
M_ v[76:79],a[76:79],a[104:107],v[76:79]
T_ a[132:133],v239,8704
M_ v[108:111],a[76:79],a[108:111],v[108:111]
s_add_u32 s55,s69,0x280000
T_ a[134:135],v239,9728
M_ v[16:19],a[80:83],a[96:99],v[16:19]
s_add_u32 m0,s75,0x8000
T_ a[136:137],v238,8960
M_ v[48:51],a[80:83],a[100:103],v[48:51]
T_ a[138:139],v238,9984
buffer_load_dwordx4 v228,s[8:11],s55 offen lds
M_ v[80:83],a[80:83],a[104:107],v[80:83]
T_ a[140:141],v239,8960
M_ v[112:115],a[80:83],a[108:111],v[112:115]
T_ a[142:143],v239,9984
M_ v[20:23],a[84:87],a[96:99],v[20:23]
M_ v[52:55],a[84:87],a[100:103],v[52:55]
M_ v[84:87],a[84:87],a[104:107],v[84:87]
buffer_load_dwordx4 v229,s[8:11],s55 offen offset:1024 lds
M_ v[116:119],a[84:87],a[108:111],v[116:119]
M_ v[24:27],a[88:91],a[96:99],v[24:27]
M_ v[56:59],a[88:91],a[100:103],v[56:59]
M_ v[88:91],a[88:91],a[104:107],v[88:91]
buffer_load_dwordx4 v230,s[8:11],s55 offen offset:2048 lds
M_ v[120:123],a[88:91],a[108:111],v[120:123]
M_ v[28:31],a[92:95],a[96:99],v[28:31]
M_ v[60:63],a[92:95],a[100:103],v[60:63]
M_ v[92:95],a[92:95],a[104:107],v[92:95]
M_ v[124:127],a[92:95],a[108:111],v[124:127]
buffer_load_dwordx4 v231,s[8:11],s55 offen offset:3072 lds
s_waitcnt lgkmcnt(0)
s_waitcnt vmcnt(12)
s_barrier
M_ v[0:3],a[112:115],a[144:147],v[0:3]
ds_read_b64_tr_b16 a[64:65],v240
M_ v[32:35],a[112:115],a[148:151],v[32:35]
T_ a[66:67],v240,1024
M_ v[64:67],a[112:115],a[152:155],v[64:67]
ds_read_b128 a[96:99],v234
M_ v[96:99],a[112:115],a[156:159],v[96:99]
R_ a[100:103],v234,2048
M_ v[4:7],a[116:119],a[144:147],v[4:7]
R_ a[104:107],v234,4096
M_ v[36:39],a[116:119],a[148:151],v[36:39]
s_add_u32 s55,s68,0x300
R_ a[108:111],v234,6144
M_ v[68:71],a[116:119],a[152:155],v[68:71]
s_lshl_b32 s56,s3,13
ds_read_b64_tr_b16 a[68:69],v241
M_ v[100:103],a[116:119],a[156:159],v[100:103]
s_add_u32 m0,s56,0x10000
T_ a[70:71],v241,1024
M_ v[8:11],a[120:123],a[144:147],v[8:11]
T_ a[72:73],v240,256
buffer_load_dwordx4 v224,s[4:7],s55 offen lds
M_ v[40:43],a[120:123],a[148:151],v[40:43]
T_ a[74:75],v240,1280
M_ v[72:75],a[120:123],a[152:155],v[72:75]
T_ a[76:77],v241,256
M_ v[104:107],a[120:123],a[156:159],v[104:107]
T_ a[78:79],v241,1280
M_ v[12:15],a[124:127],a[144:147],v[12:15]
T_ a[80:81],v240,512
M_ v[44:47],a[124:127],a[148:151],v[44:47]
T_ a[82:83],v240,1536
buffer_load_dwordx4 v225,s[4:7],s55 offen offset:1024 lds
M_ v[76:79],a[124:127],a[152:155],v[76:79]
T_ a[84:85],v241,512
M_ v[108:111],a[124:127],a[156:159],v[108:111]
T_ a[86:87],v241,1536
M_ v[16:19],a[128:131],a[144:147],v[16:19]
T_ a[88:89],v240,768
M_ v[48:51],a[128:131],a[148:151],v[48:51]
T_ a[90:91],v240,1792
buffer_load_dwordx4 v226,s[4:7],s55 offen offset:2048 lds
M_ v[80:83],a[128:131],a[152:155],v[80:83]
T_ a[92:93],v241,768
M_ v[112:115],a[128:131],a[156:159],v[112:115]
T_ a[94:95],v241,1792
M_ v[20:23],a[132:135],a[144:147],v[20:23]
M_ v[52:55],a[132:135],a[148:151],v[52:55]
M_ v[84:87],a[132:135],a[152:155],v[84:87]
buffer_load_dwordx4 v227,s[4:7],s55 offen offset:3072 lds
M_ v[116:119],a[132:135],a[156:159],v[116:119]
s_add_u32 s55,s68,0xa300
M_ v[24:27],a[136:139],a[144:147],v[24:27]
s_lshl_b32 s56,s3,13
M_ v[56:59],a[136:139],a[148:151],v[56:59]
s_add_u32 m0,s56,0x11000
M_ v[88:91],a[136:139],a[152:155],v[88:91]
buffer_load_dwordx4 v224,s[4:7],s55 offen lds
M_ v[120:123],a[136:139],a[156:159],v[120:123]
M_ v[28:31],a[140:143],a[144:147],v[28:31]
M_ v[60:63],a[140:143],a[148:151],v[60:63]
M_ v[92:95],a[140:143],a[152:155],v[92:95]
M_ v[124:127],a[140:143],a[156:159],v[124:127]
buffer_load_dwordx4 v225,s[4:7],s55 offen offset:1024 lds
s_waitcnt lgkmcnt(0)
M_ v[0:3],a[64:67],a[96:99],v[0:3]
T_ a[112:113],v240,8192
M_ v[32:35],a[64:67],a[100:103],v[32:35]
T_ a[114:115],v240,9216
M_ v[64:67],a[64:67],a[104:107],v[64:67]
ds_read_b128 a[144:147],v235
M_ v[96:99],a[64:67],a[108:111],v[96:99]
R_ a[148:151],v235,2048
M_ v[4:7],a[68:71],a[96:99],v[4:7]
R_ a[152:155],v235,4096
M_ v[36:39],a[68:71],a[100:103],v[36:39]
s_add_u32 s55,s68,0xa300
R_ a[156:159],v235,6144
M_ v[68:71],a[68:71],a[104:107],v[68:71]
s_lshl_b32 s56,s3,13
T_ a[116:117],v241,8192
M_ v[100:103],a[68:71],a[108:111],v[100:103]
s_add_u32 m0,s56,0x11000
T_ a[118:119],v241,9216
M_ v[8:11],a[72:75],a[96:99],v[8:11]
T_ a[120:121],v240,8448
buffer_load_dwordx4 v226,s[4:7],s55 offen offset:2048 lds
M_ v[40:43],a[72:75],a[100:103],v[40:43]
T_ a[122:123],v240,9472
M_ v[72:75],a[72:75],a[104:107],v[72:75]
T_ a[124:125],v241,8448
M_ v[104:107],a[72:75],a[108:111],v[104:107]
T_ a[126:127],v241,9472
M_ v[12:15],a[76:79],a[96:99],v[12:15]
T_ a[128:129],v240,8704
M_ v[44:47],a[76:79],a[100:103],v[44:47]
T_ a[130:131],v240,9728
buffer_load_dwordx4 v227,s[4:7],s55 offen offset:3072 lds
M_ v[76:79],a[76:79],a[104:107],v[76:79]
T_ a[132:133],v241,8704
M_ v[108:111],a[76:79],a[108:111],v[108:111]
s_add_u32 s55,s69,0x300000
T_ a[134:135],v241,9728
M_ v[16:19],a[80:83],a[96:99],v[16:19]
s_add_u32 m0,s75,0x18000
T_ a[136:137],v240,8960
M_ v[48:51],a[80:83],a[100:103],v[48:51]
T_ a[138:139],v240,9984
buffer_load_dwordx4 v228,s[8:11],s55 offen lds
M_ v[80:83],a[80:83],a[104:107],v[80:83]
T_ a[140:141],v241,8960
M_ v[112:115],a[80:83],a[108:111],v[112:115]
T_ a[142:143],v241,9984
M_ v[20:23],a[84:87],a[96:99],v[20:23]
M_ v[52:55],a[84:87],a[100:103],v[52:55]
M_ v[84:87],a[84:87],a[104:107],v[84:87]
buffer_load_dwordx4 v229,s[8:11],s55 offen offset:1024 lds
M_ v[116:119],a[84:87],a[108:111],v[116:119]
M_ v[24:27],a[88:91],a[96:99],v[24:27]
M_ v[56:59],a[88:91],a[100:103],v[56:59]
M_ v[88:91],a[88:91],a[104:107],v[88:91]
buffer_load_dwordx4 v230,s[8:11],s55 offen offset:2048 lds
M_ v[120:123],a[88:91],a[108:111],v[120:123]
M_ v[28:31],a[92:95],a[96:99],v[28:31]
M_ v[60:63],a[92:95],a[100:103],v[60:63]
M_ v[92:95],a[92:95],a[104:107],v[92:95]
M_ v[124:127],a[92:95],a[108:111],v[124:127]
buffer_load_dwordx4 v231,s[8:11],s55 offen offset:3072 lds
s_waitcnt lgkmcnt(0)
s_waitcnt vmcnt(12)
s_barrier
M_ v[0:3],a[112:115],a[144:147],v[0:3]
ds_read_b64_tr_b16 a[64:65],v242
M_ v[32:35],a[112:115],a[148:151],v[32:35]
T_ a[66:67],v242,1024
M_ v[64:67],a[112:115],a[152:155],v[64:67]
ds_read_b128 a[96:99],v236
M_ v[96:99],a[112:115],a[156:159],v[96:99]
R_ a[100:103],v236,2048
M_ v[4:7],a[116:119],a[144:147],v[4:7]
R_ a[104:107],v236,4096
M_ v[36:39],a[116:119],a[148:151],v[36:39]
s_add_u32 s55,s68,0x380
R_ a[108:111],v236,6144
M_ v[68:71],a[116:119],a[152:155],v[68:71]
s_lshl_b32 s56,s3,13
ds_read_b64_tr_b16 a[68:69],v243
M_ v[100:103],a[116:119],a[156:159],v[100:103]
s_add_u32 m0,s56,0x1c000
T_ a[70:71],v243,1024
M_ v[8:11],a[120:123],a[144:147],v[8:11]
T_ a[72:73],v242,256
buffer_load_dwordx4 v224,s[4:7],s55 offen lds
M_ v[40:43],a[120:123],a[148:151],v[40:43]
T_ a[74:75],v242,1280
M_ v[72:75],a[120:123],a[152:155],v[72:75]
T_ a[76:77],v243,256
M_ v[104:107],a[120:123],a[156:159],v[104:107]
T_ a[78:79],v243,1280
M_ v[12:15],a[124:127],a[144:147],v[12:15]
T_ a[80:81],v242,512
M_ v[44:47],a[124:127],a[148:151],v[44:47]
T_ a[82:83],v242,1536
buffer_load_dwordx4 v225,s[4:7],s55 offen offset:1024 lds
M_ v[76:79],a[124:127],a[152:155],v[76:79]
T_ a[84:85],v243,512
M_ v[108:111],a[124:127],a[156:159],v[108:111]
T_ a[86:87],v243,1536
M_ v[16:19],a[128:131],a[144:147],v[16:19]
T_ a[88:89],v242,768
M_ v[48:51],a[128:131],a[148:151],v[48:51]
T_ a[90:91],v242,1792
buffer_load_dwordx4 v226,s[4:7],s55 offen offset:2048 lds
M_ v[80:83],a[128:131],a[152:155],v[80:83]
T_ a[92:93],v243,768
M_ v[112:115],a[128:131],a[156:159],v[112:115]
T_ a[94:95],v243,1792
M_ v[20:23],a[132:135],a[144:147],v[20:23]
M_ v[52:55],a[132:135],a[148:151],v[52:55]
M_ v[84:87],a[132:135],a[152:155],v[84:87]
buffer_load_dwordx4 v227,s[4:7],s55 offen offset:3072 lds
M_ v[116:119],a[132:135],a[156:159],v[116:119]
s_add_u32 s55,s68,0xa380
M_ v[24:27],a[136:139],a[144:147],v[24:27]
s_lshl_b32 s56,s3,13
M_ v[56:59],a[136:139],a[148:151],v[56:59]
s_add_u32 m0,s56,0x1d000
M_ v[88:91],a[136:139],a[152:155],v[88:91]
buffer_load_dwordx4 v224,s[4:7],s55 offen lds
M_ v[120:123],a[136:139],a[156:159],v[120:123]
M_ v[28:31],a[140:143],a[144:147],v[28:31]
M_ v[60:63],a[140:143],a[148:151],v[60:63]
M_ v[92:95],a[140:143],a[152:155],v[92:95]
M_ v[124:127],a[140:143],a[156:159],v[124:127]
buffer_load_dwordx4 v225,s[4:7],s55 offen offset:1024 lds
s_waitcnt lgkmcnt(0)
M_ v[0:3],a[64:67],a[96:99],v[0:3]
T_ a[112:113],v242,8192
M_ v[32:35],a[64:67],a[100:103],v[32:35]
T_ a[114:115],v242,9216
M_ v[64:67],a[64:67],a[104:107],v[64:67]
ds_read_b128 a[144:147],v237
M_ v[96:99],a[64:67],a[108:111],v[96:99]
R_ a[148:151],v237,2048
M_ v[4:7],a[68:71],a[96:99],v[4:7]
R_ a[152:155],v237,4096
M_ v[36:39],a[68:71],a[100:103],v[36:39]
s_add_u32 s55,s68,0xa380
R_ a[156:159],v237,6144
M_ v[68:71],a[68:71],a[104:107],v[68:71]
s_lshl_b32 s56,s3,13
T_ a[116:117],v243,8192
M_ v[100:103],a[68:71],a[108:111],v[100:103]
s_add_u32 m0,s56,0x1d000
T_ a[118:119],v243,9216
M_ v[8:11],a[72:75],a[96:99],v[8:11]
T_ a[120:121],v242,8448
buffer_load_dwordx4 v226,s[4:7],s55 offen offset:2048 lds
M_ v[40:43],a[72:75],a[100:103],v[40:43]
T_ a[122:123],v242,9472
M_ v[72:75],a[72:75],a[104:107],v[72:75]
T_ a[124:125],v243,8448
M_ v[104:107],a[72:75],a[108:111],v[104:107]
T_ a[126:127],v243,9472
M_ v[12:15],a[76:79],a[96:99],v[12:15]
)A"
R"A(T_ a[128:129],v242,8704
M_ v[44:47],a[76:79],a[100:103],v[44:47]
T_ a[130:131],v242,9728
buffer_load_dwordx4 v227,s[4:7],s55 offen offset:3072 lds
M_ v[76:79],a[76:79],a[104:107],v[76:79]
T_ a[132:133],v243,8704
M_ v[108:111],a[76:79],a[108:111],v[108:111]
s_add_u32 s55,s69,0x380000
T_ a[134:135],v243,9728
M_ v[16:19],a[80:83],a[96:99],v[16:19]
s_add_u32 m0,s75,0x24000
T_ a[136:137],v242,8960
M_ v[48:51],a[80:83],a[100:103],v[48:51]
T_ a[138:139],v242,9984
buffer_load_dwordx4 v228,s[8:11],s55 offen lds
M_ v[80:83],a[80:83],a[104:107],v[80:83]
T_ a[140:141],v243,8960
M_ v[112:115],a[80:83],a[108:111],v[112:115]
T_ a[142:143],v243,9984
M_ v[20:23],a[84:87],a[96:99],v[20:23]
M_ v[52:55],a[84:87],a[100:103],v[52:55]
M_ v[84:87],a[84:87],a[104:107],v[84:87]
buffer_load_dwordx4 v229,s[8:11],s55 offen offset:1024 lds
M_ v[116:119],a[84:87],a[108:111],v[116:119]
M_ v[24:27],a[88:91],a[96:99],v[24:27]
M_ v[56:59],a[88:91],a[100:103],v[56:59]
M_ v[88:91],a[88:91],a[104:107],v[88:91]
buffer_load_dwordx4 v230,s[8:11],s55 offen offset:2048 lds
M_ v[120:123],a[88:91],a[108:111],v[120:123]
M_ v[28:31],a[92:95],a[96:99],v[28:31]
M_ v[60:63],a[92:95],a[100:103],v[60:63]
M_ v[92:95],a[92:95],a[104:107],v[92:95]
M_ v[124:127],a[92:95],a[108:111],v[124:127]
buffer_load_dwordx4 v231,s[8:11],s55 offen offset:3072 lds
s_lshl_b32 s58,s18,3
v_mbcnt_lo_u32_b32 v216,-1,0
v_mbcnt_hi_u32_b32 v216,-1,v216
v_min_u32 v216,7,v216
v_add_u32 v216,s58,v216
v_lshlrev_b32 v216,2,v216
v_add_u32 v216,0x6400400,v216
global_load_dword v217,v216,s[88:89] sc1
v_mbcnt_lo_u32_b32 v248,-1,0
v_mbcnt_hi_u32_b32 v248,-1,v248
v_lshlrev_b32 v248,4,v248
v_add_u32 v248,0x6400800,v248
global_load_dwordx4 v[244:247],v248,s[88:89] sc1
s_waitcnt lgkmcnt(0)
s_waitcnt vmcnt(14)
s_barrier
M_ v[0:3],a[112:115],a[144:147],v[0:3]
ds_read_b64_tr_b16 a[64:65],v238
M_ v[32:35],a[112:115],a[148:151],v[32:35]
T_ a[66:67],v238,1024
M_ v[64:67],a[112:115],a[152:155],v[64:67]
ds_read_b128 a[96:99],v232
M_ v[96:99],a[112:115],a[156:159],v[96:99]
R_ a[100:103],v232,2048
M_ v[4:7],a[116:119],a[144:147],v[4:7]
R_ a[104:107],v232,4096
M_ v[36:39],a[116:119],a[148:151],v[36:39]
s_add_u32 s55,s68,0x400
R_ a[108:111],v232,6144
M_ v[68:71],a[116:119],a[152:155],v[68:71]
s_lshl_b32 s56,s3,13
ds_read_b64_tr_b16 a[68:69],v239
M_ v[100:103],a[116:119],a[156:159],v[100:103]
s_add_u32 m0,s56,0x0
T_ a[70:71],v239,1024
M_ v[8:11],a[120:123],a[144:147],v[8:11]
T_ a[72:73],v238,256
buffer_load_dwordx4 v224,s[4:7],s55 offen lds
M_ v[40:43],a[120:123],a[148:151],v[40:43]
T_ a[74:75],v238,1280
M_ v[72:75],a[120:123],a[152:155],v[72:75]
T_ a[76:77],v239,256
M_ v[104:107],a[120:123],a[156:159],v[104:107]
T_ a[78:79],v239,1280
M_ v[12:15],a[124:127],a[144:147],v[12:15]
T_ a[80:81],v238,512
M_ v[44:47],a[124:127],a[148:151],v[44:47]
T_ a[82:83],v238,1536
buffer_load_dwordx4 v225,s[4:7],s55 offen offset:1024 lds
M_ v[76:79],a[124:127],a[152:155],v[76:79]
T_ a[84:85],v239,512
M_ v[108:111],a[124:127],a[156:159],v[108:111]
T_ a[86:87],v239,1536
M_ v[16:19],a[128:131],a[144:147],v[16:19]
T_ a[88:89],v238,768
M_ v[48:51],a[128:131],a[148:151],v[48:51]
T_ a[90:91],v238,1792
buffer_load_dwordx4 v226,s[4:7],s55 offen offset:2048 lds
M_ v[80:83],a[128:131],a[152:155],v[80:83]
T_ a[92:93],v239,768
M_ v[112:115],a[128:131],a[156:159],v[112:115]
T_ a[94:95],v239,1792
M_ v[20:23],a[132:135],a[144:147],v[20:23]
M_ v[52:55],a[132:135],a[148:151],v[52:55]
M_ v[84:87],a[132:135],a[152:155],v[84:87]
buffer_load_dwordx4 v227,s[4:7],s55 offen offset:3072 lds
M_ v[116:119],a[132:135],a[156:159],v[116:119]
s_add_u32 s55,s68,0xa400
M_ v[24:27],a[136:139],a[144:147],v[24:27]
s_lshl_b32 s56,s3,13
M_ v[56:59],a[136:139],a[148:151],v[56:59]
s_add_u32 m0,s56,0x1000
M_ v[88:91],a[136:139],a[152:155],v[88:91]
buffer_load_dwordx4 v224,s[4:7],s55 offen lds
M_ v[120:123],a[136:139],a[156:159],v[120:123]
M_ v[28:31],a[140:143],a[144:147],v[28:31]
M_ v[60:63],a[140:143],a[148:151],v[60:63]
M_ v[92:95],a[140:143],a[152:155],v[92:95]
M_ v[124:127],a[140:143],a[156:159],v[124:127]
buffer_load_dwordx4 v225,s[4:7],s55 offen offset:1024 lds
s_waitcnt lgkmcnt(0)
M_ v[0:3],a[64:67],a[96:99],v[0:3]
T_ a[112:113],v238,8192
M_ v[32:35],a[64:67],a[100:103],v[32:35]
T_ a[114:115],v238,9216
M_ v[64:67],a[64:67],a[104:107],v[64:67]
ds_read_b128 a[144:147],v233
M_ v[96:99],a[64:67],a[108:111],v[96:99]
R_ a[148:151],v233,2048
M_ v[4:7],a[68:71],a[96:99],v[4:7]
R_ a[152:155],v233,4096
M_ v[36:39],a[68:71],a[100:103],v[36:39]
s_add_u32 s55,s68,0xa400
R_ a[156:159],v233,6144
M_ v[68:71],a[68:71],a[104:107],v[68:71]
s_lshl_b32 s56,s3,13
T_ a[116:117],v239,8192
M_ v[100:103],a[68:71],a[108:111],v[100:103]
s_add_u32 m0,s56,0x1000
T_ a[118:119],v239,9216
M_ v[8:11],a[72:75],a[96:99],v[8:11]
T_ a[120:121],v238,8448
buffer_load_dwordx4 v226,s[4:7],s55 offen offset:2048 lds
M_ v[40:43],a[72:75],a[100:103],v[40:43]
T_ a[122:123],v238,9472
M_ v[72:75],a[72:75],a[104:107],v[72:75]
T_ a[124:125],v239,8448
M_ v[104:107],a[72:75],a[108:111],v[104:107]
T_ a[126:127],v239,9472
M_ v[12:15],a[76:79],a[96:99],v[12:15]
T_ a[128:129],v238,8704
M_ v[44:47],a[76:79],a[100:103],v[44:47]
T_ a[130:131],v238,9728
buffer_load_dwordx4 v227,s[4:7],s55 offen offset:3072 lds
M_ v[76:79],a[76:79],a[104:107],v[76:79]
T_ a[132:133],v239,8704
M_ v[108:111],a[76:79],a[108:111],v[108:111]
s_add_u32 s55,s69,0x400000
T_ a[134:135],v239,9728
M_ v[16:19],a[80:83],a[96:99],v[16:19]
s_add_u32 m0,s75,0x8000
T_ a[136:137],v238,8960
M_ v[48:51],a[80:83],a[100:103],v[48:51]
T_ a[138:139],v238,9984
buffer_load_dwordx4 v228,s[8:11],s55 offen lds
M_ v[80:83],a[80:83],a[104:107],v[80:83]
T_ a[140:141],v239,8960
M_ v[112:115],a[80:83],a[108:111],v[112:115]
T_ a[142:143],v239,9984
M_ v[20:23],a[84:87],a[96:99],v[20:23]
M_ v[52:55],a[84:87],a[100:103],v[52:55]
M_ v[84:87],a[84:87],a[104:107],v[84:87]
buffer_load_dwordx4 v229,s[8:11],s55 offen offset:1024 lds
M_ v[116:119],a[84:87],a[108:111],v[116:119]
M_ v[24:27],a[88:91],a[96:99],v[24:27]
M_ v[56:59],a[88:91],a[100:103],v[56:59]
M_ v[88:91],a[88:91],a[104:107],v[88:91]
buffer_load_dwordx4 v230,s[8:11],s55 offen offset:2048 lds
M_ v[120:123],a[88:91],a[108:111],v[120:123]
M_ v[28:31],a[92:95],a[96:99],v[28:31]
M_ v[60:63],a[92:95],a[100:103],v[60:63]
M_ v[92:95],a[92:95],a[104:107],v[92:95]
M_ v[124:127],a[92:95],a[108:111],v[124:127]
buffer_load_dwordx4 v231,s[8:11],s55 offen offset:3072 lds
s_waitcnt lgkmcnt(0)
s_waitcnt vmcnt(14)
s_barrier
M_ v[0:3],a[112:115],a[144:147],v[0:3]
ds_read_b64_tr_b16 a[64:65],v240
M_ v[32:35],a[112:115],a[148:151],v[32:35]
T_ a[66:67],v240,1024
M_ v[64:67],a[112:115],a[152:155],v[64:67]
ds_read_b128 a[96:99],v234
M_ v[96:99],a[112:115],a[156:159],v[96:99]
R_ a[100:103],v234,2048
M_ v[4:7],a[116:119],a[144:147],v[4:7]
R_ a[104:107],v234,4096
M_ v[36:39],a[116:119],a[148:151],v[36:39]
s_add_u32 s55,s68,0x480
R_ a[108:111],v234,6144
M_ v[68:71],a[116:119],a[152:155],v[68:71]
s_lshl_b32 s56,s3,13
ds_read_b64_tr_b16 a[68:69],v241
M_ v[100:103],a[116:119],a[156:159],v[100:103]
s_add_u32 m0,s56,0x10000
T_ a[70:71],v241,1024
M_ v[8:11],a[120:123],a[144:147],v[8:11]
T_ a[72:73],v240,256
buffer_load_dwordx4 v224,s[4:7],s55 offen lds
M_ v[40:43],a[120:123],a[148:151],v[40:43]
T_ a[74:75],v240,1280
M_ v[72:75],a[120:123],a[152:155],v[72:75]
T_ a[76:77],v241,256
M_ v[104:107],a[120:123],a[156:159],v[104:107]
T_ a[78:79],v241,1280
M_ v[12:15],a[124:127],a[144:147],v[12:15]
T_ a[80:81],v240,512
M_ v[44:47],a[124:127],a[148:151],v[44:47]
T_ a[82:83],v240,1536
buffer_load_dwordx4 v225,s[4:7],s55 offen offset:1024 lds
M_ v[76:79],a[124:127],a[152:155],v[76:79]
T_ a[84:85],v241,512
M_ v[108:111],a[124:127],a[156:159],v[108:111]
T_ a[86:87],v241,1536
M_ v[16:19],a[128:131],a[144:147],v[16:19]
T_ a[88:89],v240,768
M_ v[48:51],a[128:131],a[148:151],v[48:51]
T_ a[90:91],v240,1792
buffer_load_dwordx4 v226,s[4:7],s55 offen offset:2048 lds
M_ v[80:83],a[128:131],a[152:155],v[80:83]
T_ a[92:93],v241,768
M_ v[112:115],a[128:131],a[156:159],v[112:115]
T_ a[94:95],v241,1792
M_ v[20:23],a[132:135],a[144:147],v[20:23]
M_ v[52:55],a[132:135],a[148:151],v[52:55]
M_ v[84:87],a[132:135],a[152:155],v[84:87]
buffer_load_dwordx4 v227,s[4:7],s55 offen offset:3072 lds
M_ v[116:119],a[132:135],a[156:159],v[116:119]
s_add_u32 s55,s68,0xa480
M_ v[24:27],a[136:139],a[144:147],v[24:27]
s_lshl_b32 s56,s3,13
M_ v[56:59],a[136:139],a[148:151],v[56:59]
s_add_u32 m0,s56,0x11000
M_ v[88:91],a[136:139],a[152:155],v[88:91]
buffer_load_dwordx4 v224,s[4:7],s55 offen lds
M_ v[120:123],a[136:139],a[156:159],v[120:123]
M_ v[28:31],a[140:143],a[144:147],v[28:31]
M_ v[60:63],a[140:143],a[148:151],v[60:63]
M_ v[92:95],a[140:143],a[152:155],v[92:95]
M_ v[124:127],a[140:143],a[156:159],v[124:127]
buffer_load_dwordx4 v225,s[4:7],s55 offen offset:1024 lds
s_waitcnt lgkmcnt(0)
M_ v[0:3],a[64:67],a[96:99],v[0:3]
T_ a[112:113],v240,8192
M_ v[32:35],a[64:67],a[100:103],v[32:35]
T_ a[114:115],v240,9216
M_ v[64:67],a[64:67],a[104:107],v[64:67]
ds_read_b128 a[144:147],v235
M_ v[96:99],a[64:67],a[108:111],v[96:99]
R_ a[148:151],v235,2048
M_ v[4:7],a[68:71],a[96:99],v[4:7]
R_ a[152:155],v235,4096
M_ v[36:39],a[68:71],a[100:103],v[36:39]
s_add_u32 s55,s68,0xa480
R_ a[156:159],v235,6144
M_ v[68:71],a[68:71],a[104:107],v[68:71]
s_lshl_b32 s56,s3,13
T_ a[116:117],v241,8192
M_ v[100:103],a[68:71],a[108:111],v[100:103]
s_add_u32 m0,s56,0x11000
T_ a[118:119],v241,9216
M_ v[8:11],a[72:75],a[96:99],v[8:11]
T_ a[120:121],v240,8448
buffer_load_dwordx4 v226,s[4:7],s55 offen offset:2048 lds
M_ v[40:43],a[72:75],a[100:103],v[40:43]
T_ a[122:123],v240,9472
M_ v[72:75],a[72:75],a[104:107],v[72:75]
T_ a[124:125],v241,8448
M_ v[104:107],a[72:75],a[108:111],v[104:107]
T_ a[126:127],v241,9472
M_ v[12:15],a[76:79],a[96:99],v[12:15]
T_ a[128:129],v240,8704
M_ v[44:47],a[76:79],a[100:103],v[44:47]
T_ a[130:131],v240,9728
buffer_load_dwordx4 v227,s[4:7],s55 offen offset:3072 lds
M_ v[76:79],a[76:79],a[104:107],v[76:79]
T_ a[132:133],v241,8704
M_ v[108:111],a[76:79],a[108:111],v[108:111]
s_add_u32 s55,s69,0x480000
T_ a[134:135],v241,9728
M_ v[16:19],a[80:83],a[96:99],v[16:19]
s_add_u32 m0,s75,0x18000
T_ a[136:137],v240,8960
M_ v[48:51],a[80:83],a[100:103],v[48:51]
T_ a[138:139],v240,9984
buffer_load_dwordx4 v228,s[8:11],s55 offen lds
M_ v[80:83],a[80:83],a[104:107],v[80:83]
T_ a[140:141],v241,8960
M_ v[112:115],a[80:83],a[108:111],v[112:115]
T_ a[142:143],v241,9984
M_ v[20:23],a[84:87],a[96:99],v[20:23]
M_ v[52:55],a[84:87],a[100:103],v[52:55]
M_ v[84:87],a[84:87],a[104:107],v[84:87]
buffer_load_dwordx4 v229,s[8:11],s55 offen offset:1024 lds
M_ v[116:119],a[84:87],a[108:111],v[116:119]
M_ v[24:27],a[88:91],a[96:99],v[24:27]
M_ v[56:59],a[88:91],a[100:103],v[56:59]
M_ v[88:91],a[88:91],a[104:107],v[88:91]
buffer_load_dwordx4 v230,s[8:11],s55 offen offset:2048 lds
M_ v[120:123],a[88:91],a[108:111],v[120:123]
M_ v[28:31],a[92:95],a[96:99],v[28:31]
M_ v[60:63],a[92:95],a[100:103],v[60:63]
M_ v[92:95],a[92:95],a[104:107],v[92:95]
M_ v[124:127],a[92:95],a[108:111],v[124:127]
buffer_load_dwordx4 v231,s[8:11],s55 offen offset:3072 lds
s_waitcnt vmcnt(25)
v_cmp_ne_u32 vcc,s86,v217
s_cbranch_vccz LQD44_%=
s_mov_b32 s59,0x30d40
LQL44_%=:
s_sleep 2
global_load_dword v217,v216,s[88:89] sc1
s_waitcnt vmcnt(0)
v_cmp_ne_u32 vcc,s86,v217
s_cbranch_vccz LQD44_%=
s_sub_u32 s59,s59,1
s_cmp_lg_u32 s59,0
s_cbranch_scc1 LQL44_%=
LQD44_%=:
s_waitcnt vmcnt(24)
v_xor_b32 v244,s86,v244
v_xor_b32 v245,s86,v245
v_xor_b32 v246,s86,v246
v_xor_b32 v247,s86,v247
v_or3_b32 v244,v244,v245,v246
v_or_b32 v244,v244,v247
v_cmp_ne_u32 vcc,0,v244
s_cbranch_vccz LQD43_%=
s_mov_b32 s59,0x30d40
LQL43_%=:
s_sleep 2
global_load_dwordx4 v[244:247],v248,s[88:89] sc1
s_waitcnt vmcnt(0)
v_xor_b32 v244,s86,v244
v_xor_b32 v245,s86,v245
v_xor_b32 v246,s86,v246
v_xor_b32 v247,s86,v247
v_or3_b32 v244,v244,v245,v246
v_or_b32 v244,v244,v247
v_cmp_ne_u32 vcc,0,v244
s_cbranch_vccz LQD43_%=
s_sub_u32 s59,s59,1
s_cmp_lg_u32 s59,0
s_cbranch_scc1 LQL43_%=
LQD43_%=:
s_waitcnt lgkmcnt(0)
s_waitcnt vmcnt(12)
s_barrier
M_ v[0:3],a[112:115],a[144:147],v[0:3]
ds_read_b64_tr_b16 a[64:65],v242
M_ v[32:35],a[112:115],a[148:151],v[32:35]
T_ a[66:67],v242,1024
M_ v[64:67],a[112:115],a[152:155],v[64:67]
ds_read_b128 a[96:99],v236
M_ v[96:99],a[112:115],a[156:159],v[96:99]
R_ a[100:103],v236,2048
M_ v[4:7],a[116:119],a[144:147],v[4:7]
R_ a[104:107],v236,4096
M_ v[36:39],a[116:119],a[148:151],v[36:39]
R_ a[108:111],v236,6144
M_ v[68:71],a[116:119],a[152:155],v[68:71]
s_add_u32 s55,s75,0x0
ds_read_b64_tr_b16 a[68:69],v243
M_ v[100:103],a[116:119],a[156:159],v[100:103]
s_add_u32 m0,s75,0x1c000
T_ a[70:71],v243,1024
M_ v[8:11],a[120:123],a[144:147],v[8:11]
T_ a[72:73],v242,256
buffer_load_dwordx4 v208,s[12:15],s55 offen sc1 lds
M_ v[40:43],a[120:123],a[148:151],v[40:43]
T_ a[74:75],v242,1280
M_ v[72:75],a[120:123],a[152:155],v[72:75]
T_ a[76:77],v243,256
M_ v[104:107],a[120:123],a[156:159],v[104:107]
T_ a[78:79],v243,1280
M_ v[12:15],a[124:127],a[144:147],v[12:15]
T_ a[80:81],v242,512
M_ v[44:47],a[124:127],a[148:151],v[44:47]
T_ a[82:83],v242,1536
M_ v[76:79],a[124:127],a[152:155],v[76:79]
T_ a[84:85],v243,512
M_ v[108:111],a[124:127],a[156:159],v[108:111]
T_ a[86:87],v243,1536
M_ v[16:19],a[128:131],a[144:147],v[16:19]
T_ a[88:89],v242,768
buffer_load_dwordx4 v208,s[12:15],s55 offen offset:1024 sc1 lds
M_ v[48:51],a[128:131],a[148:151],v[48:51]
T_ a[90:91],v242,1792
M_ v[80:83],a[128:131],a[152:155],v[80:83]
T_ a[92:93],v243,768
M_ v[112:115],a[128:131],a[156:159],v[112:115]
T_ a[94:95],v243,1792
M_ v[20:23],a[132:135],a[144:147],v[20:23]
M_ v[52:55],a[132:135],a[148:151],v[52:55]
M_ v[84:87],a[132:135],a[152:155],v[84:87]
M_ v[116:119],a[132:135],a[156:159],v[116:119]
buffer_load_dwordx4 v208,s[12:15],s55 offen offset:2048 sc1 lds
M_ v[24:27],a[136:139],a[144:147],v[24:27]
M_ v[56:59],a[136:139],a[148:151],v[56:59]
M_ v[88:91],a[136:139],a[152:155],v[88:91]
M_ v[120:123],a[136:139],a[156:159],v[120:123]
M_ v[28:31],a[140:143],a[144:147],v[28:31]
M_ v[60:63],a[140:143],a[148:151],v[60:63]
)A"
R"A(M_ v[92:95],a[140:143],a[152:155],v[92:95]
M_ v[124:127],a[140:143],a[156:159],v[124:127]
buffer_load_dwordx4 v208,s[12:15],s55 offen offset:3072 sc1 lds
s_waitcnt lgkmcnt(0)
M_ v[0:3],a[64:67],a[96:99],v[0:3]
T_ a[112:113],v242,8192
M_ v[32:35],a[64:67],a[100:103],v[32:35]
T_ a[114:115],v242,9216
M_ v[64:67],a[64:67],a[104:107],v[64:67]
ds_read_b128 a[144:147],v237
M_ v[96:99],a[64:67],a[108:111],v[96:99]
R_ a[148:151],v237,2048
M_ v[4:7],a[68:71],a[96:99],v[4:7]
R_ a[152:155],v237,4096
M_ v[36:39],a[68:71],a[100:103],v[36:39]
R_ a[156:159],v237,6144
M_ v[68:71],a[68:71],a[104:107],v[68:71]
s_add_u32 s55,s75,0x4000
T_ a[116:117],v243,8192
M_ v[100:103],a[68:71],a[108:111],v[100:103]
s_add_u32 m0,s75,0x20000
T_ a[118:119],v243,9216
M_ v[8:11],a[72:75],a[96:99],v[8:11]
T_ a[120:121],v242,8448
buffer_load_dwordx4 v208,s[12:15],s55 offen sc1 lds
M_ v[40:43],a[72:75],a[100:103],v[40:43]
T_ a[122:123],v242,9472
M_ v[72:75],a[72:75],a[104:107],v[72:75]
T_ a[124:125],v243,8448
M_ v[104:107],a[72:75],a[108:111],v[104:107]
T_ a[126:127],v243,9472
M_ v[12:15],a[76:79],a[96:99],v[12:15]
T_ a[128:129],v242,8704
M_ v[44:47],a[76:79],a[100:103],v[44:47]
T_ a[130:131],v242,9728
M_ v[76:79],a[76:79],a[104:107],v[76:79]
T_ a[132:133],v243,8704
M_ v[108:111],a[76:79],a[108:111],v[108:111]
T_ a[134:135],v243,9728
M_ v[16:19],a[80:83],a[96:99],v[16:19]
T_ a[136:137],v242,8960
buffer_load_dwordx4 v208,s[12:15],s55 offen offset:1024 sc1 lds
M_ v[48:51],a[80:83],a[100:103],v[48:51]
T_ a[138:139],v242,9984
M_ v[80:83],a[80:83],a[104:107],v[80:83]
T_ a[140:141],v243,8960
M_ v[112:115],a[80:83],a[108:111],v[112:115]
T_ a[142:143],v243,9984
M_ v[20:23],a[84:87],a[96:99],v[20:23]
M_ v[52:55],a[84:87],a[100:103],v[52:55]
M_ v[84:87],a[84:87],a[104:107],v[84:87]
M_ v[116:119],a[84:87],a[108:111],v[116:119]
buffer_load_dwordx4 v208,s[12:15],s55 offen offset:2048 sc1 lds
M_ v[24:27],a[88:91],a[96:99],v[24:27]
M_ v[56:59],a[88:91],a[100:103],v[56:59]
M_ v[88:91],a[88:91],a[104:107],v[88:91]
M_ v[120:123],a[88:91],a[108:111],v[120:123]
M_ v[28:31],a[92:95],a[96:99],v[28:31]
M_ v[60:63],a[92:95],a[100:103],v[60:63]
M_ v[92:95],a[92:95],a[104:107],v[92:95]
M_ v[124:127],a[92:95],a[108:111],v[124:127]
buffer_load_dwordx4 v208,s[12:15],s55 offen offset:3072 sc1 lds
s_waitcnt lgkmcnt(0)
s_waitcnt vmcnt(8)
s_barrier
M_ v[0:3],a[112:115],a[144:147],v[0:3]
ds_read_b64_tr_b16 a[64:65],v238
M_ v[32:35],a[112:115],a[148:151],v[32:35]
T_ a[66:67],v238,1024
M_ v[64:67],a[112:115],a[152:155],v[64:67]
ds_read_b128 a[96:99],v232
M_ v[96:99],a[112:115],a[156:159],v[96:99]
R_ a[100:103],v232,2048
M_ v[4:7],a[116:119],a[144:147],v[4:7]
R_ a[104:107],v232,4096
M_ v[36:39],a[116:119],a[148:151],v[36:39]
R_ a[108:111],v232,6144
M_ v[68:71],a[116:119],a[152:155],v[68:71]
s_add_u32 s55,s75,0x8000
ds_read_b64_tr_b16 a[68:69],v239
M_ v[100:103],a[116:119],a[156:159],v[100:103]
s_add_u32 m0,s75,0x0
T_ a[70:71],v239,1024
M_ v[8:11],a[120:123],a[144:147],v[8:11]
T_ a[72:73],v238,256
buffer_load_dwordx4 v208,s[12:15],s55 offen sc1 lds
M_ v[40:43],a[120:123],a[148:151],v[40:43]
T_ a[74:75],v238,1280
M_ v[72:75],a[120:123],a[152:155],v[72:75]
T_ a[76:77],v239,256
M_ v[104:107],a[120:123],a[156:159],v[104:107]
T_ a[78:79],v239,1280
buffer_load_dwordx4 v208,s[12:15],s55 offen offset:1024 sc1 lds
M_ v[12:15],a[124:127],a[144:147],v[12:15]
T_ a[80:81],v238,512
M_ v[44:47],a[124:127],a[148:151],v[44:47]
T_ a[82:83],v238,1536
M_ v[76:79],a[124:127],a[152:155],v[76:79]
T_ a[84:85],v239,512
M_ v[108:111],a[124:127],a[156:159],v[108:111]
T_ a[86:87],v239,1536
buffer_load_dwordx4 v208,s[12:15],s55 offen offset:2048 sc1 lds
M_ v[16:19],a[128:131],a[144:147],v[16:19]
T_ a[88:89],v238,768
M_ v[48:51],a[128:131],a[148:151],v[48:51]
T_ a[90:91],v238,1792
M_ v[80:83],a[128:131],a[152:155],v[80:83]
T_ a[92:93],v239,768
buffer_load_dwordx4 v208,s[12:15],s55 offen offset:3072 sc1 lds
M_ v[112:115],a[128:131],a[156:159],v[112:115]
s_add_u32 s55,s75,0xc000
T_ a[94:95],v239,1792
M_ v[20:23],a[132:135],a[144:147],v[20:23]
s_add_u32 m0,s75,0x4000
M_ v[52:55],a[132:135],a[148:151],v[52:55]
buffer_load_dwordx4 v208,s[12:15],s55 offen sc1 lds
M_ v[84:87],a[132:135],a[152:155],v[84:87]
M_ v[116:119],a[132:135],a[156:159],v[116:119]
M_ v[24:27],a[136:139],a[144:147],v[24:27]
buffer_load_dwordx4 v208,s[12:15],s55 offen offset:1024 sc1 lds
M_ v[56:59],a[136:139],a[148:151],v[56:59]
M_ v[88:91],a[136:139],a[152:155],v[88:91]
M_ v[120:123],a[136:139],a[156:159],v[120:123]
M_ v[28:31],a[140:143],a[144:147],v[28:31]
buffer_load_dwordx4 v208,s[12:15],s55 offen offset:2048 sc1 lds
M_ v[60:63],a[140:143],a[148:151],v[60:63]
M_ v[92:95],a[140:143],a[152:155],v[92:95]
M_ v[124:127],a[140:143],a[156:159],v[124:127]
buffer_load_dwordx4 v208,s[12:15],s55 offen offset:3072 sc1 lds
s_waitcnt lgkmcnt(0)
M_ v[0:3],a[64:67],a[96:99],v[0:3]
T_ a[112:113],v238,8192
v_mbcnt_lo_u32_b32 v160,-1,0
v_mbcnt_hi_u32_b32 v160,-1,v160
M_ v[32:35],a[64:67],a[100:103],v[32:35]
T_ a[114:115],v238,9216
s_lshl_b32 s62,s3,6
M_ v[64:67],a[64:67],a[104:107],v[64:67]
ds_read_b128 a[144:147],v233
v_add_u32 v160,s62,v160
M_ v[96:99],a[64:67],a[108:111],v[96:99]
R_ a[148:151],v233,2048
v_min_u32 v160,159,v160
M_ v[4:7],a[68:71],a[96:99],v[4:7]
R_ a[152:155],v233,4096
s_and_b32 s62,s2,1
s_mul_i32 s62,s62,160
M_ v[36:39],a[68:71],a[100:103],v[36:39]
R_ a[156:159],v233,6144
v_add_u32 v160,s62,v160
M_ v[68:71],a[68:71],a[104:107],v[68:71]
T_ a[116:117],v239,8192
v_lshlrev_b32 v160,2,v160
M_ v[100:103],a[68:71],a[108:111],v[100:103]
T_ a[118:119],v239,9216
s_lshr_b32 s62,s2,3
M_ v[8:11],a[72:75],a[96:99],v[8:11]
T_ a[120:121],v238,8448
s_mul_i32 s62,s62,163840
s_lshr_b32 s63,s2,1
M_ v[40:43],a[72:75],a[100:103],v[40:43]
T_ a[122:123],v238,9472
s_and_b32 s63,s63,3
M_ v[72:75],a[72:75],a[104:107],v[72:75]
T_ a[124:125],v239,8448
s_mul_i32 s63,s63,1280
M_ v[104:107],a[72:75],a[108:111],v[104:107]
T_ a[126:127],v239,9472
s_add_u32 s62,s62,s63
M_ v[12:15],a[76:79],a[96:99],v[12:15]
T_ a[128:129],v238,8704
s_add_u32 s62,s62,0x4800000
s_add_u32 s60,s88,s62
s_addc_u32 s61,s89,0
M_ v[44:47],a[76:79],a[100:103],v[44:47]
T_ a[130:131],v238,9728
global_load_dword v128,v160,s[60:61] sc1
M_ v[76:79],a[76:79],a[104:107],v[76:79]
T_ a[132:133],v239,8704
s_add_u32 s60,s60,5120
s_addc_u32 s61,s61,0
M_ v[108:111],a[76:79],a[108:111],v[108:111]
T_ a[134:135],v239,9728
global_load_dword v129,v160,s[60:61] sc1
M_ v[16:19],a[80:83],a[96:99],v[16:19]
T_ a[136:137],v238,8960
s_add_u32 s60,s60,5120
s_addc_u32 s61,s61,0
global_load_dword v130,v160,s[60:61] sc1
M_ v[48:51],a[80:83],a[100:103],v[48:51]
T_ a[138:139],v238,9984
s_add_u32 s60,s60,5120
s_addc_u32 s61,s61,0
M_ v[80:83],a[80:83],a[104:107],v[80:83]
T_ a[140:141],v239,8960
global_load_dword v131,v160,s[60:61] sc1
M_ v[112:115],a[80:83],a[108:111],v[112:115]
T_ a[142:143],v239,9984
s_add_u32 s60,s60,5120
s_addc_u32 s61,s61,0
M_ v[20:23],a[84:87],a[96:99],v[20:23]
global_load_dword v132,v160,s[60:61] sc1
s_add_u32 s60,s60,5120
s_addc_u32 s61,s61,0
M_ v[52:55],a[84:87],a[100:103],v[52:55]
global_load_dword v133,v160,s[60:61] sc1
M_ v[84:87],a[84:87],a[104:107],v[84:87]
s_add_u32 s60,s60,5120
s_addc_u32 s61,s61,0
M_ v[116:119],a[84:87],a[108:111],v[116:119]
global_load_dword v134,v160,s[60:61] sc1
M_ v[24:27],a[88:91],a[96:99],v[24:27]
s_add_u32 s60,s60,5120
s_addc_u32 s61,s61,0
global_load_dword v135,v160,s[60:61] sc1
M_ v[56:59],a[88:91],a[100:103],v[56:59]
s_add_u32 s60,s60,5120
s_addc_u32 s61,s61,0
M_ v[88:91],a[88:91],a[104:107],v[88:91]
global_load_dword v136,v160,s[60:61] sc1
M_ v[120:123],a[88:91],a[108:111],v[120:123]
s_add_u32 s60,s60,5120
s_addc_u32 s61,s61,0
M_ v[28:31],a[92:95],a[96:99],v[28:31]
global_load_dword v137,v160,s[60:61] sc1
s_add_u32 s60,s60,5120
s_addc_u32 s61,s61,0
M_ v[60:63],a[92:95],a[100:103],v[60:63]
global_load_dword v138,v160,s[60:61] sc1
M_ v[92:95],a[92:95],a[104:107],v[92:95]
s_add_u32 s60,s60,5120
s_addc_u32 s61,s61,0
M_ v[124:127],a[92:95],a[108:111],v[124:127]
global_load_dword v139,v160,s[60:61] sc1
s_waitcnt lgkmcnt(0)
M_ v[0:3],a[112:115],a[144:147],v[0:3]
s_add_u32 s60,s60,5120
s_addc_u32 s61,s61,0
global_load_dword v140,v160,s[60:61] sc1
M_ v[32:35],a[112:115],a[148:151],v[32:35]
s_add_u32 s60,s60,5120
s_addc_u32 s61,s61,0
M_ v[64:67],a[112:115],a[152:155],v[64:67]
global_load_dword v141,v160,s[60:61] sc1
M_ v[96:99],a[112:115],a[156:159],v[96:99]
s_add_u32 s60,s60,5120
s_addc_u32 s61,s61,0
M_ v[4:7],a[116:119],a[144:147],v[4:7]
global_load_dword v142,v160,s[60:61] sc1
s_add_u32 s60,s60,5120
s_addc_u32 s61,s61,0
M_ v[36:39],a[116:119],a[148:151],v[36:39]
global_load_dword v143,v160,s[60:61] sc1
M_ v[68:71],a[116:119],a[152:155],v[68:71]
s_add_u32 s60,s60,5120
s_addc_u32 s61,s61,0
M_ v[100:103],a[116:119],a[156:159],v[100:103]
global_load_dword v144,v160,s[60:61] sc1
M_ v[8:11],a[120:123],a[144:147],v[8:11]
s_add_u32 s60,s60,5120
s_addc_u32 s61,s61,0
global_load_dword v145,v160,s[60:61] sc1
M_ v[40:43],a[120:123],a[148:151],v[40:43]
s_add_u32 s60,s60,5120
s_addc_u32 s61,s61,0
M_ v[72:75],a[120:123],a[152:155],v[72:75]
global_load_dword v146,v160,s[60:61] sc1
M_ v[104:107],a[120:123],a[156:159],v[104:107]
s_add_u32 s60,s60,5120
s_addc_u32 s61,s61,0
M_ v[12:15],a[124:127],a[144:147],v[12:15]
global_load_dword v147,v160,s[60:61] sc1
s_add_u32 s60,s60,5120
s_addc_u32 s61,s61,0
M_ v[44:47],a[124:127],a[148:151],v[44:47]
global_load_dword v148,v160,s[60:61] sc1
M_ v[76:79],a[124:127],a[152:155],v[76:79]
s_add_u32 s60,s60,5120
s_addc_u32 s61,s61,0
M_ v[108:111],a[124:127],a[156:159],v[108:111]
global_load_dword v149,v160,s[60:61] sc1
M_ v[16:19],a[128:131],a[144:147],v[16:19]
s_add_u32 s60,s60,5120
s_addc_u32 s61,s61,0
global_load_dword v150,v160,s[60:61] sc1
M_ v[48:51],a[128:131],a[148:151],v[48:51]
s_add_u32 s60,s60,5120
s_addc_u32 s61,s61,0
M_ v[80:83],a[128:131],a[152:155],v[80:83]
global_load_dword v151,v160,s[60:61] sc1
M_ v[112:115],a[128:131],a[156:159],v[112:115]
s_add_u32 s60,s60,5120
s_addc_u32 s61,s61,0
M_ v[20:23],a[132:135],a[144:147],v[20:23]
global_load_dword v152,v160,s[60:61] sc1
s_add_u32 s60,s60,5120
s_addc_u32 s61,s61,0
M_ v[52:55],a[132:135],a[148:151],v[52:55]
global_load_dword v153,v160,s[60:61] sc1
M_ v[84:87],a[132:135],a[152:155],v[84:87]
s_add_u32 s60,s60,5120
s_addc_u32 s61,s61,0
M_ v[116:119],a[132:135],a[156:159],v[116:119]
global_load_dword v154,v160,s[60:61] sc1
M_ v[24:27],a[136:139],a[144:147],v[24:27]
s_add_u32 s60,s60,5120
s_addc_u32 s61,s61,0
global_load_dword v155,v160,s[60:61] sc1
M_ v[56:59],a[136:139],a[148:151],v[56:59]
s_add_u32 s60,s60,5120
s_addc_u32 s61,s61,0
M_ v[88:91],a[136:139],a[152:155],v[88:91]
global_load_dword v156,v160,s[60:61] sc1
M_ v[120:123],a[136:139],a[156:159],v[120:123]
s_add_u32 s60,s60,5120
s_addc_u32 s61,s61,0
M_ v[28:31],a[140:143],a[144:147],v[28:31]
global_load_dword v157,v160,s[60:61] sc1
s_add_u32 s60,s60,5120
s_addc_u32 s61,s61,0
M_ v[60:63],a[140:143],a[148:151],v[60:63]
global_load_dword v158,v160,s[60:61] sc1
M_ v[92:95],a[140:143],a[152:155],v[92:95]
s_add_u32 s60,s60,5120
s_addc_u32 s61,s61,0
M_ v[124:127],a[140:143],a[156:159],v[124:127]
global_load_dword v159,v160,s[60:61] sc1
s_nop 7
v_mov_b32 v220,0x3e0293ee
v_mov_b32 v221,0x3e0293ee
s_and_b32 s55,s3,1
s_lshl_b32 s55,s55,14
s_add_u32 s55,s55,0x8000
v_add_u32 v223,s55,v208
s_cmp_lg_u32 s73,0
s_cbranch_scc1 LQEB_%=
v_pk_mul_f32 v[0:1],v[0:1],v[220:221]
v_pk_mul_f32 v[2:3],v[2:3],v[220:221]
v_pk_mul_f32 v[4:5],v[4:5],v[220:221]
v_pk_mul_f32 v[6:7],v[6:7],v[220:221]
C_ v210,v0,v1
C_ v211,v2,v3
C_ v212,v4,v5
C_ v213,v6,v7
AW_ a0,v210
AW_ a1,v211
AW_ a2,v212
AW_ a3,v213
ds_write_b128 v223,v[210:213]
v_mov_b64 v[0:1],0
v_mov_b64 v[2:3],0
v_mov_b64 v[4:5],0
v_mov_b64 v[6:7],0
v_pk_mul_f32 v[8:9],v[8:9],v[220:221]
v_pk_mul_f32 v[10:11],v[10:11],v[220:221]
v_pk_mul_f32 v[12:13],v[12:13],v[220:221]
v_pk_mul_f32 v[14:15],v[14:15],v[220:221]
C_ v214,v8,v9
C_ v215,v10,v11
C_ v216,v12,v13
C_ v217,v14,v15
AW_ a4,v214
AW_ a5,v215
AW_ a6,v216
AW_ a7,v217
ds_write_b128 v223,v[214:217] offset:1024
v_mov_b64 v[8:9],0
v_mov_b64 v[10:11],0
v_mov_b64 v[12:13],0
v_mov_b64 v[14:15],0
v_pk_mul_f32 v[16:17],v[16:17],v[220:221]
v_pk_mul_f32 v[18:19],v[18:19],v[220:221]
v_pk_mul_f32 v[20:21],v[20:21],v[220:221]
v_pk_mul_f32 v[22:23],v[22:23],v[220:221]
C_ v210,v16,v17
C_ v211,v18,v19
C_ v212,v20,v21
C_ v213,v22,v23
AW_ a8,v210
AW_ a9,v211
AW_ a10,v212
AW_ a11,v213
ds_write_b128 v223,v[210:213] offset:2048
v_mov_b64 v[16:17],0
v_mov_b64 v[18:19],0
v_mov_b64 v[20:21],0
v_mov_b64 v[22:23],0
v_pk_mul_f32 v[24:25],v[24:25],v[220:221]
v_pk_mul_f32 v[26:27],v[26:27],v[220:221]
v_pk_mul_f32 v[28:29],v[28:29],v[220:221]
v_pk_mul_f32 v[30:31],v[30:31],v[220:221]
C_ v214,v24,v25
C_ v215,v26,v27
C_ v216,v28,v29
C_ v217,v30,v31
AW_ a12,v214
AW_ a13,v215
AW_ a14,v216
AW_ a15,v217
ds_write_b128 v223,v[214:217] offset:3072
v_mov_b64 v[24:25],0
v_mov_b64 v[26:27],0
v_mov_b64 v[28:29],0
v_mov_b64 v[30:31],0
v_pk_mul_f32 v[32:33],v[32:33],v[220:221]
v_pk_mul_f32 v[34:35],v[34:35],v[220:221]
v_pk_mul_f32 v[36:37],v[36:37],v[220:221]
v_pk_mul_f32 v[38:39],v[38:39],v[220:221]
C_ v210,v32,v33
C_ v211,v34,v35
C_ v212,v36,v37
C_ v213,v38,v39
AW_ a16,v210
AW_ a17,v211
AW_ a18,v212
AW_ a19,v213
ds_write_b128 v223,v[210:213] offset:4096
v_mov_b64 v[32:33],0
v_mov_b64 v[34:35],0
v_mov_b64 v[36:37],0
v_mov_b64 v[38:39],0
v_pk_mul_f32 v[40:41],v[40:41],v[220:221]
v_pk_mul_f32 v[42:43],v[42:43],v[220:221]
v_pk_mul_f32 v[44:45],v[44:45],v[220:221]
v_pk_mul_f32 v[46:47],v[46:47],v[220:221]
C_ v214,v40,v41
C_ v215,v42,v43
C_ v216,v44,v45
C_ v217,v46,v47
AW_ a20,v214
AW_ a21,v215
AW_ a22,v216
AW_ a23,v217
ds_write_b128 v223,v[214:217] offset:5120
v_mov_b64 v[40:41],0
v_mov_b64 v[42:43],0
v_mov_b64 v[44:45],0
v_mov_b64 v[46:47],0
v_pk_mul_f32 v[48:49],v[48:49],v[220:221]
v_pk_mul_f32 v[50:51],v[50:51],v[220:221]
v_pk_mul_f32 v[52:53],v[52:53],v[220:221]
v_pk_mul_f32 v[54:55],v[54:55],v[220:221]
C_ v210,v48,v49
C_ v211,v50,v51
C_ v212,v52,v53
C_ v213,v54,v55
AW_ a24,v210
AW_ a25,v211
AW_ a26,v212
AW_ a27,v213
)A"
R"A(ds_write_b128 v223,v[210:213] offset:6144
v_mov_b64 v[48:49],0
v_mov_b64 v[50:51],0
v_mov_b64 v[52:53],0
v_mov_b64 v[54:55],0
v_pk_mul_f32 v[56:57],v[56:57],v[220:221]
v_pk_mul_f32 v[58:59],v[58:59],v[220:221]
v_pk_mul_f32 v[60:61],v[60:61],v[220:221]
v_pk_mul_f32 v[62:63],v[62:63],v[220:221]
C_ v214,v56,v57
C_ v215,v58,v59
C_ v216,v60,v61
C_ v217,v62,v63
AW_ a28,v214
AW_ a29,v215
AW_ a30,v216
AW_ a31,v217
ds_write_b128 v223,v[214:217] offset:7168
v_mov_b64 v[56:57],0
v_mov_b64 v[58:59],0
v_mov_b64 v[60:61],0
v_mov_b64 v[62:63],0
v_pk_mul_f32 v[64:65],v[64:65],v[220:221]
v_pk_mul_f32 v[66:67],v[66:67],v[220:221]
v_pk_mul_f32 v[68:69],v[68:69],v[220:221]
v_pk_mul_f32 v[70:71],v[70:71],v[220:221]
C_ v210,v64,v65
C_ v211,v66,v67
C_ v212,v68,v69
C_ v213,v70,v71
AW_ a32,v210
AW_ a33,v211
AW_ a34,v212
AW_ a35,v213
ds_write_b128 v223,v[210:213] offset:8192
v_mov_b64 v[64:65],0
v_mov_b64 v[66:67],0
v_mov_b64 v[68:69],0
v_mov_b64 v[70:71],0
v_pk_mul_f32 v[72:73],v[72:73],v[220:221]
v_pk_mul_f32 v[74:75],v[74:75],v[220:221]
v_pk_mul_f32 v[76:77],v[76:77],v[220:221]
v_pk_mul_f32 v[78:79],v[78:79],v[220:221]
C_ v214,v72,v73
C_ v215,v74,v75
C_ v216,v76,v77
C_ v217,v78,v79
AW_ a36,v214
AW_ a37,v215
AW_ a38,v216
AW_ a39,v217
ds_write_b128 v223,v[214:217] offset:9216
v_mov_b64 v[72:73],0
v_mov_b64 v[74:75],0
v_mov_b64 v[76:77],0
v_mov_b64 v[78:79],0
v_pk_mul_f32 v[80:81],v[80:81],v[220:221]
v_pk_mul_f32 v[82:83],v[82:83],v[220:221]
v_pk_mul_f32 v[84:85],v[84:85],v[220:221]
v_pk_mul_f32 v[86:87],v[86:87],v[220:221]
C_ v210,v80,v81
C_ v211,v82,v83
C_ v212,v84,v85
C_ v213,v86,v87
AW_ a40,v210
AW_ a41,v211
AW_ a42,v212
AW_ a43,v213
ds_write_b128 v223,v[210:213] offset:10240
v_mov_b64 v[80:81],0
v_mov_b64 v[82:83],0
v_mov_b64 v[84:85],0
v_mov_b64 v[86:87],0
v_pk_mul_f32 v[88:89],v[88:89],v[220:221]
v_pk_mul_f32 v[90:91],v[90:91],v[220:221]
v_pk_mul_f32 v[92:93],v[92:93],v[220:221]
v_pk_mul_f32 v[94:95],v[94:95],v[220:221]
C_ v214,v88,v89
C_ v215,v90,v91
C_ v216,v92,v93
C_ v217,v94,v95
AW_ a44,v214
AW_ a45,v215
AW_ a46,v216
AW_ a47,v217
ds_write_b128 v223,v[214:217] offset:11264
v_mov_b64 v[88:89],0
v_mov_b64 v[90:91],0
v_mov_b64 v[92:93],0
v_mov_b64 v[94:95],0
v_pk_mul_f32 v[96:97],v[96:97],v[220:221]
v_pk_mul_f32 v[98:99],v[98:99],v[220:221]
v_pk_mul_f32 v[100:101],v[100:101],v[220:221]
v_pk_mul_f32 v[102:103],v[102:103],v[220:221]
C_ v210,v96,v97
C_ v211,v98,v99
C_ v212,v100,v101
C_ v213,v102,v103
AW_ a48,v210
AW_ a49,v211
AW_ a50,v212
AW_ a51,v213
ds_write_b128 v223,v[210:213] offset:12288
v_mov_b64 v[96:97],0
v_mov_b64 v[98:99],0
v_mov_b64 v[100:101],0
v_mov_b64 v[102:103],0
v_pk_mul_f32 v[104:105],v[104:105],v[220:221]
v_pk_mul_f32 v[106:107],v[106:107],v[220:221]
v_pk_mul_f32 v[108:109],v[108:109],v[220:221]
v_pk_mul_f32 v[110:111],v[110:111],v[220:221]
C_ v214,v104,v105
C_ v215,v106,v107
C_ v216,v108,v109
C_ v217,v110,v111
AW_ a52,v214
AW_ a53,v215
AW_ a54,v216
AW_ a55,v217
ds_write_b128 v223,v[214:217] offset:13312
v_mov_b64 v[104:105],0
v_mov_b64 v[106:107],0
v_mov_b64 v[108:109],0
v_mov_b64 v[110:111],0
v_pk_mul_f32 v[112:113],v[112:113],v[220:221]
v_pk_mul_f32 v[114:115],v[114:115],v[220:221]
v_pk_mul_f32 v[116:117],v[116:117],v[220:221]
v_pk_mul_f32 v[118:119],v[118:119],v[220:221]
C_ v210,v112,v113
C_ v211,v114,v115
C_ v212,v116,v117
C_ v213,v118,v119
AW_ a56,v210
AW_ a57,v211
AW_ a58,v212
AW_ a59,v213
ds_write_b128 v223,v[210:213] offset:14336
v_mov_b64 v[112:113],0
v_mov_b64 v[114:115],0
v_mov_b64 v[116:117],0
v_mov_b64 v[118:119],0
v_pk_mul_f32 v[120:121],v[120:121],v[220:221]
v_pk_mul_f32 v[122:123],v[122:123],v[220:221]
v_pk_mul_f32 v[124:125],v[124:125],v[220:221]
v_pk_mul_f32 v[126:127],v[126:127],v[220:221]
C_ v214,v120,v121
C_ v215,v122,v123
C_ v216,v124,v125
C_ v217,v126,v127
AW_ a60,v214
AW_ a61,v215
AW_ a62,v216
AW_ a63,v217
ds_write_b128 v223,v[214:217] offset:15360
v_mov_b64 v[120:121],0
v_mov_b64 v[122:123],0
v_mov_b64 v[124:125],0
v_mov_b64 v[126:127],0
s_branch LQEE_%=
LQEB_%=:
v_pk_mul_f32 v[0:1],v[0:1],v[220:221]
v_pk_mul_f32 v[2:3],v[2:3],v[220:221]
v_pk_mul_f32 v[4:5],v[4:5],v[220:221]
v_pk_mul_f32 v[6:7],v[6:7],v[220:221]
C_ v210,v0,v1
C_ v211,v2,v3
C_ v212,v4,v5
C_ v213,v6,v7
AW_ a0,v210
AW_ a1,v211
AW_ a2,v212
AW_ a3,v213
v_mov_b64 v[0:1],0
v_mov_b64 v[2:3],0
v_mov_b64 v[4:5],0
v_mov_b64 v[6:7],0
v_pk_mul_f32 v[8:9],v[8:9],v[220:221]
v_pk_mul_f32 v[10:11],v[10:11],v[220:221]
v_pk_mul_f32 v[12:13],v[12:13],v[220:221]
v_pk_mul_f32 v[14:15],v[14:15],v[220:221]
C_ v214,v8,v9
C_ v215,v10,v11
C_ v216,v12,v13
C_ v217,v14,v15
AW_ a4,v214
AW_ a5,v215
AW_ a6,v216
AW_ a7,v217
v_mov_b64 v[8:9],0
v_mov_b64 v[10:11],0
v_mov_b64 v[12:13],0
v_mov_b64 v[14:15],0
v_pk_mul_f32 v[16:17],v[16:17],v[220:221]
v_pk_mul_f32 v[18:19],v[18:19],v[220:221]
v_pk_mul_f32 v[20:21],v[20:21],v[220:221]
v_pk_mul_f32 v[22:23],v[22:23],v[220:221]
C_ v210,v16,v17
C_ v211,v18,v19
C_ v212,v20,v21
C_ v213,v22,v23
AW_ a8,v210
AW_ a9,v211
AW_ a10,v212
AW_ a11,v213
v_mov_b64 v[16:17],0
v_mov_b64 v[18:19],0
v_mov_b64 v[20:21],0
v_mov_b64 v[22:23],0
v_pk_mul_f32 v[24:25],v[24:25],v[220:221]
v_pk_mul_f32 v[26:27],v[26:27],v[220:221]
v_pk_mul_f32 v[28:29],v[28:29],v[220:221]
v_pk_mul_f32 v[30:31],v[30:31],v[220:221]
C_ v214,v24,v25
C_ v215,v26,v27
C_ v216,v28,v29
C_ v217,v30,v31
AW_ a12,v214
AW_ a13,v215
AW_ a14,v216
AW_ a15,v217
v_mov_b64 v[24:25],0
v_mov_b64 v[26:27],0
v_mov_b64 v[28:29],0
v_mov_b64 v[30:31],0
v_pk_mul_f32 v[32:33],v[32:33],v[220:221]
v_pk_mul_f32 v[34:35],v[34:35],v[220:221]
v_pk_mul_f32 v[36:37],v[36:37],v[220:221]
v_pk_mul_f32 v[38:39],v[38:39],v[220:221]
C_ v210,v32,v33
C_ v211,v34,v35
C_ v212,v36,v37
C_ v213,v38,v39
AW_ a16,v210
AW_ a17,v211
AW_ a18,v212
AW_ a19,v213
v_mov_b64 v[32:33],0
v_mov_b64 v[34:35],0
v_mov_b64 v[36:37],0
v_mov_b64 v[38:39],0
v_pk_mul_f32 v[40:41],v[40:41],v[220:221]
v_pk_mul_f32 v[42:43],v[42:43],v[220:221]
v_pk_mul_f32 v[44:45],v[44:45],v[220:221]
v_pk_mul_f32 v[46:47],v[46:47],v[220:221]
C_ v214,v40,v41
C_ v215,v42,v43
C_ v216,v44,v45
C_ v217,v46,v47
AW_ a20,v214
AW_ a21,v215
AW_ a22,v216
AW_ a23,v217
v_mov_b64 v[40:41],0
v_mov_b64 v[42:43],0
v_mov_b64 v[44:45],0
v_mov_b64 v[46:47],0
v_pk_mul_f32 v[48:49],v[48:49],v[220:221]
v_pk_mul_f32 v[50:51],v[50:51],v[220:221]
v_pk_mul_f32 v[52:53],v[52:53],v[220:221]
v_pk_mul_f32 v[54:55],v[54:55],v[220:221]
C_ v210,v48,v49
C_ v211,v50,v51
C_ v212,v52,v53
C_ v213,v54,v55
AW_ a24,v210
AW_ a25,v211
AW_ a26,v212
AW_ a27,v213
v_mov_b64 v[48:49],0
v_mov_b64 v[50:51],0
v_mov_b64 v[52:53],0
v_mov_b64 v[54:55],0
v_pk_mul_f32 v[56:57],v[56:57],v[220:221]
v_pk_mul_f32 v[58:59],v[58:59],v[220:221]
v_pk_mul_f32 v[60:61],v[60:61],v[220:221]
v_pk_mul_f32 v[62:63],v[62:63],v[220:221]
C_ v214,v56,v57
C_ v215,v58,v59
C_ v216,v60,v61
C_ v217,v62,v63
AW_ a28,v214
AW_ a29,v215
AW_ a30,v216
AW_ a31,v217
v_mov_b64 v[56:57],0
v_mov_b64 v[58:59],0
v_mov_b64 v[60:61],0
v_mov_b64 v[62:63],0
v_pk_mul_f32 v[64:65],v[64:65],v[220:221]
v_pk_mul_f32 v[66:67],v[66:67],v[220:221]
v_pk_mul_f32 v[68:69],v[68:69],v[220:221]
v_pk_mul_f32 v[70:71],v[70:71],v[220:221]
C_ v210,v64,v65
C_ v211,v66,v67
C_ v212,v68,v69
C_ v213,v70,v71
AW_ a32,v210
AW_ a33,v211
AW_ a34,v212
AW_ a35,v213
v_mov_b64 v[64:65],0
v_mov_b64 v[66:67],0
v_mov_b64 v[68:69],0
v_mov_b64 v[70:71],0
v_pk_mul_f32 v[72:73],v[72:73],v[220:221]
v_pk_mul_f32 v[74:75],v[74:75],v[220:221]
v_pk_mul_f32 v[76:77],v[76:77],v[220:221]
v_pk_mul_f32 v[78:79],v[78:79],v[220:221]
C_ v214,v72,v73
C_ v215,v74,v75
C_ v216,v76,v77
C_ v217,v78,v79
AW_ a36,v214
AW_ a37,v215
AW_ a38,v216
AW_ a39,v217
v_mov_b64 v[72:73],0
v_mov_b64 v[74:75],0
v_mov_b64 v[76:77],0
v_mov_b64 v[78:79],0
v_pk_mul_f32 v[80:81],v[80:81],v[220:221]
v_pk_mul_f32 v[82:83],v[82:83],v[220:221]
v_pk_mul_f32 v[84:85],v[84:85],v[220:221]
v_pk_mul_f32 v[86:87],v[86:87],v[220:221]
C_ v210,v80,v81
C_ v211,v82,v83
C_ v212,v84,v85
C_ v213,v86,v87
AW_ a40,v210
AW_ a41,v211
AW_ a42,v212
AW_ a43,v213
v_mov_b64 v[80:81],0
v_mov_b64 v[82:83],0
v_mov_b64 v[84:85],0
v_mov_b64 v[86:87],0
v_pk_mul_f32 v[88:89],v[88:89],v[220:221]
v_pk_mul_f32 v[90:91],v[90:91],v[220:221]
v_pk_mul_f32 v[92:93],v[92:93],v[220:221]
v_pk_mul_f32 v[94:95],v[94:95],v[220:221]
C_ v214,v88,v89
C_ v215,v90,v91
C_ v216,v92,v93
C_ v217,v94,v95
AW_ a44,v214
AW_ a45,v215
AW_ a46,v216
AW_ a47,v217
v_mov_b64 v[88:89],0
v_mov_b64 v[90:91],0
v_mov_b64 v[92:93],0
v_mov_b64 v[94:95],0
v_pk_mul_f32 v[96:97],v[96:97],v[220:221]
v_pk_mul_f32 v[98:99],v[98:99],v[220:221]
v_pk_mul_f32 v[100:101],v[100:101],v[220:221]
v_pk_mul_f32 v[102:103],v[102:103],v[220:221]
C_ v210,v96,v97
C_ v211,v98,v99
C_ v212,v100,v101
C_ v213,v102,v103
AW_ a48,v210
AW_ a49,v211
AW_ a50,v212
AW_ a51,v213
v_mov_b64 v[96:97],0
v_mov_b64 v[98:99],0
v_mov_b64 v[100:101],0
v_mov_b64 v[102:103],0
v_pk_mul_f32 v[104:105],v[104:105],v[220:221]
v_pk_mul_f32 v[106:107],v[106:107],v[220:221]
v_pk_mul_f32 v[108:109],v[108:109],v[220:221]
v_pk_mul_f32 v[110:111],v[110:111],v[220:221]
C_ v214,v104,v105
C_ v215,v106,v107
C_ v216,v108,v109
C_ v217,v110,v111
AW_ a52,v214
AW_ a53,v215
AW_ a54,v216
AW_ a55,v217
v_mov_b64 v[104:105],0
v_mov_b64 v[106:107],0
v_mov_b64 v[108:109],0
v_mov_b64 v[110:111],0
v_pk_mul_f32 v[112:113],v[112:113],v[220:221]
v_pk_mul_f32 v[114:115],v[114:115],v[220:221]
v_pk_mul_f32 v[116:117],v[116:117],v[220:221]
v_pk_mul_f32 v[118:119],v[118:119],v[220:221]
C_ v210,v112,v113
C_ v211,v114,v115
C_ v212,v116,v117
C_ v213,v118,v119
AW_ a56,v210
AW_ a57,v211
AW_ a58,v212
AW_ a59,v213
v_mov_b64 v[112:113],0
v_mov_b64 v[114:115],0
v_mov_b64 v[116:117],0
v_mov_b64 v[118:119],0
v_pk_mul_f32 v[120:121],v[120:121],v[220:221]
v_pk_mul_f32 v[122:123],v[122:123],v[220:221]
v_pk_mul_f32 v[124:125],v[124:125],v[220:221]
v_pk_mul_f32 v[126:127],v[126:127],v[220:221]
C_ v214,v120,v121
C_ v215,v122,v123
C_ v216,v124,v125
C_ v217,v126,v127
AW_ a60,v214
AW_ a61,v215
AW_ a62,v216
AW_ a63,v217
v_mov_b64 v[120:121],0
v_mov_b64 v[122:123],0
v_mov_b64 v[124:125],0
v_mov_b64 v[126:127],0
LQEE_%=:
v_mov_b32 v200,0
v_mov_b32 v201,0
v_mov_b32 v202,0
v_mov_b32 v203,0
v_mov_b64 v[168:169],0
v_mov_b64 v[172:173],0
v_mov_b64 v[170:171],0
v_mov_b64 v[174:175],0
v_mov_b32 v205,0
s_mov_b64 vcc,0
s_mov_b32 s85,0
s_mov_b32 s21,0
s_mov_b32 s24,0x10000
v_add_u32 v206,0x1c000,v208
v_add_u32 v207,0x20000,v208
s_waitcnt vmcnt(40)
s_waitcnt lgkmcnt(0)
s_barrier
s_waitcnt vmcnt(15)
v_add_f32 v128,v128,v144
s_waitcnt vmcnt(14)
v_add_f32 v129,v129,v145
s_waitcnt vmcnt(13)
v_add_f32 v130,v130,v146
s_waitcnt vmcnt(12)
v_add_f32 v131,v131,v147
s_waitcnt vmcnt(11)
v_add_f32 v132,v132,v148
s_waitcnt vmcnt(10)
v_add_f32 v133,v133,v149
s_waitcnt vmcnt(9)
v_add_f32 v134,v134,v150
s_waitcnt vmcnt(8)
v_add_f32 v135,v135,v151
s_waitcnt vmcnt(7)
v_add_f32 v136,v136,v152
s_waitcnt vmcnt(6)
v_add_f32 v137,v137,v153
s_waitcnt vmcnt(5)
v_add_f32 v138,v138,v154
s_waitcnt vmcnt(4)
v_add_f32 v139,v139,v155
s_waitcnt vmcnt(3)
v_add_f32 v140,v140,v156
s_waitcnt vmcnt(2)
v_add_f32 v141,v141,v157
s_waitcnt vmcnt(1)
v_add_f32 v142,v142,v158
s_waitcnt vmcnt(0)
v_add_f32 v143,v143,v159
v_add_f32 v128,v128,v136
v_add_f32 v129,v129,v137
v_add_f32 v130,v130,v138
v_add_f32 v131,v131,v139
v_add_f32 v132,v132,v140
v_add_f32 v133,v133,v141
v_add_f32 v134,v134,v142
v_add_f32 v135,v135,v143
v_add_f32 v128,v128,v132
v_add_f32 v129,v129,v133
v_add_f32 v130,v130,v134
v_add_f32 v131,v131,v135
v_add_f32 v128,v128,v130
v_add_f32 v129,v129,v131
v_add_f32 v128,v128,v129
s_lshr_b32 s62,s2,3
s_mul_i32 s62,s62,5120
s_add_u32 s62,s62,s63
s_add_u32 s62,s62,0x4d80000
s_add_u32 s60,s88,s62
s_addc_u32 s61,s89,0
global_store_dword v160,v128,s[60:61] sc1
v_mbcnt_lo_u32_b32 v248,-1,0
v_mbcnt_hi_u32_b32 v248,-1,v248
v_lshlrev_b32 v248,4,v248
s_load_dwordx2 s[60:61],s[0:1],40
s_waitcnt lgkmcnt(0)
s_mov_b32 s64,s60
s_mov_b32 s65,s61
s_and_b32 s65,s65,0xffff
s_mov_b32 s66,0x500000
s_mov_b32 s67,0x20000
s_lshl_b32 s62,s2,2
s_add_u32 s62,s62,s3
s_lshl_b32 s62,s62,10
buffer_load_dwordx4 v[244:247],v248,s[64:67],s62 offen
s_add_u32 s62,s62,0x100000
buffer_load_dwordx4 v[244:247],v248,s[64:67],s62 offen
s_add_u32 s62,s62,0x100000
buffer_load_dwordx4 v[244:247],v248,s[64:67],s62 offen
s_add_u32 s62,s62,0x100000
buffer_load_dwordx4 v[244:247],v248,s[64:67],s62 offen
s_add_u32 s62,s62,0x100000
buffer_load_dwordx4 v[244:247],v248,s[64:67],s62 offen
ds_read_b128 a[128:131],v206
R_ a[132:135],v206,1024
R_ a[136:139],v206,2048
R_ a[140:143],v206,3072
R_ a[144:147],v206,4096
R_ a[148:151],v206,5120
R_ a[152:155],v206,6144
R_ a[156:159],v206,7168
R_ a[160:163],v206,8192
R_ a[164:167],v206,9216
R_ a[168:171],v206,10240
R_ a[172:175],v206,11264
R_ a[176:179],v206,12288
R_ a[180:183],v206,13312
R_ a[184:187],v206,14336
R_ a[188:191],v206,15360
s_and_b32 s78,s22,15
s_lshr_b64 s[22:23],s[22:23],4
s_lshl_b32 s54,s79,28
s_or_b32 s23,s23,s54
s_lshr_b32 s79,s79,4
s_waitcnt lgkmcnt(0)
s_cmp_eq_u32 s78,3
s_cselect_b32 s54,1,0
s_cmp_eq_u32 s78,4
s_cselect_b32 s54,1,s54
s_cmp_lg_u32 s54,0
s_cselect_b64 s[28:29],s[46:47],-1
s_cselect_b64 s[30:31],s[48:49],-1
s_cselect_b64 s[32:33],s[50:51],-1
s_cselect_b64 s[34:35],s[52:53],-1
M_ v[144:147],a[128:131],a[0:3],0
M_ v[144:147],a[132:135],a[4:7],v[144:147]
M_ v[144:147],a[136:139],a[8:11],v[144:147]
M_ v[144:147],a[140:143],a[12:15],v[144:147]
M_ v[148:151],a[128:131],a[16:19],0
M_ v[148:151],a[132:135],a[20:23],v[148:151]
M_ v[148:151],a[136:139],a[24:27],v[148:151]
M_ v[148:151],a[140:143],a[28:31],v[148:151]
M_ v[152:155],a[128:131],a[32:35],0
M_ v[152:155],a[132:135],a[36:39],v[152:155]
M_ v[152:155],a[136:139],a[40:43],v[152:155]
M_ v[152:155],a[140:143],a[44:47],v[152:155]
M_ v[156:159],a[128:131],a[48:51],0
M_ v[156:159],a[132:135],a[52:55],v[156:159]
M_ v[156:159],a[136:139],a[56:59],v[156:159]
M_ v[156:159],a[140:143],a[60:63],v[156:159]
s_nop 7
v_cndmask_b32 v144,v209,v144,s[28:29]
v_cndmask_b32 v145,v209,v145,s[30:31]
v_cndmask_b32 v146,v209,v146,s[32:33]
v_cndmask_b32 v147,v209,v147,s[34:35]
v_max3_f32 v210,v144,v145,v146
v_max3_f32 v211,v148,v149,v150
v_max3_f32 v212,v152,v153,v154
v_max3_f32 v213,v156,v157,v158
v_max_f32 v210,v210,v147
)A"
R"A(v_max_f32 v211,v211,v151
v_max_f32 v212,v212,v155
v_max_f32 v213,v213,v159
v_mov_b32 v214,v210
v_mov_b32 v215,v211
v_mov_b32 v216,v212
v_mov_b32 v217,v213
v_permlane32_swap_b32 v210,v214
v_permlane32_swap_b32 v211,v215
v_permlane32_swap_b32 v212,v216
v_permlane32_swap_b32 v213,v217
v_max_f32 v210,v210,v214
v_max_f32 v211,v211,v215
v_max_f32 v212,v212,v216
v_max_f32 v213,v213,v217
v_mov_b32 v214,v210
v_mov_b32 v215,v211
v_mov_b32 v216,v212
v_mov_b32 v217,v213
v_permlane16_swap_b32 v210,v214
v_permlane16_swap_b32 v211,v215
v_permlane16_swap_b32 v212,v216
v_permlane16_swap_b32 v213,v217
v_max_f32 v210,v210,v214
v_max_f32 v211,v211,v215
v_max_f32 v212,v212,v216
v_max_f32 v213,v213,v217
v_sub_f32 v184,0,v210
v_sub_f32 v185,0,v210
v_sub_f32 v186,0,v210
v_sub_f32 v187,0,v210
v_sub_f32 v188,0,v211
v_sub_f32 v189,0,v211
v_sub_f32 v190,0,v211
v_sub_f32 v191,0,v211
v_sub_f32 v192,0,v212
v_sub_f32 v193,0,v212
v_sub_f32 v194,0,v212
v_sub_f32 v195,0,v212
v_sub_f32 v196,0,v213
v_sub_f32 v197,0,v213
v_sub_f32 v198,0,v213
v_sub_f32 v199,0,v213
M_ v[128:131],a[128:131],a[0:3],v[184:187]
M_ v[128:131],a[132:135],a[4:7],v[128:131]
M_ v[128:131],a[136:139],a[8:11],v[128:131]
M_ v[128:131],a[140:143],a[12:15],v[128:131]
M_ v[132:135],a[144:147],a[0:3],v[184:187]
M_ v[132:135],a[148:151],a[4:7],v[132:135]
M_ v[132:135],a[152:155],a[8:11],v[132:135]
M_ v[132:135],a[156:159],a[12:15],v[132:135]
LDISP_%=:
s_cmp_eq_u32 s78,1
s_cbranch_scc1 LVF2_%=
s_cmp_eq_u32 s78,0
s_cbranch_scc1 LVF1_%=
s_cmp_eq_u32 s78,2
s_cbranch_scc1 LVF0_%=
s_cmp_eq_u32 s78,3
s_cbranch_scc1 LVD2_%=
s_cmp_eq_u32 s78,4
s_cbranch_scc1 LVD0_%=
s_cmp_eq_u32 s78,5
s_cbranch_scc1 LVNS2_%=
s_cmp_eq_u32 s78,6
s_cbranch_scc1 LVNS0_%=
s_cmp_eq_u32 s78,7
s_cbranch_scc1 LVN0_%=
s_cmp_eq_u32 s78,8
s_cbranch_scc1 LVSW_%=
s_branch LVEND_%=
LVF2_%=:
s_waitcnt lgkmcnt(0)
M_ v[136:139],a[160:163],a[0:3],v[184:187]
s_nop 1
E_ v176,v128
M_ v[136:139],a[164:167],a[4:7],v[136:139]
M_ v[136:139],a[168:171],a[8:11],v[136:139]
E_ v177,v129
M_ v[136:139],a[172:175],a[12:15],v[136:139]
v_add_f32 v204,v176,v177
C_ v160,v176,v177
s_cbranch_vccnz LRF2_3_%=
LBF2_3_%=:
v_add_f32 v203,v203,v205
M_ v[140:143],a[176:179],a[0:3],v[184:187]
E_ v178,v130
M_ v[140:143],a[180:183],a[4:7],v[140:143]
v_add_f32 v204,v204,v178
M_ v[140:143],a[184:187],a[8:11],v[140:143]
E_ v179,v131
M_ v[140:143],a[188:191],a[12:15],v[140:143]
v_add_f32 v204,v204,v179
C_ v161,v178,v179
M_ v[96:99],a[64:67],v[168:171],v[96:99]
s_and_b32 s56,s21,1
E_ v180,v132
M_ v[96:99],a[96:99],v[172:175],v[96:99]
ds_read_b128 a[64:67],v207
v_add_f32 v204,v204,v180
M_ v[100:103],a[68:71],v[168:171],v[100:103]
s_mul_i32 s57,s56,0xfffe4000
E_ v181,v133
M_ v[100:103],a[100:103],v[172:175],v[100:103]
R_ a[96:99],v207,8192
v_add_f32 v204,v204,v181
C_ v162,v180,v181
M_ v[104:107],a[72:75],v[168:171],v[104:107]
s_add_u32 s57,s57,0x1c000
E_ v182,v134
M_ v[104:107],a[104:107],v[172:175],v[104:107]
R_ a[68:71],v207,1024
v_add_f32 v204,v204,v182
M_ v[108:111],a[76:79],v[168:171],v[108:111]
s_add_u32 s60,s57,s75
E_ v183,v135
M_ v[108:111],a[108:111],v[172:175],v[108:111]
R_ a[100:103],v207,9216
v_add_f32 v204,v204,v183
C_ v163,v182,v183
M_ v[112:115],a[80:83],v[168:171],v[112:115]
s_add_u32 s61,s60,0x4000
E_ v176,v136
M_ v[112:115],a[112:115],v[172:175],v[112:115]
R_ a[72:75],v207,2048
v_add_f32 v204,v204,v176
M_ v[116:119],a[84:87],v[168:171],v[116:119]
s_add_u32 s64,s24,s75
E_ v177,v137
M_ v[116:119],a[116:119],v[172:175],v[116:119]
R_ a[104:107],v207,10240
v_add_f32 v204,v204,v177
C_ v164,v176,v177
M_ v[120:123],a[88:91],v[168:171],v[120:123]
s_add_u32 s65,s64,0x4000
E_ v178,v138
M_ v[120:123],a[120:123],v[172:175],v[120:123]
R_ a[76:79],v207,3072
v_add_f32 v204,v204,v178
M_ v[124:127],a[92:95],v[168:171],v[124:127]
s_mul_i32 s57,s56,0x8000
E_ v179,v139
M_ v[124:127],a[124:127],v[172:175],v[124:127]
R_ a[108:111],v207,11264
v_add_f32 v204,v204,v179
C_ v165,v178,v179
M_ v[144:147],a[128:131],a[16:19],v[188:191]
s_add_u32 s57,s57,0x10000
E_ v180,v140
M_ v[144:147],a[132:135],a[20:23],v[144:147]
R_ a[80:83],v207,4096
v_add_f32 v204,v204,v180
M_ v[144:147],a[136:139],a[24:27],v[144:147]
s_add_u32 s62,s57,s75
E_ v181,v141
M_ v[144:147],a[140:143],a[28:31],v[144:147]
R_ a[112:115],v207,12288
v_add_f32 v204,v204,v181
C_ v166,v180,v181
M_ v[148:151],a[144:147],a[16:19],v[188:191]
s_mul_i32 s57,s56,0x10000
E_ v182,v142
M_ v[148:151],a[148:151],a[20:23],v[148:151]
R_ a[84:87],v207,5120
v_add_f32 v204,v204,v182
M_ v[148:151],a[152:155],a[24:27],v[148:151]
s_add_u32 s57,s57,0x14000
E_ v183,v143
M_ v[148:151],a[156:159],a[28:31],v[148:151]
R_ a[116:119],v207,13312
v_add_f32 v204,v204,v183
C_ v167,v182,v183
M_ v[152:155],a[160:163],a[16:19],v[188:191]
s_add_u32 s63,s57,s75
E_ v176,v144
M_ v[152:155],a[164:167],a[20:23],v[152:155]
R_ a[88:91],v207,6144
v_cmp_lt_f32 vcc,s27,v204
M_ v[152:155],a[168:171],a[24:27],v[152:155]
s_add_u32 s66,s64,0x8000
E_ v177,v145
M_ v[152:155],a[172:175],a[28:31],v[152:155]
R_ a[120:123],v207,14336
v_add_f32 v205,v176,v177
C_ v168,v176,v177
M_ v[156:159],a[176:179],a[16:19],v[188:191]
s_add_u32 s67,s64,0xc000
E_ v178,v146
s_cbranch_vccnz LRF2_0_%=
LBF2_0_%=:
v_add_f32 v200,v200,v204
M_ v[156:159],a[180:183],a[20:23],v[156:159]
R_ a[92:95],v207,7168
v_add_f32 v205,v205,v178
M_ v[156:159],a[184:187],a[24:27],v[156:159]
s_add_u32 s57,s21,3
E_ v179,v147
M_ v[156:159],a[188:191],a[28:31],v[156:159]
R_ a[124:127],v207,15360
v_add_f32 v205,v205,v179
C_ v169,v178,v179
s_waitcnt lgkmcnt(8)
M_ v[0:3],a[64:67],v[160:163],v[0:3]
s_cmp_gt_u32 s57,s20
E_ v180,v148
M_ v[0:3],a[96:99],v[164:167],v[0:3]
v_add_f32 v205,v205,v180
M_ v[4:7],a[68:71],v[160:163],v[4:7]
s_cselect_b32 s57,2,1
E_ v181,v149
M_ v[4:7],a[100:103],v[164:167],v[4:7]
v_add_f32 v205,v205,v181
C_ v170,v180,v181
M_ v[8:11],a[72:75],v[160:163],v[8:11]
s_lshl_b32 s57,s57,15
E_ v182,v150
M_ v[8:11],a[104:107],v[164:167],v[8:11]
v_add_f32 v205,v205,v182
M_ v[12:15],a[76:79],v[160:163],v[12:15]
s_add_u32 s24,s24,s57
E_ v183,v151
M_ v[12:15],a[108:111],v[164:167],v[12:15]
v_add_f32 v205,v205,v183
C_ v171,v182,v183
s_waitcnt vmcnt(0) lgkmcnt(0)
s_barrier
M_ v[16:19],a[80:83],v[160:163],v[16:19]
E_ v176,v152
M_ v[16:19],a[112:115],v[164:167],v[16:19]
s_mov_b32 m0,s60
v_add_f32 v205,v205,v176
M_ v[20:23],a[84:87],v[160:163],v[20:23]
buffer_load_dwordx4 v208,s[12:15],s64 offen sc1 lds
E_ v177,v153
M_ v[20:23],a[116:119],v[164:167],v[20:23]
s_add_u32 s54,s21,1
v_add_f32 v205,v205,v177
C_ v172,v176,v177
M_ v[24:27],a[88:91],v[160:163],v[24:27]
E_ v178,v154
M_ v[24:27],a[120:123],v[164:167],v[24:27]
s_cmp_ge_u32 s54,s20
v_add_f32 v205,v205,v178
M_ v[28:31],a[92:95],v[160:163],v[28:31]
buffer_load_dwordx4 v208,s[12:15],s64 offen offset:1024 sc1 lds
E_ v179,v155
M_ v[28:31],a[124:127],v[164:167],v[28:31]
s_cselect_b32 s55,s73,0
v_add_f32 v205,v205,v179
C_ v173,v178,v179
M_ v[128:131],a[128:131],a[32:35],v[192:195]
E_ v180,v156
M_ v[128:131],a[132:135],a[36:39],v[128:131]
s_and_b32 s56,s54,1
v_add_f32 v205,v205,v180
M_ v[128:131],a[136:139],a[40:43],v[128:131]
E_ v181,v157
M_ v[128:131],a[140:143],a[44:47],v[128:131]
s_mul_i32 s57,s56,0xfffe4000
v_add_f32 v205,v205,v181
C_ v174,v180,v181
M_ v[132:135],a[144:147],a[32:35],v[192:195]
buffer_load_dwordx4 v208,s[12:15],s64 offen offset:2048 sc1 lds
E_ v182,v158
M_ v[132:135],a[148:151],a[36:39],v[132:135]
s_add_u32 s57,s57,0x1c000
v_add_f32 v205,v205,v182
M_ v[132:135],a[152:155],a[40:43],v[132:135]
E_ v183,v159
M_ v[132:135],a[156:159],a[44:47],v[132:135]
s_mul_i32 s58,s56,0x8000
v_add_f32 v205,v205,v183
C_ v175,v182,v183
M_ v[136:139],a[160:163],a[32:35],v[192:195]
buffer_load_dwordx4 v208,s[12:15],s64 offen offset:3072 sc1 lds
E_ v176,v128
M_ v[136:139],a[164:167],a[36:39],v[136:139]
s_add_u32 s58,s58,0x10000
v_cmp_lt_f32 vcc,s27,v205
M_ v[136:139],a[168:171],a[40:43],v[136:139]
E_ v177,v129
M_ v[136:139],a[172:175],a[44:47],v[136:139]
s_mov_b32 m0,s61
s_cmp_lg_u32 s55,0
v_add_f32 v204,v176,v177
C_ v160,v176,v177
M_ v[140:143],a[176:179],a[32:35],v[192:195]
buffer_load_dwordx4 v208,s[12:15],s65 offen sc1 lds
E_ v178,v130
s_cbranch_vccnz LRF2_1_%=
LBF2_1_%=:
v_add_f32 v201,v201,v205
M_ v[140:143],a[180:183],a[36:39],v[140:143]
s_cselect_b32 s25,s58,s57
v_add_f32 v204,v204,v178
M_ v[140:143],a[184:187],a[40:43],v[140:143]
E_ v179,v131
M_ v[140:143],a[188:191],a[44:47],v[140:143]
s_add_u32 s57,s57,0x4000
v_add_f32 v204,v204,v179
C_ v161,v178,v179
M_ v[32:35],a[64:67],v[168:171],v[32:35]
buffer_load_dwordx4 v208,s[12:15],s65 offen offset:1024 sc1 lds
E_ v180,v132
M_ v[32:35],a[96:99],v[172:175],v[32:35]
s_mul_i32 s58,s56,0x10000
v_add_f32 v204,v204,v180
M_ v[36:39],a[68:71],v[168:171],v[36:39]
E_ v181,v133
M_ v[36:39],a[100:103],v[172:175],v[36:39]
s_add_u32 s58,s58,0x14000
v_add_f32 v204,v204,v181
C_ v162,v180,v181
M_ v[40:43],a[72:75],v[168:171],v[40:43]
E_ v182,v134
M_ v[40:43],a[104:107],v[172:175],v[40:43]
s_cmp_lg_u32 s55,0
v_add_f32 v204,v204,v182
M_ v[44:47],a[76:79],v[168:171],v[44:47]
buffer_load_dwordx4 v208,s[12:15],s65 offen offset:2048 sc1 lds
E_ v183,v135
M_ v[44:47],a[108:111],v[172:175],v[44:47]
s_cselect_b32 s26,s58,s57
v_add_f32 v204,v204,v183
C_ v163,v182,v183
M_ v[48:51],a[80:83],v[168:171],v[48:51]
E_ v176,v136
M_ v[48:51],a[112:115],v[172:175],v[48:51]
v_add_f32 v204,v204,v176
M_ v[52:55],a[84:87],v[168:171],v[52:55]
buffer_load_dwordx4 v208,s[12:15],s65 offen offset:3072 sc1 lds
E_ v177,v137
M_ v[52:55],a[116:119],v[172:175],v[52:55]
v_add_f32 v204,v204,v177
C_ v164,v176,v177
v_add_u32 v206,s25,v208
M_ v[56:59],a[88:91],v[168:171],v[56:59]
E_ v178,v138
M_ v[56:59],a[120:123],v[172:175],v[56:59]
s_mov_b32 m0,s62
v_add_f32 v204,v204,v178
v_add_u32 v207,s26,v208
M_ v[60:63],a[92:95],v[168:171],v[60:63]
buffer_load_dwordx4 v208,s[12:15],s66 offen sc1 lds
E_ v179,v139
M_ v[60:63],a[124:127],v[172:175],v[60:63]
v_add_f32 v204,v204,v179
C_ v165,v178,v179
M_ v[144:147],a[128:131],a[48:51],v[196:199]
E_ v180,v140
M_ v[144:147],a[132:135],a[52:55],v[144:147]
ds_read_b128 a[128:131],v206
v_add_f32 v204,v204,v180
M_ v[144:147],a[136:139],a[56:59],v[144:147]
buffer_load_dwordx4 v208,s[12:15],s66 offen offset:1024 sc1 lds
E_ v181,v141
M_ v[144:147],a[140:143],a[60:63],v[144:147]
R_ a[132:135],v206,1024
v_add_f32 v204,v204,v181
C_ v166,v180,v181
M_ v[148:151],a[144:147],a[48:51],v[196:199]
E_ v182,v142
M_ v[148:151],a[148:151],a[52:55],v[148:151]
R_ a[136:139],v206,2048
v_add_f32 v204,v204,v182
M_ v[148:151],a[152:155],a[56:59],v[148:151]
E_ v183,v143
M_ v[148:151],a[156:159],a[60:63],v[148:151]
R_ a[140:143],v206,3072
v_add_f32 v204,v204,v183
C_ v167,v182,v183
M_ v[152:155],a[160:163],a[48:51],v[196:199]
buffer_load_dwordx4 v208,s[12:15],s66 offen offset:2048 sc1 lds
E_ v176,v144
M_ v[152:155],a[164:167],a[52:55],v[152:155]
R_ a[144:147],v206,4096
v_cmp_lt_f32 vcc,s27,v204
M_ v[152:155],a[168:171],a[56:59],v[152:155]
E_ v177,v145
M_ v[152:155],a[172:175],a[60:63],v[152:155]
R_ a[148:151],v206,5120
v_add_f32 v205,v176,v177
C_ v168,v176,v177
M_ v[156:159],a[176:179],a[48:51],v[196:199]
buffer_load_dwordx4 v208,s[12:15],s66 offen offset:3072 sc1 lds
E_ v178,v146
s_cbranch_vccnz LRF2_2_%=
LBF2_2_%=:
v_add_f32 v202,v202,v204
M_ v[156:159],a[180:183],a[52:55],v[156:159]
R_ a[152:155],v206,6144
v_add_f32 v205,v205,v178
M_ v[156:159],a[184:187],a[56:59],v[156:159]
E_ v179,v147
M_ v[156:159],a[188:191],a[60:63],v[156:159]
s_mov_b32 m0,s63
R_ a[156:159],v206,7168
v_add_f32 v205,v205,v179
C_ v169,v178,v179
M_ v[64:67],a[64:67],v[160:163],v[64:67]
buffer_load_dwordx4 v208,s[12:15],s67 offen sc1 lds
E_ v180,v148
M_ v[64:67],a[96:99],v[164:167],v[64:67]
R_ a[160:163],v206,8192
v_add_f32 v205,v205,v180
M_ v[68:71],a[68:71],v[160:163],v[68:71]
E_ v181,v149
M_ v[68:71],a[100:103],v[164:167],v[68:71]
R_ a[164:167],v206,9216
v_add_f32 v205,v205,v181
C_ v170,v180,v181
M_ v[72:75],a[72:75],v[160:163],v[72:75]
s_and_b32 s78,s22,15
buffer_load_dwordx4 v208,s[12:15],s67 offen offset:1024 sc1 lds
E_ v182,v150
M_ v[72:75],a[104:107],v[164:167],v[72:75]
R_ a[168:171],v206,10240
v_add_f32 v205,v205,v182
M_ v[76:79],a[76:79],v[160:163],v[76:79]
s_lshr_b64 s[22:23],s[22:23],4
E_ v183,v151
M_ v[76:79],a[108:111],v[164:167],v[76:79]
R_ a[172:175],v206,11264
v_add_f32 v205,v205,v183
C_ v171,v182,v183
M_ v[80:83],a[80:83],v[160:163],v[80:83]
s_lshl_b32 s54,s79,28
E_ v176,v152
M_ v[80:83],a[112:115],v[164:167],v[80:83]
R_ a[176:179],v206,12288
v_add_f32 v205,v205,v176
M_ v[84:87],a[84:87],v[160:163],v[84:87]
s_or_b32 s23,s23,s54
buffer_load_dwordx4 v208,s[12:15],s67 offen offset:2048 sc1 lds
E_ v177,v153
M_ v[84:87],a[116:119],v[164:167],v[84:87]
R_ a[180:183],v206,13312
v_add_f32 v205,v205,v177
C_ v172,v176,v177
M_ v[88:91],a[88:91],v[160:163],v[88:91]
s_lshr_b32 s79,s79,4
E_ v178,v154
M_ v[88:91],a[120:123],v[164:167],v[88:91]
R_ a[184:187],v206,14336
v_add_f32 v205,v205,v178
M_ v[92:95],a[92:95],v[160:163],v[92:95]
buffer_load_dwordx4 v208,s[12:15],s67 offen offset:3072 sc1 lds
E_ v179,v155
M_ v[92:95],a[124:127],v[164:167],v[92:95]
R_ a[188:191],v206,15360
v_add_f32 v205,v205,v179
C_ v173,v178,v179
s_waitcnt lgkmcnt(8)
M_ v[128:131],a[128:131],a[0:3],v[184:187]
s_add_u32 s21,s21,1
E_ v180,v156
M_ v[128:131],a[132:135],a[4:7],v[128:131]
v_add_f32 v205,v205,v180
M_ v[128:131],a[136:139],a[8:11],v[128:131]
E_ v181,v157
M_ v[128:131],a[140:143],a[12:15],v[128:131]
v_add_f32 v205,v205,v181
C_ v174,v180,v181
M_ v[132:135],a[144:147],a[0:3],v[184:187]
E_ v182,v158
M_ v[132:135],a[148:151],a[4:7],v[132:135]
v_add_f32 v205,v205,v182
M_ v[132:135],a[152:155],a[8:11],v[132:135]
E_ v183,v159
M_ v[132:135],a[156:159],a[12:15],v[132:135]
v_add_f32 v205,v205,v183
C_ v175,v182,v183
v_cmp_lt_f32 vcc,s27,v205
s_cmp_eq_u32 s78,1
s_cbranch_scc1 LVF2_%=
s_branch LDISP_%=
LVF1_%=:
s_waitcnt lgkmcnt(0)
M_ v[136:139],a[160:163],a[0:3],v[184:187]
s_nop 1
E_ v176,v128
M_ v[136:139],a[164:167],a[4:7],v[136:139]
M_ v[136:139],a[168:171],a[8:11],v[136:139]
E_ v177,v129
M_ v[136:139],a[172:175],a[12:15],v[136:139]
v_add_f32 v204,v176,v177
C_ v160,v176,v177
s_cbranch_vccnz LRF1_3_%=
LBF1_3_%=:
v_add_f32 v203,v203,v205
)A"
R"A(M_ v[140:143],a[176:179],a[0:3],v[184:187]
E_ v178,v130
M_ v[140:143],a[180:183],a[4:7],v[140:143]
v_add_f32 v204,v204,v178
M_ v[140:143],a[184:187],a[8:11],v[140:143]
E_ v179,v131
M_ v[140:143],a[188:191],a[12:15],v[140:143]
v_add_f32 v204,v204,v179
C_ v161,v178,v179
M_ v[96:99],a[64:67],v[168:171],v[96:99]
s_and_b32 s56,s21,1
E_ v180,v132
M_ v[96:99],a[96:99],v[172:175],v[96:99]
ds_read_b128 a[64:67],v207
v_add_f32 v204,v204,v180
M_ v[100:103],a[68:71],v[168:171],v[100:103]
s_mul_i32 s57,s56,0xfffe4000
E_ v181,v133
M_ v[100:103],a[100:103],v[172:175],v[100:103]
R_ a[96:99],v207,8192
v_add_f32 v204,v204,v181
C_ v162,v180,v181
M_ v[104:107],a[72:75],v[168:171],v[104:107]
s_add_u32 s57,s57,0x1c000
E_ v182,v134
M_ v[104:107],a[104:107],v[172:175],v[104:107]
R_ a[68:71],v207,1024
v_add_f32 v204,v204,v182
M_ v[108:111],a[76:79],v[168:171],v[108:111]
s_add_u32 s60,s57,s75
E_ v183,v135
M_ v[108:111],a[108:111],v[172:175],v[108:111]
R_ a[100:103],v207,9216
v_add_f32 v204,v204,v183
C_ v163,v182,v183
M_ v[112:115],a[80:83],v[168:171],v[112:115]
s_add_u32 s61,s60,0x4000
E_ v176,v136
M_ v[112:115],a[112:115],v[172:175],v[112:115]
R_ a[72:75],v207,2048
v_add_f32 v204,v204,v176
M_ v[116:119],a[84:87],v[168:171],v[116:119]
s_add_u32 s64,s24,s75
E_ v177,v137
M_ v[116:119],a[116:119],v[172:175],v[116:119]
R_ a[104:107],v207,10240
v_add_f32 v204,v204,v177
C_ v164,v176,v177
M_ v[120:123],a[88:91],v[168:171],v[120:123]
s_add_u32 s65,s64,0x4000
E_ v178,v138
M_ v[120:123],a[120:123],v[172:175],v[120:123]
R_ a[76:79],v207,3072
v_add_f32 v204,v204,v178
M_ v[124:127],a[92:95],v[168:171],v[124:127]
s_add_u32 s57,s21,3
E_ v179,v139
M_ v[124:127],a[124:127],v[172:175],v[124:127]
R_ a[108:111],v207,11264
v_add_f32 v204,v204,v179
C_ v165,v178,v179
M_ v[144:147],a[128:131],a[16:19],v[188:191]
s_cmp_gt_u32 s57,s20
E_ v180,v140
M_ v[144:147],a[132:135],a[20:23],v[144:147]
R_ a[80:83],v207,4096
v_add_f32 v204,v204,v180
M_ v[144:147],a[136:139],a[24:27],v[144:147]
s_cselect_b32 s57,2,1
E_ v181,v141
M_ v[144:147],a[140:143],a[28:31],v[144:147]
R_ a[112:115],v207,12288
v_add_f32 v204,v204,v181
C_ v166,v180,v181
M_ v[148:151],a[144:147],a[16:19],v[188:191]
s_lshl_b32 s57,s57,15
E_ v182,v142
M_ v[148:151],a[148:151],a[20:23],v[148:151]
R_ a[84:87],v207,5120
v_add_f32 v204,v204,v182
M_ v[148:151],a[152:155],a[24:27],v[148:151]
s_add_u32 s24,s24,s57
E_ v183,v143
M_ v[148:151],a[156:159],a[28:31],v[148:151]
R_ a[116:119],v207,13312
v_add_f32 v204,v204,v183
C_ v167,v182,v183
M_ v[152:155],a[160:163],a[16:19],v[188:191]
E_ v176,v144
M_ v[152:155],a[164:167],a[20:23],v[152:155]
R_ a[88:91],v207,6144
v_cmp_lt_f32 vcc,s27,v204
M_ v[152:155],a[168:171],a[24:27],v[152:155]
E_ v177,v145
M_ v[152:155],a[172:175],a[28:31],v[152:155]
R_ a[120:123],v207,14336
v_add_f32 v205,v176,v177
C_ v168,v176,v177
M_ v[156:159],a[176:179],a[16:19],v[188:191]
E_ v178,v146
s_cbranch_vccnz LRF1_0_%=
LBF1_0_%=:
v_add_f32 v200,v200,v204
M_ v[156:159],a[180:183],a[20:23],v[156:159]
R_ a[92:95],v207,7168
v_add_f32 v205,v205,v178
M_ v[156:159],a[184:187],a[24:27],v[156:159]
E_ v179,v147
M_ v[156:159],a[188:191],a[28:31],v[156:159]
R_ a[124:127],v207,15360
v_add_f32 v205,v205,v179
C_ v169,v178,v179
s_waitcnt lgkmcnt(8)
M_ v[0:3],a[64:67],v[160:163],v[0:3]
E_ v180,v148
M_ v[0:3],a[96:99],v[164:167],v[0:3]
v_add_f32 v205,v205,v180
M_ v[4:7],a[68:71],v[160:163],v[4:7]
E_ v181,v149
M_ v[4:7],a[100:103],v[164:167],v[4:7]
v_add_f32 v205,v205,v181
C_ v170,v180,v181
M_ v[8:11],a[72:75],v[160:163],v[8:11]
E_ v182,v150
M_ v[8:11],a[104:107],v[164:167],v[8:11]
v_add_f32 v205,v205,v182
M_ v[12:15],a[76:79],v[160:163],v[12:15]
E_ v183,v151
M_ v[12:15],a[108:111],v[164:167],v[12:15]
v_add_f32 v205,v205,v183
C_ v171,v182,v183
s_waitcnt vmcnt(0) lgkmcnt(0)
s_barrier
M_ v[16:19],a[80:83],v[160:163],v[16:19]
E_ v176,v152
M_ v[16:19],a[112:115],v[164:167],v[16:19]
s_mov_b32 m0,s60
v_add_f32 v205,v205,v176
M_ v[20:23],a[84:87],v[160:163],v[20:23]
buffer_load_dwordx4 v208,s[12:15],s64 offen sc1 lds
E_ v177,v153
M_ v[20:23],a[116:119],v[164:167],v[20:23]
s_add_u32 s54,s21,1
v_add_f32 v205,v205,v177
C_ v172,v176,v177
M_ v[24:27],a[88:91],v[160:163],v[24:27]
E_ v178,v154
M_ v[24:27],a[120:123],v[164:167],v[24:27]
s_cmp_ge_u32 s54,s20
v_add_f32 v205,v205,v178
M_ v[28:31],a[92:95],v[160:163],v[28:31]
E_ v179,v155
M_ v[28:31],a[124:127],v[164:167],v[28:31]
s_cselect_b32 s55,s73,0
v_add_f32 v205,v205,v179
C_ v173,v178,v179
M_ v[128:131],a[128:131],a[32:35],v[192:195]
E_ v180,v156
M_ v[128:131],a[132:135],a[36:39],v[128:131]
s_and_b32 s56,s54,1
v_add_f32 v205,v205,v180
M_ v[128:131],a[136:139],a[40:43],v[128:131]
E_ v181,v157
M_ v[128:131],a[140:143],a[44:47],v[128:131]
s_mul_i32 s57,s56,0xfffe4000
v_add_f32 v205,v205,v181
C_ v174,v180,v181
M_ v[132:135],a[144:147],a[32:35],v[192:195]
buffer_load_dwordx4 v208,s[12:15],s64 offen offset:1024 sc1 lds
E_ v182,v158
M_ v[132:135],a[148:151],a[36:39],v[132:135]
s_add_u32 s57,s57,0x1c000
v_add_f32 v205,v205,v182
M_ v[132:135],a[152:155],a[40:43],v[132:135]
E_ v183,v159
M_ v[132:135],a[156:159],a[44:47],v[132:135]
s_mul_i32 s58,s56,0x8000
v_add_f32 v205,v205,v183
C_ v175,v182,v183
M_ v[136:139],a[160:163],a[32:35],v[192:195]
E_ v176,v128
M_ v[136:139],a[164:167],a[36:39],v[136:139]
s_add_u32 s58,s58,0x10000
v_cmp_lt_f32 vcc,s27,v205
M_ v[136:139],a[168:171],a[40:43],v[136:139]
E_ v177,v129
M_ v[136:139],a[172:175],a[44:47],v[136:139]
s_cmp_lg_u32 s55,0
v_add_f32 v204,v176,v177
C_ v160,v176,v177
M_ v[140:143],a[176:179],a[32:35],v[192:195]
E_ v178,v130
s_cbranch_vccnz LRF1_1_%=
LBF1_1_%=:
v_add_f32 v201,v201,v205
M_ v[140:143],a[180:183],a[36:39],v[140:143]
s_cselect_b32 s25,s58,s57
v_add_f32 v204,v204,v178
M_ v[140:143],a[184:187],a[40:43],v[140:143]
buffer_load_dwordx4 v208,s[12:15],s64 offen offset:2048 sc1 lds
E_ v179,v131
M_ v[140:143],a[188:191],a[44:47],v[140:143]
s_add_u32 s57,s57,0x4000
v_add_f32 v204,v204,v179
C_ v161,v178,v179
M_ v[32:35],a[64:67],v[168:171],v[32:35]
E_ v180,v132
M_ v[32:35],a[96:99],v[172:175],v[32:35]
s_mul_i32 s58,s56,0x10000
v_add_f32 v204,v204,v180
M_ v[36:39],a[68:71],v[168:171],v[36:39]
E_ v181,v133
M_ v[36:39],a[100:103],v[172:175],v[36:39]
s_add_u32 s58,s58,0x14000
v_add_f32 v204,v204,v181
C_ v162,v180,v181
M_ v[40:43],a[72:75],v[168:171],v[40:43]
E_ v182,v134
M_ v[40:43],a[104:107],v[172:175],v[40:43]
s_cmp_lg_u32 s55,0
v_add_f32 v204,v204,v182
M_ v[44:47],a[76:79],v[168:171],v[44:47]
E_ v183,v135
M_ v[44:47],a[108:111],v[172:175],v[44:47]
s_cselect_b32 s26,s58,s57
v_add_f32 v204,v204,v183
C_ v163,v182,v183
M_ v[48:51],a[80:83],v[168:171],v[48:51]
buffer_load_dwordx4 v208,s[12:15],s64 offen offset:3072 sc1 lds
E_ v176,v136
M_ v[48:51],a[112:115],v[172:175],v[48:51]
v_add_f32 v204,v204,v176
M_ v[52:55],a[84:87],v[168:171],v[52:55]
E_ v177,v137
M_ v[52:55],a[116:119],v[172:175],v[52:55]
v_add_f32 v204,v204,v177
C_ v164,v176,v177
v_add_u32 v206,s25,v208
M_ v[56:59],a[88:91],v[168:171],v[56:59]
E_ v178,v138
M_ v[56:59],a[120:123],v[172:175],v[56:59]
v_add_f32 v204,v204,v178
v_add_u32 v207,s26,v208
M_ v[60:63],a[92:95],v[168:171],v[60:63]
E_ v179,v139
M_ v[60:63],a[124:127],v[172:175],v[60:63]
s_mov_b32 m0,s61
v_add_f32 v204,v204,v179
C_ v165,v178,v179
M_ v[144:147],a[128:131],a[48:51],v[196:199]
buffer_load_dwordx4 v208,s[12:15],s65 offen sc1 lds
E_ v180,v140
M_ v[144:147],a[132:135],a[52:55],v[144:147]
ds_read_b128 a[128:131],v206
v_add_f32 v204,v204,v180
M_ v[144:147],a[136:139],a[56:59],v[144:147]
E_ v181,v141
M_ v[144:147],a[140:143],a[60:63],v[144:147]
R_ a[132:135],v206,1024
v_add_f32 v204,v204,v181
C_ v166,v180,v181
M_ v[148:151],a[144:147],a[48:51],v[196:199]
E_ v182,v142
M_ v[148:151],a[148:151],a[52:55],v[148:151]
R_ a[136:139],v206,2048
v_add_f32 v204,v204,v182
M_ v[148:151],a[152:155],a[56:59],v[148:151]
E_ v183,v143
M_ v[148:151],a[156:159],a[60:63],v[148:151]
R_ a[140:143],v206,3072
v_add_f32 v204,v204,v183
C_ v167,v182,v183
M_ v[152:155],a[160:163],a[48:51],v[196:199]
E_ v176,v144
M_ v[152:155],a[164:167],a[52:55],v[152:155]
R_ a[144:147],v206,4096
v_cmp_lt_f32 vcc,s27,v204
M_ v[152:155],a[168:171],a[56:59],v[152:155]
buffer_load_dwordx4 v208,s[12:15],s65 offen offset:1024 sc1 lds
E_ v177,v145
M_ v[152:155],a[172:175],a[60:63],v[152:155]
R_ a[148:151],v206,5120
v_add_f32 v205,v176,v177
C_ v168,v176,v177
M_ v[156:159],a[176:179],a[48:51],v[196:199]
E_ v178,v146
s_cbranch_vccnz LRF1_2_%=
LBF1_2_%=:
v_add_f32 v202,v202,v204
M_ v[156:159],a[180:183],a[52:55],v[156:159]
R_ a[152:155],v206,6144
v_add_f32 v205,v205,v178
M_ v[156:159],a[184:187],a[56:59],v[156:159]
E_ v179,v147
M_ v[156:159],a[188:191],a[60:63],v[156:159]
R_ a[156:159],v206,7168
v_add_f32 v205,v205,v179
C_ v169,v178,v179
M_ v[64:67],a[64:67],v[160:163],v[64:67]
E_ v180,v148
M_ v[64:67],a[96:99],v[164:167],v[64:67]
R_ a[160:163],v206,8192
v_add_f32 v205,v205,v180
M_ v[68:71],a[68:71],v[160:163],v[68:71]
E_ v181,v149
M_ v[68:71],a[100:103],v[164:167],v[68:71]
R_ a[164:167],v206,9216
v_add_f32 v205,v205,v181
C_ v170,v180,v181
M_ v[72:75],a[72:75],v[160:163],v[72:75]
s_and_b32 s78,s22,15
buffer_load_dwordx4 v208,s[12:15],s65 offen offset:2048 sc1 lds
E_ v182,v150
M_ v[72:75],a[104:107],v[164:167],v[72:75]
R_ a[168:171],v206,10240
v_add_f32 v205,v205,v182
M_ v[76:79],a[76:79],v[160:163],v[76:79]
s_lshr_b64 s[22:23],s[22:23],4
E_ v183,v151
M_ v[76:79],a[108:111],v[164:167],v[76:79]
R_ a[172:175],v206,11264
v_add_f32 v205,v205,v183
C_ v171,v182,v183
M_ v[80:83],a[80:83],v[160:163],v[80:83]
s_lshl_b32 s54,s79,28
E_ v176,v152
M_ v[80:83],a[112:115],v[164:167],v[80:83]
R_ a[176:179],v206,12288
v_add_f32 v205,v205,v176
M_ v[84:87],a[84:87],v[160:163],v[84:87]
s_or_b32 s23,s23,s54
E_ v177,v153
M_ v[84:87],a[116:119],v[164:167],v[84:87]
R_ a[180:183],v206,13312
v_add_f32 v205,v205,v177
C_ v172,v176,v177
M_ v[88:91],a[88:91],v[160:163],v[88:91]
s_lshr_b32 s79,s79,4
E_ v178,v154
M_ v[88:91],a[120:123],v[164:167],v[88:91]
R_ a[184:187],v206,14336
v_add_f32 v205,v205,v178
M_ v[92:95],a[92:95],v[160:163],v[92:95]
buffer_load_dwordx4 v208,s[12:15],s65 offen offset:3072 sc1 lds
E_ v179,v155
M_ v[92:95],a[124:127],v[164:167],v[92:95]
R_ a[188:191],v206,15360
v_add_f32 v205,v205,v179
C_ v173,v178,v179
s_waitcnt lgkmcnt(8)
M_ v[128:131],a[128:131],a[0:3],v[184:187]
s_add_u32 s21,s21,1
E_ v180,v156
M_ v[128:131],a[132:135],a[4:7],v[128:131]
v_add_f32 v205,v205,v180
M_ v[128:131],a[136:139],a[8:11],v[128:131]
E_ v181,v157
M_ v[128:131],a[140:143],a[12:15],v[128:131]
v_add_f32 v205,v205,v181
C_ v174,v180,v181
M_ v[132:135],a[144:147],a[0:3],v[184:187]
E_ v182,v158
M_ v[132:135],a[148:151],a[4:7],v[132:135]
v_add_f32 v205,v205,v182
M_ v[132:135],a[152:155],a[8:11],v[132:135]
E_ v183,v159
M_ v[132:135],a[156:159],a[12:15],v[132:135]
v_add_f32 v205,v205,v183
C_ v175,v182,v183
v_cmp_lt_f32 vcc,s27,v205
s_cmp_eq_u32 s78,0
s_cbranch_scc1 LVF1_%=
s_branch LDISP_%=
LVF0_%=:
s_waitcnt lgkmcnt(0)
M_ v[136:139],a[160:163],a[0:3],v[184:187]
s_nop 1
E_ v176,v128
M_ v[136:139],a[164:167],a[4:7],v[136:139]
M_ v[136:139],a[168:171],a[8:11],v[136:139]
E_ v177,v129
M_ v[136:139],a[172:175],a[12:15],v[136:139]
v_add_f32 v204,v176,v177
C_ v160,v176,v177
s_cbranch_vccnz LRF0_3_%=
LBF0_3_%=:
v_add_f32 v203,v203,v205
M_ v[140:143],a[176:179],a[0:3],v[184:187]
E_ v178,v130
M_ v[140:143],a[180:183],a[4:7],v[140:143]
v_add_f32 v204,v204,v178
M_ v[140:143],a[184:187],a[8:11],v[140:143]
E_ v179,v131
M_ v[140:143],a[188:191],a[12:15],v[140:143]
v_add_f32 v204,v204,v179
C_ v161,v178,v179
M_ v[96:99],a[64:67],v[168:171],v[96:99]
E_ v180,v132
M_ v[96:99],a[96:99],v[172:175],v[96:99]
ds_read_b128 a[64:67],v207
v_add_f32 v204,v204,v180
M_ v[100:103],a[68:71],v[168:171],v[100:103]
E_ v181,v133
M_ v[100:103],a[100:103],v[172:175],v[100:103]
R_ a[96:99],v207,8192
v_add_f32 v204,v204,v181
C_ v162,v180,v181
M_ v[104:107],a[72:75],v[168:171],v[104:107]
E_ v182,v134
M_ v[104:107],a[104:107],v[172:175],v[104:107]
R_ a[68:71],v207,1024
v_add_f32 v204,v204,v182
M_ v[108:111],a[76:79],v[168:171],v[108:111]
E_ v183,v135
M_ v[108:111],a[108:111],v[172:175],v[108:111]
R_ a[100:103],v207,9216
v_add_f32 v204,v204,v183
C_ v163,v182,v183
M_ v[112:115],a[80:83],v[168:171],v[112:115]
E_ v176,v136
M_ v[112:115],a[112:115],v[172:175],v[112:115]
R_ a[72:75],v207,2048
v_add_f32 v204,v204,v176
M_ v[116:119],a[84:87],v[168:171],v[116:119]
E_ v177,v137
M_ v[116:119],a[116:119],v[172:175],v[116:119]
R_ a[104:107],v207,10240
v_add_f32 v204,v204,v177
C_ v164,v176,v177
M_ v[120:123],a[88:91],v[168:171],v[120:123]
E_ v178,v138
M_ v[120:123],a[120:123],v[172:175],v[120:123]
R_ a[76:79],v207,3072
v_add_f32 v204,v204,v178
M_ v[124:127],a[92:95],v[168:171],v[124:127]
E_ v179,v139
M_ v[124:127],a[124:127],v[172:175],v[124:127]
R_ a[108:111],v207,11264
v_add_f32 v204,v204,v179
C_ v165,v178,v179
M_ v[144:147],a[128:131],a[16:19],v[188:191]
E_ v180,v140
M_ v[144:147],a[132:135],a[20:23],v[144:147]
R_ a[80:83],v207,4096
v_add_f32 v204,v204,v180
M_ v[144:147],a[136:139],a[24:27],v[144:147]
E_ v181,v141
M_ v[144:147],a[140:143],a[28:31],v[144:147]
R_ a[112:115],v207,12288
v_add_f32 v204,v204,v181
C_ v166,v180,v181
M_ v[148:151],a[144:147],a[16:19],v[188:191]
E_ v182,v142
M_ v[148:151],a[148:151],a[20:23],v[148:151]
R_ a[84:87],v207,5120
v_add_f32 v204,v204,v182
M_ v[148:151],a[152:155],a[24:27],v[148:151]
E_ v183,v143
M_ v[148:151],a[156:159],a[28:31],v[148:151]
R_ a[116:119],v207,13312
v_add_f32 v204,v204,v183
C_ v167,v182,v183
M_ v[152:155],a[160:163],a[16:19],v[188:191]
E_ v176,v144
M_ v[152:155],a[164:167],a[20:23],v[152:155]
R_ a[88:91],v207,6144
v_cmp_lt_f32 vcc,s27,v204
M_ v[152:155],a[168:171],a[24:27],v[152:155]
E_ v177,v145
M_ v[152:155],a[172:175],a[28:31],v[152:155]
R_ a[120:123],v207,14336
v_add_f32 v205,v176,v177
C_ v168,v176,v177
M_ v[156:159],a[176:179],a[16:19],v[188:191]
E_ v178,v146
s_cbranch_vccnz LRF0_0_%=
LBF0_0_%=:
v_add_f32 v200,v200,v204
M_ v[156:159],a[180:183],a[20:23],v[156:159]
R_ a[92:95],v207,7168
v_add_f32 v205,v205,v178
M_ v[156:159],a[184:187],a[24:27],v[156:159]
E_ v179,v147
M_ v[156:159],a[188:191],a[28:31],v[156:159]
R_ a[124:127],v207,15360
)A"
R"A(v_add_f32 v205,v205,v179
C_ v169,v178,v179
s_waitcnt lgkmcnt(8)
M_ v[0:3],a[64:67],v[160:163],v[0:3]
E_ v180,v148
M_ v[0:3],a[96:99],v[164:167],v[0:3]
v_add_f32 v205,v205,v180
M_ v[4:7],a[68:71],v[160:163],v[4:7]
E_ v181,v149
M_ v[4:7],a[100:103],v[164:167],v[4:7]
v_add_f32 v205,v205,v181
C_ v170,v180,v181
M_ v[8:11],a[72:75],v[160:163],v[8:11]
E_ v182,v150
M_ v[8:11],a[104:107],v[164:167],v[8:11]
v_add_f32 v205,v205,v182
M_ v[12:15],a[76:79],v[160:163],v[12:15]
E_ v183,v151
M_ v[12:15],a[108:111],v[164:167],v[12:15]
v_add_f32 v205,v205,v183
C_ v171,v182,v183
s_waitcnt vmcnt(0) lgkmcnt(0)
s_barrier
M_ v[16:19],a[80:83],v[160:163],v[16:19]
E_ v176,v152
M_ v[16:19],a[112:115],v[164:167],v[16:19]
v_add_f32 v205,v205,v176
M_ v[20:23],a[84:87],v[160:163],v[20:23]
E_ v177,v153
M_ v[20:23],a[116:119],v[164:167],v[20:23]
s_add_u32 s54,s21,1
v_add_f32 v205,v205,v177
C_ v172,v176,v177
M_ v[24:27],a[88:91],v[160:163],v[24:27]
E_ v178,v154
M_ v[24:27],a[120:123],v[164:167],v[24:27]
s_cmp_ge_u32 s54,s20
v_add_f32 v205,v205,v178
M_ v[28:31],a[92:95],v[160:163],v[28:31]
E_ v179,v155
M_ v[28:31],a[124:127],v[164:167],v[28:31]
s_cselect_b32 s55,s73,0
v_add_f32 v205,v205,v179
C_ v173,v178,v179
M_ v[128:131],a[128:131],a[32:35],v[192:195]
E_ v180,v156
M_ v[128:131],a[132:135],a[36:39],v[128:131]
s_and_b32 s56,s54,1
v_add_f32 v205,v205,v180
M_ v[128:131],a[136:139],a[40:43],v[128:131]
E_ v181,v157
M_ v[128:131],a[140:143],a[44:47],v[128:131]
s_mul_i32 s57,s56,0xfffe4000
v_add_f32 v205,v205,v181
C_ v174,v180,v181
M_ v[132:135],a[144:147],a[32:35],v[192:195]
E_ v182,v158
M_ v[132:135],a[148:151],a[36:39],v[132:135]
s_add_u32 s57,s57,0x1c000
v_add_f32 v205,v205,v182
M_ v[132:135],a[152:155],a[40:43],v[132:135]
E_ v183,v159
M_ v[132:135],a[156:159],a[44:47],v[132:135]
s_mul_i32 s58,s56,0x8000
v_add_f32 v205,v205,v183
C_ v175,v182,v183
M_ v[136:139],a[160:163],a[32:35],v[192:195]
E_ v176,v128
M_ v[136:139],a[164:167],a[36:39],v[136:139]
s_add_u32 s58,s58,0x10000
v_cmp_lt_f32 vcc,s27,v205
M_ v[136:139],a[168:171],a[40:43],v[136:139]
E_ v177,v129
M_ v[136:139],a[172:175],a[44:47],v[136:139]
s_cmp_lg_u32 s55,0
v_add_f32 v204,v176,v177
C_ v160,v176,v177
M_ v[140:143],a[176:179],a[32:35],v[192:195]
E_ v178,v130
s_cbranch_vccnz LRF0_1_%=
LBF0_1_%=:
v_add_f32 v201,v201,v205
M_ v[140:143],a[180:183],a[36:39],v[140:143]
s_cselect_b32 s25,s58,s57
v_add_f32 v204,v204,v178
M_ v[140:143],a[184:187],a[40:43],v[140:143]
E_ v179,v131
M_ v[140:143],a[188:191],a[44:47],v[140:143]
s_add_u32 s57,s57,0x4000
v_add_f32 v204,v204,v179
C_ v161,v178,v179
M_ v[32:35],a[64:67],v[168:171],v[32:35]
E_ v180,v132
M_ v[32:35],a[96:99],v[172:175],v[32:35]
s_mul_i32 s58,s56,0x10000
v_add_f32 v204,v204,v180
M_ v[36:39],a[68:71],v[168:171],v[36:39]
E_ v181,v133
M_ v[36:39],a[100:103],v[172:175],v[36:39]
s_add_u32 s58,s58,0x14000
v_add_f32 v204,v204,v181
C_ v162,v180,v181
M_ v[40:43],a[72:75],v[168:171],v[40:43]
E_ v182,v134
M_ v[40:43],a[104:107],v[172:175],v[40:43]
s_cmp_lg_u32 s55,0
v_add_f32 v204,v204,v182
M_ v[44:47],a[76:79],v[168:171],v[44:47]
E_ v183,v135
M_ v[44:47],a[108:111],v[172:175],v[44:47]
s_cselect_b32 s26,s58,s57
v_add_f32 v204,v204,v183
C_ v163,v182,v183
M_ v[48:51],a[80:83],v[168:171],v[48:51]
E_ v176,v136
M_ v[48:51],a[112:115],v[172:175],v[48:51]
v_add_f32 v204,v204,v176
M_ v[52:55],a[84:87],v[168:171],v[52:55]
E_ v177,v137
M_ v[52:55],a[116:119],v[172:175],v[52:55]
v_add_f32 v204,v204,v177
C_ v164,v176,v177
v_add_u32 v206,s25,v208
M_ v[56:59],a[88:91],v[168:171],v[56:59]
E_ v178,v138
M_ v[56:59],a[120:123],v[172:175],v[56:59]
v_add_f32 v204,v204,v178
v_add_u32 v207,s26,v208
M_ v[60:63],a[92:95],v[168:171],v[60:63]
E_ v179,v139
M_ v[60:63],a[124:127],v[172:175],v[60:63]
v_add_f32 v204,v204,v179
C_ v165,v178,v179
M_ v[144:147],a[128:131],a[48:51],v[196:199]
E_ v180,v140
M_ v[144:147],a[132:135],a[52:55],v[144:147]
ds_read_b128 a[128:131],v206
v_add_f32 v204,v204,v180
M_ v[144:147],a[136:139],a[56:59],v[144:147]
E_ v181,v141
M_ v[144:147],a[140:143],a[60:63],v[144:147]
R_ a[132:135],v206,1024
v_add_f32 v204,v204,v181
C_ v166,v180,v181
M_ v[148:151],a[144:147],a[48:51],v[196:199]
E_ v182,v142
M_ v[148:151],a[148:151],a[52:55],v[148:151]
R_ a[136:139],v206,2048
v_add_f32 v204,v204,v182
M_ v[148:151],a[152:155],a[56:59],v[148:151]
E_ v183,v143
M_ v[148:151],a[156:159],a[60:63],v[148:151]
R_ a[140:143],v206,3072
v_add_f32 v204,v204,v183
C_ v167,v182,v183
M_ v[152:155],a[160:163],a[48:51],v[196:199]
E_ v176,v144
M_ v[152:155],a[164:167],a[52:55],v[152:155]
R_ a[144:147],v206,4096
v_cmp_lt_f32 vcc,s27,v204
M_ v[152:155],a[168:171],a[56:59],v[152:155]
E_ v177,v145
M_ v[152:155],a[172:175],a[60:63],v[152:155]
R_ a[148:151],v206,5120
v_add_f32 v205,v176,v177
C_ v168,v176,v177
M_ v[156:159],a[176:179],a[48:51],v[196:199]
E_ v178,v146
s_cbranch_vccnz LRF0_2_%=
LBF0_2_%=:
v_add_f32 v202,v202,v204
M_ v[156:159],a[180:183],a[52:55],v[156:159]
R_ a[152:155],v206,6144
v_add_f32 v205,v205,v178
M_ v[156:159],a[184:187],a[56:59],v[156:159]
E_ v179,v147
M_ v[156:159],a[188:191],a[60:63],v[156:159]
R_ a[156:159],v206,7168
v_add_f32 v205,v205,v179
C_ v169,v178,v179
M_ v[64:67],a[64:67],v[160:163],v[64:67]
E_ v180,v148
M_ v[64:67],a[96:99],v[164:167],v[64:67]
R_ a[160:163],v206,8192
v_add_f32 v205,v205,v180
M_ v[68:71],a[68:71],v[160:163],v[68:71]
E_ v181,v149
M_ v[68:71],a[100:103],v[164:167],v[68:71]
R_ a[164:167],v206,9216
v_add_f32 v205,v205,v181
C_ v170,v180,v181
M_ v[72:75],a[72:75],v[160:163],v[72:75]
s_and_b32 s78,s22,15
E_ v182,v150
M_ v[72:75],a[104:107],v[164:167],v[72:75]
R_ a[168:171],v206,10240
v_add_f32 v205,v205,v182
M_ v[76:79],a[76:79],v[160:163],v[76:79]
s_lshr_b64 s[22:23],s[22:23],4
E_ v183,v151
M_ v[76:79],a[108:111],v[164:167],v[76:79]
R_ a[172:175],v206,11264
v_add_f32 v205,v205,v183
C_ v171,v182,v183
M_ v[80:83],a[80:83],v[160:163],v[80:83]
s_lshl_b32 s54,s79,28
E_ v176,v152
M_ v[80:83],a[112:115],v[164:167],v[80:83]
R_ a[176:179],v206,12288
v_add_f32 v205,v205,v176
M_ v[84:87],a[84:87],v[160:163],v[84:87]
s_or_b32 s23,s23,s54
E_ v177,v153
M_ v[84:87],a[116:119],v[164:167],v[84:87]
R_ a[180:183],v206,13312
v_add_f32 v205,v205,v177
C_ v172,v176,v177
M_ v[88:91],a[88:91],v[160:163],v[88:91]
s_lshr_b32 s79,s79,4
E_ v178,v154
M_ v[88:91],a[120:123],v[164:167],v[88:91]
R_ a[184:187],v206,14336
v_add_f32 v205,v205,v178
M_ v[92:95],a[92:95],v[160:163],v[92:95]
E_ v179,v155
M_ v[92:95],a[124:127],v[164:167],v[92:95]
R_ a[188:191],v206,15360
v_add_f32 v205,v205,v179
C_ v173,v178,v179
s_waitcnt lgkmcnt(8)
M_ v[128:131],a[128:131],a[0:3],v[184:187]
s_add_u32 s21,s21,1
E_ v180,v156
M_ v[128:131],a[132:135],a[4:7],v[128:131]
v_add_f32 v205,v205,v180
M_ v[128:131],a[136:139],a[8:11],v[128:131]
E_ v181,v157
M_ v[128:131],a[140:143],a[12:15],v[128:131]
v_add_f32 v205,v205,v181
C_ v174,v180,v181
M_ v[132:135],a[144:147],a[0:3],v[184:187]
E_ v182,v158
M_ v[132:135],a[148:151],a[4:7],v[132:135]
v_add_f32 v205,v205,v182
M_ v[132:135],a[152:155],a[8:11],v[132:135]
E_ v183,v159
M_ v[132:135],a[156:159],a[12:15],v[132:135]
v_add_f32 v205,v205,v183
C_ v175,v182,v183
v_cmp_lt_f32 vcc,s27,v205
s_cmp_eq_u32 s78,2
s_cbranch_scc1 LVF0_%=
s_branch LDISP_%=
LVD2_%=:
s_waitcnt lgkmcnt(0)
M_ v[136:139],a[160:163],a[0:3],v[184:187]
s_nop 1
v_cndmask_b32 v128,v209,v128,s[46:47]
E_ v176,v128
M_ v[136:139],a[164:167],a[4:7],v[136:139]
M_ v[136:139],a[168:171],a[8:11],v[136:139]
v_cndmask_b32 v129,v209,v129,s[48:49]
E_ v177,v129
M_ v[136:139],a[172:175],a[12:15],v[136:139]
v_add_f32 v204,v176,v177
C_ v160,v176,v177
s_cbranch_vccnz LRD2_3_%=
LBD2_3_%=:
v_add_f32 v203,v203,v205
M_ v[140:143],a[176:179],a[0:3],v[184:187]
v_cndmask_b32 v130,v209,v130,s[50:51]
E_ v178,v130
M_ v[140:143],a[180:183],a[4:7],v[140:143]
v_add_f32 v204,v204,v178
M_ v[140:143],a[184:187],a[8:11],v[140:143]
v_cndmask_b32 v131,v209,v131,s[52:53]
E_ v179,v131
M_ v[140:143],a[188:191],a[12:15],v[140:143]
v_add_f32 v204,v204,v179
C_ v161,v178,v179
M_ v[96:99],a[64:67],v[168:171],v[96:99]
s_and_b32 s56,s21,1
v_mov_b32 v132,v209
E_ v180,v132
M_ v[96:99],a[96:99],v[172:175],v[96:99]
ds_read_b128 a[64:67],v207
v_add_f32 v204,v204,v180
M_ v[100:103],a[68:71],v[168:171],v[100:103]
s_mul_i32 s57,s56,0xfffe4000
v_mov_b32 v133,v209
E_ v181,v133
M_ v[100:103],a[100:103],v[172:175],v[100:103]
R_ a[96:99],v207,8192
v_add_f32 v204,v204,v181
C_ v162,v180,v181
M_ v[104:107],a[72:75],v[168:171],v[104:107]
s_add_u32 s57,s57,0x1c000
v_mov_b32 v134,v209
E_ v182,v134
M_ v[104:107],a[104:107],v[172:175],v[104:107]
R_ a[68:71],v207,1024
v_add_f32 v204,v204,v182
M_ v[108:111],a[76:79],v[168:171],v[108:111]
s_add_u32 s60,s57,s75
v_mov_b32 v135,v209
E_ v183,v135
M_ v[108:111],a[108:111],v[172:175],v[108:111]
R_ a[100:103],v207,9216
v_add_f32 v204,v204,v183
C_ v163,v182,v183
M_ v[112:115],a[80:83],v[168:171],v[112:115]
s_add_u32 s61,s60,0x4000
v_mov_b32 v136,v209
E_ v176,v136
M_ v[112:115],a[112:115],v[172:175],v[112:115]
R_ a[72:75],v207,2048
v_add_f32 v204,v204,v176
M_ v[116:119],a[84:87],v[168:171],v[116:119]
s_add_u32 s64,s24,s75
v_mov_b32 v137,v209
E_ v177,v137
M_ v[116:119],a[116:119],v[172:175],v[116:119]
R_ a[104:107],v207,10240
v_add_f32 v204,v204,v177
C_ v164,v176,v177
M_ v[120:123],a[88:91],v[168:171],v[120:123]
s_add_u32 s65,s64,0x4000
v_mov_b32 v138,v209
E_ v178,v138
M_ v[120:123],a[120:123],v[172:175],v[120:123]
R_ a[76:79],v207,3072
v_add_f32 v204,v204,v178
M_ v[124:127],a[92:95],v[168:171],v[124:127]
s_mul_i32 s57,s56,0x8000
v_mov_b32 v139,v209
E_ v179,v139
M_ v[124:127],a[124:127],v[172:175],v[124:127]
R_ a[108:111],v207,11264
v_add_f32 v204,v204,v179
C_ v165,v178,v179
M_ v[144:147],a[128:131],a[16:19],v[188:191]
s_add_u32 s57,s57,0x10000
v_mov_b32 v140,v209
E_ v180,v140
M_ v[144:147],a[132:135],a[20:23],v[144:147]
R_ a[80:83],v207,4096
v_add_f32 v204,v204,v180
M_ v[144:147],a[136:139],a[24:27],v[144:147]
s_add_u32 s62,s57,s75
v_mov_b32 v141,v209
E_ v181,v141
M_ v[144:147],a[140:143],a[28:31],v[144:147]
R_ a[112:115],v207,12288
v_add_f32 v204,v204,v181
C_ v166,v180,v181
M_ v[148:151],a[144:147],a[16:19],v[188:191]
s_mul_i32 s57,s56,0x10000
v_mov_b32 v142,v209
E_ v182,v142
M_ v[148:151],a[148:151],a[20:23],v[148:151]
R_ a[84:87],v207,5120
v_add_f32 v204,v204,v182
M_ v[148:151],a[152:155],a[24:27],v[148:151]
s_add_u32 s57,s57,0x14000
v_mov_b32 v143,v209
E_ v183,v143
M_ v[148:151],a[156:159],a[28:31],v[148:151]
R_ a[116:119],v207,13312
v_add_f32 v204,v204,v183
C_ v167,v182,v183
M_ v[152:155],a[160:163],a[16:19],v[188:191]
s_add_u32 s63,s57,s75
E_ v176,v144
M_ v[152:155],a[164:167],a[20:23],v[152:155]
R_ a[88:91],v207,6144
v_cmp_lt_f32 vcc,s27,v204
M_ v[152:155],a[168:171],a[24:27],v[152:155]
s_add_u32 s66,s64,0x8000
E_ v177,v145
M_ v[152:155],a[172:175],a[28:31],v[152:155]
R_ a[120:123],v207,14336
v_add_f32 v205,v176,v177
C_ v168,v176,v177
M_ v[156:159],a[176:179],a[16:19],v[188:191]
s_add_u32 s67,s64,0xc000
E_ v178,v146
s_cbranch_vccnz LRD2_0_%=
LBD2_0_%=:
v_add_f32 v200,v200,v204
M_ v[156:159],a[180:183],a[20:23],v[156:159]
R_ a[92:95],v207,7168
v_add_f32 v205,v205,v178
M_ v[156:159],a[184:187],a[24:27],v[156:159]
s_add_u32 s57,s21,3
E_ v179,v147
M_ v[156:159],a[188:191],a[28:31],v[156:159]
R_ a[124:127],v207,15360
v_add_f32 v205,v205,v179
C_ v169,v178,v179
s_waitcnt lgkmcnt(8)
M_ v[0:3],a[64:67],v[160:163],v[0:3]
s_cmp_gt_u32 s57,s20
v_cndmask_b32 v148,v209,v148,s[46:47]
E_ v180,v148
M_ v[0:3],a[96:99],v[164:167],v[0:3]
v_add_f32 v205,v205,v180
M_ v[4:7],a[68:71],v[160:163],v[4:7]
s_cselect_b32 s57,2,1
v_cndmask_b32 v149,v209,v149,s[48:49]
E_ v181,v149
M_ v[4:7],a[100:103],v[164:167],v[4:7]
v_add_f32 v205,v205,v181
C_ v170,v180,v181
M_ v[8:11],a[72:75],v[160:163],v[8:11]
s_lshl_b32 s57,s57,15
v_cndmask_b32 v150,v209,v150,s[50:51]
E_ v182,v150
M_ v[8:11],a[104:107],v[164:167],v[8:11]
v_add_f32 v205,v205,v182
M_ v[12:15],a[76:79],v[160:163],v[12:15]
s_add_u32 s24,s24,s57
v_cndmask_b32 v151,v209,v151,s[52:53]
E_ v183,v151
M_ v[12:15],a[108:111],v[164:167],v[12:15]
v_add_f32 v205,v205,v183
C_ v171,v182,v183
s_waitcnt vmcnt(0) lgkmcnt(0)
s_barrier
M_ v[16:19],a[80:83],v[160:163],v[16:19]
v_mov_b32 v152,v209
E_ v176,v152
M_ v[16:19],a[112:115],v[164:167],v[16:19]
s_mov_b32 m0,s60
v_add_f32 v205,v205,v176
M_ v[20:23],a[84:87],v[160:163],v[20:23]
buffer_load_dwordx4 v208,s[12:15],s64 offen sc1 lds
v_mov_b32 v153,v209
E_ v177,v153
M_ v[20:23],a[116:119],v[164:167],v[20:23]
s_add_u32 s54,s21,1
v_add_f32 v205,v205,v177
C_ v172,v176,v177
M_ v[24:27],a[88:91],v[160:163],v[24:27]
v_mov_b32 v154,v209
E_ v178,v154
M_ v[24:27],a[120:123],v[164:167],v[24:27]
s_cmp_ge_u32 s54,s20
v_add_f32 v205,v205,v178
M_ v[28:31],a[92:95],v[160:163],v[28:31]
buffer_load_dwordx4 v208,s[12:15],s64 offen offset:1024 sc1 lds
v_mov_b32 v155,v209
E_ v179,v155
M_ v[28:31],a[124:127],v[164:167],v[28:31]
s_cselect_b32 s55,s73,0
v_add_f32 v205,v205,v179
C_ v173,v178,v179
M_ v[128:131],a[128:131],a[32:35],v[192:195]
v_mov_b32 v156,v209
E_ v180,v156
M_ v[128:131],a[132:135],a[36:39],v[128:131]
s_and_b32 s56,s54,1
v_add_f32 v205,v205,v180
M_ v[128:131],a[136:139],a[40:43],v[128:131]
v_mov_b32 v157,v209
E_ v181,v157
M_ v[128:131],a[140:143],a[44:47],v[128:131]
s_mul_i32 s57,s56,0xfffe4000
v_add_f32 v205,v205,v181
C_ v174,v180,v181
M_ v[132:135],a[144:147],a[32:35],v[192:195]
buffer_load_dwordx4 v208,s[12:15],s64 offen offset:2048 sc1 lds
v_mov_b32 v158,v209
E_ v182,v158
M_ v[132:135],a[148:151],a[36:39],v[132:135]
s_add_u32 s57,s57,0x1c000
v_add_f32 v205,v205,v182
M_ v[132:135],a[152:155],a[40:43],v[132:135]
v_mov_b32 v159,v209
E_ v183,v159
M_ v[132:135],a[156:159],a[44:47],v[132:135]
s_mul_i32 s58,s56,0x8000
v_add_f32 v205,v205,v183
C_ v175,v182,v183
M_ v[136:139],a[160:163],a[32:35],v[192:195]
buffer_load_dwordx4 v208,s[12:15],s64 offen offset:3072 sc1 lds
E_ v176,v128
M_ v[136:139],a[164:167],a[36:39],v[136:139]
s_add_u32 s58,s58,0x10000
v_cmp_lt_f32 vcc,s27,v205
M_ v[136:139],a[168:171],a[40:43],v[136:139]
E_ v177,v129
M_ v[136:139],a[172:175],a[44:47],v[136:139]
s_mov_b32 m0,s61
s_cmp_lg_u32 s55,0
v_add_f32 v204,v176,v177
C_ v160,v176,v177
M_ v[140:143],a[176:179],a[32:35],v[192:195]
)A"
R"A(buffer_load_dwordx4 v208,s[12:15],s65 offen sc1 lds
E_ v178,v130
s_cbranch_vccnz LRD2_1_%=
LBD2_1_%=:
v_add_f32 v201,v201,v205
M_ v[140:143],a[180:183],a[36:39],v[140:143]
s_cselect_b32 s25,s58,s57
v_add_f32 v204,v204,v178
M_ v[140:143],a[184:187],a[40:43],v[140:143]
E_ v179,v131
M_ v[140:143],a[188:191],a[44:47],v[140:143]
s_add_u32 s57,s57,0x4000
v_add_f32 v204,v204,v179
C_ v161,v178,v179
M_ v[32:35],a[64:67],v[168:171],v[32:35]
buffer_load_dwordx4 v208,s[12:15],s65 offen offset:1024 sc1 lds
E_ v180,v132
M_ v[32:35],a[96:99],v[172:175],v[32:35]
s_mul_i32 s58,s56,0x10000
v_add_f32 v204,v204,v180
M_ v[36:39],a[68:71],v[168:171],v[36:39]
E_ v181,v133
M_ v[36:39],a[100:103],v[172:175],v[36:39]
s_add_u32 s58,s58,0x14000
v_add_f32 v204,v204,v181
C_ v162,v180,v181
M_ v[40:43],a[72:75],v[168:171],v[40:43]
E_ v182,v134
M_ v[40:43],a[104:107],v[172:175],v[40:43]
s_cmp_lg_u32 s55,0
v_add_f32 v204,v204,v182
M_ v[44:47],a[76:79],v[168:171],v[44:47]
buffer_load_dwordx4 v208,s[12:15],s65 offen offset:2048 sc1 lds
E_ v183,v135
M_ v[44:47],a[108:111],v[172:175],v[44:47]
s_cselect_b32 s26,s58,s57
v_add_f32 v204,v204,v183
C_ v163,v182,v183
M_ v[48:51],a[80:83],v[168:171],v[48:51]
v_cndmask_b32 v136,v209,v136,s[46:47]
E_ v176,v136
M_ v[48:51],a[112:115],v[172:175],v[48:51]
v_add_f32 v204,v204,v176
M_ v[52:55],a[84:87],v[168:171],v[52:55]
buffer_load_dwordx4 v208,s[12:15],s65 offen offset:3072 sc1 lds
v_cndmask_b32 v137,v209,v137,s[48:49]
E_ v177,v137
M_ v[52:55],a[116:119],v[172:175],v[52:55]
v_add_f32 v204,v204,v177
C_ v164,v176,v177
v_add_u32 v206,s25,v208
M_ v[56:59],a[88:91],v[168:171],v[56:59]
v_cndmask_b32 v138,v209,v138,s[50:51]
E_ v178,v138
M_ v[56:59],a[120:123],v[172:175],v[56:59]
s_mov_b32 m0,s62
v_add_f32 v204,v204,v178
v_add_u32 v207,s26,v208
M_ v[60:63],a[92:95],v[168:171],v[60:63]
buffer_load_dwordx4 v208,s[12:15],s66 offen sc1 lds
v_cndmask_b32 v139,v209,v139,s[52:53]
E_ v179,v139
M_ v[60:63],a[124:127],v[172:175],v[60:63]
v_add_f32 v204,v204,v179
C_ v165,v178,v179
M_ v[144:147],a[128:131],a[48:51],v[196:199]
v_mov_b32 v140,v209
E_ v180,v140
M_ v[144:147],a[132:135],a[52:55],v[144:147]
ds_read_b128 a[128:131],v206
v_add_f32 v204,v204,v180
M_ v[144:147],a[136:139],a[56:59],v[144:147]
buffer_load_dwordx4 v208,s[12:15],s66 offen offset:1024 sc1 lds
v_mov_b32 v141,v209
E_ v181,v141
M_ v[144:147],a[140:143],a[60:63],v[144:147]
R_ a[132:135],v206,1024
v_add_f32 v204,v204,v181
C_ v166,v180,v181
M_ v[148:151],a[144:147],a[48:51],v[196:199]
v_mov_b32 v142,v209
E_ v182,v142
M_ v[148:151],a[148:151],a[52:55],v[148:151]
R_ a[136:139],v206,2048
v_add_f32 v204,v204,v182
M_ v[148:151],a[152:155],a[56:59],v[148:151]
v_mov_b32 v143,v209
E_ v183,v143
M_ v[148:151],a[156:159],a[60:63],v[148:151]
R_ a[140:143],v206,3072
v_add_f32 v204,v204,v183
C_ v167,v182,v183
M_ v[152:155],a[160:163],a[48:51],v[196:199]
buffer_load_dwordx4 v208,s[12:15],s66 offen offset:2048 sc1 lds
E_ v176,v144
M_ v[152:155],a[164:167],a[52:55],v[152:155]
R_ a[144:147],v206,4096
v_cmp_lt_f32 vcc,s27,v204
M_ v[152:155],a[168:171],a[56:59],v[152:155]
E_ v177,v145
M_ v[152:155],a[172:175],a[60:63],v[152:155]
R_ a[148:151],v206,5120
v_add_f32 v205,v176,v177
C_ v168,v176,v177
M_ v[156:159],a[176:179],a[48:51],v[196:199]
buffer_load_dwordx4 v208,s[12:15],s66 offen offset:3072 sc1 lds
E_ v178,v146
s_cbranch_vccnz LRD2_2_%=
LBD2_2_%=:
v_add_f32 v202,v202,v204
M_ v[156:159],a[180:183],a[52:55],v[156:159]
R_ a[152:155],v206,6144
v_add_f32 v205,v205,v178
M_ v[156:159],a[184:187],a[56:59],v[156:159]
E_ v179,v147
M_ v[156:159],a[188:191],a[60:63],v[156:159]
s_mov_b32 m0,s63
R_ a[156:159],v206,7168
v_add_f32 v205,v205,v179
C_ v169,v178,v179
M_ v[64:67],a[64:67],v[160:163],v[64:67]
buffer_load_dwordx4 v208,s[12:15],s67 offen sc1 lds
E_ v180,v148
M_ v[64:67],a[96:99],v[164:167],v[64:67]
R_ a[160:163],v206,8192
v_add_f32 v205,v205,v180
M_ v[68:71],a[68:71],v[160:163],v[68:71]
E_ v181,v149
M_ v[68:71],a[100:103],v[164:167],v[68:71]
R_ a[164:167],v206,9216
v_add_f32 v205,v205,v181
C_ v170,v180,v181
M_ v[72:75],a[72:75],v[160:163],v[72:75]
s_and_b32 s78,s22,15
buffer_load_dwordx4 v208,s[12:15],s67 offen offset:1024 sc1 lds
E_ v182,v150
M_ v[72:75],a[104:107],v[164:167],v[72:75]
R_ a[168:171],v206,10240
v_add_f32 v205,v205,v182
M_ v[76:79],a[76:79],v[160:163],v[76:79]
s_lshr_b64 s[22:23],s[22:23],4
E_ v183,v151
M_ v[76:79],a[108:111],v[164:167],v[76:79]
R_ a[172:175],v206,11264
v_add_f32 v205,v205,v183
C_ v171,v182,v183
M_ v[80:83],a[80:83],v[160:163],v[80:83]
s_lshl_b32 s54,s79,28
E_ v176,v152
M_ v[80:83],a[112:115],v[164:167],v[80:83]
R_ a[176:179],v206,12288
v_add_f32 v205,v205,v176
M_ v[84:87],a[84:87],v[160:163],v[84:87]
s_or_b32 s23,s23,s54
buffer_load_dwordx4 v208,s[12:15],s67 offen offset:2048 sc1 lds
E_ v177,v153
M_ v[84:87],a[116:119],v[164:167],v[84:87]
R_ a[180:183],v206,13312
v_add_f32 v205,v205,v177
C_ v172,v176,v177
M_ v[88:91],a[88:91],v[160:163],v[88:91]
s_lshr_b32 s79,s79,4
E_ v178,v154
M_ v[88:91],a[120:123],v[164:167],v[88:91]
R_ a[184:187],v206,14336
v_add_f32 v205,v205,v178
M_ v[92:95],a[92:95],v[160:163],v[92:95]
buffer_load_dwordx4 v208,s[12:15],s67 offen offset:3072 sc1 lds
E_ v179,v155
M_ v[92:95],a[124:127],v[164:167],v[92:95]
R_ a[188:191],v206,15360
v_add_f32 v205,v205,v179
C_ v173,v178,v179
s_waitcnt lgkmcnt(8)
M_ v[128:131],a[128:131],a[0:3],v[184:187]
s_add_u32 s21,s21,1
v_cndmask_b32 v156,v209,v156,s[46:47]
E_ v180,v156
M_ v[128:131],a[132:135],a[4:7],v[128:131]
v_add_f32 v205,v205,v180
M_ v[128:131],a[136:139],a[8:11],v[128:131]
v_cndmask_b32 v157,v209,v157,s[48:49]
E_ v181,v157
M_ v[128:131],a[140:143],a[12:15],v[128:131]
v_add_f32 v205,v205,v181
C_ v174,v180,v181
M_ v[132:135],a[144:147],a[0:3],v[184:187]
v_cndmask_b32 v158,v209,v158,s[50:51]
E_ v182,v158
M_ v[132:135],a[148:151],a[4:7],v[132:135]
v_add_f32 v205,v205,v182
M_ v[132:135],a[152:155],a[8:11],v[132:135]
v_cndmask_b32 v159,v209,v159,s[52:53]
E_ v183,v159
M_ v[132:135],a[156:159],a[12:15],v[132:135]
v_add_f32 v205,v205,v183
C_ v175,v182,v183
v_cmp_lt_f32 vcc,s27,v205
s_cmp_eq_u32 s78,3
s_cbranch_scc1 LVD2_%=
s_branch LDISP_%=
LVD0_%=:
s_waitcnt lgkmcnt(0)
M_ v[136:139],a[160:163],a[0:3],v[184:187]
s_nop 1
v_cndmask_b32 v128,v209,v128,s[46:47]
E_ v176,v128
M_ v[136:139],a[164:167],a[4:7],v[136:139]
M_ v[136:139],a[168:171],a[8:11],v[136:139]
v_cndmask_b32 v129,v209,v129,s[48:49]
E_ v177,v129
M_ v[136:139],a[172:175],a[12:15],v[136:139]
v_add_f32 v204,v176,v177
C_ v160,v176,v177
s_cbranch_vccnz LRD0_3_%=
LBD0_3_%=:
v_add_f32 v203,v203,v205
M_ v[140:143],a[176:179],a[0:3],v[184:187]
v_cndmask_b32 v130,v209,v130,s[50:51]
E_ v178,v130
M_ v[140:143],a[180:183],a[4:7],v[140:143]
v_add_f32 v204,v204,v178
M_ v[140:143],a[184:187],a[8:11],v[140:143]
v_cndmask_b32 v131,v209,v131,s[52:53]
E_ v179,v131
M_ v[140:143],a[188:191],a[12:15],v[140:143]
v_add_f32 v204,v204,v179
C_ v161,v178,v179
M_ v[96:99],a[64:67],v[168:171],v[96:99]
v_mov_b32 v132,v209
E_ v180,v132
M_ v[96:99],a[96:99],v[172:175],v[96:99]
ds_read_b128 a[64:67],v207
v_add_f32 v204,v204,v180
M_ v[100:103],a[68:71],v[168:171],v[100:103]
v_mov_b32 v133,v209
E_ v181,v133
M_ v[100:103],a[100:103],v[172:175],v[100:103]
R_ a[96:99],v207,8192
v_add_f32 v204,v204,v181
C_ v162,v180,v181
M_ v[104:107],a[72:75],v[168:171],v[104:107]
v_mov_b32 v134,v209
E_ v182,v134
M_ v[104:107],a[104:107],v[172:175],v[104:107]
R_ a[68:71],v207,1024
v_add_f32 v204,v204,v182
M_ v[108:111],a[76:79],v[168:171],v[108:111]
v_mov_b32 v135,v209
E_ v183,v135
M_ v[108:111],a[108:111],v[172:175],v[108:111]
R_ a[100:103],v207,9216
v_add_f32 v204,v204,v183
C_ v163,v182,v183
M_ v[112:115],a[80:83],v[168:171],v[112:115]
v_mov_b32 v136,v209
E_ v176,v136
M_ v[112:115],a[112:115],v[172:175],v[112:115]
R_ a[72:75],v207,2048
v_add_f32 v204,v204,v176
M_ v[116:119],a[84:87],v[168:171],v[116:119]
v_mov_b32 v137,v209
E_ v177,v137
M_ v[116:119],a[116:119],v[172:175],v[116:119]
R_ a[104:107],v207,10240
v_add_f32 v204,v204,v177
C_ v164,v176,v177
M_ v[120:123],a[88:91],v[168:171],v[120:123]
v_mov_b32 v138,v209
E_ v178,v138
M_ v[120:123],a[120:123],v[172:175],v[120:123]
R_ a[76:79],v207,3072
v_add_f32 v204,v204,v178
M_ v[124:127],a[92:95],v[168:171],v[124:127]
v_mov_b32 v139,v209
E_ v179,v139
M_ v[124:127],a[124:127],v[172:175],v[124:127]
R_ a[108:111],v207,11264
v_add_f32 v204,v204,v179
C_ v165,v178,v179
M_ v[144:147],a[128:131],a[16:19],v[188:191]
v_mov_b32 v140,v209
E_ v180,v140
M_ v[144:147],a[132:135],a[20:23],v[144:147]
R_ a[80:83],v207,4096
v_add_f32 v204,v204,v180
M_ v[144:147],a[136:139],a[24:27],v[144:147]
v_mov_b32 v141,v209
E_ v181,v141
M_ v[144:147],a[140:143],a[28:31],v[144:147]
R_ a[112:115],v207,12288
v_add_f32 v204,v204,v181
C_ v166,v180,v181
M_ v[148:151],a[144:147],a[16:19],v[188:191]
v_mov_b32 v142,v209
E_ v182,v142
M_ v[148:151],a[148:151],a[20:23],v[148:151]
R_ a[84:87],v207,5120
v_add_f32 v204,v204,v182
M_ v[148:151],a[152:155],a[24:27],v[148:151]
v_mov_b32 v143,v209
E_ v183,v143
M_ v[148:151],a[156:159],a[28:31],v[148:151]
R_ a[116:119],v207,13312
v_add_f32 v204,v204,v183
C_ v167,v182,v183
M_ v[152:155],a[160:163],a[16:19],v[188:191]
E_ v176,v144
M_ v[152:155],a[164:167],a[20:23],v[152:155]
R_ a[88:91],v207,6144
v_cmp_lt_f32 vcc,s27,v204
M_ v[152:155],a[168:171],a[24:27],v[152:155]
E_ v177,v145
M_ v[152:155],a[172:175],a[28:31],v[152:155]
R_ a[120:123],v207,14336
v_add_f32 v205,v176,v177
C_ v168,v176,v177
M_ v[156:159],a[176:179],a[16:19],v[188:191]
E_ v178,v146
s_cbranch_vccnz LRD0_0_%=
LBD0_0_%=:
v_add_f32 v200,v200,v204
M_ v[156:159],a[180:183],a[20:23],v[156:159]
R_ a[92:95],v207,7168
v_add_f32 v205,v205,v178
M_ v[156:159],a[184:187],a[24:27],v[156:159]
E_ v179,v147
M_ v[156:159],a[188:191],a[28:31],v[156:159]
R_ a[124:127],v207,15360
v_add_f32 v205,v205,v179
C_ v169,v178,v179
s_waitcnt lgkmcnt(8)
M_ v[0:3],a[64:67],v[160:163],v[0:3]
v_cndmask_b32 v148,v209,v148,s[46:47]
E_ v180,v148
M_ v[0:3],a[96:99],v[164:167],v[0:3]
v_add_f32 v205,v205,v180
M_ v[4:7],a[68:71],v[160:163],v[4:7]
v_cndmask_b32 v149,v209,v149,s[48:49]
E_ v181,v149
M_ v[4:7],a[100:103],v[164:167],v[4:7]
v_add_f32 v205,v205,v181
C_ v170,v180,v181
M_ v[8:11],a[72:75],v[160:163],v[8:11]
v_cndmask_b32 v150,v209,v150,s[50:51]
E_ v182,v150
M_ v[8:11],a[104:107],v[164:167],v[8:11]
v_add_f32 v205,v205,v182
M_ v[12:15],a[76:79],v[160:163],v[12:15]
v_cndmask_b32 v151,v209,v151,s[52:53]
E_ v183,v151
M_ v[12:15],a[108:111],v[164:167],v[12:15]
v_add_f32 v205,v205,v183
C_ v171,v182,v183
s_waitcnt vmcnt(0) lgkmcnt(0)
s_barrier
M_ v[16:19],a[80:83],v[160:163],v[16:19]
v_mov_b32 v152,v209
E_ v176,v152
M_ v[16:19],a[112:115],v[164:167],v[16:19]
v_add_f32 v205,v205,v176
M_ v[20:23],a[84:87],v[160:163],v[20:23]
v_mov_b32 v153,v209
E_ v177,v153
M_ v[20:23],a[116:119],v[164:167],v[20:23]
s_add_u32 s54,s21,1
v_add_f32 v205,v205,v177
C_ v172,v176,v177
M_ v[24:27],a[88:91],v[160:163],v[24:27]
v_mov_b32 v154,v209
E_ v178,v154
M_ v[24:27],a[120:123],v[164:167],v[24:27]
s_cmp_ge_u32 s54,s20
v_add_f32 v205,v205,v178
M_ v[28:31],a[92:95],v[160:163],v[28:31]
v_mov_b32 v155,v209
E_ v179,v155
M_ v[28:31],a[124:127],v[164:167],v[28:31]
s_cselect_b32 s55,s73,0
v_add_f32 v205,v205,v179
C_ v173,v178,v179
M_ v[128:131],a[128:131],a[32:35],v[192:195]
v_mov_b32 v156,v209
E_ v180,v156
M_ v[128:131],a[132:135],a[36:39],v[128:131]
s_and_b32 s56,s54,1
v_add_f32 v205,v205,v180
M_ v[128:131],a[136:139],a[40:43],v[128:131]
v_mov_b32 v157,v209
E_ v181,v157
M_ v[128:131],a[140:143],a[44:47],v[128:131]
s_mul_i32 s57,s56,0xfffe4000
v_add_f32 v205,v205,v181
C_ v174,v180,v181
M_ v[132:135],a[144:147],a[32:35],v[192:195]
v_mov_b32 v158,v209
E_ v182,v158
M_ v[132:135],a[148:151],a[36:39],v[132:135]
s_add_u32 s57,s57,0x1c000
v_add_f32 v205,v205,v182
M_ v[132:135],a[152:155],a[40:43],v[132:135]
v_mov_b32 v159,v209
E_ v183,v159
M_ v[132:135],a[156:159],a[44:47],v[132:135]
s_mul_i32 s58,s56,0x8000
v_add_f32 v205,v205,v183
C_ v175,v182,v183
M_ v[136:139],a[160:163],a[32:35],v[192:195]
E_ v176,v128
M_ v[136:139],a[164:167],a[36:39],v[136:139]
s_add_u32 s58,s58,0x10000
v_cmp_lt_f32 vcc,s27,v205
M_ v[136:139],a[168:171],a[40:43],v[136:139]
E_ v177,v129
M_ v[136:139],a[172:175],a[44:47],v[136:139]
s_cmp_lg_u32 s55,0
v_add_f32 v204,v176,v177
C_ v160,v176,v177
M_ v[140:143],a[176:179],a[32:35],v[192:195]
E_ v178,v130
s_cbranch_vccnz LRD0_1_%=
LBD0_1_%=:
v_add_f32 v201,v201,v205
M_ v[140:143],a[180:183],a[36:39],v[140:143]
s_cselect_b32 s25,s58,s57
v_add_f32 v204,v204,v178
M_ v[140:143],a[184:187],a[40:43],v[140:143]
E_ v179,v131
M_ v[140:143],a[188:191],a[44:47],v[140:143]
s_add_u32 s57,s57,0x4000
v_add_f32 v204,v204,v179
C_ v161,v178,v179
M_ v[32:35],a[64:67],v[168:171],v[32:35]
E_ v180,v132
M_ v[32:35],a[96:99],v[172:175],v[32:35]
s_mul_i32 s58,s56,0x10000
v_add_f32 v204,v204,v180
M_ v[36:39],a[68:71],v[168:171],v[36:39]
E_ v181,v133
M_ v[36:39],a[100:103],v[172:175],v[36:39]
s_add_u32 s58,s58,0x14000
v_add_f32 v204,v204,v181
C_ v162,v180,v181
M_ v[40:43],a[72:75],v[168:171],v[40:43]
E_ v182,v134
M_ v[40:43],a[104:107],v[172:175],v[40:43]
s_cmp_lg_u32 s55,0
v_add_f32 v204,v204,v182
M_ v[44:47],a[76:79],v[168:171],v[44:47]
E_ v183,v135
M_ v[44:47],a[108:111],v[172:175],v[44:47]
s_cselect_b32 s26,s58,s57
v_add_f32 v204,v204,v183
C_ v163,v182,v183
M_ v[48:51],a[80:83],v[168:171],v[48:51]
v_cndmask_b32 v136,v209,v136,s[46:47]
E_ v176,v136
M_ v[48:51],a[112:115],v[172:175],v[48:51]
v_add_f32 v204,v204,v176
M_ v[52:55],a[84:87],v[168:171],v[52:55]
v_cndmask_b32 v137,v209,v137,s[48:49]
E_ v177,v137
M_ v[52:55],a[116:119],v[172:175],v[52:55]
v_add_f32 v204,v204,v177
C_ v164,v176,v177
v_add_u32 v206,s25,v208
M_ v[56:59],a[88:91],v[168:171],v[56:59]
v_cndmask_b32 v138,v209,v138,s[50:51]
E_ v178,v138
M_ v[56:59],a[120:123],v[172:175],v[56:59]
v_add_f32 v204,v204,v178
v_add_u32 v207,s26,v208
M_ v[60:63],a[92:95],v[168:171],v[60:63]
v_cndmask_b32 v139,v209,v139,s[52:53]
E_ v179,v139
M_ v[60:63],a[124:127],v[172:175],v[60:63]
v_add_f32 v204,v204,v179
C_ v165,v178,v179
M_ v[144:147],a[128:131],a[48:51],v[196:199]
v_mov_b32 v140,v209
E_ v180,v140
M_ v[144:147],a[132:135],a[52:55],v[144:147]
)A"
R"A(ds_read_b128 a[128:131],v206
v_add_f32 v204,v204,v180
M_ v[144:147],a[136:139],a[56:59],v[144:147]
v_mov_b32 v141,v209
E_ v181,v141
M_ v[144:147],a[140:143],a[60:63],v[144:147]
R_ a[132:135],v206,1024
v_add_f32 v204,v204,v181
C_ v166,v180,v181
M_ v[148:151],a[144:147],a[48:51],v[196:199]
v_mov_b32 v142,v209
E_ v182,v142
M_ v[148:151],a[148:151],a[52:55],v[148:151]
R_ a[136:139],v206,2048
v_add_f32 v204,v204,v182
M_ v[148:151],a[152:155],a[56:59],v[148:151]
v_mov_b32 v143,v209
E_ v183,v143
M_ v[148:151],a[156:159],a[60:63],v[148:151]
R_ a[140:143],v206,3072
v_add_f32 v204,v204,v183
C_ v167,v182,v183
M_ v[152:155],a[160:163],a[48:51],v[196:199]
E_ v176,v144
M_ v[152:155],a[164:167],a[52:55],v[152:155]
R_ a[144:147],v206,4096
v_cmp_lt_f32 vcc,s27,v204
M_ v[152:155],a[168:171],a[56:59],v[152:155]
E_ v177,v145
M_ v[152:155],a[172:175],a[60:63],v[152:155]
R_ a[148:151],v206,5120
v_add_f32 v205,v176,v177
C_ v168,v176,v177
M_ v[156:159],a[176:179],a[48:51],v[196:199]
E_ v178,v146
s_cbranch_vccnz LRD0_2_%=
LBD0_2_%=:
v_add_f32 v202,v202,v204
M_ v[156:159],a[180:183],a[52:55],v[156:159]
R_ a[152:155],v206,6144
v_add_f32 v205,v205,v178
M_ v[156:159],a[184:187],a[56:59],v[156:159]
E_ v179,v147
M_ v[156:159],a[188:191],a[60:63],v[156:159]
R_ a[156:159],v206,7168
v_add_f32 v205,v205,v179
C_ v169,v178,v179
M_ v[64:67],a[64:67],v[160:163],v[64:67]
E_ v180,v148
M_ v[64:67],a[96:99],v[164:167],v[64:67]
R_ a[160:163],v206,8192
v_add_f32 v205,v205,v180
M_ v[68:71],a[68:71],v[160:163],v[68:71]
E_ v181,v149
M_ v[68:71],a[100:103],v[164:167],v[68:71]
R_ a[164:167],v206,9216
v_add_f32 v205,v205,v181
C_ v170,v180,v181
M_ v[72:75],a[72:75],v[160:163],v[72:75]
s_and_b32 s78,s22,15
E_ v182,v150
M_ v[72:75],a[104:107],v[164:167],v[72:75]
R_ a[168:171],v206,10240
v_add_f32 v205,v205,v182
M_ v[76:79],a[76:79],v[160:163],v[76:79]
s_lshr_b64 s[22:23],s[22:23],4
E_ v183,v151
M_ v[76:79],a[108:111],v[164:167],v[76:79]
R_ a[172:175],v206,11264
v_add_f32 v205,v205,v183
C_ v171,v182,v183
M_ v[80:83],a[80:83],v[160:163],v[80:83]
s_lshl_b32 s54,s79,28
E_ v176,v152
M_ v[80:83],a[112:115],v[164:167],v[80:83]
R_ a[176:179],v206,12288
v_add_f32 v205,v205,v176
M_ v[84:87],a[84:87],v[160:163],v[84:87]
s_or_b32 s23,s23,s54
E_ v177,v153
M_ v[84:87],a[116:119],v[164:167],v[84:87]
R_ a[180:183],v206,13312
v_add_f32 v205,v205,v177
C_ v172,v176,v177
M_ v[88:91],a[88:91],v[160:163],v[88:91]
s_lshr_b32 s79,s79,4
E_ v178,v154
M_ v[88:91],a[120:123],v[164:167],v[88:91]
R_ a[184:187],v206,14336
v_add_f32 v205,v205,v178
M_ v[92:95],a[92:95],v[160:163],v[92:95]
E_ v179,v155
M_ v[92:95],a[124:127],v[164:167],v[92:95]
R_ a[188:191],v206,15360
v_add_f32 v205,v205,v179
C_ v173,v178,v179
s_waitcnt lgkmcnt(8)
M_ v[128:131],a[128:131],a[0:3],v[184:187]
s_add_u32 s21,s21,1
v_cndmask_b32 v156,v209,v156,s[46:47]
E_ v180,v156
M_ v[128:131],a[132:135],a[4:7],v[128:131]
v_add_f32 v205,v205,v180
M_ v[128:131],a[136:139],a[8:11],v[128:131]
v_cndmask_b32 v157,v209,v157,s[48:49]
E_ v181,v157
M_ v[128:131],a[140:143],a[12:15],v[128:131]
v_add_f32 v205,v205,v181
C_ v174,v180,v181
M_ v[132:135],a[144:147],a[0:3],v[184:187]
v_cndmask_b32 v158,v209,v158,s[50:51]
E_ v182,v158
M_ v[132:135],a[148:151],a[4:7],v[132:135]
v_add_f32 v205,v205,v182
M_ v[132:135],a[152:155],a[8:11],v[132:135]
v_cndmask_b32 v159,v209,v159,s[52:53]
E_ v183,v159
M_ v[132:135],a[156:159],a[12:15],v[132:135]
v_add_f32 v205,v205,v183
C_ v175,v182,v183
v_cmp_lt_f32 vcc,s27,v205
s_cmp_eq_u32 s78,4
s_cbranch_scc1 LVD0_%=
s_branch LDISP_%=
LVNS2_%=:
s_nop 3
s_cbranch_vccnz LRNS2_3_%=
LBNS2_3_%=:
v_add_f32 v203,v203,v205
s_and_b32 s56,s21,1
s_mul_i32 s57,s56,0xfffe4000
s_add_u32 s57,s57,0x1c000
s_add_u32 s60,s57,s75
s_add_u32 s61,s60,0x4000
s_add_u32 s64,s24,s75
s_add_u32 s65,s64,0x4000
s_mul_i32 s57,s56,0x8000
s_add_u32 s57,s57,0x10000
s_add_u32 s62,s57,s75
s_mul_i32 s57,s56,0x10000
s_add_u32 s57,s57,0x14000
s_add_u32 s63,s57,s75
s_add_u32 s66,s64,0x8000
s_add_u32 s67,s64,0xc000
s_add_u32 s57,s21,3
s_cmp_gt_u32 s57,s20
s_cselect_b32 s57,2,1
s_lshl_b32 s57,s57,15
s_add_u32 s24,s24,s57
s_add_u32 s54,s21,1
s_cmp_ge_u32 s54,s20
s_cselect_b32 s55,s73,0
s_and_b32 s56,s54,1
s_mul_i32 s57,s56,0xfffe4000
s_add_u32 s57,s57,0x1c000
s_mul_i32 s58,s56,0x8000
s_add_u32 s58,s58,0x10000
s_cmp_lg_u32 s55,0
s_cselect_b32 s25,s58,s57
s_add_u32 s57,s57,0x4000
s_mul_i32 s58,s56,0x10000
s_add_u32 s58,s58,0x14000
s_cmp_lg_u32 s55,0
s_cselect_b32 s26,s58,s57
s_waitcnt vmcnt(0) lgkmcnt(0)
s_barrier
s_mov_b32 m0,s60
s_nop 0
buffer_load_dwordx4 v208,s[12:15],s64 offen sc1 lds
buffer_load_dwordx4 v208,s[12:15],s64 offen offset:1024 sc1 lds
buffer_load_dwordx4 v208,s[12:15],s64 offen offset:2048 sc1 lds
buffer_load_dwordx4 v208,s[12:15],s64 offen offset:3072 sc1 lds
s_mov_b32 m0,s61
s_nop 0
buffer_load_dwordx4 v208,s[12:15],s65 offen sc1 lds
buffer_load_dwordx4 v208,s[12:15],s65 offen offset:1024 sc1 lds
buffer_load_dwordx4 v208,s[12:15],s65 offen offset:2048 sc1 lds
buffer_load_dwordx4 v208,s[12:15],s65 offen offset:3072 sc1 lds
s_mov_b32 m0,s62
s_nop 0
buffer_load_dwordx4 v208,s[12:15],s66 offen sc1 lds
buffer_load_dwordx4 v208,s[12:15],s66 offen offset:1024 sc1 lds
buffer_load_dwordx4 v208,s[12:15],s66 offen offset:2048 sc1 lds
buffer_load_dwordx4 v208,s[12:15],s66 offen offset:3072 sc1 lds
s_mov_b32 m0,s63
s_nop 0
buffer_load_dwordx4 v208,s[12:15],s67 offen sc1 lds
buffer_load_dwordx4 v208,s[12:15],s67 offen offset:1024 sc1 lds
buffer_load_dwordx4 v208,s[12:15],s67 offen offset:2048 sc1 lds
buffer_load_dwordx4 v208,s[12:15],s67 offen offset:3072 sc1 lds
v_add_u32 v206,s25,v208
v_add_u32 v207,s26,v208
ds_read_b128 a[128:131],v206
R_ a[132:135],v206,1024
R_ a[136:139],v206,2048
R_ a[140:143],v206,3072
R_ a[144:147],v206,4096
R_ a[148:151],v206,5120
R_ a[152:155],v206,6144
R_ a[156:159],v206,7168
R_ a[160:163],v206,8192
R_ a[164:167],v206,9216
R_ a[168:171],v206,10240
R_ a[172:175],v206,11264
R_ a[176:179],v206,12288
R_ a[180:183],v206,13312
R_ a[184:187],v206,14336
R_ a[188:191],v206,15360
s_lshl_b32 s54,s2,2
s_add_u32 s54,s54,0x6400c00
v_mov_b32 v246,s54
v_mov_b32 v247,s86
s_mov_b64 exec,1
global_store_dword v246,v247,s[88:89] sc1
s_mov_b64 exec,-1
s_and_b32 s55,s3,1
s_lshl_b32 s55,s55,14
s_add_u32 s55,s55,0x8000
v_add_u32 v223,s55,v208
ds_read_b128 a[0:3],v223
R_ a[4:7],v223,1024
R_ a[8:11],v223,2048
R_ a[12:15],v223,3072
R_ a[16:19],v223,4096
R_ a[20:23],v223,5120
R_ a[24:27],v223,6144
R_ a[28:31],v223,7168
R_ a[32:35],v223,8192
R_ a[36:39],v223,9216
R_ a[40:43],v223,10240
R_ a[44:47],v223,11264
R_ a[48:51],v223,12288
R_ a[52:55],v223,13312
R_ a[56:59],v223,14336
R_ a[60:63],v223,15360
M_ v[96:99],a[64:67],v[168:171],v[96:99]
M_ v[96:99],a[96:99],v[172:175],v[96:99]
M_ v[100:103],a[68:71],v[168:171],v[100:103]
M_ v[100:103],a[100:103],v[172:175],v[100:103]
M_ v[104:107],a[72:75],v[168:171],v[104:107]
M_ v[104:107],a[104:107],v[172:175],v[104:107]
M_ v[108:111],a[76:79],v[168:171],v[108:111]
M_ v[108:111],a[108:111],v[172:175],v[108:111]
M_ v[112:115],a[80:83],v[168:171],v[112:115]
M_ v[112:115],a[112:115],v[172:175],v[112:115]
M_ v[116:119],a[84:87],v[168:171],v[116:119]
M_ v[116:119],a[116:119],v[172:175],v[116:119]
M_ v[120:123],a[88:91],v[168:171],v[120:123]
M_ v[120:123],a[120:123],v[172:175],v[120:123]
M_ v[124:127],a[92:95],v[168:171],v[124:127]
M_ v[124:127],a[124:127],v[172:175],v[124:127]
s_nop 7
v_mov_b32 v214,v200
v_mov_b32 v215,v201
v_mov_b32 v216,v202
v_mov_b32 v217,v203
v_permlane32_swap_b32 v200,v214
v_permlane32_swap_b32 v201,v215
v_permlane32_swap_b32 v202,v216
v_permlane32_swap_b32 v203,v217
v_add_f32 v200,v200,v214
v_add_f32 v201,v201,v215
v_add_f32 v202,v202,v216
v_add_f32 v203,v203,v217
v_mov_b32 v214,v200
v_mov_b32 v215,v201
v_mov_b32 v216,v202
v_mov_b32 v217,v203
v_permlane16_swap_b32 v200,v214
v_permlane16_swap_b32 v201,v215
v_permlane16_swap_b32 v202,v216
v_permlane16_swap_b32 v203,v217
v_add_f32 v200,v200,v214
v_add_f32 v201,v201,v215
v_add_f32 v202,v202,v216
v_add_f32 v203,v203,v217
v_rcp_f32 v224,v200
v_rcp_f32 v226,v201
v_rcp_f32 v228,v202
v_rcp_f32 v230,v203
s_lshl_b32 s55,s18,2
s_add_u32 s54,s71,0
s_lshl_b32 s54,s54,7
s_add_u32 s54,s54,s55
s_lshl_b32 s54,s54,10
s_add_u32 s76,s16,s54
s_addc_u32 s77,s17,0
v_pk_mul_f32 v[0:1],v[0:1],v[224:225] op_sel_hi:[1,0]
v_pk_mul_f32 v[2:3],v[2:3],v[224:225] op_sel_hi:[1,0]
v_pk_mul_f32 v[4:5],v[4:5],v[224:225] op_sel_hi:[1,0]
v_pk_mul_f32 v[6:7],v[6:7],v[224:225] op_sel_hi:[1,0]
C_ v232,v0,v1
C_ v233,v2,v3
C_ v234,v4,v5
C_ v235,v6,v7
global_store_dwordx4 v208,v[232:235],s[76:77] sc1
v_pk_mul_f32 v[8:9],v[8:9],v[224:225] op_sel_hi:[1,0]
v_pk_mul_f32 v[10:11],v[10:11],v[224:225] op_sel_hi:[1,0]
v_pk_mul_f32 v[12:13],v[12:13],v[224:225] op_sel_hi:[1,0]
v_pk_mul_f32 v[14:15],v[14:15],v[224:225] op_sel_hi:[1,0]
C_ v236,v8,v9
C_ v237,v10,v11
C_ v238,v12,v13
C_ v239,v14,v15
global_store_dwordx4 v208,v[236:239],s[76:77] offset:1024 sc1
v_pk_mul_f32 v[16:17],v[16:17],v[224:225] op_sel_hi:[1,0]
v_pk_mul_f32 v[18:19],v[18:19],v[224:225] op_sel_hi:[1,0]
v_pk_mul_f32 v[20:21],v[20:21],v[224:225] op_sel_hi:[1,0]
v_pk_mul_f32 v[22:23],v[22:23],v[224:225] op_sel_hi:[1,0]
C_ v240,v16,v17
C_ v241,v18,v19
C_ v242,v20,v21
C_ v243,v22,v23
global_store_dwordx4 v208,v[240:243],s[76:77] offset:2048 sc1
v_pk_mul_f32 v[24:25],v[24:25],v[224:225] op_sel_hi:[1,0]
v_pk_mul_f32 v[26:27],v[26:27],v[224:225] op_sel_hi:[1,0]
v_pk_mul_f32 v[28:29],v[28:29],v[224:225] op_sel_hi:[1,0]
v_pk_mul_f32 v[30:31],v[30:31],v[224:225] op_sel_hi:[1,0]
C_ v244,v24,v25
C_ v245,v26,v27
C_ v246,v28,v29
C_ v247,v30,v31
global_store_dwordx4 v208,v[244:247],s[76:77] offset:3072 sc1
s_add_u32 s54,s71,1
s_lshl_b32 s54,s54,7
s_add_u32 s54,s54,s55
s_lshl_b32 s54,s54,10
s_add_u32 s76,s16,s54
s_addc_u32 s77,s17,0
v_pk_mul_f32 v[32:33],v[32:33],v[226:227] op_sel_hi:[1,0]
v_pk_mul_f32 v[34:35],v[34:35],v[226:227] op_sel_hi:[1,0]
v_pk_mul_f32 v[36:37],v[36:37],v[226:227] op_sel_hi:[1,0]
v_pk_mul_f32 v[38:39],v[38:39],v[226:227] op_sel_hi:[1,0]
C_ v232,v32,v33
C_ v233,v34,v35
C_ v234,v36,v37
C_ v235,v38,v39
global_store_dwordx4 v208,v[232:235],s[76:77] sc1
v_pk_mul_f32 v[40:41],v[40:41],v[226:227] op_sel_hi:[1,0]
v_pk_mul_f32 v[42:43],v[42:43],v[226:227] op_sel_hi:[1,0]
v_pk_mul_f32 v[44:45],v[44:45],v[226:227] op_sel_hi:[1,0]
v_pk_mul_f32 v[46:47],v[46:47],v[226:227] op_sel_hi:[1,0]
C_ v236,v40,v41
C_ v237,v42,v43
C_ v238,v44,v45
C_ v239,v46,v47
global_store_dwordx4 v208,v[236:239],s[76:77] offset:1024 sc1
v_pk_mul_f32 v[48:49],v[48:49],v[226:227] op_sel_hi:[1,0]
v_pk_mul_f32 v[50:51],v[50:51],v[226:227] op_sel_hi:[1,0]
v_pk_mul_f32 v[52:53],v[52:53],v[226:227] op_sel_hi:[1,0]
v_pk_mul_f32 v[54:55],v[54:55],v[226:227] op_sel_hi:[1,0]
C_ v240,v48,v49
C_ v241,v50,v51
C_ v242,v52,v53
C_ v243,v54,v55
global_store_dwordx4 v208,v[240:243],s[76:77] offset:2048 sc1
v_pk_mul_f32 v[56:57],v[56:57],v[226:227] op_sel_hi:[1,0]
v_pk_mul_f32 v[58:59],v[58:59],v[226:227] op_sel_hi:[1,0]
v_pk_mul_f32 v[60:61],v[60:61],v[226:227] op_sel_hi:[1,0]
v_pk_mul_f32 v[62:63],v[62:63],v[226:227] op_sel_hi:[1,0]
C_ v244,v56,v57
C_ v245,v58,v59
C_ v246,v60,v61
C_ v247,v62,v63
global_store_dwordx4 v208,v[244:247],s[76:77] offset:3072 sc1
s_add_u32 s54,s71,2
s_lshl_b32 s54,s54,7
s_add_u32 s54,s54,s55
s_lshl_b32 s54,s54,10
s_add_u32 s76,s16,s54
s_addc_u32 s77,s17,0
v_pk_mul_f32 v[64:65],v[64:65],v[228:229] op_sel_hi:[1,0]
v_pk_mul_f32 v[66:67],v[66:67],v[228:229] op_sel_hi:[1,0]
v_pk_mul_f32 v[68:69],v[68:69],v[228:229] op_sel_hi:[1,0]
v_pk_mul_f32 v[70:71],v[70:71],v[228:229] op_sel_hi:[1,0]
C_ v232,v64,v65
C_ v233,v66,v67
C_ v234,v68,v69
C_ v235,v70,v71
global_store_dwordx4 v208,v[232:235],s[76:77] sc1
v_pk_mul_f32 v[72:73],v[72:73],v[228:229] op_sel_hi:[1,0]
v_pk_mul_f32 v[74:75],v[74:75],v[228:229] op_sel_hi:[1,0]
v_pk_mul_f32 v[76:77],v[76:77],v[228:229] op_sel_hi:[1,0]
v_pk_mul_f32 v[78:79],v[78:79],v[228:229] op_sel_hi:[1,0]
C_ v236,v72,v73
C_ v237,v74,v75
C_ v238,v76,v77
C_ v239,v78,v79
global_store_dwordx4 v208,v[236:239],s[76:77] offset:1024 sc1
v_pk_mul_f32 v[80:81],v[80:81],v[228:229] op_sel_hi:[1,0]
v_pk_mul_f32 v[82:83],v[82:83],v[228:229] op_sel_hi:[1,0]
v_pk_mul_f32 v[84:85],v[84:85],v[228:229] op_sel_hi:[1,0]
v_pk_mul_f32 v[86:87],v[86:87],v[228:229] op_sel_hi:[1,0]
C_ v240,v80,v81
C_ v241,v82,v83
C_ v242,v84,v85
C_ v243,v86,v87
global_store_dwordx4 v208,v[240:243],s[76:77] offset:2048 sc1
v_pk_mul_f32 v[88:89],v[88:89],v[228:229] op_sel_hi:[1,0]
v_pk_mul_f32 v[90:91],v[90:91],v[228:229] op_sel_hi:[1,0]
v_pk_mul_f32 v[92:93],v[92:93],v[228:229] op_sel_hi:[1,0]
v_pk_mul_f32 v[94:95],v[94:95],v[228:229] op_sel_hi:[1,0]
C_ v244,v88,v89
C_ v245,v90,v91
C_ v246,v92,v93
C_ v247,v94,v95
global_store_dwordx4 v208,v[244:247],s[76:77] offset:3072 sc1
s_add_u32 s54,s71,3
s_lshl_b32 s54,s54,7
s_add_u32 s54,s54,s55
s_lshl_b32 s54,s54,10
s_add_u32 s76,s16,s54
s_addc_u32 s77,s17,0
v_pk_mul_f32 v[96:97],v[96:97],v[230:231] op_sel_hi:[1,0]
v_pk_mul_f32 v[98:99],v[98:99],v[230:231] op_sel_hi:[1,0]
v_pk_mul_f32 v[100:101],v[100:101],v[230:231] op_sel_hi:[1,0]
v_pk_mul_f32 v[102:103],v[102:103],v[230:231] op_sel_hi:[1,0]
C_ v232,v96,v97
C_ v233,v98,v99
C_ v234,v100,v101
C_ v235,v102,v103
global_store_dwordx4 v208,v[232:235],s[76:77] sc1
v_pk_mul_f32 v[104:105],v[104:105],v[230:231] op_sel_hi:[1,0]
v_pk_mul_f32 v[106:107],v[106:107],v[230:231] op_sel_hi:[1,0]
v_pk_mul_f32 v[108:109],v[108:109],v[230:231] op_sel_hi:[1,0]
v_pk_mul_f32 v[110:111],v[110:111],v[230:231] op_sel_hi:[1,0]
C_ v236,v104,v105
C_ v237,v106,v107
C_ v238,v108,v109
C_ v239,v110,v111
global_store_dwordx4 v208,v[236:239],s[76:77] offset:1024 sc1
v_pk_mul_f32 v[112:113],v[112:113],v[230:231] op_sel_hi:[1,0]
v_pk_mul_f32 v[114:115],v[114:115],v[230:231] op_sel_hi:[1,0]
v_pk_mul_f32 v[116:117],v[116:117],v[230:231] op_sel_hi:[1,0]
v_pk_mul_f32 v[118:119],v[118:119],v[230:231] op_sel_hi:[1,0]
C_ v240,v112,v113
C_ v241,v114,v115
C_ v242,v116,v117
C_ v243,v118,v119
global_store_dwordx4 v208,v[240:243],s[76:77] offset:2048 sc1
v_pk_mul_f32 v[120:121],v[120:121],v[230:231] op_sel_hi:[1,0]
)A"
R"A(v_pk_mul_f32 v[122:123],v[122:123],v[230:231] op_sel_hi:[1,0]
v_pk_mul_f32 v[124:125],v[124:125],v[230:231] op_sel_hi:[1,0]
v_pk_mul_f32 v[126:127],v[126:127],v[230:231] op_sel_hi:[1,0]
C_ v244,v120,v121
C_ v245,v122,v123
C_ v246,v124,v125
C_ v247,v126,v127
global_store_dwordx4 v208,v[244:247],s[76:77] offset:3072 sc1
v_mov_b64 v[0:1],0
v_mov_b64 v[2:3],0
v_mov_b64 v[4:5],0
v_mov_b64 v[6:7],0
v_mov_b64 v[8:9],0
v_mov_b64 v[10:11],0
v_mov_b64 v[12:13],0
v_mov_b64 v[14:15],0
v_mov_b64 v[16:17],0
v_mov_b64 v[18:19],0
v_mov_b64 v[20:21],0
v_mov_b64 v[22:23],0
v_mov_b64 v[24:25],0
v_mov_b64 v[26:27],0
v_mov_b64 v[28:29],0
v_mov_b64 v[30:31],0
v_mov_b64 v[32:33],0
v_mov_b64 v[34:35],0
v_mov_b64 v[36:37],0
v_mov_b64 v[38:39],0
v_mov_b64 v[40:41],0
v_mov_b64 v[42:43],0
v_mov_b64 v[44:45],0
v_mov_b64 v[46:47],0
v_mov_b64 v[48:49],0
v_mov_b64 v[50:51],0
v_mov_b64 v[52:53],0
v_mov_b64 v[54:55],0
v_mov_b64 v[56:57],0
v_mov_b64 v[58:59],0
v_mov_b64 v[60:61],0
v_mov_b64 v[62:63],0
v_mov_b64 v[64:65],0
v_mov_b64 v[66:67],0
v_mov_b64 v[68:69],0
v_mov_b64 v[70:71],0
v_mov_b64 v[72:73],0
v_mov_b64 v[74:75],0
v_mov_b64 v[76:77],0
v_mov_b64 v[78:79],0
v_mov_b64 v[80:81],0
v_mov_b64 v[82:83],0
v_mov_b64 v[84:85],0
v_mov_b64 v[86:87],0
v_mov_b64 v[88:89],0
v_mov_b64 v[90:91],0
v_mov_b64 v[92:93],0
v_mov_b64 v[94:95],0
v_mov_b64 v[96:97],0
v_mov_b64 v[98:99],0
v_mov_b64 v[100:101],0
v_mov_b64 v[102:103],0
v_mov_b64 v[104:105],0
v_mov_b64 v[106:107],0
v_mov_b64 v[108:109],0
v_mov_b64 v[110:111],0
v_mov_b64 v[112:113],0
v_mov_b64 v[114:115],0
v_mov_b64 v[116:117],0
v_mov_b64 v[118:119],0
v_mov_b64 v[120:121],0
v_mov_b64 v[122:123],0
v_mov_b64 v[124:125],0
v_mov_b64 v[126:127],0
v_mov_b32 v200,0
v_mov_b32 v201,0
v_mov_b32 v202,0
v_mov_b32 v203,0
v_mov_b64 v[168:169],0
v_mov_b64 v[172:173],0
v_mov_b64 v[170:171],0
v_mov_b64 v[174:175],0
v_mov_b32 v205,0
s_mov_b64 vcc,0
s_and_b32 s78,s22,15
s_lshr_b64 s[22:23],s[22:23],4
s_lshl_b32 s54,s79,28
s_or_b32 s23,s23,s54
s_lshr_b32 s79,s79,4
s_add_u32 s21,s21,1
s_waitcnt lgkmcnt(0)
s_cmp_eq_u32 s78,3
s_cselect_b32 s54,1,0
s_cmp_eq_u32 s78,4
s_cselect_b32 s54,1,s54
s_cmp_lg_u32 s54,0
s_cselect_b64 s[28:29],s[46:47],-1
s_cselect_b64 s[30:31],s[48:49],-1
s_cselect_b64 s[32:33],s[50:51],-1
s_cselect_b64 s[34:35],s[52:53],-1
M_ v[144:147],a[128:131],a[0:3],0
M_ v[144:147],a[132:135],a[4:7],v[144:147]
M_ v[144:147],a[136:139],a[8:11],v[144:147]
M_ v[144:147],a[140:143],a[12:15],v[144:147]
M_ v[148:151],a[128:131],a[16:19],0
M_ v[148:151],a[132:135],a[20:23],v[148:151]
M_ v[148:151],a[136:139],a[24:27],v[148:151]
M_ v[148:151],a[140:143],a[28:31],v[148:151]
M_ v[152:155],a[128:131],a[32:35],0
M_ v[152:155],a[132:135],a[36:39],v[152:155]
M_ v[152:155],a[136:139],a[40:43],v[152:155]
M_ v[152:155],a[140:143],a[44:47],v[152:155]
M_ v[156:159],a[128:131],a[48:51],0
M_ v[156:159],a[132:135],a[52:55],v[156:159]
M_ v[156:159],a[136:139],a[56:59],v[156:159]
M_ v[156:159],a[140:143],a[60:63],v[156:159]
s_nop 7
v_cndmask_b32 v144,v209,v144,s[28:29]
v_cndmask_b32 v145,v209,v145,s[30:31]
v_cndmask_b32 v146,v209,v146,s[32:33]
v_cndmask_b32 v147,v209,v147,s[34:35]
v_max3_f32 v210,v144,v145,v146
v_max3_f32 v211,v148,v149,v150
v_max3_f32 v212,v152,v153,v154
v_max3_f32 v213,v156,v157,v158
v_max_f32 v210,v210,v147
v_max_f32 v211,v211,v151
v_max_f32 v212,v212,v155
v_max_f32 v213,v213,v159
v_mov_b32 v214,v210
v_mov_b32 v215,v211
v_mov_b32 v216,v212
v_mov_b32 v217,v213
v_permlane32_swap_b32 v210,v214
v_permlane32_swap_b32 v211,v215
v_permlane32_swap_b32 v212,v216
v_permlane32_swap_b32 v213,v217
v_max_f32 v210,v210,v214
v_max_f32 v211,v211,v215
v_max_f32 v212,v212,v216
v_max_f32 v213,v213,v217
v_mov_b32 v214,v210
v_mov_b32 v215,v211
v_mov_b32 v216,v212
v_mov_b32 v217,v213
v_permlane16_swap_b32 v210,v214
v_permlane16_swap_b32 v211,v215
v_permlane16_swap_b32 v212,v216
v_permlane16_swap_b32 v213,v217
v_max_f32 v210,v210,v214
v_max_f32 v211,v211,v215
v_max_f32 v212,v212,v216
v_max_f32 v213,v213,v217
v_sub_f32 v184,0,v210
v_sub_f32 v185,0,v210
v_sub_f32 v186,0,v210
v_sub_f32 v187,0,v210
v_sub_f32 v188,0,v211
v_sub_f32 v189,0,v211
v_sub_f32 v190,0,v211
v_sub_f32 v191,0,v211
v_sub_f32 v192,0,v212
v_sub_f32 v193,0,v212
v_sub_f32 v194,0,v212
v_sub_f32 v195,0,v212
v_sub_f32 v196,0,v213
v_sub_f32 v197,0,v213
v_sub_f32 v198,0,v213
v_sub_f32 v199,0,v213
M_ v[128:131],a[128:131],a[0:3],v[184:187]
M_ v[128:131],a[132:135],a[4:7],v[128:131]
M_ v[128:131],a[136:139],a[8:11],v[128:131]
M_ v[128:131],a[140:143],a[12:15],v[128:131]
M_ v[132:135],a[144:147],a[0:3],v[184:187]
M_ v[132:135],a[148:151],a[4:7],v[132:135]
M_ v[132:135],a[152:155],a[8:11],v[132:135]
M_ v[132:135],a[156:159],a[12:15],v[132:135]
v_mov_b32 v205,0
s_mov_b64 vcc,0
s_branch LDISP_%=
LVNS0_%=:
s_nop 3
s_cbranch_vccnz LRNS0_3_%=
LBNS0_3_%=:
v_add_f32 v203,v203,v205
s_add_u32 s54,s21,1
s_cmp_ge_u32 s54,s20
s_cselect_b32 s55,s73,0
s_and_b32 s56,s54,1
s_mul_i32 s57,s56,0xfffe4000
s_add_u32 s57,s57,0x1c000
s_mul_i32 s58,s56,0x8000
s_add_u32 s58,s58,0x10000
s_cmp_lg_u32 s55,0
s_cselect_b32 s25,s58,s57
s_add_u32 s57,s57,0x4000
s_mul_i32 s58,s56,0x10000
s_add_u32 s58,s58,0x14000
s_cmp_lg_u32 s55,0
s_cselect_b32 s26,s58,s57
s_waitcnt vmcnt(0) lgkmcnt(0)
s_barrier
v_add_u32 v206,s25,v208
v_add_u32 v207,s26,v208
ds_read_b128 a[128:131],v206
R_ a[132:135],v206,1024
R_ a[136:139],v206,2048
R_ a[140:143],v206,3072
R_ a[144:147],v206,4096
R_ a[148:151],v206,5120
R_ a[152:155],v206,6144
R_ a[156:159],v206,7168
R_ a[160:163],v206,8192
R_ a[164:167],v206,9216
R_ a[168:171],v206,10240
R_ a[172:175],v206,11264
R_ a[176:179],v206,12288
R_ a[180:183],v206,13312
R_ a[184:187],v206,14336
R_ a[188:191],v206,15360
s_lshl_b32 s54,s2,2
s_add_u32 s54,s54,0x6400c00
v_mov_b32 v246,s54
v_mov_b32 v247,s86
s_mov_b64 exec,1
global_store_dword v246,v247,s[88:89] sc1
s_mov_b64 exec,-1
s_and_b32 s55,s3,1
s_lshl_b32 s55,s55,14
s_add_u32 s55,s55,0x8000
v_add_u32 v223,s55,v208
ds_read_b128 a[0:3],v223
R_ a[4:7],v223,1024
R_ a[8:11],v223,2048
R_ a[12:15],v223,3072
R_ a[16:19],v223,4096
R_ a[20:23],v223,5120
R_ a[24:27],v223,6144
R_ a[28:31],v223,7168
R_ a[32:35],v223,8192
R_ a[36:39],v223,9216
R_ a[40:43],v223,10240
R_ a[44:47],v223,11264
R_ a[48:51],v223,12288
R_ a[52:55],v223,13312
R_ a[56:59],v223,14336
R_ a[60:63],v223,15360
M_ v[96:99],a[64:67],v[168:171],v[96:99]
M_ v[96:99],a[96:99],v[172:175],v[96:99]
M_ v[100:103],a[68:71],v[168:171],v[100:103]
M_ v[100:103],a[100:103],v[172:175],v[100:103]
M_ v[104:107],a[72:75],v[168:171],v[104:107]
M_ v[104:107],a[104:107],v[172:175],v[104:107]
M_ v[108:111],a[76:79],v[168:171],v[108:111]
M_ v[108:111],a[108:111],v[172:175],v[108:111]
M_ v[112:115],a[80:83],v[168:171],v[112:115]
M_ v[112:115],a[112:115],v[172:175],v[112:115]
M_ v[116:119],a[84:87],v[168:171],v[116:119]
M_ v[116:119],a[116:119],v[172:175],v[116:119]
M_ v[120:123],a[88:91],v[168:171],v[120:123]
M_ v[120:123],a[120:123],v[172:175],v[120:123]
M_ v[124:127],a[92:95],v[168:171],v[124:127]
M_ v[124:127],a[124:127],v[172:175],v[124:127]
s_nop 7
v_mov_b32 v214,v200
v_mov_b32 v215,v201
v_mov_b32 v216,v202
v_mov_b32 v217,v203
v_permlane32_swap_b32 v200,v214
v_permlane32_swap_b32 v201,v215
v_permlane32_swap_b32 v202,v216
v_permlane32_swap_b32 v203,v217
v_add_f32 v200,v200,v214
v_add_f32 v201,v201,v215
v_add_f32 v202,v202,v216
v_add_f32 v203,v203,v217
v_mov_b32 v214,v200
v_mov_b32 v215,v201
v_mov_b32 v216,v202
v_mov_b32 v217,v203
v_permlane16_swap_b32 v200,v214
v_permlane16_swap_b32 v201,v215
v_permlane16_swap_b32 v202,v216
v_permlane16_swap_b32 v203,v217
v_add_f32 v200,v200,v214
v_add_f32 v201,v201,v215
v_add_f32 v202,v202,v216
v_add_f32 v203,v203,v217
v_rcp_f32 v224,v200
v_rcp_f32 v226,v201
v_rcp_f32 v228,v202
v_rcp_f32 v230,v203
s_lshl_b32 s55,s18,2
s_add_u32 s54,s71,0
s_lshl_b32 s54,s54,7
s_add_u32 s54,s54,s55
s_lshl_b32 s54,s54,10
s_add_u32 s76,s16,s54
s_addc_u32 s77,s17,0
v_pk_mul_f32 v[0:1],v[0:1],v[224:225] op_sel_hi:[1,0]
v_pk_mul_f32 v[2:3],v[2:3],v[224:225] op_sel_hi:[1,0]
v_pk_mul_f32 v[4:5],v[4:5],v[224:225] op_sel_hi:[1,0]
v_pk_mul_f32 v[6:7],v[6:7],v[224:225] op_sel_hi:[1,0]
C_ v232,v0,v1
C_ v233,v2,v3
C_ v234,v4,v5
C_ v235,v6,v7
global_store_dwordx4 v208,v[232:235],s[76:77] sc1
v_pk_mul_f32 v[8:9],v[8:9],v[224:225] op_sel_hi:[1,0]
v_pk_mul_f32 v[10:11],v[10:11],v[224:225] op_sel_hi:[1,0]
v_pk_mul_f32 v[12:13],v[12:13],v[224:225] op_sel_hi:[1,0]
v_pk_mul_f32 v[14:15],v[14:15],v[224:225] op_sel_hi:[1,0]
C_ v236,v8,v9
C_ v237,v10,v11
C_ v238,v12,v13
C_ v239,v14,v15
global_store_dwordx4 v208,v[236:239],s[76:77] offset:1024 sc1
v_pk_mul_f32 v[16:17],v[16:17],v[224:225] op_sel_hi:[1,0]
v_pk_mul_f32 v[18:19],v[18:19],v[224:225] op_sel_hi:[1,0]
v_pk_mul_f32 v[20:21],v[20:21],v[224:225] op_sel_hi:[1,0]
v_pk_mul_f32 v[22:23],v[22:23],v[224:225] op_sel_hi:[1,0]
C_ v240,v16,v17
C_ v241,v18,v19
C_ v242,v20,v21
C_ v243,v22,v23
global_store_dwordx4 v208,v[240:243],s[76:77] offset:2048 sc1
v_pk_mul_f32 v[24:25],v[24:25],v[224:225] op_sel_hi:[1,0]
v_pk_mul_f32 v[26:27],v[26:27],v[224:225] op_sel_hi:[1,0]
v_pk_mul_f32 v[28:29],v[28:29],v[224:225] op_sel_hi:[1,0]
v_pk_mul_f32 v[30:31],v[30:31],v[224:225] op_sel_hi:[1,0]
C_ v244,v24,v25
C_ v245,v26,v27
C_ v246,v28,v29
C_ v247,v30,v31
global_store_dwordx4 v208,v[244:247],s[76:77] offset:3072 sc1
s_add_u32 s54,s71,1
s_lshl_b32 s54,s54,7
s_add_u32 s54,s54,s55
s_lshl_b32 s54,s54,10
s_add_u32 s76,s16,s54
s_addc_u32 s77,s17,0
v_pk_mul_f32 v[32:33],v[32:33],v[226:227] op_sel_hi:[1,0]
v_pk_mul_f32 v[34:35],v[34:35],v[226:227] op_sel_hi:[1,0]
v_pk_mul_f32 v[36:37],v[36:37],v[226:227] op_sel_hi:[1,0]
v_pk_mul_f32 v[38:39],v[38:39],v[226:227] op_sel_hi:[1,0]
C_ v232,v32,v33
C_ v233,v34,v35
C_ v234,v36,v37
C_ v235,v38,v39
global_store_dwordx4 v208,v[232:235],s[76:77] sc1
v_pk_mul_f32 v[40:41],v[40:41],v[226:227] op_sel_hi:[1,0]
v_pk_mul_f32 v[42:43],v[42:43],v[226:227] op_sel_hi:[1,0]
v_pk_mul_f32 v[44:45],v[44:45],v[226:227] op_sel_hi:[1,0]
v_pk_mul_f32 v[46:47],v[46:47],v[226:227] op_sel_hi:[1,0]
C_ v236,v40,v41
C_ v237,v42,v43
C_ v238,v44,v45
C_ v239,v46,v47
global_store_dwordx4 v208,v[236:239],s[76:77] offset:1024 sc1
v_pk_mul_f32 v[48:49],v[48:49],v[226:227] op_sel_hi:[1,0]
v_pk_mul_f32 v[50:51],v[50:51],v[226:227] op_sel_hi:[1,0]
v_pk_mul_f32 v[52:53],v[52:53],v[226:227] op_sel_hi:[1,0]
v_pk_mul_f32 v[54:55],v[54:55],v[226:227] op_sel_hi:[1,0]
C_ v240,v48,v49
C_ v241,v50,v51
C_ v242,v52,v53
C_ v243,v54,v55
global_store_dwordx4 v208,v[240:243],s[76:77] offset:2048 sc1
v_pk_mul_f32 v[56:57],v[56:57],v[226:227] op_sel_hi:[1,0]
v_pk_mul_f32 v[58:59],v[58:59],v[226:227] op_sel_hi:[1,0]
v_pk_mul_f32 v[60:61],v[60:61],v[226:227] op_sel_hi:[1,0]
v_pk_mul_f32 v[62:63],v[62:63],v[226:227] op_sel_hi:[1,0]
C_ v244,v56,v57
C_ v245,v58,v59
C_ v246,v60,v61
C_ v247,v62,v63
global_store_dwordx4 v208,v[244:247],s[76:77] offset:3072 sc1
s_add_u32 s54,s71,2
s_lshl_b32 s54,s54,7
s_add_u32 s54,s54,s55
s_lshl_b32 s54,s54,10
s_add_u32 s76,s16,s54
s_addc_u32 s77,s17,0
v_pk_mul_f32 v[64:65],v[64:65],v[228:229] op_sel_hi:[1,0]
v_pk_mul_f32 v[66:67],v[66:67],v[228:229] op_sel_hi:[1,0]
v_pk_mul_f32 v[68:69],v[68:69],v[228:229] op_sel_hi:[1,0]
v_pk_mul_f32 v[70:71],v[70:71],v[228:229] op_sel_hi:[1,0]
C_ v232,v64,v65
C_ v233,v66,v67
C_ v234,v68,v69
C_ v235,v70,v71
global_store_dwordx4 v208,v[232:235],s[76:77] sc1
v_pk_mul_f32 v[72:73],v[72:73],v[228:229] op_sel_hi:[1,0]
v_pk_mul_f32 v[74:75],v[74:75],v[228:229] op_sel_hi:[1,0]
v_pk_mul_f32 v[76:77],v[76:77],v[228:229] op_sel_hi:[1,0]
v_pk_mul_f32 v[78:79],v[78:79],v[228:229] op_sel_hi:[1,0]
C_ v236,v72,v73
C_ v237,v74,v75
C_ v238,v76,v77
C_ v239,v78,v79
global_store_dwordx4 v208,v[236:239],s[76:77] offset:1024 sc1
v_pk_mul_f32 v[80:81],v[80:81],v[228:229] op_sel_hi:[1,0]
v_pk_mul_f32 v[82:83],v[82:83],v[228:229] op_sel_hi:[1,0]
v_pk_mul_f32 v[84:85],v[84:85],v[228:229] op_sel_hi:[1,0]
v_pk_mul_f32 v[86:87],v[86:87],v[228:229] op_sel_hi:[1,0]
C_ v240,v80,v81
C_ v241,v82,v83
C_ v242,v84,v85
C_ v243,v86,v87
global_store_dwordx4 v208,v[240:243],s[76:77] offset:2048 sc1
v_pk_mul_f32 v[88:89],v[88:89],v[228:229] op_sel_hi:[1,0]
v_pk_mul_f32 v[90:91],v[90:91],v[228:229] op_sel_hi:[1,0]
v_pk_mul_f32 v[92:93],v[92:93],v[228:229] op_sel_hi:[1,0]
v_pk_mul_f32 v[94:95],v[94:95],v[228:229] op_sel_hi:[1,0]
C_ v244,v88,v89
C_ v245,v90,v91
C_ v246,v92,v93
C_ v247,v94,v95
global_store_dwordx4 v208,v[244:247],s[76:77] offset:3072 sc1
s_add_u32 s54,s71,3
s_lshl_b32 s54,s54,7
s_add_u32 s54,s54,s55
s_lshl_b32 s54,s54,10
s_add_u32 s76,s16,s54
s_addc_u32 s77,s17,0
v_pk_mul_f32 v[96:97],v[96:97],v[230:231] op_sel_hi:[1,0]
v_pk_mul_f32 v[98:99],v[98:99],v[230:231] op_sel_hi:[1,0]
v_pk_mul_f32 v[100:101],v[100:101],v[230:231] op_sel_hi:[1,0]
v_pk_mul_f32 v[102:103],v[102:103],v[230:231] op_sel_hi:[1,0]
C_ v232,v96,v97
C_ v233,v98,v99
C_ v234,v100,v101
C_ v235,v102,v103
global_store_dwordx4 v208,v[232:235],s[76:77] sc1
v_pk_mul_f32 v[104:105],v[104:105],v[230:231] op_sel_hi:[1,0]
v_pk_mul_f32 v[106:107],v[106:107],v[230:231] op_sel_hi:[1,0]
v_pk_mul_f32 v[108:109],v[108:109],v[230:231] op_sel_hi:[1,0]
v_pk_mul_f32 v[110:111],v[110:111],v[230:231] op_sel_hi:[1,0]
C_ v236,v104,v105
C_ v237,v106,v107
C_ v238,v108,v109
C_ v239,v110,v111
global_store_dwordx4 v208,v[236:239],s[76:77] offset:1024 sc1
v_pk_mul_f32 v[112:113],v[112:113],v[230:231] op_sel_hi:[1,0]
v_pk_mul_f32 v[114:115],v[114:115],v[230:231] op_sel_hi:[1,0]
v_pk_mul_f32 v[116:117],v[116:117],v[230:231] op_sel_hi:[1,0]
v_pk_mul_f32 v[118:119],v[118:119],v[230:231] op_sel_hi:[1,0]
C_ v240,v112,v113
C_ v241,v114,v115
C_ v242,v116,v117
C_ v243,v118,v119
global_store_dwordx4 v208,v[240:243],s[76:77] offset:2048 sc1
v_pk_mul_f32 v[120:121],v[120:121],v[230:231] op_sel_hi:[1,0]
v_pk_mul_f32 v[122:123],v[122:123],v[230:231] op_sel_hi:[1,0]
v_pk_mul_f32 v[124:125],v[124:125],v[230:231] op_sel_hi:[1,0]
v_pk_mul_f32 v[126:127],v[126:127],v[230:231] op_sel_hi:[1,0]
C_ v244,v120,v121
C_ v245,v122,v123
C_ v246,v124,v125
C_ v247,v126,v127
global_store_dwordx4 v208,v[244:247],s[76:77] offset:3072 sc1
v_mov_b64 v[0:1],0
v_mov_b64 v[2:3],0
v_mov_b64 v[4:5],0
)A"
R"A(v_mov_b64 v[6:7],0
v_mov_b64 v[8:9],0
v_mov_b64 v[10:11],0
v_mov_b64 v[12:13],0
v_mov_b64 v[14:15],0
v_mov_b64 v[16:17],0
v_mov_b64 v[18:19],0
v_mov_b64 v[20:21],0
v_mov_b64 v[22:23],0
v_mov_b64 v[24:25],0
v_mov_b64 v[26:27],0
v_mov_b64 v[28:29],0
v_mov_b64 v[30:31],0
v_mov_b64 v[32:33],0
v_mov_b64 v[34:35],0
v_mov_b64 v[36:37],0
v_mov_b64 v[38:39],0
v_mov_b64 v[40:41],0
v_mov_b64 v[42:43],0
v_mov_b64 v[44:45],0
v_mov_b64 v[46:47],0
v_mov_b64 v[48:49],0
v_mov_b64 v[50:51],0
v_mov_b64 v[52:53],0
v_mov_b64 v[54:55],0
v_mov_b64 v[56:57],0
v_mov_b64 v[58:59],0
v_mov_b64 v[60:61],0
v_mov_b64 v[62:63],0
v_mov_b64 v[64:65],0
v_mov_b64 v[66:67],0
v_mov_b64 v[68:69],0
v_mov_b64 v[70:71],0
v_mov_b64 v[72:73],0
v_mov_b64 v[74:75],0
v_mov_b64 v[76:77],0
v_mov_b64 v[78:79],0
v_mov_b64 v[80:81],0
v_mov_b64 v[82:83],0
v_mov_b64 v[84:85],0
v_mov_b64 v[86:87],0
v_mov_b64 v[88:89],0
v_mov_b64 v[90:91],0
v_mov_b64 v[92:93],0
v_mov_b64 v[94:95],0
v_mov_b64 v[96:97],0
v_mov_b64 v[98:99],0
v_mov_b64 v[100:101],0
v_mov_b64 v[102:103],0
v_mov_b64 v[104:105],0
v_mov_b64 v[106:107],0
v_mov_b64 v[108:109],0
v_mov_b64 v[110:111],0
v_mov_b64 v[112:113],0
v_mov_b64 v[114:115],0
v_mov_b64 v[116:117],0
v_mov_b64 v[118:119],0
v_mov_b64 v[120:121],0
v_mov_b64 v[122:123],0
v_mov_b64 v[124:125],0
v_mov_b64 v[126:127],0
v_mov_b32 v200,0
v_mov_b32 v201,0
v_mov_b32 v202,0
v_mov_b32 v203,0
v_mov_b64 v[168:169],0
v_mov_b64 v[172:173],0
v_mov_b64 v[170:171],0
v_mov_b64 v[174:175],0
v_mov_b32 v205,0
s_mov_b64 vcc,0
s_and_b32 s78,s22,15
s_lshr_b64 s[22:23],s[22:23],4
s_lshl_b32 s54,s79,28
s_or_b32 s23,s23,s54
s_lshr_b32 s79,s79,4
s_add_u32 s21,s21,1
s_waitcnt lgkmcnt(0)
s_cmp_eq_u32 s78,3
s_cselect_b32 s54,1,0
s_cmp_eq_u32 s78,4
s_cselect_b32 s54,1,s54
s_cmp_lg_u32 s54,0
s_cselect_b64 s[28:29],s[46:47],-1
s_cselect_b64 s[30:31],s[48:49],-1
s_cselect_b64 s[32:33],s[50:51],-1
s_cselect_b64 s[34:35],s[52:53],-1
M_ v[144:147],a[128:131],a[0:3],0
M_ v[144:147],a[132:135],a[4:7],v[144:147]
M_ v[144:147],a[136:139],a[8:11],v[144:147]
M_ v[144:147],a[140:143],a[12:15],v[144:147]
M_ v[148:151],a[128:131],a[16:19],0
M_ v[148:151],a[132:135],a[20:23],v[148:151]
M_ v[148:151],a[136:139],a[24:27],v[148:151]
M_ v[148:151],a[140:143],a[28:31],v[148:151]
M_ v[152:155],a[128:131],a[32:35],0
M_ v[152:155],a[132:135],a[36:39],v[152:155]
M_ v[152:155],a[136:139],a[40:43],v[152:155]
M_ v[152:155],a[140:143],a[44:47],v[152:155]
M_ v[156:159],a[128:131],a[48:51],0
M_ v[156:159],a[132:135],a[52:55],v[156:159]
M_ v[156:159],a[136:139],a[56:59],v[156:159]
M_ v[156:159],a[140:143],a[60:63],v[156:159]
s_nop 7
v_cndmask_b32 v144,v209,v144,s[28:29]
v_cndmask_b32 v145,v209,v145,s[30:31]
v_cndmask_b32 v146,v209,v146,s[32:33]
v_cndmask_b32 v147,v209,v147,s[34:35]
v_max3_f32 v210,v144,v145,v146
v_max3_f32 v211,v148,v149,v150
v_max3_f32 v212,v152,v153,v154
v_max3_f32 v213,v156,v157,v158
v_max_f32 v210,v210,v147
v_max_f32 v211,v211,v151
v_max_f32 v212,v212,v155
v_max_f32 v213,v213,v159
v_mov_b32 v214,v210
v_mov_b32 v215,v211
v_mov_b32 v216,v212
v_mov_b32 v217,v213
v_permlane32_swap_b32 v210,v214
v_permlane32_swap_b32 v211,v215
v_permlane32_swap_b32 v212,v216
v_permlane32_swap_b32 v213,v217
v_max_f32 v210,v210,v214
v_max_f32 v211,v211,v215
v_max_f32 v212,v212,v216
v_max_f32 v213,v213,v217
v_mov_b32 v214,v210
v_mov_b32 v215,v211
v_mov_b32 v216,v212
v_mov_b32 v217,v213
v_permlane16_swap_b32 v210,v214
v_permlane16_swap_b32 v211,v215
v_permlane16_swap_b32 v212,v216
v_permlane16_swap_b32 v213,v217
v_max_f32 v210,v210,v214
v_max_f32 v211,v211,v215
v_max_f32 v212,v212,v216
v_max_f32 v213,v213,v217
v_sub_f32 v184,0,v210
v_sub_f32 v185,0,v210
v_sub_f32 v186,0,v210
v_sub_f32 v187,0,v210
v_sub_f32 v188,0,v211
v_sub_f32 v189,0,v211
v_sub_f32 v190,0,v211
v_sub_f32 v191,0,v211
v_sub_f32 v192,0,v212
v_sub_f32 v193,0,v212
v_sub_f32 v194,0,v212
v_sub_f32 v195,0,v212
v_sub_f32 v196,0,v213
v_sub_f32 v197,0,v213
v_sub_f32 v198,0,v213
v_sub_f32 v199,0,v213
M_ v[128:131],a[128:131],a[0:3],v[184:187]
M_ v[128:131],a[132:135],a[4:7],v[128:131]
M_ v[128:131],a[136:139],a[8:11],v[128:131]
M_ v[128:131],a[140:143],a[12:15],v[128:131]
M_ v[132:135],a[144:147],a[0:3],v[184:187]
M_ v[132:135],a[148:151],a[4:7],v[132:135]
M_ v[132:135],a[152:155],a[8:11],v[132:135]
M_ v[132:135],a[156:159],a[12:15],v[132:135]
v_mov_b32 v205,0
s_mov_b64 vcc,0
s_branch LDISP_%=
LVN0_%=:
s_nop 3
s_cbranch_vccnz LRN0_3_%=
LBN0_3_%=:
v_add_f32 v203,v203,v205
s_waitcnt vmcnt(0) lgkmcnt(0)
s_barrier
s_and_b32 s78,s22,15
s_lshr_b64 s[22:23],s[22:23],4
s_lshl_b32 s54,s79,28
s_or_b32 s23,s23,s54
s_lshr_b32 s79,s79,4
s_add_u32 s21,s21,1
v_mov_b32 v205,0
s_mov_b64 vcc,0
s_branch LDISP_%=
LVSW_%=:
s_nop 3
s_cbranch_vccnz LRSW_3_%=
LBSW_3_%=:
v_add_f32 v203,v203,v205
s_and_b32 s55,s3,1
s_lshl_b32 s55,s55,14
s_add_u32 s55,s55,0x8000
v_add_u32 v223,s55,v208
ds_read_b128 a[0:3],v223
R_ a[4:7],v223,1024
R_ a[8:11],v223,2048
R_ a[12:15],v223,3072
R_ a[16:19],v223,4096
R_ a[20:23],v223,5120
R_ a[24:27],v223,6144
R_ a[28:31],v223,7168
R_ a[32:35],v223,8192
R_ a[36:39],v223,9216
R_ a[40:43],v223,10240
R_ a[44:47],v223,11264
R_ a[48:51],v223,12288
R_ a[52:55],v223,13312
R_ a[56:59],v223,14336
R_ a[60:63],v223,15360
M_ v[96:99],a[64:67],v[168:171],v[96:99]
M_ v[96:99],a[96:99],v[172:175],v[96:99]
M_ v[100:103],a[68:71],v[168:171],v[100:103]
M_ v[100:103],a[100:103],v[172:175],v[100:103]
M_ v[104:107],a[72:75],v[168:171],v[104:107]
M_ v[104:107],a[104:107],v[172:175],v[104:107]
M_ v[108:111],a[76:79],v[168:171],v[108:111]
M_ v[108:111],a[108:111],v[172:175],v[108:111]
M_ v[112:115],a[80:83],v[168:171],v[112:115]
M_ v[112:115],a[112:115],v[172:175],v[112:115]
M_ v[116:119],a[84:87],v[168:171],v[116:119]
M_ v[116:119],a[116:119],v[172:175],v[116:119]
M_ v[120:123],a[88:91],v[168:171],v[120:123]
M_ v[120:123],a[120:123],v[172:175],v[120:123]
M_ v[124:127],a[92:95],v[168:171],v[124:127]
M_ v[124:127],a[124:127],v[172:175],v[124:127]
s_nop 7
v_mov_b32 v214,v200
v_mov_b32 v215,v201
v_mov_b32 v216,v202
v_mov_b32 v217,v203
v_permlane32_swap_b32 v200,v214
v_permlane32_swap_b32 v201,v215
v_permlane32_swap_b32 v202,v216
v_permlane32_swap_b32 v203,v217
v_add_f32 v200,v200,v214
v_add_f32 v201,v201,v215
v_add_f32 v202,v202,v216
v_add_f32 v203,v203,v217
v_mov_b32 v214,v200
v_mov_b32 v215,v201
v_mov_b32 v216,v202
v_mov_b32 v217,v203
v_permlane16_swap_b32 v200,v214
v_permlane16_swap_b32 v201,v215
v_permlane16_swap_b32 v202,v216
v_permlane16_swap_b32 v203,v217
v_add_f32 v200,v200,v214
v_add_f32 v201,v201,v215
v_add_f32 v202,v202,v216
v_add_f32 v203,v203,v217
v_rcp_f32 v224,v200
v_rcp_f32 v226,v201
v_rcp_f32 v228,v202
v_rcp_f32 v230,v203
s_lshl_b32 s55,s18,2
s_add_u32 s54,s71,0
s_lshl_b32 s54,s54,7
s_add_u32 s54,s54,s55
s_lshl_b32 s54,s54,10
s_add_u32 s76,s16,s54
s_addc_u32 s77,s17,0
v_pk_mul_f32 v[0:1],v[0:1],v[224:225] op_sel_hi:[1,0]
v_pk_mul_f32 v[2:3],v[2:3],v[224:225] op_sel_hi:[1,0]
v_pk_mul_f32 v[4:5],v[4:5],v[224:225] op_sel_hi:[1,0]
v_pk_mul_f32 v[6:7],v[6:7],v[224:225] op_sel_hi:[1,0]
C_ v232,v0,v1
C_ v233,v2,v3
C_ v234,v4,v5
C_ v235,v6,v7
global_store_dwordx4 v208,v[232:235],s[76:77] sc1
v_pk_mul_f32 v[8:9],v[8:9],v[224:225] op_sel_hi:[1,0]
v_pk_mul_f32 v[10:11],v[10:11],v[224:225] op_sel_hi:[1,0]
v_pk_mul_f32 v[12:13],v[12:13],v[224:225] op_sel_hi:[1,0]
v_pk_mul_f32 v[14:15],v[14:15],v[224:225] op_sel_hi:[1,0]
C_ v236,v8,v9
C_ v237,v10,v11
C_ v238,v12,v13
C_ v239,v14,v15
global_store_dwordx4 v208,v[236:239],s[76:77] offset:1024 sc1
v_pk_mul_f32 v[16:17],v[16:17],v[224:225] op_sel_hi:[1,0]
v_pk_mul_f32 v[18:19],v[18:19],v[224:225] op_sel_hi:[1,0]
v_pk_mul_f32 v[20:21],v[20:21],v[224:225] op_sel_hi:[1,0]
v_pk_mul_f32 v[22:23],v[22:23],v[224:225] op_sel_hi:[1,0]
C_ v240,v16,v17
C_ v241,v18,v19
C_ v242,v20,v21
C_ v243,v22,v23
global_store_dwordx4 v208,v[240:243],s[76:77] offset:2048 sc1
v_pk_mul_f32 v[24:25],v[24:25],v[224:225] op_sel_hi:[1,0]
v_pk_mul_f32 v[26:27],v[26:27],v[224:225] op_sel_hi:[1,0]
v_pk_mul_f32 v[28:29],v[28:29],v[224:225] op_sel_hi:[1,0]
v_pk_mul_f32 v[30:31],v[30:31],v[224:225] op_sel_hi:[1,0]
C_ v244,v24,v25
C_ v245,v26,v27
C_ v246,v28,v29
C_ v247,v30,v31
global_store_dwordx4 v208,v[244:247],s[76:77] offset:3072 sc1
s_add_u32 s54,s71,1
s_lshl_b32 s54,s54,7
s_add_u32 s54,s54,s55
s_lshl_b32 s54,s54,10
s_add_u32 s76,s16,s54
s_addc_u32 s77,s17,0
v_pk_mul_f32 v[32:33],v[32:33],v[226:227] op_sel_hi:[1,0]
v_pk_mul_f32 v[34:35],v[34:35],v[226:227] op_sel_hi:[1,0]
v_pk_mul_f32 v[36:37],v[36:37],v[226:227] op_sel_hi:[1,0]
v_pk_mul_f32 v[38:39],v[38:39],v[226:227] op_sel_hi:[1,0]
C_ v232,v32,v33
C_ v233,v34,v35
C_ v234,v36,v37
C_ v235,v38,v39
global_store_dwordx4 v208,v[232:235],s[76:77] sc1
v_pk_mul_f32 v[40:41],v[40:41],v[226:227] op_sel_hi:[1,0]
v_pk_mul_f32 v[42:43],v[42:43],v[226:227] op_sel_hi:[1,0]
v_pk_mul_f32 v[44:45],v[44:45],v[226:227] op_sel_hi:[1,0]
v_pk_mul_f32 v[46:47],v[46:47],v[226:227] op_sel_hi:[1,0]
C_ v236,v40,v41
C_ v237,v42,v43
C_ v238,v44,v45
C_ v239,v46,v47
global_store_dwordx4 v208,v[236:239],s[76:77] offset:1024 sc1
v_pk_mul_f32 v[48:49],v[48:49],v[226:227] op_sel_hi:[1,0]
v_pk_mul_f32 v[50:51],v[50:51],v[226:227] op_sel_hi:[1,0]
v_pk_mul_f32 v[52:53],v[52:53],v[226:227] op_sel_hi:[1,0]
v_pk_mul_f32 v[54:55],v[54:55],v[226:227] op_sel_hi:[1,0]
C_ v240,v48,v49
C_ v241,v50,v51
C_ v242,v52,v53
C_ v243,v54,v55
global_store_dwordx4 v208,v[240:243],s[76:77] offset:2048 sc1
v_pk_mul_f32 v[56:57],v[56:57],v[226:227] op_sel_hi:[1,0]
v_pk_mul_f32 v[58:59],v[58:59],v[226:227] op_sel_hi:[1,0]
v_pk_mul_f32 v[60:61],v[60:61],v[226:227] op_sel_hi:[1,0]
v_pk_mul_f32 v[62:63],v[62:63],v[226:227] op_sel_hi:[1,0]
C_ v244,v56,v57
C_ v245,v58,v59
C_ v246,v60,v61
C_ v247,v62,v63
global_store_dwordx4 v208,v[244:247],s[76:77] offset:3072 sc1
s_add_u32 s54,s71,2
s_lshl_b32 s54,s54,7
s_add_u32 s54,s54,s55
s_lshl_b32 s54,s54,10
s_add_u32 s76,s16,s54
s_addc_u32 s77,s17,0
v_pk_mul_f32 v[64:65],v[64:65],v[228:229] op_sel_hi:[1,0]
v_pk_mul_f32 v[66:67],v[66:67],v[228:229] op_sel_hi:[1,0]
v_pk_mul_f32 v[68:69],v[68:69],v[228:229] op_sel_hi:[1,0]
v_pk_mul_f32 v[70:71],v[70:71],v[228:229] op_sel_hi:[1,0]
C_ v232,v64,v65
C_ v233,v66,v67
C_ v234,v68,v69
C_ v235,v70,v71
global_store_dwordx4 v208,v[232:235],s[76:77] sc1
v_pk_mul_f32 v[72:73],v[72:73],v[228:229] op_sel_hi:[1,0]
v_pk_mul_f32 v[74:75],v[74:75],v[228:229] op_sel_hi:[1,0]
v_pk_mul_f32 v[76:77],v[76:77],v[228:229] op_sel_hi:[1,0]
v_pk_mul_f32 v[78:79],v[78:79],v[228:229] op_sel_hi:[1,0]
C_ v236,v72,v73
C_ v237,v74,v75
C_ v238,v76,v77
C_ v239,v78,v79
global_store_dwordx4 v208,v[236:239],s[76:77] offset:1024 sc1
v_pk_mul_f32 v[80:81],v[80:81],v[228:229] op_sel_hi:[1,0]
v_pk_mul_f32 v[82:83],v[82:83],v[228:229] op_sel_hi:[1,0]
v_pk_mul_f32 v[84:85],v[84:85],v[228:229] op_sel_hi:[1,0]
v_pk_mul_f32 v[86:87],v[86:87],v[228:229] op_sel_hi:[1,0]
C_ v240,v80,v81
C_ v241,v82,v83
C_ v242,v84,v85
C_ v243,v86,v87
global_store_dwordx4 v208,v[240:243],s[76:77] offset:2048 sc1
v_pk_mul_f32 v[88:89],v[88:89],v[228:229] op_sel_hi:[1,0]
v_pk_mul_f32 v[90:91],v[90:91],v[228:229] op_sel_hi:[1,0]
v_pk_mul_f32 v[92:93],v[92:93],v[228:229] op_sel_hi:[1,0]
v_pk_mul_f32 v[94:95],v[94:95],v[228:229] op_sel_hi:[1,0]
C_ v244,v88,v89
C_ v245,v90,v91
C_ v246,v92,v93
C_ v247,v94,v95
global_store_dwordx4 v208,v[244:247],s[76:77] offset:3072 sc1
s_add_u32 s54,s71,3
s_lshl_b32 s54,s54,7
s_add_u32 s54,s54,s55
s_lshl_b32 s54,s54,10
s_add_u32 s76,s16,s54
s_addc_u32 s77,s17,0
v_pk_mul_f32 v[96:97],v[96:97],v[230:231] op_sel_hi:[1,0]
v_pk_mul_f32 v[98:99],v[98:99],v[230:231] op_sel_hi:[1,0]
v_pk_mul_f32 v[100:101],v[100:101],v[230:231] op_sel_hi:[1,0]
v_pk_mul_f32 v[102:103],v[102:103],v[230:231] op_sel_hi:[1,0]
C_ v232,v96,v97
C_ v233,v98,v99
C_ v234,v100,v101
C_ v235,v102,v103
global_store_dwordx4 v208,v[232:235],s[76:77] sc1
v_pk_mul_f32 v[104:105],v[104:105],v[230:231] op_sel_hi:[1,0]
v_pk_mul_f32 v[106:107],v[106:107],v[230:231] op_sel_hi:[1,0]
v_pk_mul_f32 v[108:109],v[108:109],v[230:231] op_sel_hi:[1,0]
v_pk_mul_f32 v[110:111],v[110:111],v[230:231] op_sel_hi:[1,0]
C_ v236,v104,v105
C_ v237,v106,v107
C_ v238,v108,v109
C_ v239,v110,v111
global_store_dwordx4 v208,v[236:239],s[76:77] offset:1024 sc1
v_pk_mul_f32 v[112:113],v[112:113],v[230:231] op_sel_hi:[1,0]
v_pk_mul_f32 v[114:115],v[114:115],v[230:231] op_sel_hi:[1,0]
v_pk_mul_f32 v[116:117],v[116:117],v[230:231] op_sel_hi:[1,0]
v_pk_mul_f32 v[118:119],v[118:119],v[230:231] op_sel_hi:[1,0]
C_ v240,v112,v113
C_ v241,v114,v115
C_ v242,v116,v117
C_ v243,v118,v119
global_store_dwordx4 v208,v[240:243],s[76:77] offset:2048 sc1
v_pk_mul_f32 v[120:121],v[120:121],v[230:231] op_sel_hi:[1,0]
v_pk_mul_f32 v[122:123],v[122:123],v[230:231] op_sel_hi:[1,0]
v_pk_mul_f32 v[124:125],v[124:125],v[230:231] op_sel_hi:[1,0]
v_pk_mul_f32 v[126:127],v[126:127],v[230:231] op_sel_hi:[1,0]
C_ v244,v120,v121
C_ v245,v122,v123
C_ v246,v124,v125
C_ v247,v126,v127
global_store_dwordx4 v208,v[244:247],s[76:77] offset:3072 sc1
v_mov_b64 v[0:1],0
v_mov_b64 v[2:3],0
v_mov_b64 v[4:5],0
v_mov_b64 v[6:7],0
v_mov_b64 v[8:9],0
v_mov_b64 v[10:11],0
v_mov_b64 v[12:13],0
v_mov_b64 v[14:15],0
v_mov_b64 v[16:17],0
v_mov_b64 v[18:19],0
v_mov_b64 v[20:21],0
v_mov_b64 v[22:23],0
v_mov_b64 v[24:25],0
v_mov_b64 v[26:27],0
v_mov_b64 v[28:29],0
v_mov_b64 v[30:31],0
v_mov_b64 v[32:33],0
v_mov_b64 v[34:35],0
v_mov_b64 v[36:37],0
v_mov_b64 v[38:39],0
v_mov_b64 v[40:41],0
v_mov_b64 v[42:43],0
v_mov_b64 v[44:45],0
v_mov_b64 v[46:47],0
v_mov_b64 v[48:49],0
v_mov_b64 v[50:51],0
v_mov_b64 v[52:53],0
v_mov_b64 v[54:55],0
v_mov_b64 v[56:57],0
v_mov_b64 v[58:59],0
v_mov_b64 v[60:61],0
v_mov_b64 v[62:63],0
v_mov_b64 v[64:65],0
v_mov_b64 v[66:67],0
v_mov_b64 v[68:69],0
v_mov_b64 v[70:71],0
v_mov_b64 v[72:73],0
v_mov_b64 v[74:75],0
v_mov_b64 v[76:77],0
v_mov_b64 v[78:79],0
v_mov_b64 v[80:81],0
v_mov_b64 v[82:83],0
v_mov_b64 v[84:85],0
v_mov_b64 v[86:87],0
v_mov_b64 v[88:89],0
v_mov_b64 v[90:91],0
v_mov_b64 v[92:93],0
v_mov_b64 v[94:95],0
v_mov_b64 v[96:97],0
v_mov_b64 v[98:99],0
v_mov_b64 v[100:101],0
v_mov_b64 v[102:103],0
v_mov_b64 v[104:105],0
v_mov_b64 v[106:107],0
)A"
R"A(v_mov_b64 v[108:109],0
v_mov_b64 v[110:111],0
v_mov_b64 v[112:113],0
v_mov_b64 v[114:115],0
v_mov_b64 v[116:117],0
v_mov_b64 v[118:119],0
v_mov_b64 v[120:121],0
v_mov_b64 v[122:123],0
v_mov_b64 v[124:125],0
v_mov_b64 v[126:127],0
v_mov_b32 v200,0
v_mov_b32 v201,0
v_mov_b32 v202,0
v_mov_b32 v203,0
v_mov_b64 v[168:169],0
v_mov_b64 v[172:173],0
v_mov_b64 v[170:171],0
v_mov_b64 v[174:175],0
v_mov_b32 v205,0
s_mov_b64 vcc,0
s_and_b32 s78,s22,15
s_lshr_b64 s[22:23],s[22:23],4
s_lshl_b32 s54,s79,28
s_or_b32 s23,s23,s54
s_lshr_b32 s79,s79,4
s_waitcnt lgkmcnt(0)
s_cmp_eq_u32 s78,3
s_cselect_b32 s54,1,0
s_cmp_eq_u32 s78,4
s_cselect_b32 s54,1,s54
s_cmp_lg_u32 s54,0
s_cselect_b64 s[28:29],s[46:47],-1
s_cselect_b64 s[30:31],s[48:49],-1
s_cselect_b64 s[32:33],s[50:51],-1
s_cselect_b64 s[34:35],s[52:53],-1
M_ v[144:147],a[128:131],a[0:3],0
M_ v[144:147],a[132:135],a[4:7],v[144:147]
M_ v[144:147],a[136:139],a[8:11],v[144:147]
M_ v[144:147],a[140:143],a[12:15],v[144:147]
M_ v[148:151],a[128:131],a[16:19],0
M_ v[148:151],a[132:135],a[20:23],v[148:151]
M_ v[148:151],a[136:139],a[24:27],v[148:151]
M_ v[148:151],a[140:143],a[28:31],v[148:151]
M_ v[152:155],a[128:131],a[32:35],0
M_ v[152:155],a[132:135],a[36:39],v[152:155]
M_ v[152:155],a[136:139],a[40:43],v[152:155]
M_ v[152:155],a[140:143],a[44:47],v[152:155]
M_ v[156:159],a[128:131],a[48:51],0
M_ v[156:159],a[132:135],a[52:55],v[156:159]
M_ v[156:159],a[136:139],a[56:59],v[156:159]
M_ v[156:159],a[140:143],a[60:63],v[156:159]
s_nop 7
v_cndmask_b32 v144,v209,v144,s[28:29]
v_cndmask_b32 v145,v209,v145,s[30:31]
v_cndmask_b32 v146,v209,v146,s[32:33]
v_cndmask_b32 v147,v209,v147,s[34:35]
v_max3_f32 v210,v144,v145,v146
v_max3_f32 v211,v148,v149,v150
v_max3_f32 v212,v152,v153,v154
v_max3_f32 v213,v156,v157,v158
v_max_f32 v210,v210,v147
v_max_f32 v211,v211,v151
v_max_f32 v212,v212,v155
v_max_f32 v213,v213,v159
v_mov_b32 v214,v210
v_mov_b32 v215,v211
v_mov_b32 v216,v212
v_mov_b32 v217,v213
v_permlane32_swap_b32 v210,v214
v_permlane32_swap_b32 v211,v215
v_permlane32_swap_b32 v212,v216
v_permlane32_swap_b32 v213,v217
v_max_f32 v210,v210,v214
v_max_f32 v211,v211,v215
v_max_f32 v212,v212,v216
v_max_f32 v213,v213,v217
v_mov_b32 v214,v210
v_mov_b32 v215,v211
v_mov_b32 v216,v212
v_mov_b32 v217,v213
v_permlane16_swap_b32 v210,v214
v_permlane16_swap_b32 v211,v215
v_permlane16_swap_b32 v212,v216
v_permlane16_swap_b32 v213,v217
v_max_f32 v210,v210,v214
v_max_f32 v211,v211,v215
v_max_f32 v212,v212,v216
v_max_f32 v213,v213,v217
v_sub_f32 v184,0,v210
v_sub_f32 v185,0,v210
v_sub_f32 v186,0,v210
v_sub_f32 v187,0,v210
v_sub_f32 v188,0,v211
v_sub_f32 v189,0,v211
v_sub_f32 v190,0,v211
v_sub_f32 v191,0,v211
v_sub_f32 v192,0,v212
v_sub_f32 v193,0,v212
v_sub_f32 v194,0,v212
v_sub_f32 v195,0,v212
v_sub_f32 v196,0,v213
v_sub_f32 v197,0,v213
v_sub_f32 v198,0,v213
v_sub_f32 v199,0,v213
M_ v[128:131],a[128:131],a[0:3],v[184:187]
M_ v[128:131],a[132:135],a[4:7],v[128:131]
M_ v[128:131],a[136:139],a[8:11],v[128:131]
M_ v[128:131],a[140:143],a[12:15],v[128:131]
M_ v[132:135],a[144:147],a[0:3],v[184:187]
M_ v[132:135],a[148:151],a[4:7],v[132:135]
M_ v[132:135],a[152:155],a[8:11],v[132:135]
M_ v[132:135],a[156:159],a[12:15],v[132:135]
s_branch LDISP_%=
LVEND_%=:
s_nop 3
s_cbranch_vccnz LREND_3_%=
LBEND_3_%=:
v_add_f32 v203,v203,v205
s_and_b32 s90,s2,3
s_lshl_b32 s90,s90,6
v_mbcnt_lo_u32_b32 v253,-1,0
v_mbcnt_hi_u32_b32 v253,-1,v253
v_min_u32 v253,63,v253
v_add_u32 v253,s90,v253
v_lshlrev_b32 v253,2,v253
v_add_u32 v253,0x6400c00,v253
global_load_dword v254,v253,s[88:89] sc1
M_ v[96:99],a[64:67],v[168:171],v[96:99]
M_ v[96:99],a[96:99],v[172:175],v[96:99]
M_ v[100:103],a[68:71],v[168:171],v[100:103]
M_ v[100:103],a[100:103],v[172:175],v[100:103]
M_ v[104:107],a[72:75],v[168:171],v[104:107]
M_ v[104:107],a[104:107],v[172:175],v[104:107]
M_ v[108:111],a[76:79],v[168:171],v[108:111]
M_ v[108:111],a[108:111],v[172:175],v[108:111]
M_ v[112:115],a[80:83],v[168:171],v[112:115]
M_ v[112:115],a[112:115],v[172:175],v[112:115]
M_ v[116:119],a[84:87],v[168:171],v[116:119]
M_ v[116:119],a[116:119],v[172:175],v[116:119]
M_ v[120:123],a[88:91],v[168:171],v[120:123]
M_ v[120:123],a[120:123],v[172:175],v[120:123]
M_ v[124:127],a[92:95],v[168:171],v[124:127]
M_ v[124:127],a[124:127],v[172:175],v[124:127]
s_nop 7
s_mul_i32 s54,s3,20480
v_add_u32 v223,s54,v208
s_xor_b32 s55,s3,2
s_mul_i32 s55,s55,20480
v_add_u32 v222,s55,v208
s_cmp_lg_u32 s73,0
s_cbranch_scc1 LMB_%=
v_mov_b32 v212,v192
v_mov_b32 v213,v202
v_mov_b32 v214,v196
v_mov_b32 v215,v203
ds_write_b128 v223,v[212:215] offset:16384
ds_write_b128 v223,v[64:67]
ds_write_b128 v223,v[68:71] offset:1024
ds_write_b128 v223,v[72:75] offset:2048
ds_write_b128 v223,v[76:79] offset:3072
ds_write_b128 v223,v[80:83] offset:4096
ds_write_b128 v223,v[84:87] offset:5120
ds_write_b128 v223,v[88:91] offset:6144
ds_write_b128 v223,v[92:95] offset:7168
ds_write_b128 v223,v[96:99] offset:8192
ds_write_b128 v223,v[100:103] offset:9216
ds_write_b128 v223,v[104:107] offset:10240
ds_write_b128 v223,v[108:111] offset:11264
ds_write_b128 v223,v[112:115] offset:12288
ds_write_b128 v223,v[116:119] offset:13312
ds_write_b128 v223,v[120:123] offset:14336
ds_write_b128 v223,v[124:127] offset:15360
s_waitcnt lgkmcnt(0)
s_barrier
R_ v[216:219],v222,16384
ds_read_b128 v[128:131],v222
R_ v[132:135],v222,1024
R_ v[136:139],v222,2048
R_ v[140:143],v222,3072
R_ v[144:147],v222,4096
R_ v[148:151],v222,5120
R_ v[152:155],v222,6144
R_ v[156:159],v222,7168
R_ v[160:163],v222,8192
R_ v[164:167],v222,9216
R_ v[168:171],v222,10240
R_ v[172:175],v222,11264
R_ v[176:179],v222,12288
R_ v[180:183],v222,13312
R_ v[242:245],v222,14336
R_ v[246:249],v222,15360
v_mov_b32 v224,v200
s_waitcnt lgkmcnt(12)
v_mov_b32 v226,v217
v_mov_b32 v225,v201
v_mov_b32 v227,v219
v_mov_b32 v228,v224
v_mov_b32 v229,v225
v_mov_b32 v230,v226
v_mov_b32 v231,v227
v_permlane32_swap_b32 v224,v228
v_permlane32_swap_b32 v225,v229
v_permlane32_swap_b32 v226,v230
v_permlane32_swap_b32 v227,v231
v_add_f32 v224,v224,v228
v_add_f32 v225,v225,v229
v_add_f32 v226,v226,v230
v_add_f32 v227,v227,v231
v_mov_b32 v228,v224
v_mov_b32 v229,v225
v_mov_b32 v230,v226
v_mov_b32 v231,v227
v_permlane16_swap_b32 v224,v228
v_permlane16_swap_b32 v225,v229
v_permlane16_swap_b32 v226,v230
v_permlane16_swap_b32 v227,v231
v_add_f32 v224,v224,v228
v_add_f32 v225,v225,v229
v_add_f32 v226,v226,v230
v_add_f32 v227,v227,v231
v_mov_b32 v228,0x7149f2ca
v_cmp_lt_f32 s[28:29],0,v224
v_cmp_lt_f32 s[30:31],0,v226
s_nop 0
v_cndmask_b32 v224,v228,v184,s[28:29]
v_cndmask_b32 v226,v228,v216,s[30:31]
v_min_f32 v240,v224,v226
v_sub_f32 v232,v240,v224
v_sub_f32 v236,v240,v226
E_ v232,v232
E_ v236,v236
v_cmp_lt_f32 s[28:29],0,v225
v_cmp_lt_f32 s[30:31],0,v227
s_nop 0
v_cndmask_b32 v225,v228,v188,s[28:29]
v_cndmask_b32 v227,v228,v218,s[30:31]
v_min_f32 v241,v225,v227
v_sub_f32 v234,v241,v225
v_sub_f32 v238,v241,v227
E_ v234,v234
E_ v238,v238
v_mul_f32 v200,v200,v232
v_fmac_f32 v200,v217,v236
v_pk_mul_f32 v[0:1],v[0:1],v[232:233] op_sel_hi:[1,0]
v_pk_fma_f32 v[0:1],v[128:129],v[236:237],v[0:1] op_sel_hi:[1,0,1]
v_pk_mul_f32 v[2:3],v[2:3],v[232:233] op_sel_hi:[1,0]
v_pk_fma_f32 v[2:3],v[130:131],v[236:237],v[2:3] op_sel_hi:[1,0,1]
v_pk_mul_f32 v[4:5],v[4:5],v[232:233] op_sel_hi:[1,0]
v_pk_fma_f32 v[4:5],v[132:133],v[236:237],v[4:5] op_sel_hi:[1,0,1]
v_pk_mul_f32 v[6:7],v[6:7],v[232:233] op_sel_hi:[1,0]
v_pk_fma_f32 v[6:7],v[134:135],v[236:237],v[6:7] op_sel_hi:[1,0,1]
v_pk_mul_f32 v[8:9],v[8:9],v[232:233] op_sel_hi:[1,0]
v_pk_fma_f32 v[8:9],v[136:137],v[236:237],v[8:9] op_sel_hi:[1,0,1]
v_pk_mul_f32 v[10:11],v[10:11],v[232:233] op_sel_hi:[1,0]
v_pk_fma_f32 v[10:11],v[138:139],v[236:237],v[10:11] op_sel_hi:[1,0,1]
v_pk_mul_f32 v[12:13],v[12:13],v[232:233] op_sel_hi:[1,0]
v_pk_fma_f32 v[12:13],v[140:141],v[236:237],v[12:13] op_sel_hi:[1,0,1]
v_pk_mul_f32 v[14:15],v[14:15],v[232:233] op_sel_hi:[1,0]
v_pk_fma_f32 v[14:15],v[142:143],v[236:237],v[14:15] op_sel_hi:[1,0,1]
v_pk_mul_f32 v[16:17],v[16:17],v[232:233] op_sel_hi:[1,0]
s_waitcnt lgkmcnt(0)
v_pk_fma_f32 v[16:17],v[144:145],v[236:237],v[16:17] op_sel_hi:[1,0,1]
v_pk_mul_f32 v[18:19],v[18:19],v[232:233] op_sel_hi:[1,0]
v_pk_fma_f32 v[18:19],v[146:147],v[236:237],v[18:19] op_sel_hi:[1,0,1]
v_pk_mul_f32 v[20:21],v[20:21],v[232:233] op_sel_hi:[1,0]
v_pk_fma_f32 v[20:21],v[148:149],v[236:237],v[20:21] op_sel_hi:[1,0,1]
v_pk_mul_f32 v[22:23],v[22:23],v[232:233] op_sel_hi:[1,0]
v_pk_fma_f32 v[22:23],v[150:151],v[236:237],v[22:23] op_sel_hi:[1,0,1]
v_pk_mul_f32 v[24:25],v[24:25],v[232:233] op_sel_hi:[1,0]
v_pk_fma_f32 v[24:25],v[152:153],v[236:237],v[24:25] op_sel_hi:[1,0,1]
v_pk_mul_f32 v[26:27],v[26:27],v[232:233] op_sel_hi:[1,0]
v_pk_fma_f32 v[26:27],v[154:155],v[236:237],v[26:27] op_sel_hi:[1,0,1]
v_pk_mul_f32 v[28:29],v[28:29],v[232:233] op_sel_hi:[1,0]
v_pk_fma_f32 v[28:29],v[156:157],v[236:237],v[28:29] op_sel_hi:[1,0,1]
v_pk_mul_f32 v[30:31],v[30:31],v[232:233] op_sel_hi:[1,0]
v_pk_fma_f32 v[30:31],v[158:159],v[236:237],v[30:31] op_sel_hi:[1,0,1]
v_mul_f32 v201,v201,v234
v_fmac_f32 v201,v219,v238
v_pk_mul_f32 v[32:33],v[32:33],v[234:235] op_sel_hi:[1,0]
v_pk_fma_f32 v[32:33],v[160:161],v[238:239],v[32:33] op_sel_hi:[1,0,1]
v_pk_mul_f32 v[34:35],v[34:35],v[234:235] op_sel_hi:[1,0]
v_pk_fma_f32 v[34:35],v[162:163],v[238:239],v[34:35] op_sel_hi:[1,0,1]
v_pk_mul_f32 v[36:37],v[36:37],v[234:235] op_sel_hi:[1,0]
v_pk_fma_f32 v[36:37],v[164:165],v[238:239],v[36:37] op_sel_hi:[1,0,1]
v_pk_mul_f32 v[38:39],v[38:39],v[234:235] op_sel_hi:[1,0]
v_pk_fma_f32 v[38:39],v[166:167],v[238:239],v[38:39] op_sel_hi:[1,0,1]
v_pk_mul_f32 v[40:41],v[40:41],v[234:235] op_sel_hi:[1,0]
v_pk_fma_f32 v[40:41],v[168:169],v[238:239],v[40:41] op_sel_hi:[1,0,1]
v_pk_mul_f32 v[42:43],v[42:43],v[234:235] op_sel_hi:[1,0]
v_pk_fma_f32 v[42:43],v[170:171],v[238:239],v[42:43] op_sel_hi:[1,0,1]
v_pk_mul_f32 v[44:45],v[44:45],v[234:235] op_sel_hi:[1,0]
v_pk_fma_f32 v[44:45],v[172:173],v[238:239],v[44:45] op_sel_hi:[1,0,1]
v_pk_mul_f32 v[46:47],v[46:47],v[234:235] op_sel_hi:[1,0]
v_pk_fma_f32 v[46:47],v[174:175],v[238:239],v[46:47] op_sel_hi:[1,0,1]
v_pk_mul_f32 v[48:49],v[48:49],v[234:235] op_sel_hi:[1,0]
v_pk_fma_f32 v[48:49],v[176:177],v[238:239],v[48:49] op_sel_hi:[1,0,1]
v_pk_mul_f32 v[50:51],v[50:51],v[234:235] op_sel_hi:[1,0]
v_pk_fma_f32 v[50:51],v[178:179],v[238:239],v[50:51] op_sel_hi:[1,0,1]
v_pk_mul_f32 v[52:53],v[52:53],v[234:235] op_sel_hi:[1,0]
v_pk_fma_f32 v[52:53],v[180:181],v[238:239],v[52:53] op_sel_hi:[1,0,1]
v_pk_mul_f32 v[54:55],v[54:55],v[234:235] op_sel_hi:[1,0]
v_pk_fma_f32 v[54:55],v[182:183],v[238:239],v[54:55] op_sel_hi:[1,0,1]
v_pk_mul_f32 v[56:57],v[56:57],v[234:235] op_sel_hi:[1,0]
v_pk_fma_f32 v[56:57],v[242:243],v[238:239],v[56:57] op_sel_hi:[1,0,1]
v_pk_mul_f32 v[58:59],v[58:59],v[234:235] op_sel_hi:[1,0]
v_pk_fma_f32 v[58:59],v[244:245],v[238:239],v[58:59] op_sel_hi:[1,0,1]
v_pk_mul_f32 v[60:61],v[60:61],v[234:235] op_sel_hi:[1,0]
v_pk_fma_f32 v[60:61],v[246:247],v[238:239],v[60:61] op_sel_hi:[1,0,1]
v_pk_mul_f32 v[62:63],v[62:63],v[234:235] op_sel_hi:[1,0]
v_pk_fma_f32 v[62:63],v[248:249],v[238:239],v[62:63] op_sel_hi:[1,0,1]
v_mov_b32 v214,v200
v_mov_b32 v215,v201
s_nop 0
v_permlane32_swap_b32 v200,v214
v_permlane32_swap_b32 v201,v215
v_add_f32 v200,v200,v214
v_add_f32 v201,v201,v215
v_mov_b32 v214,v200
v_mov_b32 v215,v201
s_nop 0
v_permlane16_swap_b32 v200,v214
v_permlane16_swap_b32 v201,v215
v_add_f32 v200,v200,v214
v_add_f32 v201,v201,v215
v_rcp_f32 v224,v200
v_rcp_f32 v226,v201
s_lshl_b32 s55,s18,2
s_add_u32 s54,s70,0
s_lshl_b32 s54,s54,7
s_add_u32 s54,s54,s55
s_lshl_b32 s54,s54,10
s_add_u32 s76,s16,s54
s_addc_u32 s77,s17,0
v_pk_mul_f32 v[0:1],v[0:1],v[224:225] op_sel_hi:[1,0]
v_pk_mul_f32 v[2:3],v[2:3],v[224:225] op_sel_hi:[1,0]
v_pk_mul_f32 v[4:5],v[4:5],v[224:225] op_sel_hi:[1,0]
v_pk_mul_f32 v[6:7],v[6:7],v[224:225] op_sel_hi:[1,0]
C_ v232,v0,v1
C_ v233,v2,v3
C_ v234,v4,v5
C_ v235,v6,v7
global_store_dwordx4 v208,v[232:235],s[76:77] sc1
v_pk_mul_f32 v[8:9],v[8:9],v[224:225] op_sel_hi:[1,0]
v_pk_mul_f32 v[10:11],v[10:11],v[224:225] op_sel_hi:[1,0]
v_pk_mul_f32 v[12:13],v[12:13],v[224:225] op_sel_hi:[1,0]
v_pk_mul_f32 v[14:15],v[14:15],v[224:225] op_sel_hi:[1,0]
C_ v236,v8,v9
C_ v237,v10,v11
C_ v238,v12,v13
C_ v239,v14,v15
global_store_dwordx4 v208,v[236:239],s[76:77] offset:1024 sc1
v_pk_mul_f32 v[16:17],v[16:17],v[224:225] op_sel_hi:[1,0]
v_pk_mul_f32 v[18:19],v[18:19],v[224:225] op_sel_hi:[1,0]
v_pk_mul_f32 v[20:21],v[20:21],v[224:225] op_sel_hi:[1,0]
v_pk_mul_f32 v[22:23],v[22:23],v[224:225] op_sel_hi:[1,0]
C_ v240,v16,v17
C_ v241,v18,v19
C_ v242,v20,v21
C_ v243,v22,v23
global_store_dwordx4 v208,v[240:243],s[76:77] offset:2048 sc1
v_pk_mul_f32 v[24:25],v[24:25],v[224:225] op_sel_hi:[1,0]
v_pk_mul_f32 v[26:27],v[26:27],v[224:225] op_sel_hi:[1,0]
v_pk_mul_f32 v[28:29],v[28:29],v[224:225] op_sel_hi:[1,0]
v_pk_mul_f32 v[30:31],v[30:31],v[224:225] op_sel_hi:[1,0]
C_ v244,v24,v25
C_ v245,v26,v27
C_ v246,v28,v29
C_ v247,v30,v31
global_store_dwordx4 v208,v[244:247],s[76:77] offset:3072 sc1
s_add_u32 s54,s70,1
s_lshl_b32 s54,s54,7
s_add_u32 s54,s54,s55
s_lshl_b32 s54,s54,10
s_add_u32 s76,s16,s54
s_addc_u32 s77,s17,0
v_pk_mul_f32 v[32:33],v[32:33],v[226:227] op_sel_hi:[1,0]
v_pk_mul_f32 v[34:35],v[34:35],v[226:227] op_sel_hi:[1,0]
v_pk_mul_f32 v[36:37],v[36:37],v[226:227] op_sel_hi:[1,0]
v_pk_mul_f32 v[38:39],v[38:39],v[226:227] op_sel_hi:[1,0]
C_ v232,v32,v33
C_ v233,v34,v35
C_ v234,v36,v37
C_ v235,v38,v39
global_store_dwordx4 v208,v[232:235],s[76:77] sc1
v_pk_mul_f32 v[40:41],v[40:41],v[226:227] op_sel_hi:[1,0]
v_pk_mul_f32 v[42:43],v[42:43],v[226:227] op_sel_hi:[1,0]
v_pk_mul_f32 v[44:45],v[44:45],v[226:227] op_sel_hi:[1,0]
v_pk_mul_f32 v[46:47],v[46:47],v[226:227] op_sel_hi:[1,0]
C_ v236,v40,v41
C_ v237,v42,v43
C_ v238,v44,v45
C_ v239,v46,v47
global_store_dwordx4 v208,v[236:239],s[76:77] offset:1024 sc1
v_pk_mul_f32 v[48:49],v[48:49],v[226:227] op_sel_hi:[1,0]
v_pk_mul_f32 v[50:51],v[50:51],v[226:227] op_sel_hi:[1,0]
v_pk_mul_f32 v[52:53],v[52:53],v[226:227] op_sel_hi:[1,0]
)A"
R"A(v_pk_mul_f32 v[54:55],v[54:55],v[226:227] op_sel_hi:[1,0]
C_ v240,v48,v49
C_ v241,v50,v51
C_ v242,v52,v53
C_ v243,v54,v55
global_store_dwordx4 v208,v[240:243],s[76:77] offset:2048 sc1
v_pk_mul_f32 v[56:57],v[56:57],v[226:227] op_sel_hi:[1,0]
v_pk_mul_f32 v[58:59],v[58:59],v[226:227] op_sel_hi:[1,0]
v_pk_mul_f32 v[60:61],v[60:61],v[226:227] op_sel_hi:[1,0]
v_pk_mul_f32 v[62:63],v[62:63],v[226:227] op_sel_hi:[1,0]
C_ v244,v56,v57
C_ v245,v58,v59
C_ v246,v60,v61
C_ v247,v62,v63
global_store_dwordx4 v208,v[244:247],s[76:77] offset:3072 sc1
s_branch LEND_%=
LMB_%=:
s_nop 0
v_mov_b32 v212,v184
v_mov_b32 v213,v200
v_mov_b32 v214,v188
v_mov_b32 v215,v201
ds_write_b128 v223,v[212:215] offset:16384
ds_write_b128 v223,v[0:3]
ds_write_b128 v223,v[4:7] offset:1024
ds_write_b128 v223,v[8:11] offset:2048
ds_write_b128 v223,v[12:15] offset:3072
ds_write_b128 v223,v[16:19] offset:4096
ds_write_b128 v223,v[20:23] offset:5120
ds_write_b128 v223,v[24:27] offset:6144
ds_write_b128 v223,v[28:31] offset:7168
ds_write_b128 v223,v[32:35] offset:8192
ds_write_b128 v223,v[36:39] offset:9216
ds_write_b128 v223,v[40:43] offset:10240
ds_write_b128 v223,v[44:47] offset:11264
ds_write_b128 v223,v[48:51] offset:12288
ds_write_b128 v223,v[52:55] offset:13312
ds_write_b128 v223,v[56:59] offset:14336
ds_write_b128 v223,v[60:63] offset:15360
s_waitcnt lgkmcnt(0)
s_barrier
R_ v[216:219],v222,16384
ds_read_b128 v[128:131],v222
R_ v[132:135],v222,1024
R_ v[136:139],v222,2048
R_ v[140:143],v222,3072
R_ v[144:147],v222,4096
R_ v[148:151],v222,5120
R_ v[152:155],v222,6144
R_ v[156:159],v222,7168
R_ v[160:163],v222,8192
R_ v[164:167],v222,9216
R_ v[168:171],v222,10240
R_ v[172:175],v222,11264
R_ v[176:179],v222,12288
R_ v[180:183],v222,13312
R_ v[242:245],v222,14336
R_ v[246:249],v222,15360
v_mov_b32 v224,v202
s_waitcnt lgkmcnt(12)
v_mov_b32 v226,v217
v_mov_b32 v225,v203
v_mov_b32 v227,v219
v_mov_b32 v228,v224
v_mov_b32 v229,v225
v_mov_b32 v230,v226
v_mov_b32 v231,v227
v_permlane32_swap_b32 v224,v228
v_permlane32_swap_b32 v225,v229
v_permlane32_swap_b32 v226,v230
v_permlane32_swap_b32 v227,v231
v_add_f32 v224,v224,v228
v_add_f32 v225,v225,v229
v_add_f32 v226,v226,v230
v_add_f32 v227,v227,v231
v_mov_b32 v228,v224
v_mov_b32 v229,v225
v_mov_b32 v230,v226
v_mov_b32 v231,v227
v_permlane16_swap_b32 v224,v228
v_permlane16_swap_b32 v225,v229
v_permlane16_swap_b32 v226,v230
v_permlane16_swap_b32 v227,v231
v_add_f32 v224,v224,v228
v_add_f32 v225,v225,v229
v_add_f32 v226,v226,v230
v_add_f32 v227,v227,v231
v_mov_b32 v228,0x7149f2ca
v_cmp_lt_f32 s[28:29],0,v224
v_cmp_lt_f32 s[30:31],0,v226
s_nop 0
v_cndmask_b32 v224,v228,v192,s[28:29]
v_cndmask_b32 v226,v228,v216,s[30:31]
v_min_f32 v240,v224,v226
v_sub_f32 v232,v240,v224
v_sub_f32 v236,v240,v226
E_ v232,v232
E_ v236,v236
v_cmp_lt_f32 s[28:29],0,v225
v_cmp_lt_f32 s[30:31],0,v227
s_nop 0
v_cndmask_b32 v225,v228,v196,s[28:29]
v_cndmask_b32 v227,v228,v218,s[30:31]
v_min_f32 v241,v225,v227
v_sub_f32 v234,v241,v225
v_sub_f32 v238,v241,v227
E_ v234,v234
E_ v238,v238
v_mul_f32 v202,v202,v232
v_fmac_f32 v202,v217,v236
v_pk_mul_f32 v[64:65],v[64:65],v[232:233] op_sel_hi:[1,0]
v_pk_fma_f32 v[64:65],v[128:129],v[236:237],v[64:65] op_sel_hi:[1,0,1]
v_pk_mul_f32 v[66:67],v[66:67],v[232:233] op_sel_hi:[1,0]
v_pk_fma_f32 v[66:67],v[130:131],v[236:237],v[66:67] op_sel_hi:[1,0,1]
v_pk_mul_f32 v[68:69],v[68:69],v[232:233] op_sel_hi:[1,0]
v_pk_fma_f32 v[68:69],v[132:133],v[236:237],v[68:69] op_sel_hi:[1,0,1]
v_pk_mul_f32 v[70:71],v[70:71],v[232:233] op_sel_hi:[1,0]
v_pk_fma_f32 v[70:71],v[134:135],v[236:237],v[70:71] op_sel_hi:[1,0,1]
v_pk_mul_f32 v[72:73],v[72:73],v[232:233] op_sel_hi:[1,0]
v_pk_fma_f32 v[72:73],v[136:137],v[236:237],v[72:73] op_sel_hi:[1,0,1]
v_pk_mul_f32 v[74:75],v[74:75],v[232:233] op_sel_hi:[1,0]
v_pk_fma_f32 v[74:75],v[138:139],v[236:237],v[74:75] op_sel_hi:[1,0,1]
v_pk_mul_f32 v[76:77],v[76:77],v[232:233] op_sel_hi:[1,0]
v_pk_fma_f32 v[76:77],v[140:141],v[236:237],v[76:77] op_sel_hi:[1,0,1]
v_pk_mul_f32 v[78:79],v[78:79],v[232:233] op_sel_hi:[1,0]
v_pk_fma_f32 v[78:79],v[142:143],v[236:237],v[78:79] op_sel_hi:[1,0,1]
v_pk_mul_f32 v[80:81],v[80:81],v[232:233] op_sel_hi:[1,0]
s_waitcnt lgkmcnt(0)
v_pk_fma_f32 v[80:81],v[144:145],v[236:237],v[80:81] op_sel_hi:[1,0,1]
v_pk_mul_f32 v[82:83],v[82:83],v[232:233] op_sel_hi:[1,0]
v_pk_fma_f32 v[82:83],v[146:147],v[236:237],v[82:83] op_sel_hi:[1,0,1]
v_pk_mul_f32 v[84:85],v[84:85],v[232:233] op_sel_hi:[1,0]
v_pk_fma_f32 v[84:85],v[148:149],v[236:237],v[84:85] op_sel_hi:[1,0,1]
v_pk_mul_f32 v[86:87],v[86:87],v[232:233] op_sel_hi:[1,0]
v_pk_fma_f32 v[86:87],v[150:151],v[236:237],v[86:87] op_sel_hi:[1,0,1]
v_pk_mul_f32 v[88:89],v[88:89],v[232:233] op_sel_hi:[1,0]
v_pk_fma_f32 v[88:89],v[152:153],v[236:237],v[88:89] op_sel_hi:[1,0,1]
v_pk_mul_f32 v[90:91],v[90:91],v[232:233] op_sel_hi:[1,0]
v_pk_fma_f32 v[90:91],v[154:155],v[236:237],v[90:91] op_sel_hi:[1,0,1]
v_pk_mul_f32 v[92:93],v[92:93],v[232:233] op_sel_hi:[1,0]
v_pk_fma_f32 v[92:93],v[156:157],v[236:237],v[92:93] op_sel_hi:[1,0,1]
v_pk_mul_f32 v[94:95],v[94:95],v[232:233] op_sel_hi:[1,0]
v_pk_fma_f32 v[94:95],v[158:159],v[236:237],v[94:95] op_sel_hi:[1,0,1]
v_mul_f32 v203,v203,v234
v_fmac_f32 v203,v219,v238
v_pk_mul_f32 v[96:97],v[96:97],v[234:235] op_sel_hi:[1,0]
v_pk_fma_f32 v[96:97],v[160:161],v[238:239],v[96:97] op_sel_hi:[1,0,1]
v_pk_mul_f32 v[98:99],v[98:99],v[234:235] op_sel_hi:[1,0]
v_pk_fma_f32 v[98:99],v[162:163],v[238:239],v[98:99] op_sel_hi:[1,0,1]
v_pk_mul_f32 v[100:101],v[100:101],v[234:235] op_sel_hi:[1,0]
v_pk_fma_f32 v[100:101],v[164:165],v[238:239],v[100:101] op_sel_hi:[1,0,1]
v_pk_mul_f32 v[102:103],v[102:103],v[234:235] op_sel_hi:[1,0]
v_pk_fma_f32 v[102:103],v[166:167],v[238:239],v[102:103] op_sel_hi:[1,0,1]
v_pk_mul_f32 v[104:105],v[104:105],v[234:235] op_sel_hi:[1,0]
v_pk_fma_f32 v[104:105],v[168:169],v[238:239],v[104:105] op_sel_hi:[1,0,1]
v_pk_mul_f32 v[106:107],v[106:107],v[234:235] op_sel_hi:[1,0]
v_pk_fma_f32 v[106:107],v[170:171],v[238:239],v[106:107] op_sel_hi:[1,0,1]
v_pk_mul_f32 v[108:109],v[108:109],v[234:235] op_sel_hi:[1,0]
v_pk_fma_f32 v[108:109],v[172:173],v[238:239],v[108:109] op_sel_hi:[1,0,1]
v_pk_mul_f32 v[110:111],v[110:111],v[234:235] op_sel_hi:[1,0]
v_pk_fma_f32 v[110:111],v[174:175],v[238:239],v[110:111] op_sel_hi:[1,0,1]
v_pk_mul_f32 v[112:113],v[112:113],v[234:235] op_sel_hi:[1,0]
v_pk_fma_f32 v[112:113],v[176:177],v[238:239],v[112:113] op_sel_hi:[1,0,1]
v_pk_mul_f32 v[114:115],v[114:115],v[234:235] op_sel_hi:[1,0]
v_pk_fma_f32 v[114:115],v[178:179],v[238:239],v[114:115] op_sel_hi:[1,0,1]
v_pk_mul_f32 v[116:117],v[116:117],v[234:235] op_sel_hi:[1,0]
v_pk_fma_f32 v[116:117],v[180:181],v[238:239],v[116:117] op_sel_hi:[1,0,1]
v_pk_mul_f32 v[118:119],v[118:119],v[234:235] op_sel_hi:[1,0]
v_pk_fma_f32 v[118:119],v[182:183],v[238:239],v[118:119] op_sel_hi:[1,0,1]
v_pk_mul_f32 v[120:121],v[120:121],v[234:235] op_sel_hi:[1,0]
v_pk_fma_f32 v[120:121],v[242:243],v[238:239],v[120:121] op_sel_hi:[1,0,1]
v_pk_mul_f32 v[122:123],v[122:123],v[234:235] op_sel_hi:[1,0]
v_pk_fma_f32 v[122:123],v[244:245],v[238:239],v[122:123] op_sel_hi:[1,0,1]
v_pk_mul_f32 v[124:125],v[124:125],v[234:235] op_sel_hi:[1,0]
v_pk_fma_f32 v[124:125],v[246:247],v[238:239],v[124:125] op_sel_hi:[1,0,1]
v_pk_mul_f32 v[126:127],v[126:127],v[234:235] op_sel_hi:[1,0]
v_pk_fma_f32 v[126:127],v[248:249],v[238:239],v[126:127] op_sel_hi:[1,0,1]
v_mov_b32 v214,v202
v_mov_b32 v215,v203
s_nop 0
v_permlane32_swap_b32 v202,v214
v_permlane32_swap_b32 v203,v215
v_add_f32 v202,v202,v214
v_add_f32 v203,v203,v215
v_mov_b32 v214,v202
v_mov_b32 v215,v203
s_nop 0
v_permlane16_swap_b32 v202,v214
v_permlane16_swap_b32 v203,v215
v_add_f32 v202,v202,v214
v_add_f32 v203,v203,v215
v_rcp_f32 v224,v202
v_rcp_f32 v226,v203
s_lshl_b32 s55,s18,2
s_add_u32 s54,s70,2
s_lshl_b32 s54,s54,7
s_add_u32 s54,s54,s55
s_lshl_b32 s54,s54,10
s_add_u32 s76,s16,s54
s_addc_u32 s77,s17,0
v_pk_mul_f32 v[64:65],v[64:65],v[224:225] op_sel_hi:[1,0]
v_pk_mul_f32 v[66:67],v[66:67],v[224:225] op_sel_hi:[1,0]
v_pk_mul_f32 v[68:69],v[68:69],v[224:225] op_sel_hi:[1,0]
v_pk_mul_f32 v[70:71],v[70:71],v[224:225] op_sel_hi:[1,0]
C_ v232,v64,v65
C_ v233,v66,v67
C_ v234,v68,v69
C_ v235,v70,v71
global_store_dwordx4 v208,v[232:235],s[76:77] sc1
v_pk_mul_f32 v[72:73],v[72:73],v[224:225] op_sel_hi:[1,0]
v_pk_mul_f32 v[74:75],v[74:75],v[224:225] op_sel_hi:[1,0]
v_pk_mul_f32 v[76:77],v[76:77],v[224:225] op_sel_hi:[1,0]
v_pk_mul_f32 v[78:79],v[78:79],v[224:225] op_sel_hi:[1,0]
C_ v236,v72,v73
C_ v237,v74,v75
C_ v238,v76,v77
C_ v239,v78,v79
global_store_dwordx4 v208,v[236:239],s[76:77] offset:1024 sc1
v_pk_mul_f32 v[80:81],v[80:81],v[224:225] op_sel_hi:[1,0]
v_pk_mul_f32 v[82:83],v[82:83],v[224:225] op_sel_hi:[1,0]
v_pk_mul_f32 v[84:85],v[84:85],v[224:225] op_sel_hi:[1,0]
v_pk_mul_f32 v[86:87],v[86:87],v[224:225] op_sel_hi:[1,0]
C_ v240,v80,v81
C_ v241,v82,v83
C_ v242,v84,v85
C_ v243,v86,v87
global_store_dwordx4 v208,v[240:243],s[76:77] offset:2048 sc1
v_pk_mul_f32 v[88:89],v[88:89],v[224:225] op_sel_hi:[1,0]
v_pk_mul_f32 v[90:91],v[90:91],v[224:225] op_sel_hi:[1,0]
v_pk_mul_f32 v[92:93],v[92:93],v[224:225] op_sel_hi:[1,0]
v_pk_mul_f32 v[94:95],v[94:95],v[224:225] op_sel_hi:[1,0]
C_ v244,v88,v89
C_ v245,v90,v91
C_ v246,v92,v93
C_ v247,v94,v95
global_store_dwordx4 v208,v[244:247],s[76:77] offset:3072 sc1
s_add_u32 s54,s70,3
s_lshl_b32 s54,s54,7
s_add_u32 s54,s54,s55
s_lshl_b32 s54,s54,10
s_add_u32 s76,s16,s54
s_addc_u32 s77,s17,0
v_pk_mul_f32 v[96:97],v[96:97],v[226:227] op_sel_hi:[1,0]
v_pk_mul_f32 v[98:99],v[98:99],v[226:227] op_sel_hi:[1,0]
v_pk_mul_f32 v[100:101],v[100:101],v[226:227] op_sel_hi:[1,0]
v_pk_mul_f32 v[102:103],v[102:103],v[226:227] op_sel_hi:[1,0]
C_ v232,v96,v97
C_ v233,v98,v99
C_ v234,v100,v101
C_ v235,v102,v103
global_store_dwordx4 v208,v[232:235],s[76:77] sc1
v_pk_mul_f32 v[104:105],v[104:105],v[226:227] op_sel_hi:[1,0]
v_pk_mul_f32 v[106:107],v[106:107],v[226:227] op_sel_hi:[1,0]
v_pk_mul_f32 v[108:109],v[108:109],v[226:227] op_sel_hi:[1,0]
v_pk_mul_f32 v[110:111],v[110:111],v[226:227] op_sel_hi:[1,0]
C_ v236,v104,v105
C_ v237,v106,v107
C_ v238,v108,v109
C_ v239,v110,v111
global_store_dwordx4 v208,v[236:239],s[76:77] offset:1024 sc1
v_pk_mul_f32 v[112:113],v[112:113],v[226:227] op_sel_hi:[1,0]
v_pk_mul_f32 v[114:115],v[114:115],v[226:227] op_sel_hi:[1,0]
v_pk_mul_f32 v[116:117],v[116:117],v[226:227] op_sel_hi:[1,0]
v_pk_mul_f32 v[118:119],v[118:119],v[226:227] op_sel_hi:[1,0]
C_ v240,v112,v113
C_ v241,v114,v115
C_ v242,v116,v117
C_ v243,v118,v119
global_store_dwordx4 v208,v[240:243],s[76:77] offset:2048 sc1
v_pk_mul_f32 v[120:121],v[120:121],v[226:227] op_sel_hi:[1,0]
v_pk_mul_f32 v[122:123],v[122:123],v[226:227] op_sel_hi:[1,0]
v_pk_mul_f32 v[124:125],v[124:125],v[226:227] op_sel_hi:[1,0]
v_pk_mul_f32 v[126:127],v[126:127],v[226:227] op_sel_hi:[1,0]
C_ v244,v120,v121
C_ v245,v122,v123
C_ v246,v124,v125
C_ v247,v126,v127
global_store_dwordx4 v208,v[244:247],s[76:77] offset:3072 sc1
LEND_%=:
s_waitcnt vmcnt(8)
v_cmp_ne_u32 vcc,s86,v254
s_cbranch_vccz LQD47_%=
s_mov_b32 s59,0x30d40
LQL47_%=:
s_sleep 2
global_load_dword v254,v253,s[88:89] sc1
s_waitcnt vmcnt(0)
v_cmp_ne_u32 vcc,s86,v254
s_cbranch_vccz LQD47_%=
s_sub_u32 s59,s59,1
s_cmp_lg_u32 s59,0
s_cbranch_scc1 LQL47_%=
LQD47_%=:
v_mbcnt_lo_u32_b32 v89,-1,0
v_mbcnt_hi_u32_b32 v89,-1,v89
v_lshlrev_b32 v88,4,v89
v_lshrrev_b32 v215,2,v88
v_lshrrev_b32 v216,4,v89
v_lshlrev_b32 v216,8,v216
v_add_u32 v216,64,v216
v_lshrrev_b32 v217,2,v216
v_add_u32 v218,0x10000,v88
s_and_b32 s28,s2,3
s_lshl_b32 s28,s28,3
s_lshl_b32 s29,s3,1
s_add_u32 s28,s28,s29
s_add_u32 s31,s28,0
s_mul_i32 s28,s31,5120
s_add_u32 s28,s28,0x4d80000
s_add_u32 s36,s88,s28
s_addc_u32 s37,s89,0
global_load_dwordx4 v[110:113],v88,s[36:37] sc1
global_load_dword v114,v215,s[36:37] offset:1024 sc1
global_load_dwordx4 v[116:119],v88,s[36:37] offset:1280 sc1
global_load_dword v120,v215,s[36:37] offset:2304 sc1
global_load_dwordx4 v[122:125],v216,s[36:37] offset:1280 sc1
global_load_dword v126,v217,s[36:37] offset:2304 sc1
s_add_u32 s36,s36,2560
s_addc_u32 s37,s37,0
global_load_dwordx4 v[128:131],v88,s[36:37] sc1
global_load_dword v132,v215,s[36:37] offset:1024 sc1
global_load_dwordx4 v[134:137],v88,s[36:37] offset:1280 sc1
global_load_dword v138,v215,s[36:37] offset:2304 sc1
global_load_dwordx4 v[140:143],v216,s[36:37] offset:1280 sc1
global_load_dword v144,v217,s[36:37] offset:2304 sc1
s_lshr_b32 s28,s2,2
s_lshl_b32 s28,s28,6
s_add_u32 s28,s28,s31
s_lshl_b32 s28,s28,11
s_add_u32 s28,s28,0x4000000
s_add_u32 s38,s88,s28
s_addc_u32 s39,s89,0
global_load_dwordx4 v[182:185],v88,s[38:39] sc1
global_load_dwordx4 v[186:189],v88,s[38:39] offset:1024 sc1
global_load_dwordx4 v[190:193],v218,s[38:39] sc1
global_load_dwordx4 v[194:197],v218,s[38:39] offset:1024 sc1
s_and_b32 s28,s2,3
s_lshl_b32 s28,s28,3
s_lshl_b32 s29,s3,1
s_add_u32 s28,s28,s29
s_add_u32 s31,s28,1
s_mul_i32 s28,s31,5120
s_add_u32 s28,s28,0x4d80000
s_add_u32 s36,s88,s28
s_addc_u32 s37,s89,0
global_load_dwordx4 v[146:149],v88,s[36:37] sc1
global_load_dword v150,v215,s[36:37] offset:1024 sc1
global_load_dwordx4 v[152:155],v88,s[36:37] offset:1280 sc1
global_load_dword v156,v215,s[36:37] offset:2304 sc1
global_load_dwordx4 v[158:161],v216,s[36:37] offset:1280 sc1
global_load_dword v162,v217,s[36:37] offset:2304 sc1
s_add_u32 s36,s36,2560
s_addc_u32 s37,s37,0
global_load_dwordx4 v[164:167],v88,s[36:37] sc1
global_load_dword v168,v215,s[36:37] offset:1024 sc1
global_load_dwordx4 v[170:173],v88,s[36:37] offset:1280 sc1
global_load_dword v174,v215,s[36:37] offset:2304 sc1
global_load_dwordx4 v[176:179],v216,s[36:37] offset:1280 sc1
global_load_dword v180,v217,s[36:37] offset:2304 sc1
s_lshr_b32 s28,s2,2
s_lshl_b32 s28,s28,6
s_add_u32 s28,s28,s31
s_lshl_b32 s28,s28,11
s_add_u32 s28,s28,0x4000000
s_add_u32 s38,s88,s28
s_addc_u32 s39,s89,0
global_load_dwordx4 v[198:201],v88,s[38:39] sc1
global_load_dwordx4 v[202:205],v88,s[38:39] offset:1024 sc1
global_load_dwordx4 v[206:209],v218,s[38:39] sc1
)A"
R"A(global_load_dwordx4 v[210:213],v218,s[38:39] offset:1024 sc1
s_load_dwordx2 s[12:13],s[0:1],80
s_load_dwordx2 s[8:9],s[0:1],40
v_mbcnt_lo_u32_b32 v89,-1,0
v_mbcnt_hi_u32_b32 v89,-1,v89
v_lshlrev_b32 v88,4,v89
s_and_b32 s54,s2,7
s_lshr_b32 s55,s2,3
s_and_b32 s18,s54,3
s_lshr_b32 s54,s54,2
s_lshl_b32 s54,s54,3
s_and_b32 s56,s55,7
s_add_u32 s16,s54,s56
s_lshr_b32 s17,s55,3
s_lshl_b32 s26,s16,2
s_add_u32 s26,s26,s17
s_lshl_b32 s54,s18,10
s_lshl_b32 s55,s3,4
s_add_u32 s54,s54,s55
s_mul_i32 s54,s54,1280
s_mul_i32 s55,s17,320
s_add_u32 s22,s54,s55
s_mul_i32 s24,s3,5120
v_mov_b32 v80,v88
v_add_u32 v81,0x20000,v88
v_add_u32 v92,0,v89
v_mul_u32_u24 v93,0xcccd,v92
v_lshrrev_b32 v93,20,v93
v_mul_u32_u24 v94,20,v93
v_sub_u32 v92,v92,v94
v_lshrrev_b32 v94,1,v93
v_and_b32 v94,2,v94
v_xor_b32 v92,v92,v94
v_mul_u32_u24 v93,0x500,v93
v_lshl_add_u32 v92,v92,4,v93
v_subrev_u32 v100,0x0,v92
v_add_u32 v92,64,v89
v_mul_u32_u24 v93,0xcccd,v92
v_lshrrev_b32 v93,20,v93
v_mul_u32_u24 v94,20,v93
v_sub_u32 v92,v92,v94
v_lshrrev_b32 v94,1,v93
v_and_b32 v94,2,v94
v_xor_b32 v92,v92,v94
v_mul_u32_u24 v93,0x500,v93
v_lshl_add_u32 v92,v92,4,v93
v_subrev_u32 v101,0x400,v92
v_add_u32 v92,128,v89
v_mul_u32_u24 v93,0xcccd,v92
v_lshrrev_b32 v93,20,v93
v_mul_u32_u24 v94,20,v93
v_sub_u32 v92,v92,v94
v_lshrrev_b32 v94,1,v93
v_and_b32 v94,2,v94
v_xor_b32 v92,v92,v94
v_mul_u32_u24 v93,0x500,v93
v_lshl_add_u32 v92,v92,4,v93
v_subrev_u32 v102,0x800,v92
v_add_u32 v92,192,v89
v_mul_u32_u24 v93,0xcccd,v92
v_lshrrev_b32 v93,20,v93
v_mul_u32_u24 v94,20,v93
v_sub_u32 v92,v92,v94
v_lshrrev_b32 v94,1,v93
v_and_b32 v94,2,v94
v_xor_b32 v92,v92,v94
v_mul_u32_u24 v93,0x500,v93
v_lshl_add_u32 v92,v92,4,v93
v_subrev_u32 v103,0xc00,v92
v_add_u32 v92,256,v89
v_mul_u32_u24 v93,0xcccd,v92
v_lshrrev_b32 v93,20,v93
v_mul_u32_u24 v94,20,v93
v_sub_u32 v92,v92,v94
v_lshrrev_b32 v94,1,v93
v_and_b32 v94,2,v94
v_xor_b32 v92,v92,v94
v_mul_u32_u24 v93,0x500,v93
v_lshl_add_u32 v92,v92,4,v93
v_subrev_u32 v104,0x0,v92
v_lshrrev_b32 v92,2,v89
v_mul_u32_u24 v92,0x140,v92
v_and_b32 v93,3,v89
v_lshl_add_u32 v92,v93,3,v92
v_lshrrev_b32 v93,4,v89
v_and_b32 v93,1,v93
v_lshl_add_u32 v82,v93,5,v92
v_xor_b32 v93,1,v93
v_lshl_add_u32 v83,v93,5,v92
v_add_u32 v84,0xa000,v82
v_add_u32 v85,0xa000,v83
v_add_u32 v86,0x14000,v82
v_add_u32 v87,0x14000,v83
s_waitcnt lgkmcnt(0)
s_and_b32 s9,s9,0xffff
s_mov_b32 s10,0x500000
s_mov_b32 s11,0x20000
s_lshl_b32 s54,s16,3
s_lshl_b32 s55,s3,1
s_add_u32 s54,s54,s55
s_lshl_b32 s54,s54,7
s_lshl_b32 s55,s18,5
s_add_u32 s54,s54,s55
s_lshl_b32 s54,s54,10
s_add_u32 s54,s54,0x2000000
s_add_u32 s4,s88,s54
s_addc_u32 s5,s89,0
s_add_u32 s55,s22,0x0
s_add_u32 m0,s24,0x14000
s_nop 0
buffer_load_dwordx4 v100,s[8:11],s55 offen lds
buffer_load_dwordx4 v101,s[8:11],s55 offen offset:1024 lds
buffer_load_dwordx4 v102,s[8:11],s55 offen offset:2048 lds
buffer_load_dwordx4 v103,s[8:11],s55 offen offset:3072 lds
s_add_u32 m0,s24,0x15000
s_nop 0
buffer_load_dwordx4 v104,s[8:11],s55 offen lds
s_add_u32 s55,s22,0x14000
s_add_u32 m0,s24,0x19000
s_nop 0
buffer_load_dwordx4 v100,s[8:11],s55 offen lds
buffer_load_dwordx4 v101,s[8:11],s55 offen offset:1024 lds
buffer_load_dwordx4 v102,s[8:11],s55 offen offset:2048 lds
buffer_load_dwordx4 v103,s[8:11],s55 offen offset:3072 lds
s_add_u32 m0,s24,0x1a000
s_nop 0
buffer_load_dwordx4 v104,s[8:11],s55 offen lds
s_waitcnt vmcnt(42)
s_barrier
s_and_b32 s32,s2,7
s_lshr_b32 s33,s2,3
s_and_b32 s34,s33,7
s_lshr_b32 s33,s33,3
s_lshl_b32 s33,s33,3
s_add_u32 s32,s32,s33
s_lshl_b32 s34,s34,5
s_add_u32 s32,s32,s34
s_cmp_lg_u32 s3,0
s_cbranch_scc1 LPB48_%=
s_lshl_b32 s54,s32,2
s_add_u32 s54,s54,0x6401000
v_mov_b32 v222,s54
v_mov_b32 v223,s86
s_mov_b64 exec,1
global_store_dword v222,v223,s[88:89] sc1
s_mov_b64 exec,-1
LPB48_%=:
s_sub_u32 s32,15,s16
s_min_u32 s32,s32,s16
s_lshl_b32 s32,s32,5
s_lshl_b32 s33,s18,3
s_add_u32 s32,s32,s33
s_add_u32 s55,s22,0x28000
s_add_u32 m0,s24,0x0
s_nop 0
buffer_load_dwordx4 v100,s[8:11],s55 offen lds
buffer_load_dwordx4 v101,s[8:11],s55 offen offset:1024 lds
buffer_load_dwordx4 v102,s[8:11],s55 offen offset:2048 lds
buffer_load_dwordx4 v103,s[8:11],s55 offen offset:3072 lds
s_add_u32 m0,s24,0x1000
s_nop 0
buffer_load_dwordx4 v104,s[8:11],s55 offen lds
s_add_u32 s55,s22,0x3c000
s_add_u32 m0,s24,0x5000
s_nop 0
buffer_load_dwordx4 v100,s[8:11],s55 offen lds
buffer_load_dwordx4 v101,s[8:11],s55 offen offset:1024 lds
buffer_load_dwordx4 v102,s[8:11],s55 offen offset:2048 lds
buffer_load_dwordx4 v103,s[8:11],s55 offen offset:3072 lds
s_add_u32 m0,s24,0x6000
s_nop 0
buffer_load_dwordx4 v104,s[8:11],s55 offen lds
s_add_u32 s55,s22,0x50000
s_add_u32 m0,s24,0xa000
s_nop 0
buffer_load_dwordx4 v100,s[8:11],s55 offen lds
buffer_load_dwordx4 v101,s[8:11],s55 offen offset:1024 lds
buffer_load_dwordx4 v102,s[8:11],s55 offen offset:2048 lds
buffer_load_dwordx4 v103,s[8:11],s55 offen offset:3072 lds
s_add_u32 m0,s24,0xb000
s_nop 0
buffer_load_dwordx4 v104,s[8:11],s55 offen lds
v_mbcnt_lo_u32_b32 v221,-1,0
v_mbcnt_hi_u32_b32 v221,-1,v221
v_min_u32 v221,7,v221
v_add_u32 v221,s32,v221
v_lshlrev_b32 v221,2,v221
v_add_u32 v221,0x6401000,v221
global_load_dword v222,v221,s[88:89] sc1
s_waitcnt vmcnt(57)
C_ v110,v110,v111
C_ v111,v112,v113
s_waitcnt vmcnt(56)
C_ v112,v114,0
v_mov_b32 v113,0
s_waitcnt vmcnt(55)
C_ v116,v116,v117
C_ v117,v118,v119
s_waitcnt vmcnt(54)
C_ v118,v120,0
v_mov_b32 v119,0
s_waitcnt vmcnt(53)
C_ v122,v122,v123
C_ v123,v124,v125
s_waitcnt vmcnt(52)
C_ v124,v126,0
v_mov_b32 v125,0
s_waitcnt vmcnt(51)
C_ v128,v128,v129
C_ v129,v130,v131
s_waitcnt vmcnt(50)
C_ v130,v132,0
v_mov_b32 v131,0
s_waitcnt vmcnt(49)
C_ v134,v134,v135
C_ v135,v136,v137
s_waitcnt vmcnt(48)
C_ v136,v138,0
v_mov_b32 v137,0
s_waitcnt vmcnt(47)
C_ v140,v140,v141
C_ v141,v142,v143
s_waitcnt vmcnt(46)
C_ v142,v144,0
v_mov_b32 v143,0
s_waitcnt vmcnt(41)
C_ v146,v146,v147
C_ v147,v148,v149
s_waitcnt vmcnt(40)
C_ v148,v150,0
v_mov_b32 v149,0
s_waitcnt vmcnt(39)
C_ v152,v152,v153
C_ v153,v154,v155
s_waitcnt vmcnt(38)
C_ v154,v156,0
v_mov_b32 v155,0
s_waitcnt vmcnt(37)
C_ v158,v158,v159
C_ v159,v160,v161
s_waitcnt vmcnt(36)
C_ v160,v162,0
v_mov_b32 v161,0
s_waitcnt vmcnt(35)
C_ v164,v164,v165
C_ v165,v166,v167
s_waitcnt vmcnt(34)
C_ v166,v168,0
v_mov_b32 v167,0
s_waitcnt vmcnt(33)
C_ v170,v170,v171
C_ v171,v172,v173
s_waitcnt vmcnt(32)
C_ v172,v174,0
v_mov_b32 v173,0
s_waitcnt vmcnt(31)
C_ v176,v176,v177
C_ v177,v178,v179
s_waitcnt vmcnt(30)
C_ v178,v180,0
v_mov_b32 v179,0
M_ a[0:3],v[110:113],v[182:185],0
M_ a[0:3],v[128:131],v[186:189],a[0:3]
M_ a[4:7],v[116:119],v[182:185],0
M_ a[4:7],v[134:137],v[186:189],a[4:7]
M_ a[8:11],v[122:125],v[182:185],0
M_ a[8:11],v[140:143],v[186:189],a[8:11]
M_ a[12:15],v[110:113],v[190:193],0
M_ a[12:15],v[128:131],v[194:197],a[12:15]
M_ a[16:19],v[116:119],v[190:193],0
M_ a[16:19],v[134:137],v[194:197],a[16:19]
M_ a[20:23],v[122:125],v[190:193],0
M_ a[20:23],v[140:143],v[194:197],a[20:23]
s_waitcnt vmcnt(29)
M_ a[24:27],v[146:149],v[198:201],0
s_waitcnt vmcnt(28)
M_ a[24:27],v[164:167],v[202:205],a[24:27]
M_ a[28:31],v[152:155],v[198:201],0
M_ a[28:31],v[170:173],v[202:205],a[28:31]
M_ a[32:35],v[158:161],v[198:201],0
M_ a[32:35],v[176:179],v[202:205],a[32:35]
s_waitcnt vmcnt(27)
M_ a[36:39],v[146:149],v[206:209],0
s_waitcnt vmcnt(26)
M_ a[36:39],v[164:167],v[210:213],a[36:39]
M_ a[40:43],v[152:155],v[206:209],0
M_ a[40:43],v[170:173],v[210:213],a[40:43]
M_ a[44:47],v[158:161],v[206:209],0
M_ a[44:47],v[176:179],v[210:213],a[44:47]
v_and_b32 v219,15,v89
v_lshrrev_b32 v220,4,v89
s_mul_i32 s28,s3,80
s_add_u32 s28,s28,0x20000
v_mul_u32_u24 v218,336,v219
v_lshl_add_u32 v215,v220,3,v218
v_add_u32 v215,s28,v215
v_add_u32 v216,s28,v218
v_add_u32 v216,32,v216
v_cmp_eq_u32 vcc,0,v220
v_add_u32 v218,0x20140,v218
s_nop 0
v_add_u32 v217,40,v216
v_cndmask_b32 v216,v218,v216,vcc
v_cndmask_b32 v217,v218,v217,vcc
AR_ v110,a0
AR_ v114,a4
AR_ v111,a1
AR_ v115,a5
AR_ v112,a2
AR_ v116,a6
AR_ v113,a3
AR_ v117,a7
AR_ v118,a8
AR_ v120,a12
AR_ v124,a16
AR_ v121,a13
AR_ v125,a17
AR_ v122,a14
AR_ v126,a18
AR_ v123,a15
AR_ v127,a19
AR_ v128,a20
AR_ v130,a24
AR_ v134,a28
AR_ v131,a25
AR_ v135,a29
AR_ v132,a26
AR_ v136,a30
AR_ v133,a27
AR_ v137,a31
AR_ v138,a32
AR_ v140,a36
AR_ v144,a40
AR_ v141,a37
AR_ v145,a41
AR_ v142,a38
AR_ v146,a42
AR_ v143,a39
AR_ v147,a43
AR_ v148,a44
v_add_f32 v118,0x358637bd,v118
v_rcp_f32 v118,v118
s_nop 0
v_mul_f32 v110,v110,v118
v_mul_f32 v114,v114,v118
v_mul_f32 v111,v111,v118
v_mul_f32 v115,v115,v118
v_mul_f32 v112,v112,v118
v_mul_f32 v116,v116,v118
v_mul_f32 v113,v113,v118
v_mul_f32 v117,v117,v118
C_ v110,v110,v111
C_ v111,v112,v113
C_ v114,v114,v115
C_ v115,v116,v117
ds_write_b64 v215,v[110:111]
ds_write_b64 v216,v[114:115]
v_add_f32 v128,0x358637bd,v128
v_rcp_f32 v128,v128
s_nop 0
v_mul_f32 v120,v120,v128
v_mul_f32 v124,v124,v128
v_mul_f32 v121,v121,v128
v_mul_f32 v125,v125,v128
v_mul_f32 v122,v122,v128
v_mul_f32 v126,v126,v128
v_mul_f32 v123,v123,v128
v_mul_f32 v127,v127,v128
C_ v120,v120,v121
C_ v121,v122,v123
C_ v124,v124,v125
C_ v125,v126,v127
ds_write_b64 v215,v[120:121] offset:5376
ds_write_b64 v216,v[124:125] offset:5376
v_add_f32 v138,0x358637bd,v138
v_rcp_f32 v138,v138
s_nop 0
v_mul_f32 v130,v130,v138
v_mul_f32 v134,v134,v138
v_mul_f32 v131,v131,v138
v_mul_f32 v135,v135,v138
v_mul_f32 v132,v132,v138
v_mul_f32 v136,v136,v138
v_mul_f32 v133,v133,v138
v_mul_f32 v137,v137,v138
C_ v130,v130,v131
C_ v131,v132,v133
C_ v134,v134,v135
C_ v135,v136,v137
ds_write_b64 v215,v[130:131] offset:40
ds_write_b64 v217,v[134:135]
v_add_f32 v148,0x358637bd,v148
v_rcp_f32 v148,v148
s_nop 0
v_mul_f32 v140,v140,v148
v_mul_f32 v144,v144,v148
v_mul_f32 v141,v141,v148
v_mul_f32 v145,v145,v148
v_mul_f32 v142,v142,v148
v_mul_f32 v146,v146,v148
v_mul_f32 v143,v143,v148
v_mul_f32 v147,v147,v148
C_ v140,v140,v141
C_ v141,v142,v143
C_ v144,v144,v145
C_ v145,v146,v147
ds_write_b64 v215,v[140:141] offset:5416
ds_write_b64 v217,v[144:145] offset:5376
s_waitcnt vmcnt(0)
v_cmp_ne_u32 vcc,s86,v222
s_cbranch_vccz LQD49_%=
s_mov_b32 s59,0x30d40
LQL49_%=:
s_sleep 2
global_load_dword v222,v221,s[88:89] sc1
s_waitcnt vmcnt(0)
v_cmp_ne_u32 vcc,s86,v222
s_cbranch_vccz LQD49_%=
s_sub_u32 s59,s59,1
s_cmp_lg_u32 s59,0
s_cbranch_scc1 LQL49_%=
LQD49_%=:
global_load_dwordx4 a[160:163],v80,s[4:5] sc1
global_load_dwordx4 a[164:167],v80,s[4:5] offset:1024 sc1
global_load_dwordx4 a[168:171],v81,s[4:5] sc1
global_load_dwordx4 a[172:175],v81,s[4:5] offset:1024 sc1
global_load_dwordx4 a[176:179],v80,s[4:5] offset:2048 sc1
global_load_dwordx4 a[180:183],v80,s[4:5] offset:3072 sc1
global_load_dwordx4 a[184:187],v81,s[4:5] offset:2048 sc1
global_load_dwordx4 a[188:191],v81,s[4:5] offset:3072 sc1
s_add_u32 s4,s4,4096
s_addc_u32 s5,s5,0
global_load_dwordx4 a[192:195],v80,s[4:5] sc1
global_load_dwordx4 a[196:199],v80,s[4:5] offset:1024 sc1
global_load_dwordx4 a[200:203],v81,s[4:5] sc1
global_load_dwordx4 a[204:207],v81,s[4:5] offset:1024 sc1
s_waitcnt lgkmcnt(0)
s_barrier
v_and_b32 v215,15,v89
v_mul_u32_u24 v215,336,v215
v_lshrrev_b32 v216,4,v89
v_lshl_add_u32 v215,v216,4,v215
v_add_u32 v215,0x20000,v215
s_add_u32 s28,s3,0
s_cmp_ge_u32 s28,5
s_cselect_b32 s29,1,0
s_mul_i32 s30,s29,5
s_sub_u32 s30,s28,s30
s_mul_i32 s31,s29,5376
s_lshl_b32 s28,s30,6
s_add_u32 s31,s31,s28
v_add_u32 v216,s31,v215
ds_read_b128 v[150:153],v216
s_lshr_b32 s28,s2,2
s_lshl_b32 s28,s28,1
s_add_u32 s28,s28,s29
s_mul_i32 s28,s28,20
s_and_b32 s31,s2,3
s_mul_i32 s31,s31,5
s_add_u32 s28,s28,s31
s_add_u32 s28,s28,s30
s_lshl_b32 s28,s28,10
s_add_u32 s28,s28,0x4e00000
s_add_u32 s36,s88,s28
s_addc_u32 s37,s89,0
s_waitcnt lgkmcnt(0)
global_store_dwordx4 v88,v[150:153],s[36:37] sc1
s_add_u32 s28,s3,4
s_cmp_ge_u32 s28,5
s_cselect_b32 s29,1,0
s_mul_i32 s30,s29,5
s_sub_u32 s30,s28,s30
s_mul_i32 s31,s29,5376
s_lshl_b32 s28,s30,6
s_add_u32 s31,s31,s28
v_add_u32 v216,s31,v215
ds_read_b128 v[154:157],v216
s_lshr_b32 s28,s2,2
s_lshl_b32 s28,s28,1
s_add_u32 s28,s28,s29
s_mul_i32 s28,s28,20
s_and_b32 s31,s2,3
s_mul_i32 s31,s31,5
s_add_u32 s28,s28,s31
s_add_u32 s28,s28,s30
s_lshl_b32 s28,s28,10
s_add_u32 s28,s28,0x4e00000
s_add_u32 s36,s88,s28
s_addc_u32 s37,s89,0
s_waitcnt lgkmcnt(0)
global_store_dwordx4 v88,v[154:157],s[36:37] sc1
s_add_u32 s28,s3,8
s_min_u32 s28,s28,9
s_cmp_ge_u32 s28,5
s_cselect_b32 s29,1,0
s_mul_i32 s30,s29,5
s_sub_u32 s30,s28,s30
s_mul_i32 s31,s29,5376
s_lshl_b32 s28,s30,6
s_add_u32 s31,s31,s28
v_add_u32 v216,s31,v215
ds_read_b128 v[158:161],v216
s_lshr_b32 s28,s2,2
s_lshl_b32 s28,s28,1
s_add_u32 s28,s28,s29
s_mul_i32 s28,s28,20
s_and_b32 s31,s2,3
s_mul_i32 s31,s31,5
s_add_u32 s28,s28,s31
s_add_u32 s28,s28,s30
s_lshl_b32 s28,s28,10
s_add_u32 s28,s28,0x4e00000
s_add_u32 s36,s88,s28
s_addc_u32 s37,s89,0
s_waitcnt lgkmcnt(0)
global_store_dwordx4 v88,v[158:161],s[36:37] sc1
s_waitcnt vmcnt(36)
s_barrier
ds_read_b64_tr_b16 a[0:1],v86
T_ a[2:3],v86,5120
T_ a[4:5],v86,10240
T_ a[6:7],v86,15360
ds_read_b64_tr_b16 a[8:9],v87
T_ a[10:11],v87,5120
T_ a[12:13],v87,10240
T_ a[14:15],v87,15360
T_ a[16:17],v86,64
T_ a[18:19],v86,5184
T_ a[20:21],v86,10304
T_ a[22:23],v86,15424
T_ a[24:25],v87,64
T_ a[26:27],v87,5184
T_ a[28:29],v87,10304
T_ a[30:31],v87,15424
T_ a[32:33],v86,128
T_ a[34:35],v86,5248
T_ a[36:37],v86,10368
T_ a[38:39],v86,15488
T_ a[40:41],v87,128
T_ a[42:43],v87,5248
T_ a[44:45],v87,10368
T_ a[46:47],v87,15488
T_ a[48:49],v86,192
T_ a[50:51],v86,5312
T_ a[52:53],v86,10432
T_ a[54:55],v86,15552
T_ a[56:57],v87,192
T_ a[58:59],v87,5312
T_ a[60:61],v87,10432
T_ a[62:63],v87,15552
T_ a[64:65],v86,256
T_ a[66:67],v86,5376
T_ a[68:69],v86,10496
T_ a[70:71],v86,15616
T_ a[72:73],v87,256
T_ a[74:75],v87,5376
T_ a[76:77],v87,10496
T_ a[78:79],v87,15616
s_waitcnt vmcnt(26)
s_waitcnt lgkmcnt(0)
s_barrier
s_waitcnt vmcnt(14)
M_ v[0:3],a[0:3],a[160:163],0
T_ a[80:81],v86,20480
s_waitcnt vmcnt(13)
M_ v[0:3],a[4:7],a[164:167],v[0:3]
T_ a[82:83],v86,25600
s_waitcnt vmcnt(12)
M_ v[4:7],a[0:3],a[168:171],0
T_ a[84:85],v86,30720
global_load_dwordx4 a[208:211],v80,s[4:5] offset:2048 sc1
s_waitcnt vmcnt(12)
M_ v[4:7],a[4:7],a[172:175],v[4:7]
)A"
R"A(T_ a[86:87],v86,35840
M_ v[8:11],a[8:11],a[160:163],0
T_ a[88:89],v87,20480
M_ v[8:11],a[12:15],a[164:167],v[8:11]
T_ a[90:91],v87,25600
M_ v[12:15],a[8:11],a[168:171],0
T_ a[92:93],v87,30720
global_load_dwordx4 a[212:215],v80,s[4:5] offset:3072 sc1
M_ v[12:15],a[12:15],a[172:175],v[12:15]
T_ a[94:95],v87,35840
M_ v[16:19],a[16:19],a[160:163],0
T_ a[96:97],v86,20544
M_ v[16:19],a[20:23],a[164:167],v[16:19]
T_ a[98:99],v86,25664
M_ v[20:23],a[16:19],a[168:171],0
T_ a[100:101],v86,30784
global_load_dwordx4 a[216:219],v81,s[4:5] offset:2048 sc1
M_ v[20:23],a[20:23],a[172:175],v[20:23]
T_ a[102:103],v86,35904
M_ v[24:27],a[24:27],a[160:163],0
T_ a[104:105],v87,20544
M_ v[24:27],a[28:31],a[164:167],v[24:27]
T_ a[106:107],v87,25664
M_ v[28:31],a[24:27],a[168:171],0
T_ a[108:109],v87,30784
M_ v[28:31],a[28:31],a[172:175],v[28:31]
T_ a[110:111],v87,35904
global_load_dwordx4 a[220:223],v81,s[4:5] offset:3072 sc1
M_ v[32:35],a[32:35],a[160:163],0
T_ a[112:113],v86,20608
M_ v[32:35],a[36:39],a[164:167],v[32:35]
s_add_u32 s55,s22,0x64000
T_ a[114:115],v86,25728
M_ v[36:39],a[32:35],a[168:171],0
s_add_u32 m0,s24,0xf000
T_ a[116:117],v86,30848
M_ v[36:39],a[36:39],a[172:175],v[36:39]
T_ a[118:119],v86,35968
buffer_load_dwordx4 v100,s[8:11],s55 offen lds
M_ v[40:43],a[40:43],a[160:163],0
T_ a[120:121],v87,20608
M_ v[40:43],a[44:47],a[164:167],v[40:43]
T_ a[122:123],v87,25728
M_ v[44:47],a[40:43],a[168:171],0
T_ a[124:125],v87,30848
M_ v[44:47],a[44:47],a[172:175],v[44:47]
T_ a[126:127],v87,35968
buffer_load_dwordx4 v101,s[8:11],s55 offen offset:1024 lds
M_ v[48:51],a[48:51],a[160:163],0
T_ a[128:129],v86,20672
M_ v[48:51],a[52:55],a[164:167],v[48:51]
T_ a[130:131],v86,25792
M_ v[52:55],a[48:51],a[168:171],0
T_ a[132:133],v86,30912
M_ v[52:55],a[52:55],a[172:175],v[52:55]
T_ a[134:135],v86,36032
M_ v[56:59],a[56:59],a[160:163],0
T_ a[136:137],v87,20672
buffer_load_dwordx4 v102,s[8:11],s55 offen offset:2048 lds
M_ v[56:59],a[60:63],a[164:167],v[56:59]
T_ a[138:139],v87,25792
M_ v[60:63],a[56:59],a[168:171],0
T_ a[140:141],v87,30912
M_ v[60:63],a[60:63],a[172:175],v[60:63]
T_ a[142:143],v87,36032
M_ v[64:67],a[64:67],a[160:163],0
T_ a[144:145],v86,20736
buffer_load_dwordx4 v103,s[8:11],s55 offen offset:3072 lds
M_ v[64:67],a[68:71],a[164:167],v[64:67]
T_ a[146:147],v86,25856
M_ v[68:71],a[64:67],a[168:171],0
T_ a[148:149],v86,30976
M_ v[68:71],a[68:71],a[172:175],v[68:71]
s_add_u32 m0,s24,0x10000
T_ a[150:151],v86,36096
M_ v[72:75],a[72:75],a[160:163],0
T_ a[152:153],v87,20736
buffer_load_dwordx4 v104,s[8:11],s55 offen lds
M_ v[72:75],a[76:79],a[164:167],v[72:75]
T_ a[154:155],v87,25856
M_ v[76:79],a[72:75],a[168:171],0
T_ a[156:157],v87,30976
M_ v[76:79],a[76:79],a[172:175],v[76:79]
T_ a[158:159],v87,36096
s_waitcnt vmcnt(19) lgkmcnt(6)
M_ v[0:3],a[80:83],a[176:179],v[0:3]
ds_read_b64_tr_b16 a[0:1],v82
s_waitcnt vmcnt(18)
M_ v[0:3],a[84:87],a[180:183],v[0:3]
s_add_u32 s4,s4,4096
s_addc_u32 s5,s5,0
T_ a[2:3],v82,5120
s_waitcnt vmcnt(17)
M_ v[4:7],a[80:83],a[184:187],v[4:7]
T_ a[4:5],v82,10240
global_load_dwordx4 a[160:163],v80,s[4:5] sc1
s_waitcnt vmcnt(17)
M_ v[4:7],a[84:87],a[188:191],v[4:7]
T_ a[6:7],v82,15360
M_ v[8:11],a[88:91],a[176:179],v[8:11]
ds_read_b64_tr_b16 a[8:9],v83
M_ v[8:11],a[92:95],a[180:183],v[8:11]
T_ a[10:11],v83,5120
M_ v[12:15],a[88:91],a[184:187],v[12:15]
T_ a[12:13],v83,10240
global_load_dwordx4 a[164:167],v80,s[4:5] offset:1024 sc1
M_ v[12:15],a[92:95],a[188:191],v[12:15]
T_ a[14:15],v83,15360
M_ v[16:19],a[96:99],a[176:179],v[16:19]
T_ a[16:17],v82,64
M_ v[16:19],a[100:103],a[180:183],v[16:19]
T_ a[18:19],v82,5184
M_ v[20:23],a[96:99],a[184:187],v[20:23]
T_ a[20:21],v82,10304
global_load_dwordx4 a[168:171],v81,s[4:5] sc1
M_ v[20:23],a[100:103],a[188:191],v[20:23]
T_ a[22:23],v82,15424
M_ v[24:27],a[104:107],a[176:179],v[24:27]
T_ a[24:25],v83,64
M_ v[24:27],a[108:111],a[180:183],v[24:27]
T_ a[26:27],v83,5184
M_ v[28:31],a[104:107],a[184:187],v[28:31]
T_ a[28:29],v83,10304
M_ v[28:31],a[108:111],a[188:191],v[28:31]
T_ a[30:31],v83,15424
global_load_dwordx4 a[172:175],v81,s[4:5] offset:1024 sc1
M_ v[32:35],a[112:115],a[176:179],v[32:35]
T_ a[32:33],v82,128
M_ v[32:35],a[116:119],a[180:183],v[32:35]
s_add_u32 s55,s22,0x78000
T_ a[34:35],v82,5248
M_ v[36:39],a[112:115],a[184:187],v[36:39]
s_add_u32 m0,s24,0x14000
T_ a[36:37],v82,10368
M_ v[36:39],a[116:119],a[188:191],v[36:39]
T_ a[38:39],v82,15488
buffer_load_dwordx4 v100,s[8:11],s55 offen lds
M_ v[40:43],a[120:123],a[176:179],v[40:43]
T_ a[40:41],v83,128
M_ v[40:43],a[124:127],a[180:183],v[40:43]
T_ a[42:43],v83,5248
M_ v[44:47],a[120:123],a[184:187],v[44:47]
T_ a[44:45],v83,10368
M_ v[44:47],a[124:127],a[188:191],v[44:47]
T_ a[46:47],v83,15488
buffer_load_dwordx4 v101,s[8:11],s55 offen offset:1024 lds
M_ v[48:51],a[128:131],a[176:179],v[48:51]
T_ a[48:49],v82,192
M_ v[48:51],a[132:135],a[180:183],v[48:51]
T_ a[50:51],v82,5312
M_ v[52:55],a[128:131],a[184:187],v[52:55]
T_ a[52:53],v82,10432
M_ v[52:55],a[132:135],a[188:191],v[52:55]
T_ a[54:55],v82,15552
M_ v[56:59],a[136:139],a[176:179],v[56:59]
T_ a[56:57],v83,192
buffer_load_dwordx4 v102,s[8:11],s55 offen offset:2048 lds
M_ v[56:59],a[140:143],a[180:183],v[56:59]
T_ a[58:59],v83,5312
M_ v[60:63],a[136:139],a[184:187],v[60:63]
T_ a[60:61],v83,10432
M_ v[60:63],a[140:143],a[188:191],v[60:63]
T_ a[62:63],v83,15552
M_ v[64:67],a[144:147],a[176:179],v[64:67]
T_ a[64:65],v82,256
buffer_load_dwordx4 v103,s[8:11],s55 offen offset:3072 lds
s_waitcnt lgkmcnt(6)
M_ v[64:67],a[148:151],a[180:183],v[64:67]
T_ a[66:67],v82,5376
M_ v[68:71],a[144:147],a[184:187],v[68:71]
T_ a[68:69],v82,10496
M_ v[68:71],a[148:151],a[188:191],v[68:71]
s_add_u32 m0,s24,0x15000
T_ a[70:71],v82,15616
M_ v[72:75],a[152:155],a[176:179],v[72:75]
T_ a[72:73],v83,256
buffer_load_dwordx4 v104,s[8:11],s55 offen lds
M_ v[72:75],a[156:159],a[180:183],v[72:75]
T_ a[74:75],v83,5376
M_ v[76:79],a[152:155],a[184:187],v[76:79]
T_ a[76:77],v83,10496
M_ v[76:79],a[156:159],a[188:191],v[76:79]
T_ a[78:79],v83,15616
s_waitcnt vmcnt(18)
s_waitcnt vmcnt(34)
s_waitcnt lgkmcnt(0)
s_barrier
s_cmp_lg_u32 s3,0
s_cbranch_scc1 LPB52_%=
s_lshl_b32 s54,s2,2
s_add_u32 s54,s54,0x6401400
v_mov_b32 v222,s54
v_mov_b32 v223,s86
s_mov_b64 exec,1
global_store_dword v222,v223,s[88:89] sc1
s_mov_b64 exec,-1
LPB52_%=:
M_ v[0:3],a[0:3],a[192:195],v[0:3]
T_ a[80:81],v82,20480
M_ v[0:3],a[4:7],a[196:199],v[0:3]
T_ a[82:83],v82,25600
M_ v[4:7],a[0:3],a[200:203],v[4:7]
T_ a[84:85],v82,30720
global_load_dwordx4 a[176:179],v80,s[4:5] offset:2048 sc1
M_ v[4:7],a[4:7],a[204:207],v[4:7]
T_ a[86:87],v82,35840
M_ v[8:11],a[8:11],a[192:195],v[8:11]
T_ a[88:89],v83,20480
M_ v[8:11],a[12:15],a[196:199],v[8:11]
T_ a[90:91],v83,25600
M_ v[12:15],a[8:11],a[200:203],v[12:15]
T_ a[92:93],v83,30720
global_load_dwordx4 a[180:183],v80,s[4:5] offset:3072 sc1
M_ v[12:15],a[12:15],a[204:207],v[12:15]
T_ a[94:95],v83,35840
M_ v[16:19],a[16:19],a[192:195],v[16:19]
T_ a[96:97],v82,20544
M_ v[16:19],a[20:23],a[196:199],v[16:19]
T_ a[98:99],v82,25664
M_ v[20:23],a[16:19],a[200:203],v[20:23]
T_ a[100:101],v82,30784
global_load_dwordx4 a[184:187],v81,s[4:5] offset:2048 sc1
M_ v[20:23],a[20:23],a[204:207],v[20:23]
T_ a[102:103],v82,35904
M_ v[24:27],a[24:27],a[192:195],v[24:27]
T_ a[104:105],v83,20544
M_ v[24:27],a[28:31],a[196:199],v[24:27]
T_ a[106:107],v83,25664
M_ v[28:31],a[24:27],a[200:203],v[28:31]
T_ a[108:109],v83,30784
M_ v[28:31],a[28:31],a[204:207],v[28:31]
T_ a[110:111],v83,35904
global_load_dwordx4 a[188:191],v81,s[4:5] offset:3072 sc1
M_ v[32:35],a[32:35],a[192:195],v[32:35]
T_ a[112:113],v82,20608
M_ v[32:35],a[36:39],a[196:199],v[32:35]
s_add_u32 s55,s22,0x8c000
T_ a[114:115],v82,25728
M_ v[36:39],a[32:35],a[200:203],v[36:39]
s_add_u32 m0,s24,0x19000
T_ a[116:117],v82,30848
M_ v[36:39],a[36:39],a[204:207],v[36:39]
T_ a[118:119],v82,35968
buffer_load_dwordx4 v100,s[8:11],s55 offen lds
M_ v[40:43],a[40:43],a[192:195],v[40:43]
T_ a[120:121],v83,20608
M_ v[40:43],a[44:47],a[196:199],v[40:43]
T_ a[122:123],v83,25728
M_ v[44:47],a[40:43],a[200:203],v[44:47]
T_ a[124:125],v83,30848
M_ v[44:47],a[44:47],a[204:207],v[44:47]
T_ a[126:127],v83,35968
buffer_load_dwordx4 v101,s[8:11],s55 offen offset:1024 lds
M_ v[48:51],a[48:51],a[192:195],v[48:51]
T_ a[128:129],v82,20672
M_ v[48:51],a[52:55],a[196:199],v[48:51]
T_ a[130:131],v82,25792
M_ v[52:55],a[48:51],a[200:203],v[52:55]
T_ a[132:133],v82,30912
M_ v[52:55],a[52:55],a[204:207],v[52:55]
T_ a[134:135],v82,36032
M_ v[56:59],a[56:59],a[192:195],v[56:59]
T_ a[136:137],v83,20672
buffer_load_dwordx4 v102,s[8:11],s55 offen offset:2048 lds
M_ v[56:59],a[60:63],a[196:199],v[56:59]
T_ a[138:139],v83,25792
M_ v[60:63],a[56:59],a[200:203],v[60:63]
T_ a[140:141],v83,30912
M_ v[60:63],a[60:63],a[204:207],v[60:63]
T_ a[142:143],v83,36032
M_ v[64:67],a[64:67],a[192:195],v[64:67]
T_ a[144:145],v82,20736
buffer_load_dwordx4 v103,s[8:11],s55 offen offset:3072 lds
M_ v[64:67],a[68:71],a[196:199],v[64:67]
T_ a[146:147],v82,25856
M_ v[68:71],a[64:67],a[200:203],v[68:71]
T_ a[148:149],v82,30976
M_ v[68:71],a[68:71],a[204:207],v[68:71]
s_add_u32 m0,s24,0x1a000
T_ a[150:151],v82,36096
M_ v[72:75],a[72:75],a[192:195],v[72:75]
T_ a[152:153],v83,20736
buffer_load_dwordx4 v104,s[8:11],s55 offen lds
M_ v[72:75],a[76:79],a[196:199],v[72:75]
T_ a[154:155],v83,25856
M_ v[76:79],a[72:75],a[200:203],v[76:79]
T_ a[156:157],v83,30976
M_ v[76:79],a[76:79],a[204:207],v[76:79]
T_ a[158:159],v83,36096
s_waitcnt vmcnt(26) lgkmcnt(6)
M_ v[0:3],a[80:83],a[208:211],v[0:3]
ds_read_b64_tr_b16 a[0:1],v84
s_and_b32 s28,s2,7
s_waitcnt vmcnt(25)
M_ v[0:3],a[84:87],a[212:215],v[0:3]
s_add_u32 s4,s4,4096
s_addc_u32 s5,s5,0
T_ a[2:3],v84,5120
s_lshr_b32 s29,s2,3
s_waitcnt vmcnt(24)
M_ v[4:7],a[80:83],a[216:219],v[4:7]
T_ a[4:5],v84,10240
global_load_dwordx4 a[192:195],v80,s[4:5] sc1
s_waitcnt vmcnt(24)
M_ v[4:7],a[84:87],a[220:223],v[4:7]
T_ a[6:7],v84,15360
s_lshr_b32 s30,s28,1
M_ v[8:11],a[88:91],a[208:211],v[8:11]
ds_read_b64_tr_b16 a[8:9],v85
M_ v[8:11],a[92:95],a[212:215],v[8:11]
T_ a[10:11],v85,5120
s_lshl_b32 s30,s30,3
M_ v[12:15],a[88:91],a[216:219],v[12:15]
T_ a[12:13],v85,10240
global_load_dwordx4 a[196:199],v80,s[4:5] offset:1024 sc1
M_ v[12:15],a[92:95],a[220:223],v[12:15]
T_ a[14:15],v85,15360
s_and_b32 s48,s29,7
M_ v[16:19],a[96:99],a[208:211],v[16:19]
T_ a[16:17],v84,64
M_ v[16:19],a[100:103],a[212:215],v[16:19]
T_ a[18:19],v84,5184
s_add_u32 s48,s48,s30
M_ v[20:23],a[96:99],a[216:219],v[20:23]
T_ a[20:21],v84,10304
global_load_dwordx4 a[200:203],v81,s[4:5] sc1
s_and_b32 s28,s28,1
M_ v[20:23],a[100:103],a[220:223],v[20:23]
T_ a[22:23],v84,15424
M_ v[24:27],a[104:107],a[208:211],v[24:27]
T_ a[24:25],v85,64
s_lshl_b32 s28,s28,2
M_ v[24:27],a[108:111],a[212:215],v[24:27]
T_ a[26:27],v85,5184
M_ v[28:31],a[104:107],a[216:219],v[28:31]
T_ a[28:29],v85,10304
s_lshr_b32 s49,s29,3
M_ v[28:31],a[108:111],a[220:223],v[28:31]
T_ a[30:31],v85,15424
global_load_dwordx4 a[204:207],v81,s[4:5] offset:1024 sc1
M_ v[32:35],a[112:115],a[208:211],v[32:35]
T_ a[32:33],v84,128
s_add_u32 s49,s49,s28
M_ v[32:35],a[116:119],a[212:215],v[32:35]
s_add_u32 s55,s22,0xa0000
T_ a[34:35],v84,5248
M_ v[36:39],a[112:115],a[216:219],v[36:39]
s_add_u32 m0,s24,0x0
T_ a[36:37],v84,10368
s_add_u32 s44,s88,0x3a00000
s_addc_u32 s45,s89,0
M_ v[36:39],a[116:119],a[220:223],v[36:39]
T_ a[38:39],v84,15488
buffer_load_dwordx4 v100,s[8:11],s55 offen lds
M_ v[40:43],a[120:123],a[208:211],v[40:43]
T_ a[40:41],v85,128
s_and_b32 s45,s45,0xffff
M_ v[40:43],a[124:127],a[212:215],v[40:43]
T_ a[42:43],v85,5248
s_mov_b32 s46,0xc8000
M_ v[44:47],a[120:123],a[216:219],v[44:47]
T_ a[44:45],v85,10368
M_ v[44:47],a[124:127],a[220:223],v[44:47]
T_ a[46:47],v85,15488
buffer_load_dwordx4 v101,s[8:11],s55 offen offset:1024 lds
s_mov_b32 s47,0x20000
M_ v[48:51],a[128:131],a[208:211],v[48:51]
T_ a[48:49],v84,192
M_ v[48:51],a[132:135],a[212:215],v[48:51]
T_ a[50:51],v84,5312
s_lshr_b32 s28,s49,1
M_ v[52:55],a[128:131],a[216:219],v[52:55]
T_ a[52:53],v84,10432
M_ v[52:55],a[132:135],a[220:223],v[52:55]
T_ a[54:55],v84,15552
s_mul_i32 s28,s28,20
M_ v[56:59],a[136:139],a[208:211],v[56:59]
T_ a[56:57],v85,192
buffer_load_dwordx4 v102,s[8:11],s55 offen offset:2048 lds
M_ v[56:59],a[140:143],a[212:215],v[56:59]
T_ a[58:59],v85,5312
s_add_u32 s28,s28,s3
M_ v[60:63],a[136:139],a[216:219],v[60:63]
T_ a[60:61],v85,10432
s_mul_i32 s50,s28,10240
M_ v[60:63],a[140:143],a[220:223],v[60:63]
T_ a[62:63],v85,15552
M_ v[64:67],a[144:147],a[208:211],v[64:67]
T_ a[64:65],v84,256
buffer_load_dwordx4 v103,s[8:11],s55 offen offset:3072 lds
s_and_b32 s28,s49,1
s_waitcnt lgkmcnt(4)
M_ v[64:67],a[148:151],a[212:215],v[64:67]
T_ a[66:67],v84,5376
M_ v[68:71],a[144:147],a[216:219],v[68:71]
T_ a[68:69],v84,10496
s_mul_i32 s28,s28,5120
M_ v[68:71],a[148:151],a[220:223],v[68:71]
s_add_u32 m0,s24,0x1000
T_ a[70:71],v84,15616
M_ v[72:75],a[152:155],a[208:211],v[72:75]
T_ a[72:73],v85,256
buffer_load_dwordx4 v104,s[8:11],s55 offen lds
s_add_u32 s50,s50,s28
M_ v[72:75],a[156:159],a[212:215],v[72:75]
T_ a[74:75],v85,5376
M_ v[76:79],a[152:155],a[216:219],v[76:79]
T_ a[76:77],v85,10496
s_mul_i32 s51,s3,5120
M_ v[76:79],a[156:159],a[220:223],v[76:79]
T_ a[78:79],v85,15616
s_waitcnt vmcnt(18)
s_waitcnt lgkmcnt(0)
s_barrier
M_ v[0:3],a[0:3],a[160:163],v[0:3]
T_ a[80:81],v84,20480
M_ v[0:3],a[4:7],a[164:167],v[0:3]
T_ a[82:83],v84,25600
M_ v[4:7],a[0:3],a[168:171],v[4:7]
T_ a[84:85],v84,30720
global_load_dwordx4 a[208:211],v80,s[4:5] offset:2048 sc1
M_ v[4:7],a[4:7],a[172:175],v[4:7]
T_ a[86:87],v84,35840
M_ v[8:11],a[8:11],a[160:163],v[8:11]
T_ a[88:89],v85,20480
M_ v[8:11],a[12:15],a[164:167],v[8:11]
T_ a[90:91],v85,25600
M_ v[12:15],a[8:11],a[168:171],v[12:15]
T_ a[92:93],v85,30720
global_load_dwordx4 a[212:215],v80,s[4:5] offset:3072 sc1
M_ v[12:15],a[12:15],a[172:175],v[12:15]
T_ a[94:95],v85,35840
M_ v[16:19],a[16:19],a[160:163],v[16:19]
T_ a[96:97],v84,20544
M_ v[16:19],a[20:23],a[164:167],v[16:19]
T_ a[98:99],v84,25664
M_ v[20:23],a[16:19],a[168:171],v[20:23]
T_ a[100:101],v84,30784
)A"
R"A(global_load_dwordx4 a[216:219],v81,s[4:5] offset:2048 sc1
M_ v[20:23],a[20:23],a[172:175],v[20:23]
T_ a[102:103],v84,35904
M_ v[24:27],a[24:27],a[160:163],v[24:27]
T_ a[104:105],v85,20544
M_ v[24:27],a[28:31],a[164:167],v[24:27]
T_ a[106:107],v85,25664
M_ v[28:31],a[24:27],a[168:171],v[28:31]
T_ a[108:109],v85,30784
M_ v[28:31],a[28:31],a[172:175],v[28:31]
T_ a[110:111],v85,35904
global_load_dwordx4 a[220:223],v81,s[4:5] offset:3072 sc1
M_ v[32:35],a[32:35],a[160:163],v[32:35]
T_ a[112:113],v84,20608
M_ v[32:35],a[36:39],a[164:167],v[32:35]
s_add_u32 s55,s22,0xb4000
T_ a[114:115],v84,25728
M_ v[36:39],a[32:35],a[168:171],v[36:39]
s_add_u32 m0,s24,0x5000
T_ a[116:117],v84,30848
M_ v[36:39],a[36:39],a[172:175],v[36:39]
T_ a[118:119],v84,35968
buffer_load_dwordx4 v100,s[8:11],s55 offen lds
M_ v[40:43],a[40:43],a[160:163],v[40:43]
T_ a[120:121],v85,20608
M_ v[40:43],a[44:47],a[164:167],v[40:43]
T_ a[122:123],v85,25728
M_ v[44:47],a[40:43],a[168:171],v[44:47]
T_ a[124:125],v85,30848
M_ v[44:47],a[44:47],a[172:175],v[44:47]
T_ a[126:127],v85,35968
buffer_load_dwordx4 v101,s[8:11],s55 offen offset:1024 lds
M_ v[48:51],a[48:51],a[160:163],v[48:51]
T_ a[128:129],v84,20672
M_ v[48:51],a[52:55],a[164:167],v[48:51]
T_ a[130:131],v84,25792
M_ v[52:55],a[48:51],a[168:171],v[52:55]
T_ a[132:133],v84,30912
M_ v[52:55],a[52:55],a[172:175],v[52:55]
T_ a[134:135],v84,36032
M_ v[56:59],a[56:59],a[160:163],v[56:59]
T_ a[136:137],v85,20672
buffer_load_dwordx4 v102,s[8:11],s55 offen offset:2048 lds
M_ v[56:59],a[60:63],a[164:167],v[56:59]
T_ a[138:139],v85,25792
M_ v[60:63],a[56:59],a[168:171],v[60:63]
T_ a[140:141],v85,30912
M_ v[60:63],a[60:63],a[172:175],v[60:63]
T_ a[142:143],v85,36032
M_ v[64:67],a[64:67],a[160:163],v[64:67]
T_ a[144:145],v84,20736
buffer_load_dwordx4 v103,s[8:11],s55 offen offset:3072 lds
M_ v[64:67],a[68:71],a[164:167],v[64:67]
T_ a[146:147],v84,25856
M_ v[68:71],a[64:67],a[168:171],v[68:71]
T_ a[148:149],v84,30976
M_ v[68:71],a[68:71],a[172:175],v[68:71]
s_add_u32 m0,s24,0x6000
T_ a[150:151],v84,36096
M_ v[72:75],a[72:75],a[160:163],v[72:75]
T_ a[152:153],v85,20736
buffer_load_dwordx4 v104,s[8:11],s55 offen lds
M_ v[72:75],a[76:79],a[164:167],v[72:75]
T_ a[154:155],v85,25856
M_ v[76:79],a[72:75],a[168:171],v[76:79]
T_ a[156:157],v85,30976
M_ v[76:79],a[76:79],a[172:175],v[76:79]
T_ a[158:159],v85,36096
s_waitcnt vmcnt(26) lgkmcnt(6)
M_ v[0:3],a[80:83],a[176:179],v[0:3]
ds_read_b64_tr_b16 a[0:1],v86
s_waitcnt vmcnt(25)
M_ v[0:3],a[84:87],a[180:183],v[0:3]
s_add_u32 s4,s4,4096
s_addc_u32 s5,s5,0
T_ a[2:3],v86,5120
s_waitcnt vmcnt(24)
M_ v[4:7],a[80:83],a[184:187],v[4:7]
T_ a[4:5],v86,10240
global_load_dwordx4 a[160:163],v80,s[4:5] sc1
s_waitcnt vmcnt(24)
M_ v[4:7],a[84:87],a[188:191],v[4:7]
T_ a[6:7],v86,15360
M_ v[8:11],a[88:91],a[176:179],v[8:11]
ds_read_b64_tr_b16 a[8:9],v87
M_ v[8:11],a[92:95],a[180:183],v[8:11]
T_ a[10:11],v87,5120
M_ v[12:15],a[88:91],a[184:187],v[12:15]
T_ a[12:13],v87,10240
global_load_dwordx4 a[164:167],v80,s[4:5] offset:1024 sc1
M_ v[12:15],a[92:95],a[188:191],v[12:15]
T_ a[14:15],v87,15360
M_ v[16:19],a[96:99],a[176:179],v[16:19]
T_ a[16:17],v86,64
M_ v[16:19],a[100:103],a[180:183],v[16:19]
T_ a[18:19],v86,5184
M_ v[20:23],a[96:99],a[184:187],v[20:23]
T_ a[20:21],v86,10304
global_load_dwordx4 a[168:171],v81,s[4:5] sc1
M_ v[20:23],a[100:103],a[188:191],v[20:23]
T_ a[22:23],v86,15424
M_ v[24:27],a[104:107],a[176:179],v[24:27]
T_ a[24:25],v87,64
M_ v[24:27],a[108:111],a[180:183],v[24:27]
T_ a[26:27],v87,5184
M_ v[28:31],a[104:107],a[184:187],v[28:31]
T_ a[28:29],v87,10304
M_ v[28:31],a[108:111],a[188:191],v[28:31]
T_ a[30:31],v87,15424
global_load_dwordx4 a[172:175],v81,s[4:5] offset:1024 sc1
M_ v[32:35],a[112:115],a[176:179],v[32:35]
T_ a[32:33],v86,128
M_ v[32:35],a[116:119],a[180:183],v[32:35]
s_add_u32 s55,s22,0xc8000
T_ a[34:35],v86,5248
M_ v[36:39],a[112:115],a[184:187],v[36:39]
s_add_u32 m0,s24,0xa000
T_ a[36:37],v86,10368
M_ v[36:39],a[116:119],a[188:191],v[36:39]
T_ a[38:39],v86,15488
buffer_load_dwordx4 v100,s[8:11],s55 offen lds
M_ v[40:43],a[120:123],a[176:179],v[40:43]
T_ a[40:41],v87,128
M_ v[40:43],a[124:127],a[180:183],v[40:43]
T_ a[42:43],v87,5248
M_ v[44:47],a[120:123],a[184:187],v[44:47]
T_ a[44:45],v87,10368
M_ v[44:47],a[124:127],a[188:191],v[44:47]
T_ a[46:47],v87,15488
buffer_load_dwordx4 v101,s[8:11],s55 offen offset:1024 lds
M_ v[48:51],a[128:131],a[176:179],v[48:51]
T_ a[48:49],v86,192
M_ v[48:51],a[132:135],a[180:183],v[48:51]
T_ a[50:51],v86,5312
M_ v[52:55],a[128:131],a[184:187],v[52:55]
T_ a[52:53],v86,10432
M_ v[52:55],a[132:135],a[188:191],v[52:55]
T_ a[54:55],v86,15552
M_ v[56:59],a[136:139],a[176:179],v[56:59]
T_ a[56:57],v87,192
buffer_load_dwordx4 v102,s[8:11],s55 offen offset:2048 lds
M_ v[56:59],a[140:143],a[180:183],v[56:59]
T_ a[58:59],v87,5312
M_ v[60:63],a[136:139],a[184:187],v[60:63]
T_ a[60:61],v87,10432
M_ v[60:63],a[140:143],a[188:191],v[60:63]
T_ a[62:63],v87,15552
M_ v[64:67],a[144:147],a[176:179],v[64:67]
T_ a[64:65],v86,256
buffer_load_dwordx4 v103,s[8:11],s55 offen offset:3072 lds
s_waitcnt lgkmcnt(6)
M_ v[64:67],a[148:151],a[180:183],v[64:67]
T_ a[66:67],v86,5376
M_ v[68:71],a[144:147],a[184:187],v[68:71]
T_ a[68:69],v86,10496
M_ v[68:71],a[148:151],a[188:191],v[68:71]
s_add_u32 m0,s24,0xb000
T_ a[70:71],v86,15616
M_ v[72:75],a[152:155],a[176:179],v[72:75]
T_ a[72:73],v87,256
buffer_load_dwordx4 v104,s[8:11],s55 offen lds
M_ v[72:75],a[156:159],a[180:183],v[72:75]
T_ a[74:75],v87,5376
M_ v[76:79],a[152:155],a[184:187],v[76:79]
T_ a[76:77],v87,10496
M_ v[76:79],a[156:159],a[188:191],v[76:79]
T_ a[78:79],v87,15616
s_waitcnt vmcnt(18)
s_waitcnt lgkmcnt(0)
s_barrier
M_ v[0:3],a[0:3],a[192:195],v[0:3]
T_ a[80:81],v86,20480
s_lshl_b32 s32,s48,3
M_ v[0:3],a[4:7],a[196:199],v[0:3]
T_ a[82:83],v86,25600
M_ v[4:7],a[0:3],a[200:203],v[4:7]
T_ a[84:85],v86,30720
global_load_dwordx4 a[176:179],v80,s[4:5] offset:2048 sc1
M_ v[4:7],a[4:7],a[204:207],v[4:7]
T_ a[86:87],v86,35840
M_ v[8:11],a[8:11],a[192:195],v[8:11]
T_ a[88:89],v87,20480
M_ v[8:11],a[12:15],a[196:199],v[8:11]
T_ a[90:91],v87,25600
v_mbcnt_lo_u32_b32 v221,-1,0
M_ v[12:15],a[8:11],a[200:203],v[12:15]
T_ a[92:93],v87,30720
global_load_dwordx4 a[180:183],v80,s[4:5] offset:3072 sc1
M_ v[12:15],a[12:15],a[204:207],v[12:15]
T_ a[94:95],v87,35840
M_ v[16:19],a[16:19],a[192:195],v[16:19]
T_ a[96:97],v86,20544
M_ v[16:19],a[20:23],a[196:199],v[16:19]
T_ a[98:99],v86,25664
M_ v[20:23],a[16:19],a[200:203],v[20:23]
T_ a[100:101],v86,30784
global_load_dwordx4 a[184:187],v81,s[4:5] offset:2048 sc1
v_mbcnt_hi_u32_b32 v221,-1,v221
M_ v[20:23],a[20:23],a[204:207],v[20:23]
T_ a[102:103],v86,35904
M_ v[24:27],a[24:27],a[192:195],v[24:27]
T_ a[104:105],v87,20544
M_ v[24:27],a[28:31],a[196:199],v[24:27]
T_ a[106:107],v87,25664
M_ v[28:31],a[24:27],a[200:203],v[28:31]
T_ a[108:109],v87,30784
M_ v[28:31],a[28:31],a[204:207],v[28:31]
T_ a[110:111],v87,35904
global_load_dwordx4 a[188:191],v81,s[4:5] offset:3072 sc1
v_min_u32 v221,7,v221
M_ v[32:35],a[32:35],a[192:195],v[32:35]
T_ a[112:113],v86,20608
M_ v[32:35],a[36:39],a[196:199],v[32:35]
s_add_u32 s55,s22,0xdc000
T_ a[114:115],v86,25728
M_ v[36:39],a[32:35],a[200:203],v[36:39]
s_add_u32 m0,s24,0xf000
T_ a[116:117],v86,30848
M_ v[36:39],a[36:39],a[204:207],v[36:39]
T_ a[118:119],v86,35968
buffer_load_dwordx4 v100,s[8:11],s55 offen lds
M_ v[40:43],a[40:43],a[192:195],v[40:43]
T_ a[120:121],v87,20608
v_add_u32 v221,s32,v221
M_ v[40:43],a[44:47],a[196:199],v[40:43]
T_ a[122:123],v87,25728
M_ v[44:47],a[40:43],a[200:203],v[44:47]
T_ a[124:125],v87,30848
M_ v[44:47],a[44:47],a[204:207],v[44:47]
T_ a[126:127],v87,35968
buffer_load_dwordx4 v101,s[8:11],s55 offen offset:1024 lds
M_ v[48:51],a[48:51],a[192:195],v[48:51]
T_ a[128:129],v86,20672
M_ v[48:51],a[52:55],a[196:199],v[48:51]
T_ a[130:131],v86,25792
v_lshlrev_b32 v221,2,v221
M_ v[52:55],a[48:51],a[200:203],v[52:55]
T_ a[132:133],v86,30912
M_ v[52:55],a[52:55],a[204:207],v[52:55]
T_ a[134:135],v86,36032
M_ v[56:59],a[56:59],a[192:195],v[56:59]
T_ a[136:137],v87,20672
buffer_load_dwordx4 v102,s[8:11],s55 offen offset:2048 lds
M_ v[56:59],a[60:63],a[196:199],v[56:59]
T_ a[138:139],v87,25792
M_ v[60:63],a[56:59],a[200:203],v[60:63]
T_ a[140:141],v87,30912
v_add_u32 v221,0x6401400,v221
M_ v[60:63],a[60:63],a[204:207],v[60:63]
T_ a[142:143],v87,36032
M_ v[64:67],a[64:67],a[192:195],v[64:67]
T_ a[144:145],v86,20736
buffer_load_dwordx4 v103,s[8:11],s55 offen offset:3072 lds
M_ v[64:67],a[68:71],a[196:199],v[64:67]
T_ a[146:147],v86,25856
M_ v[68:71],a[64:67],a[200:203],v[68:71]
T_ a[148:149],v86,30976
M_ v[68:71],a[68:71],a[204:207],v[68:71]
s_add_u32 m0,s24,0x10000
T_ a[150:151],v86,36096
global_load_dword v222,v221,s[88:89] sc1
M_ v[72:75],a[72:75],a[192:195],v[72:75]
T_ a[152:153],v87,20736
buffer_load_dwordx4 v104,s[8:11],s55 offen lds
M_ v[72:75],a[76:79],a[196:199],v[72:75]
T_ a[154:155],v87,25856
M_ v[76:79],a[72:75],a[200:203],v[76:79]
T_ a[156:157],v87,30976
M_ v[76:79],a[76:79],a[204:207],v[76:79]
T_ a[158:159],v87,36096
s_waitcnt vmcnt(27) lgkmcnt(5)
M_ v[0:3],a[80:83],a[208:211],v[0:3]
ds_read_b64_tr_b16 a[0:1],v82
s_waitcnt vmcnt(26)
M_ v[0:3],a[84:87],a[212:215],v[0:3]
s_add_u32 s4,s4,4096
s_addc_u32 s5,s5,0
T_ a[2:3],v82,5120
s_waitcnt vmcnt(25)
M_ v[4:7],a[80:83],a[216:219],v[4:7]
T_ a[4:5],v82,10240
global_load_dwordx4 a[192:195],v80,s[4:5] sc1
s_waitcnt vmcnt(25)
M_ v[4:7],a[84:87],a[220:223],v[4:7]
T_ a[6:7],v82,15360
M_ v[8:11],a[88:91],a[208:211],v[8:11]
ds_read_b64_tr_b16 a[8:9],v83
M_ v[8:11],a[92:95],a[212:215],v[8:11]
T_ a[10:11],v83,5120
M_ v[12:15],a[88:91],a[216:219],v[12:15]
T_ a[12:13],v83,10240
global_load_dwordx4 a[196:199],v80,s[4:5] offset:1024 sc1
M_ v[12:15],a[92:95],a[220:223],v[12:15]
T_ a[14:15],v83,15360
M_ v[16:19],a[96:99],a[208:211],v[16:19]
T_ a[16:17],v82,64
M_ v[16:19],a[100:103],a[212:215],v[16:19]
T_ a[18:19],v82,5184
M_ v[20:23],a[96:99],a[216:219],v[20:23]
T_ a[20:21],v82,10304
global_load_dwordx4 a[200:203],v81,s[4:5] sc1
M_ v[20:23],a[100:103],a[220:223],v[20:23]
T_ a[22:23],v82,15424
M_ v[24:27],a[104:107],a[208:211],v[24:27]
T_ a[24:25],v83,64
M_ v[24:27],a[108:111],a[212:215],v[24:27]
T_ a[26:27],v83,5184
M_ v[28:31],a[104:107],a[216:219],v[28:31]
T_ a[28:29],v83,10304
M_ v[28:31],a[108:111],a[220:223],v[28:31]
T_ a[30:31],v83,15424
global_load_dwordx4 a[204:207],v81,s[4:5] offset:1024 sc1
M_ v[32:35],a[112:115],a[208:211],v[32:35]
T_ a[32:33],v82,128
M_ v[32:35],a[116:119],a[212:215],v[32:35]
s_add_u32 s55,s22,0xf0000
T_ a[34:35],v82,5248
M_ v[36:39],a[112:115],a[216:219],v[36:39]
s_add_u32 m0,s24,0x14000
T_ a[36:37],v82,10368
M_ v[36:39],a[116:119],a[220:223],v[36:39]
T_ a[38:39],v82,15488
buffer_load_dwordx4 v100,s[8:11],s55 offen lds
M_ v[40:43],a[120:123],a[208:211],v[40:43]
T_ a[40:41],v83,128
M_ v[40:43],a[124:127],a[212:215],v[40:43]
T_ a[42:43],v83,5248
M_ v[44:47],a[120:123],a[216:219],v[44:47]
T_ a[44:45],v83,10368
M_ v[44:47],a[124:127],a[220:223],v[44:47]
T_ a[46:47],v83,15488
buffer_load_dwordx4 v101,s[8:11],s55 offen offset:1024 lds
M_ v[48:51],a[128:131],a[208:211],v[48:51]
T_ a[48:49],v82,192
M_ v[48:51],a[132:135],a[212:215],v[48:51]
T_ a[50:51],v82,5312
M_ v[52:55],a[128:131],a[216:219],v[52:55]
T_ a[52:53],v82,10432
M_ v[52:55],a[132:135],a[220:223],v[52:55]
T_ a[54:55],v82,15552
M_ v[56:59],a[136:139],a[208:211],v[56:59]
T_ a[56:57],v83,192
buffer_load_dwordx4 v102,s[8:11],s55 offen offset:2048 lds
M_ v[56:59],a[140:143],a[212:215],v[56:59]
T_ a[58:59],v83,5312
M_ v[60:63],a[136:139],a[216:219],v[60:63]
T_ a[60:61],v83,10432
M_ v[60:63],a[140:143],a[220:223],v[60:63]
T_ a[62:63],v83,15552
M_ v[64:67],a[144:147],a[208:211],v[64:67]
T_ a[64:65],v82,256
buffer_load_dwordx4 v103,s[8:11],s55 offen offset:3072 lds
s_waitcnt lgkmcnt(6)
M_ v[64:67],a[148:151],a[212:215],v[64:67]
T_ a[66:67],v82,5376
M_ v[68:71],a[144:147],a[216:219],v[68:71]
T_ a[68:69],v82,10496
M_ v[68:71],a[148:151],a[220:223],v[68:71]
s_add_u32 m0,s24,0x15000
T_ a[70:71],v82,15616
M_ v[72:75],a[152:155],a[208:211],v[72:75]
T_ a[72:73],v83,256
buffer_load_dwordx4 v104,s[8:11],s55 offen lds
M_ v[72:75],a[156:159],a[212:215],v[72:75]
T_ a[74:75],v83,5376
M_ v[76:79],a[152:155],a[216:219],v[76:79]
T_ a[76:77],v83,10496
M_ v[76:79],a[156:159],a[220:223],v[76:79]
T_ a[78:79],v83,15616
s_waitcnt vmcnt(19)
s_waitcnt lgkmcnt(0)
s_barrier
M_ v[0:3],a[0:3],a[160:163],v[0:3]
T_ a[80:81],v82,20480
s_waitcnt vmcnt(10)
v_cmp_ne_u32 vcc,s86,v222
s_cbranch_vccz LQD51_%=
s_mov_b32 s59,0x30d40
LQL51_%=:
s_sleep 2
global_load_dword v222,v221,s[88:89] sc1
s_waitcnt vmcnt(0)
v_cmp_ne_u32 vcc,s86,v222
s_cbranch_vccz LQD51_%=
s_sub_u32 s59,s59,1
s_cmp_lg_u32 s59,0
s_cbranch_scc1 LQL51_%=
LQD51_%=:
M_ v[0:3],a[4:7],a[164:167],v[0:3]
T_ a[82:83],v82,25600
M_ v[4:7],a[0:3],a[168:171],v[4:7]
T_ a[84:85],v82,30720
global_load_dwordx4 a[208:211],v80,s[4:5] offset:2048 sc1
M_ v[4:7],a[4:7],a[172:175],v[4:7]
T_ a[86:87],v82,35840
M_ v[8:11],a[8:11],a[160:163],v[8:11]
T_ a[88:89],v83,20480
M_ v[8:11],a[12:15],a[164:167],v[8:11]
T_ a[90:91],v83,25600
M_ v[12:15],a[8:11],a[168:171],v[12:15]
T_ a[92:93],v83,30720
global_load_dwordx4 a[212:215],v80,s[4:5] offset:3072 sc1
M_ v[12:15],a[12:15],a[172:175],v[12:15]
T_ a[94:95],v83,35840
M_ v[16:19],a[16:19],a[160:163],v[16:19]
T_ a[96:97],v82,20544
M_ v[16:19],a[20:23],a[164:167],v[16:19]
T_ a[98:99],v82,25664
M_ v[20:23],a[16:19],a[168:171],v[20:23]
T_ a[100:101],v82,30784
global_load_dwordx4 a[216:219],v81,s[4:5] offset:2048 sc1
M_ v[20:23],a[20:23],a[172:175],v[20:23]
T_ a[102:103],v82,35904
M_ v[24:27],a[24:27],a[160:163],v[24:27]
T_ a[104:105],v83,20544
M_ v[24:27],a[28:31],a[164:167],v[24:27]
T_ a[106:107],v83,25664
M_ v[28:31],a[24:27],a[168:171],v[28:31]
T_ a[108:109],v83,30784
M_ v[28:31],a[28:31],a[172:175],v[28:31]
T_ a[110:111],v83,35904
global_load_dwordx4 a[220:223],v81,s[4:5] offset:3072 sc1
M_ v[32:35],a[32:35],a[160:163],v[32:35]
T_ a[112:113],v82,20608
M_ v[32:35],a[36:39],a[164:167],v[32:35]
s_add_u32 s55,s22,0x104000
T_ a[114:115],v82,25728
)A"
R"A(M_ v[36:39],a[32:35],a[168:171],v[36:39]
s_add_u32 m0,s24,0x19000
T_ a[116:117],v82,30848
M_ v[36:39],a[36:39],a[172:175],v[36:39]
T_ a[118:119],v82,35968
buffer_load_dwordx4 v100,s[8:11],s55 offen lds
M_ v[40:43],a[40:43],a[160:163],v[40:43]
T_ a[120:121],v83,20608
M_ v[40:43],a[44:47],a[164:167],v[40:43]
T_ a[122:123],v83,25728
M_ v[44:47],a[40:43],a[168:171],v[44:47]
T_ a[124:125],v83,30848
M_ v[44:47],a[44:47],a[172:175],v[44:47]
T_ a[126:127],v83,35968
buffer_load_dwordx4 v101,s[8:11],s55 offen offset:1024 lds
M_ v[48:51],a[48:51],a[160:163],v[48:51]
T_ a[128:129],v82,20672
M_ v[48:51],a[52:55],a[164:167],v[48:51]
T_ a[130:131],v82,25792
M_ v[52:55],a[48:51],a[168:171],v[52:55]
T_ a[132:133],v82,30912
M_ v[52:55],a[52:55],a[172:175],v[52:55]
T_ a[134:135],v82,36032
M_ v[56:59],a[56:59],a[160:163],v[56:59]
T_ a[136:137],v83,20672
buffer_load_dwordx4 v102,s[8:11],s55 offen offset:2048 lds
M_ v[56:59],a[60:63],a[164:167],v[56:59]
T_ a[138:139],v83,25792
M_ v[60:63],a[56:59],a[168:171],v[60:63]
T_ a[140:141],v83,30912
M_ v[60:63],a[60:63],a[172:175],v[60:63]
T_ a[142:143],v83,36032
M_ v[64:67],a[64:67],a[160:163],v[64:67]
T_ a[144:145],v82,20736
buffer_load_dwordx4 v103,s[8:11],s55 offen offset:3072 lds
M_ v[64:67],a[68:71],a[164:167],v[64:67]
T_ a[146:147],v82,25856
M_ v[68:71],a[64:67],a[168:171],v[68:71]
T_ a[148:149],v82,30976
M_ v[68:71],a[68:71],a[172:175],v[68:71]
s_add_u32 m0,s24,0x1a000
T_ a[150:151],v82,36096
M_ v[72:75],a[72:75],a[160:163],v[72:75]
T_ a[152:153],v83,20736
buffer_load_dwordx4 v104,s[8:11],s55 offen lds
M_ v[72:75],a[76:79],a[164:167],v[72:75]
T_ a[154:155],v83,25856
M_ v[76:79],a[72:75],a[168:171],v[76:79]
T_ a[156:157],v83,30976
M_ v[76:79],a[76:79],a[172:175],v[76:79]
T_ a[158:159],v83,36096
s_waitcnt lgkmcnt(6)
M_ v[0:3],a[80:83],a[176:179],v[0:3]
ds_read_b64_tr_b16 a[0:1],v84
s_lshl_b32 s28,s48,2
M_ v[0:3],a[84:87],a[180:183],v[0:3]
s_add_u32 s4,s4,4096
s_addc_u32 s5,s5,0
T_ a[2:3],v84,5120
M_ v[4:7],a[80:83],a[184:187],v[4:7]
T_ a[4:5],v84,10240
global_load_dwordx4 a[160:163],v80,s[4:5] sc1
s_add_u32 s28,s28,s3
M_ v[4:7],a[84:87],a[188:191],v[4:7]
T_ a[6:7],v84,15360
M_ v[8:11],a[88:91],a[176:179],v[8:11]
ds_read_b64_tr_b16 a[8:9],v85
M_ v[8:11],a[92:95],a[180:183],v[8:11]
T_ a[10:11],v85,5120
s_mul_i32 s28,s28,20480
M_ v[12:15],a[88:91],a[184:187],v[12:15]
T_ a[12:13],v85,10240
global_load_dwordx4 a[164:167],v80,s[4:5] offset:1024 sc1
M_ v[12:15],a[92:95],a[188:191],v[12:15]
T_ a[14:15],v85,15360
M_ v[16:19],a[96:99],a[176:179],v[16:19]
T_ a[16:17],v84,64
s_add_u32 s28,s28,0x4e00000
M_ v[16:19],a[100:103],a[180:183],v[16:19]
T_ a[18:19],v84,5184
M_ v[20:23],a[96:99],a[184:187],v[20:23]
T_ a[20:21],v84,10304
global_load_dwordx4 a[168:171],v81,s[4:5] sc1
M_ v[20:23],a[100:103],a[188:191],v[20:23]
T_ a[22:23],v84,15424
s_add_u32 s38,s88,s28
s_addc_u32 s39,s89,0
M_ v[24:27],a[104:107],a[176:179],v[24:27]
T_ a[24:25],v85,64
M_ v[24:27],a[108:111],a[180:183],v[24:27]
T_ a[26:27],v85,5184
M_ v[28:31],a[104:107],a[184:187],v[28:31]
T_ a[28:29],v85,10304
global_load_dwordx4 v[110:113],v88,s[38:39] sc1
M_ v[28:31],a[108:111],a[188:191],v[28:31]
T_ a[30:31],v85,15424
global_load_dwordx4 a[172:175],v81,s[4:5] offset:1024 sc1
M_ v[32:35],a[112:115],a[176:179],v[32:35]
T_ a[32:33],v84,128
M_ v[32:35],a[116:119],a[180:183],v[32:35]
s_add_u32 s55,s22,0x118000
T_ a[34:35],v84,5248
global_load_dwordx4 v[114:117],v88,s[38:39] offset:1024 sc1
M_ v[36:39],a[112:115],a[184:187],v[36:39]
s_add_u32 m0,s24,0x0
T_ a[36:37],v84,10368
M_ v[36:39],a[116:119],a[188:191],v[36:39]
T_ a[38:39],v84,15488
buffer_load_dwordx4 v100,s[8:11],s55 offen lds
M_ v[40:43],a[120:123],a[176:179],v[40:43]
T_ a[40:41],v85,128
global_load_dwordx4 v[118:121],v88,s[38:39] offset:2048 sc1
M_ v[40:43],a[124:127],a[180:183],v[40:43]
T_ a[42:43],v85,5248
M_ v[44:47],a[120:123],a[184:187],v[44:47]
T_ a[44:45],v85,10368
global_load_dwordx4 v[122:125],v88,s[38:39] offset:3072 sc1
M_ v[44:47],a[124:127],a[188:191],v[44:47]
T_ a[46:47],v85,15488
buffer_load_dwordx4 v101,s[8:11],s55 offen offset:1024 lds
M_ v[48:51],a[128:131],a[176:179],v[48:51]
T_ a[48:49],v84,192
M_ v[48:51],a[132:135],a[180:183],v[48:51]
T_ a[50:51],v84,5312
s_add_u32 s38,s38,4096
s_addc_u32 s39,s39,0
M_ v[52:55],a[128:131],a[184:187],v[52:55]
T_ a[52:53],v84,10432
M_ v[52:55],a[132:135],a[188:191],v[52:55]
T_ a[54:55],v84,15552
M_ v[56:59],a[136:139],a[176:179],v[56:59]
T_ a[56:57],v85,192
buffer_load_dwordx4 v102,s[8:11],s55 offen offset:2048 lds
global_load_dwordx4 v[126:129],v88,s[38:39] sc1
M_ v[56:59],a[140:143],a[180:183],v[56:59]
T_ a[58:59],v85,5312
M_ v[60:63],a[136:139],a[184:187],v[60:63]
T_ a[60:61],v85,10432
M_ v[60:63],a[140:143],a[188:191],v[60:63]
T_ a[62:63],v85,15552
global_load_dwordx4 v[130:133],v88,s[38:39] offset:1024 sc1
M_ v[64:67],a[144:147],a[176:179],v[64:67]
T_ a[64:65],v84,256
buffer_load_dwordx4 v103,s[8:11],s55 offen offset:3072 lds
s_waitcnt lgkmcnt(5)
M_ v[64:67],a[148:151],a[180:183],v[64:67]
T_ a[66:67],v84,5376
M_ v[68:71],a[144:147],a[184:187],v[68:71]
T_ a[68:69],v84,10496
global_load_dwordx4 v[134:137],v88,s[38:39] offset:2048 sc1
M_ v[68:71],a[148:151],a[188:191],v[68:71]
s_add_u32 m0,s24,0x1000
T_ a[70:71],v84,15616
M_ v[72:75],a[152:155],a[176:179],v[72:75]
T_ a[72:73],v85,256
buffer_load_dwordx4 v104,s[8:11],s55 offen lds
M_ v[72:75],a[156:159],a[180:183],v[72:75]
T_ a[74:75],v85,5376
global_load_dwordx4 v[138:141],v88,s[38:39] offset:3072 sc1
M_ v[76:79],a[152:155],a[184:187],v[76:79]
T_ a[76:77],v85,10496
M_ v[76:79],a[156:159],a[188:191],v[76:79]
T_ a[78:79],v85,15616
s_waitcnt vmcnt(26)
s_waitcnt lgkmcnt(0)
s_barrier
M_ v[0:3],a[0:3],a[192:195],v[0:3]
T_ a[80:81],v84,20480
s_add_u32 s38,s38,4096
s_addc_u32 s39,s39,0
M_ v[0:3],a[4:7],a[196:199],v[0:3]
T_ a[82:83],v84,25600
M_ v[4:7],a[0:3],a[200:203],v[4:7]
T_ a[84:85],v84,30720
global_load_dwordx4 a[176:179],v80,s[4:5] offset:2048 sc1
global_load_dwordx4 v[142:145],v88,s[38:39] sc1
M_ v[4:7],a[4:7],a[204:207],v[4:7]
T_ a[86:87],v84,35840
M_ v[8:11],a[8:11],a[192:195],v[8:11]
T_ a[88:89],v85,20480
M_ v[8:11],a[12:15],a[196:199],v[8:11]
T_ a[90:91],v85,25600
global_load_dwordx4 v[146:149],v88,s[38:39] offset:1024 sc1
M_ v[12:15],a[8:11],a[200:203],v[12:15]
T_ a[92:93],v85,30720
global_load_dwordx4 a[180:183],v80,s[4:5] offset:3072 sc1
M_ v[12:15],a[12:15],a[204:207],v[12:15]
T_ a[94:95],v85,35840
M_ v[16:19],a[16:19],a[192:195],v[16:19]
T_ a[96:97],v84,20544
global_load_dwordx4 v[150:153],v88,s[38:39] offset:2048 sc1
M_ v[16:19],a[20:23],a[196:199],v[16:19]
T_ a[98:99],v84,25664
M_ v[20:23],a[16:19],a[200:203],v[20:23]
T_ a[100:101],v84,30784
global_load_dwordx4 a[184:187],v81,s[4:5] offset:2048 sc1
global_load_dwordx4 v[154:157],v88,s[38:39] offset:3072 sc1
M_ v[20:23],a[20:23],a[204:207],v[20:23]
T_ a[102:103],v84,35904
M_ v[24:27],a[24:27],a[192:195],v[24:27]
T_ a[104:105],v85,20544
M_ v[24:27],a[28:31],a[196:199],v[24:27]
T_ a[106:107],v85,25664
s_add_u32 s38,s38,4096
s_addc_u32 s39,s39,0
M_ v[28:31],a[24:27],a[200:203],v[28:31]
T_ a[108:109],v85,30784
M_ v[28:31],a[28:31],a[204:207],v[28:31]
T_ a[110:111],v85,35904
global_load_dwordx4 a[188:191],v81,s[4:5] offset:3072 sc1
M_ v[32:35],a[32:35],a[192:195],v[32:35]
T_ a[112:113],v84,20608
global_load_dwordx4 v[158:161],v88,s[38:39] sc1
M_ v[32:35],a[36:39],a[196:199],v[32:35]
s_add_u32 s55,s22,0x12c000
T_ a[114:115],v84,25728
M_ v[36:39],a[32:35],a[200:203],v[36:39]
s_add_u32 m0,s24,0x5000
T_ a[116:117],v84,30848
global_load_dwordx4 v[162:165],v88,s[38:39] offset:1024 sc1
M_ v[36:39],a[36:39],a[204:207],v[36:39]
T_ a[118:119],v84,35968
buffer_load_dwordx4 v100,s[8:11],s55 offen lds
M_ v[40:43],a[40:43],a[192:195],v[40:43]
T_ a[120:121],v85,20608
M_ v[40:43],a[44:47],a[196:199],v[40:43]
T_ a[122:123],v85,25728
global_load_dwordx4 v[166:169],v88,s[38:39] offset:2048 sc1
M_ v[44:47],a[40:43],a[200:203],v[44:47]
T_ a[124:125],v85,30848
M_ v[44:47],a[44:47],a[204:207],v[44:47]
T_ a[126:127],v85,35968
buffer_load_dwordx4 v101,s[8:11],s55 offen offset:1024 lds
M_ v[48:51],a[48:51],a[192:195],v[48:51]
T_ a[128:129],v84,20672
global_load_dwordx4 v[170:173],v88,s[38:39] offset:3072 sc1
M_ v[48:51],a[52:55],a[196:199],v[48:51]
T_ a[130:131],v84,25792
M_ v[52:55],a[48:51],a[200:203],v[52:55]
T_ a[132:133],v84,30912
s_add_u32 s38,s38,4096
s_addc_u32 s39,s39,0
M_ v[52:55],a[52:55],a[204:207],v[52:55]
T_ a[134:135],v84,36032
M_ v[56:59],a[56:59],a[192:195],v[56:59]
T_ a[136:137],v85,20672
buffer_load_dwordx4 v102,s[8:11],s55 offen offset:2048 lds
M_ v[56:59],a[60:63],a[196:199],v[56:59]
T_ a[138:139],v85,25792
global_load_dwordx4 v[174:177],v88,s[38:39] sc1
M_ v[60:63],a[56:59],a[200:203],v[60:63]
T_ a[140:141],v85,30912
M_ v[60:63],a[60:63],a[204:207],v[60:63]
T_ a[142:143],v85,36032
M_ v[64:67],a[64:67],a[192:195],v[64:67]
T_ a[144:145],v84,20736
buffer_load_dwordx4 v103,s[8:11],s55 offen offset:3072 lds
global_load_dwordx4 v[178:181],v88,s[38:39] offset:1024 sc1
M_ v[64:67],a[68:71],a[196:199],v[64:67]
T_ a[146:147],v84,25856
M_ v[68:71],a[64:67],a[200:203],v[68:71]
T_ a[148:149],v84,30976
global_load_dwordx4 v[182:185],v88,s[38:39] offset:2048 sc1
M_ v[68:71],a[68:71],a[204:207],v[68:71]
s_add_u32 m0,s24,0x6000
T_ a[150:151],v84,36096
M_ v[72:75],a[72:75],a[192:195],v[72:75]
T_ a[152:153],v85,20736
buffer_load_dwordx4 v104,s[8:11],s55 offen lds
M_ v[72:75],a[76:79],a[196:199],v[72:75]
T_ a[154:155],v85,25856
global_load_dwordx4 v[186:189],v88,s[38:39] offset:3072 sc1
M_ v[76:79],a[72:75],a[200:203],v[76:79]
T_ a[156:157],v85,30976
M_ v[76:79],a[76:79],a[204:207],v[76:79]
T_ a[158:159],v85,36096
s_waitcnt vmcnt(46) lgkmcnt(5)
M_ v[0:3],a[80:83],a[208:211],v[0:3]
ds_read_b64_tr_b16 a[0:1],v86
s_waitcnt vmcnt(45)
M_ v[0:3],a[84:87],a[212:215],v[0:3]
s_add_u32 s4,s4,4096
s_addc_u32 s5,s5,0
T_ a[2:3],v86,5120
s_waitcnt vmcnt(44)
M_ v[4:7],a[80:83],a[216:219],v[4:7]
T_ a[4:5],v86,10240
global_load_dwordx4 a[192:195],v80,s[4:5] sc1
s_waitcnt vmcnt(44)
M_ v[4:7],a[84:87],a[220:223],v[4:7]
T_ a[6:7],v86,15360
M_ v[8:11],a[88:91],a[208:211],v[8:11]
ds_read_b64_tr_b16 a[8:9],v87
M_ v[8:11],a[92:95],a[212:215],v[8:11]
T_ a[10:11],v87,5120
M_ v[12:15],a[88:91],a[216:219],v[12:15]
T_ a[12:13],v87,10240
global_load_dwordx4 a[196:199],v80,s[4:5] offset:1024 sc1
M_ v[12:15],a[92:95],a[220:223],v[12:15]
T_ a[14:15],v87,15360
M_ v[16:19],a[96:99],a[208:211],v[16:19]
T_ a[16:17],v86,64
M_ v[16:19],a[100:103],a[212:215],v[16:19]
T_ a[18:19],v86,5184
M_ v[20:23],a[96:99],a[216:219],v[20:23]
T_ a[20:21],v86,10304
global_load_dwordx4 a[200:203],v81,s[4:5] sc1
M_ v[20:23],a[100:103],a[220:223],v[20:23]
T_ a[22:23],v86,15424
M_ v[24:27],a[104:107],a[208:211],v[24:27]
T_ a[24:25],v87,64
M_ v[24:27],a[108:111],a[212:215],v[24:27]
T_ a[26:27],v87,5184
M_ v[28:31],a[104:107],a[216:219],v[28:31]
T_ a[28:29],v87,10304
M_ v[28:31],a[108:111],a[220:223],v[28:31]
T_ a[30:31],v87,15424
global_load_dwordx4 a[204:207],v81,s[4:5] offset:1024 sc1
M_ v[32:35],a[112:115],a[208:211],v[32:35]
T_ a[32:33],v86,128
M_ v[32:35],a[116:119],a[212:215],v[32:35]
s_add_u32 s35,s50,0x0
T_ a[34:35],v86,5248
M_ v[36:39],a[112:115],a[216:219],v[36:39]
s_add_u32 m0,s51,0x1e000
T_ a[36:37],v86,10368
M_ v[36:39],a[116:119],a[220:223],v[36:39]
T_ a[38:39],v86,15488
buffer_load_dwordx4 v88,s[44:47],s35 offen sc1 lds
M_ v[40:43],a[120:123],a[208:211],v[40:43]
T_ a[40:41],v87,128
M_ v[40:43],a[124:127],a[212:215],v[40:43]
T_ a[42:43],v87,5248
M_ v[44:47],a[120:123],a[216:219],v[44:47]
T_ a[44:45],v87,10368
M_ v[44:47],a[124:127],a[220:223],v[44:47]
T_ a[46:47],v87,15488
buffer_load_dwordx4 v88,s[44:47],s35 offen offset:1024 sc1 lds
M_ v[48:51],a[128:131],a[208:211],v[48:51]
T_ a[48:49],v86,192
M_ v[48:51],a[132:135],a[212:215],v[48:51]
T_ a[50:51],v86,5312
M_ v[52:55],a[128:131],a[216:219],v[52:55]
T_ a[52:53],v86,10432
M_ v[52:55],a[132:135],a[220:223],v[52:55]
T_ a[54:55],v86,15552
M_ v[56:59],a[136:139],a[208:211],v[56:59]
T_ a[56:57],v87,192
buffer_load_dwordx4 v88,s[44:47],s35 offen offset:2048 sc1 lds
M_ v[56:59],a[140:143],a[212:215],v[56:59]
T_ a[58:59],v87,5312
M_ v[60:63],a[136:139],a[216:219],v[60:63]
T_ a[60:61],v87,10432
M_ v[60:63],a[140:143],a[220:223],v[60:63]
T_ a[62:63],v87,15552
M_ v[64:67],a[144:147],a[208:211],v[64:67]
T_ a[64:65],v86,256
buffer_load_dwordx4 v88,s[44:47],s35 offen offset:3072 sc1 lds
s_waitcnt lgkmcnt(6)
M_ v[64:67],a[148:151],a[212:215],v[64:67]
T_ a[66:67],v86,5376
M_ v[68:71],a[144:147],a[216:219],v[68:71]
s_add_u32 s35,s35,4096
T_ a[68:69],v86,10496
M_ v[68:71],a[148:151],a[220:223],v[68:71]
s_add_u32 m0,s51,0x1f000
T_ a[70:71],v86,15616
M_ v[72:75],a[152:155],a[208:211],v[72:75]
T_ a[72:73],v87,256
buffer_load_dwordx4 v88,s[44:47],s35 offen sc1 lds
M_ v[72:75],a[156:159],a[212:215],v[72:75]
T_ a[74:75],v87,5376
M_ v[76:79],a[152:155],a[216:219],v[76:79]
T_ a[76:77],v87,10496
M_ v[76:79],a[156:159],a[220:223],v[76:79]
T_ a[78:79],v87,15616
s_waitcnt vmcnt(31)
s_waitcnt lgkmcnt(0)
s_barrier
M_ v[0:3],a[0:3],a[160:163],v[0:3]
T_ a[80:81],v86,20480
M_ v[0:3],a[4:7],a[164:167],v[0:3]
T_ a[82:83],v86,25600
M_ v[4:7],a[0:3],a[168:171],v[4:7]
T_ a[84:85],v86,30720
global_load_dwordx4 a[208:211],v80,s[4:5] offset:2048 sc1
M_ v[4:7],a[4:7],a[172:175],v[4:7]
T_ a[86:87],v86,35840
M_ v[8:11],a[8:11],a[160:163],v[8:11]
T_ a[88:89],v87,20480
M_ v[8:11],a[12:15],a[164:167],v[8:11]
T_ a[90:91],v87,25600
M_ v[12:15],a[8:11],a[168:171],v[12:15]
T_ a[92:93],v87,30720
global_load_dwordx4 a[212:215],v80,s[4:5] offset:3072 sc1
M_ v[12:15],a[12:15],a[172:175],v[12:15]
T_ a[94:95],v87,35840
M_ v[16:19],a[16:19],a[160:163],v[16:19]
T_ a[96:97],v86,20544
M_ v[16:19],a[20:23],a[164:167],v[16:19]
T_ a[98:99],v86,25664
M_ v[20:23],a[16:19],a[168:171],v[20:23]
T_ a[100:101],v86,30784
global_load_dwordx4 a[216:219],v81,s[4:5] offset:2048 sc1
M_ v[20:23],a[20:23],a[172:175],v[20:23]
T_ a[102:103],v86,35904
M_ v[24:27],a[24:27],a[160:163],v[24:27]
T_ a[104:105],v87,20544
M_ v[24:27],a[28:31],a[164:167],v[24:27]
T_ a[106:107],v87,25664
M_ v[28:31],a[24:27],a[168:171],v[28:31]
T_ a[108:109],v87,30784
)A"
R"A(M_ v[28:31],a[28:31],a[172:175],v[28:31]
T_ a[110:111],v87,35904
global_load_dwordx4 a[220:223],v81,s[4:5] offset:3072 sc1
M_ v[32:35],a[32:35],a[160:163],v[32:35]
T_ a[112:113],v86,20608
M_ v[32:35],a[36:39],a[164:167],v[32:35]
s_add_u32 s35,s50,0xa000
T_ a[114:115],v86,25728
M_ v[36:39],a[32:35],a[168:171],v[36:39]
s_add_u32 m0,s51,0x23000
T_ a[116:117],v86,30848
M_ v[36:39],a[36:39],a[172:175],v[36:39]
T_ a[118:119],v86,35968
buffer_load_dwordx4 v88,s[44:47],s35 offen sc1 lds
M_ v[40:43],a[40:43],a[160:163],v[40:43]
T_ a[120:121],v87,20608
M_ v[40:43],a[44:47],a[164:167],v[40:43]
T_ a[122:123],v87,25728
M_ v[44:47],a[40:43],a[168:171],v[44:47]
T_ a[124:125],v87,30848
M_ v[44:47],a[44:47],a[172:175],v[44:47]
T_ a[126:127],v87,35968
buffer_load_dwordx4 v88,s[44:47],s35 offen offset:1024 sc1 lds
M_ v[48:51],a[48:51],a[160:163],v[48:51]
T_ a[128:129],v86,20672
M_ v[48:51],a[52:55],a[164:167],v[48:51]
T_ a[130:131],v86,25792
M_ v[52:55],a[48:51],a[168:171],v[52:55]
T_ a[132:133],v86,30912
M_ v[52:55],a[52:55],a[172:175],v[52:55]
T_ a[134:135],v86,36032
M_ v[56:59],a[56:59],a[160:163],v[56:59]
T_ a[136:137],v87,20672
buffer_load_dwordx4 v88,s[44:47],s35 offen offset:2048 sc1 lds
M_ v[56:59],a[60:63],a[164:167],v[56:59]
T_ a[138:139],v87,25792
M_ v[60:63],a[56:59],a[168:171],v[60:63]
T_ a[140:141],v87,30912
M_ v[60:63],a[60:63],a[172:175],v[60:63]
T_ a[142:143],v87,36032
M_ v[64:67],a[64:67],a[160:163],v[64:67]
T_ a[144:145],v86,20736
buffer_load_dwordx4 v88,s[44:47],s35 offen offset:3072 sc1 lds
M_ v[64:67],a[68:71],a[164:167],v[64:67]
T_ a[146:147],v86,25856
M_ v[68:71],a[64:67],a[168:171],v[68:71]
s_add_u32 s35,s35,4096
T_ a[148:149],v86,30976
M_ v[68:71],a[68:71],a[172:175],v[68:71]
s_add_u32 m0,s51,0x24000
T_ a[150:151],v86,36096
M_ v[72:75],a[72:75],a[160:163],v[72:75]
T_ a[152:153],v87,20736
buffer_load_dwordx4 v88,s[44:47],s35 offen sc1 lds
M_ v[72:75],a[76:79],a[164:167],v[72:75]
T_ a[154:155],v87,25856
M_ v[76:79],a[72:75],a[168:171],v[76:79]
T_ a[156:157],v87,30976
M_ v[76:79],a[76:79],a[172:175],v[76:79]
T_ a[158:159],v87,36096
s_waitcnt vmcnt(38) lgkmcnt(6)
M_ v[0:3],a[80:83],a[176:179],v[0:3]
ds_read_b64_tr_b16 a[0:1],v82
s_waitcnt vmcnt(35)
M_ v[0:3],a[84:87],a[180:183],v[0:3]
T_ a[2:3],v82,5120
s_waitcnt vmcnt(33)
M_ v[4:7],a[80:83],a[184:187],v[4:7]
T_ a[4:5],v82,10240
s_waitcnt vmcnt(31)
M_ v[4:7],a[84:87],a[188:191],v[4:7]
T_ a[6:7],v82,15360
M_ v[8:11],a[88:91],a[176:179],v[8:11]
ds_read_b64_tr_b16 a[8:9],v83
M_ v[8:11],a[92:95],a[180:183],v[8:11]
T_ a[10:11],v83,5120
M_ v[12:15],a[88:91],a[184:187],v[12:15]
T_ a[12:13],v83,10240
M_ v[12:15],a[92:95],a[188:191],v[12:15]
T_ a[14:15],v83,15360
M_ v[16:19],a[96:99],a[176:179],v[16:19]
T_ a[16:17],v82,64
M_ v[16:19],a[100:103],a[180:183],v[16:19]
T_ a[18:19],v82,5184
M_ v[20:23],a[96:99],a[184:187],v[20:23]
T_ a[20:21],v82,10304
M_ v[20:23],a[100:103],a[188:191],v[20:23]
T_ a[22:23],v82,15424
M_ v[24:27],a[104:107],a[176:179],v[24:27]
T_ a[24:25],v83,64
M_ v[24:27],a[108:111],a[180:183],v[24:27]
T_ a[26:27],v83,5184
M_ v[28:31],a[104:107],a[184:187],v[28:31]
T_ a[28:29],v83,10304
M_ v[28:31],a[108:111],a[188:191],v[28:31]
T_ a[30:31],v83,15424
M_ v[32:35],a[112:115],a[176:179],v[32:35]
T_ a[32:33],v82,128
M_ v[32:35],a[116:119],a[180:183],v[32:35]
T_ a[34:35],v82,5248
M_ v[36:39],a[112:115],a[184:187],v[36:39]
T_ a[36:37],v82,10368
M_ v[36:39],a[116:119],a[188:191],v[36:39]
T_ a[38:39],v82,15488
M_ v[40:43],a[120:123],a[176:179],v[40:43]
T_ a[40:41],v83,128
M_ v[40:43],a[124:127],a[180:183],v[40:43]
T_ a[42:43],v83,5248
M_ v[44:47],a[120:123],a[184:187],v[44:47]
T_ a[44:45],v83,10368
M_ v[44:47],a[124:127],a[188:191],v[44:47]
T_ a[46:47],v83,15488
M_ v[48:51],a[128:131],a[176:179],v[48:51]
T_ a[48:49],v82,192
M_ v[48:51],a[132:135],a[180:183],v[48:51]
T_ a[50:51],v82,5312
M_ v[52:55],a[128:131],a[184:187],v[52:55]
T_ a[52:53],v82,10432
M_ v[52:55],a[132:135],a[188:191],v[52:55]
T_ a[54:55],v82,15552
M_ v[56:59],a[136:139],a[176:179],v[56:59]
T_ a[56:57],v83,192
M_ v[56:59],a[140:143],a[180:183],v[56:59]
T_ a[58:59],v83,5312
M_ v[60:63],a[136:139],a[184:187],v[60:63]
T_ a[60:61],v83,10432
M_ v[60:63],a[140:143],a[188:191],v[60:63]
T_ a[62:63],v83,15552
M_ v[64:67],a[144:147],a[176:179],v[64:67]
T_ a[64:65],v82,256
s_waitcnt lgkmcnt(7)
M_ v[64:67],a[148:151],a[180:183],v[64:67]
T_ a[66:67],v82,5376
M_ v[68:71],a[144:147],a[184:187],v[68:71]
T_ a[68:69],v82,10496
M_ v[68:71],a[148:151],a[188:191],v[68:71]
T_ a[70:71],v82,15616
M_ v[72:75],a[152:155],a[176:179],v[72:75]
T_ a[72:73],v83,256
M_ v[72:75],a[156:159],a[180:183],v[72:75]
T_ a[74:75],v83,5376
M_ v[76:79],a[152:155],a[184:187],v[76:79]
T_ a[76:77],v83,10496
M_ v[76:79],a[156:159],a[188:191],v[76:79]
T_ a[78:79],v83,15616
s_waitcnt vmcnt(19)
s_waitcnt lgkmcnt(0)
s_barrier
s_waitcnt vmcnt(17)
M_ v[0:3],a[0:3],a[192:195],v[0:3]
s_add_u32 s35,s50,0x14000
T_ a[80:81],v82,20480
s_waitcnt vmcnt(16)
M_ v[0:3],a[4:7],a[196:199],v[0:3]
s_add_u32 m0,s51,0xa000
T_ a[82:83],v82,25600
s_waitcnt vmcnt(15)
M_ v[4:7],a[0:3],a[200:203],v[4:7]
T_ a[84:85],v82,30720
buffer_load_dwordx4 v88,s[44:47],s35 offen sc1 lds
s_waitcnt vmcnt(15)
M_ v[4:7],a[4:7],a[204:207],v[4:7]
T_ a[86:87],v82,35840
M_ v[8:11],a[8:11],a[192:195],v[8:11]
T_ a[88:89],v83,20480
M_ v[8:11],a[12:15],a[196:199],v[8:11]
T_ a[90:91],v83,25600
M_ v[12:15],a[8:11],a[200:203],v[12:15]
T_ a[92:93],v83,30720
buffer_load_dwordx4 v88,s[44:47],s35 offen offset:1024 sc1 lds
M_ v[12:15],a[12:15],a[204:207],v[12:15]
T_ a[94:95],v83,35840
M_ v[16:19],a[16:19],a[192:195],v[16:19]
T_ a[96:97],v82,20544
M_ v[16:19],a[20:23],a[196:199],v[16:19]
T_ a[98:99],v82,25664
M_ v[20:23],a[16:19],a[200:203],v[20:23]
T_ a[100:101],v82,30784
buffer_load_dwordx4 v88,s[44:47],s35 offen offset:2048 sc1 lds
M_ v[20:23],a[20:23],a[204:207],v[20:23]
T_ a[102:103],v82,35904
M_ v[24:27],a[24:27],a[192:195],v[24:27]
T_ a[104:105],v83,20544
M_ v[24:27],a[28:31],a[196:199],v[24:27]
T_ a[106:107],v83,25664
buffer_load_dwordx4 v88,s[44:47],s35 offen offset:3072 sc1 lds
M_ v[28:31],a[24:27],a[200:203],v[28:31]
T_ a[108:109],v83,30784
M_ v[28:31],a[28:31],a[204:207],v[28:31]
s_add_u32 s35,s35,4096
T_ a[110:111],v83,35904
M_ v[32:35],a[32:35],a[192:195],v[32:35]
s_add_u32 m0,s51,0xb000
T_ a[112:113],v82,20608
M_ v[32:35],a[36:39],a[196:199],v[32:35]
T_ a[114:115],v82,25728
buffer_load_dwordx4 v88,s[44:47],s35 offen sc1 lds
M_ v[36:39],a[32:35],a[200:203],v[36:39]
T_ a[116:117],v82,30848
M_ v[36:39],a[36:39],a[204:207],v[36:39]
s_add_u32 s35,s50,0x1e000
T_ a[118:119],v82,35968
M_ v[40:43],a[40:43],a[192:195],v[40:43]
s_add_u32 m0,s51,0xf000
T_ a[120:121],v83,20608
M_ v[40:43],a[44:47],a[196:199],v[40:43]
T_ a[122:123],v83,25728
buffer_load_dwordx4 v88,s[44:47],s35 offen sc1 lds
M_ v[44:47],a[40:43],a[200:203],v[44:47]
T_ a[124:125],v83,30848
M_ v[44:47],a[44:47],a[204:207],v[44:47]
T_ a[126:127],v83,35968
M_ v[48:51],a[48:51],a[192:195],v[48:51]
T_ a[128:129],v82,20672
M_ v[48:51],a[52:55],a[196:199],v[48:51]
T_ a[130:131],v82,25792
buffer_load_dwordx4 v88,s[44:47],s35 offen offset:1024 sc1 lds
M_ v[52:55],a[48:51],a[200:203],v[52:55]
T_ a[132:133],v82,30912
M_ v[52:55],a[52:55],a[204:207],v[52:55]
T_ a[134:135],v82,36032
M_ v[56:59],a[56:59],a[192:195],v[56:59]
T_ a[136:137],v83,20672
buffer_load_dwordx4 v88,s[44:47],s35 offen offset:2048 sc1 lds
M_ v[56:59],a[60:63],a[196:199],v[56:59]
T_ a[138:139],v83,25792
M_ v[60:63],a[56:59],a[200:203],v[60:63]
T_ a[140:141],v83,30912
M_ v[60:63],a[60:63],a[204:207],v[60:63]
T_ a[142:143],v83,36032
M_ v[64:67],a[64:67],a[192:195],v[64:67]
T_ a[144:145],v82,20736
buffer_load_dwordx4 v88,s[44:47],s35 offen offset:3072 sc1 lds
M_ v[64:67],a[68:71],a[196:199],v[64:67]
T_ a[146:147],v82,25856
M_ v[68:71],a[64:67],a[200:203],v[68:71]
s_add_u32 s35,s35,4096
T_ a[148:149],v82,30976
M_ v[68:71],a[68:71],a[204:207],v[68:71]
s_add_u32 m0,s51,0x10000
T_ a[150:151],v82,36096
M_ v[72:75],a[72:75],a[192:195],v[72:75]
T_ a[152:153],v83,20736
buffer_load_dwordx4 v88,s[44:47],s35 offen sc1 lds
M_ v[72:75],a[76:79],a[196:199],v[72:75]
T_ a[154:155],v83,25856
M_ v[76:79],a[72:75],a[200:203],v[76:79]
T_ a[156:157],v83,30976
M_ v[76:79],a[76:79],a[204:207],v[76:79]
T_ a[158:159],v83,36096
s_waitcnt vmcnt(18) lgkmcnt(6)
M_ v[0:3],a[80:83],a[208:211],v[0:3]
s_add_u32 s35,s50,0x28000
s_waitcnt vmcnt(17)
M_ v[0:3],a[84:87],a[212:215],v[0:3]
s_add_u32 m0,s51,0x14000
s_waitcnt vmcnt(16)
M_ v[4:7],a[80:83],a[216:219],v[4:7]
buffer_load_dwordx4 v88,s[44:47],s35 offen sc1 lds
s_waitcnt vmcnt(16)
M_ v[4:7],a[84:87],a[220:223],v[4:7]
M_ v[8:11],a[88:91],a[208:211],v[8:11]
M_ v[8:11],a[92:95],a[212:215],v[8:11]
M_ v[12:15],a[88:91],a[216:219],v[12:15]
M_ v[12:15],a[92:95],a[220:223],v[12:15]
M_ v[16:19],a[96:99],a[208:211],v[16:19]
M_ v[16:19],a[100:103],a[212:215],v[16:19]
M_ v[20:23],a[96:99],a[216:219],v[20:23]
buffer_load_dwordx4 v88,s[44:47],s35 offen offset:1024 sc1 lds
M_ v[20:23],a[100:103],a[220:223],v[20:23]
M_ v[24:27],a[104:107],a[208:211],v[24:27]
M_ v[24:27],a[108:111],a[212:215],v[24:27]
M_ v[28:31],a[104:107],a[216:219],v[28:31]
M_ v[28:31],a[108:111],a[220:223],v[28:31]
M_ v[32:35],a[112:115],a[208:211],v[32:35]
M_ v[32:35],a[116:119],a[212:215],v[32:35]
M_ v[36:39],a[112:115],a[216:219],v[36:39]
M_ v[36:39],a[116:119],a[220:223],v[36:39]
buffer_load_dwordx4 v88,s[44:47],s35 offen offset:2048 sc1 lds
M_ v[40:43],a[120:123],a[208:211],v[40:43]
M_ v[40:43],a[124:127],a[212:215],v[40:43]
M_ v[44:47],a[120:123],a[216:219],v[44:47]
M_ v[44:47],a[124:127],a[220:223],v[44:47]
M_ v[48:51],a[128:131],a[208:211],v[48:51]
M_ v[48:51],a[132:135],a[212:215],v[48:51]
M_ v[52:55],a[128:131],a[216:219],v[52:55]
M_ v[52:55],a[132:135],a[220:223],v[52:55]
M_ v[56:59],a[136:139],a[208:211],v[56:59]
buffer_load_dwordx4 v88,s[44:47],s35 offen offset:3072 sc1 lds
M_ v[56:59],a[140:143],a[212:215],v[56:59]
M_ v[60:63],a[136:139],a[216:219],v[60:63]
M_ v[60:63],a[140:143],a[220:223],v[60:63]
M_ v[64:67],a[144:147],a[208:211],v[64:67]
s_waitcnt lgkmcnt(0)
M_ v[64:67],a[148:151],a[212:215],v[64:67]
M_ v[68:71],a[144:147],a[216:219],v[68:71]
s_add_u32 s35,s35,4096
M_ v[68:71],a[148:151],a[220:223],v[68:71]
s_add_u32 m0,s51,0x15000
M_ v[72:75],a[152:155],a[208:211],v[72:75]
buffer_load_dwordx4 v88,s[44:47],s35 offen sc1 lds
M_ v[72:75],a[156:159],a[212:215],v[72:75]
M_ v[76:79],a[152:155],a[216:219],v[76:79]
M_ v[76:79],a[156:159],a[220:223],v[76:79]
s_waitcnt vmcnt(0)
s_nop 7
s_cmp_lg_u32 s18,0
s_cbranch_scc1 LSD53n0_%=
s_lshl_b32 s28,s26,2
s_add_u32 s28,s28,1
s_lshl_b32 s28,s28,2
s_add_u32 s28,s28,0
s_lshl_b32 s28,s28,2
s_add_u32 s28,s28,s3
s_mul_i32 s28,s28,3072
s_add_u32 s28,s28,0x5200000
s_add_u32 s36,s88,s28
s_addc_u32 s37,s89,0
C_ v224,v24,v25
C_ v225,v26,v27
C_ v226,v28,v29
C_ v227,v30,v31
global_store_dwordx4 v88,v[224:227],s[36:37] sc1
C_ v228,v32,v33
C_ v229,v34,v35
C_ v230,v36,v37
C_ v231,v38,v39
global_store_dwordx4 v88,v[228:231],s[36:37] offset:1024 sc1
C_ v232,v40,v41
C_ v233,v42,v43
C_ v234,v44,v45
C_ v235,v46,v47
global_store_dwordx4 v88,v[232:235],s[36:37] offset:2048 sc1
s_lshl_b32 s28,s26,2
s_add_u32 s28,s28,2
s_lshl_b32 s28,s28,2
s_add_u32 s28,s28,0
s_lshl_b32 s28,s28,2
s_add_u32 s28,s28,s3
s_mul_i32 s28,s28,3072
s_add_u32 s28,s28,0x5200000
s_add_u32 s36,s88,s28
s_addc_u32 s37,s89,0
C_ v236,v48,v49
C_ v237,v50,v51
C_ v238,v52,v53
C_ v239,v54,v55
global_store_dwordx4 v88,v[236:239],s[36:37] sc1
C_ v240,v56,v57
C_ v241,v58,v59
C_ v242,v60,v61
C_ v243,v62,v63
global_store_dwordx4 v88,v[240:243],s[36:37] offset:1024 sc1
s_lshl_b32 s28,s26,2
s_add_u32 s28,s28,3
s_lshl_b32 s28,s28,2
s_add_u32 s28,s28,0
s_lshl_b32 s28,s28,2
s_add_u32 s28,s28,s3
s_mul_i32 s28,s28,3072
s_add_u32 s28,s28,0x5200000
s_add_u32 s36,s88,s28
s_addc_u32 s37,s89,0
C_ v224,v64,v65
C_ v225,v66,v67
C_ v226,v68,v69
C_ v227,v70,v71
global_store_dwordx4 v88,v[224:227],s[36:37] sc1
C_ v228,v72,v73
C_ v229,v74,v75
C_ v230,v76,v77
C_ v231,v78,v79
global_store_dwordx4 v88,v[228:231],s[36:37] offset:1024 sc1
s_branch LSD53e_%=
LSD53n0_%=:
s_cmp_lg_u32 s18,1
s_cbranch_scc1 LSD53n1_%=
s_lshl_b32 s28,s26,2
s_add_u32 s28,s28,0
s_lshl_b32 s28,s28,2
s_add_u32 s28,s28,1
s_lshl_b32 s28,s28,2
s_add_u32 s28,s28,s3
s_mul_i32 s28,s28,3072
s_add_u32 s28,s28,0x5200000
s_add_u32 s36,s88,s28
s_addc_u32 s37,s89,0
C_ v224,v0,v1
C_ v225,v2,v3
C_ v226,v4,v5
C_ v227,v6,v7
global_store_dwordx4 v88,v[224:227],s[36:37] sc1
C_ v228,v8,v9
C_ v229,v10,v11
C_ v230,v12,v13
C_ v231,v14,v15
global_store_dwordx4 v88,v[228:231],s[36:37] offset:1024 sc1
C_ v232,v16,v17
C_ v233,v18,v19
C_ v234,v20,v21
C_ v235,v22,v23
global_store_dwordx4 v88,v[232:235],s[36:37] offset:2048 sc1
s_lshl_b32 s28,s26,2
s_add_u32 s28,s28,2
s_lshl_b32 s28,s28,2
s_add_u32 s28,s28,1
s_lshl_b32 s28,s28,2
s_add_u32 s28,s28,s3
s_mul_i32 s28,s28,3072
s_add_u32 s28,s28,0x5200000
s_add_u32 s36,s88,s28
s_addc_u32 s37,s89,0
C_ v236,v48,v49
C_ v237,v50,v51
C_ v238,v52,v53
C_ v239,v54,v55
global_store_dwordx4 v88,v[236:239],s[36:37] sc1
C_ v240,v56,v57
C_ v241,v58,v59
C_ v242,v60,v61
C_ v243,v62,v63
global_store_dwordx4 v88,v[240:243],s[36:37] offset:1024 sc1
s_lshl_b32 s28,s26,2
s_add_u32 s28,s28,3
s_lshl_b32 s28,s28,2
s_add_u32 s28,s28,1
s_lshl_b32 s28,s28,2
s_add_u32 s28,s28,s3
s_mul_i32 s28,s28,3072
s_add_u32 s28,s28,0x5200000
s_add_u32 s36,s88,s28
s_addc_u32 s37,s89,0
C_ v224,v64,v65
C_ v225,v66,v67
C_ v226,v68,v69
C_ v227,v70,v71
global_store_dwordx4 v88,v[224:227],s[36:37] sc1
C_ v228,v72,v73
C_ v229,v74,v75
C_ v230,v76,v77
C_ v231,v78,v79
global_store_dwordx4 v88,v[228:231],s[36:37] offset:1024 sc1
s_branch LSD53e_%=
LSD53n1_%=:
s_cmp_lg_u32 s18,2
s_cbranch_scc1 LSD53n2_%=
s_lshl_b32 s28,s26,2
s_add_u32 s28,s28,0
s_lshl_b32 s28,s28,2
s_add_u32 s28,s28,2
s_lshl_b32 s28,s28,2
s_add_u32 s28,s28,s3
s_mul_i32 s28,s28,3072
s_add_u32 s28,s28,0x5200000
s_add_u32 s36,s88,s28
s_addc_u32 s37,s89,0
C_ v224,v0,v1
C_ v225,v2,v3
C_ v226,v4,v5
C_ v227,v6,v7
global_store_dwordx4 v88,v[224:227],s[36:37] sc1
C_ v228,v8,v9
C_ v229,v10,v11
C_ v230,v12,v13
C_ v231,v14,v15
)A"
R"A(global_store_dwordx4 v88,v[228:231],s[36:37] offset:1024 sc1
C_ v232,v16,v17
C_ v233,v18,v19
C_ v234,v20,v21
C_ v235,v22,v23
global_store_dwordx4 v88,v[232:235],s[36:37] offset:2048 sc1
s_lshl_b32 s28,s26,2
s_add_u32 s28,s28,1
s_lshl_b32 s28,s28,2
s_add_u32 s28,s28,2
s_lshl_b32 s28,s28,2
s_add_u32 s28,s28,s3
s_mul_i32 s28,s28,3072
s_add_u32 s28,s28,0x5200000
s_add_u32 s36,s88,s28
s_addc_u32 s37,s89,0
C_ v236,v24,v25
C_ v237,v26,v27
C_ v238,v28,v29
C_ v239,v30,v31
global_store_dwordx4 v88,v[236:239],s[36:37] sc1
C_ v240,v32,v33
C_ v241,v34,v35
C_ v242,v36,v37
C_ v243,v38,v39
global_store_dwordx4 v88,v[240:243],s[36:37] offset:1024 sc1
C_ v224,v40,v41
C_ v225,v42,v43
C_ v226,v44,v45
C_ v227,v46,v47
global_store_dwordx4 v88,v[224:227],s[36:37] offset:2048 sc1
s_lshl_b32 s28,s26,2
s_add_u32 s28,s28,3
s_lshl_b32 s28,s28,2
s_add_u32 s28,s28,2
s_lshl_b32 s28,s28,2
s_add_u32 s28,s28,s3
s_mul_i32 s28,s28,3072
s_add_u32 s28,s28,0x5200000
s_add_u32 s36,s88,s28
s_addc_u32 s37,s89,0
C_ v228,v64,v65
C_ v229,v66,v67
C_ v230,v68,v69
C_ v231,v70,v71
global_store_dwordx4 v88,v[228:231],s[36:37] sc1
C_ v232,v72,v73
C_ v233,v74,v75
C_ v234,v76,v77
C_ v235,v78,v79
global_store_dwordx4 v88,v[232:235],s[36:37] offset:1024 sc1
s_branch LSD53e_%=
LSD53n2_%=:
s_lshl_b32 s28,s26,2
s_add_u32 s28,s28,0
s_lshl_b32 s28,s28,2
s_add_u32 s28,s28,3
s_lshl_b32 s28,s28,2
s_add_u32 s28,s28,s3
s_mul_i32 s28,s28,3072
s_add_u32 s28,s28,0x5200000
s_add_u32 s36,s88,s28
s_addc_u32 s37,s89,0
C_ v224,v0,v1
C_ v225,v2,v3
C_ v226,v4,v5
C_ v227,v6,v7
global_store_dwordx4 v88,v[224:227],s[36:37] sc1
C_ v228,v8,v9
C_ v229,v10,v11
C_ v230,v12,v13
C_ v231,v14,v15
global_store_dwordx4 v88,v[228:231],s[36:37] offset:1024 sc1
C_ v232,v16,v17
C_ v233,v18,v19
C_ v234,v20,v21
C_ v235,v22,v23
global_store_dwordx4 v88,v[232:235],s[36:37] offset:2048 sc1
s_lshl_b32 s28,s26,2
s_add_u32 s28,s28,1
s_lshl_b32 s28,s28,2
s_add_u32 s28,s28,3
s_lshl_b32 s28,s28,2
s_add_u32 s28,s28,s3
s_mul_i32 s28,s28,3072
s_add_u32 s28,s28,0x5200000
s_add_u32 s36,s88,s28
s_addc_u32 s37,s89,0
C_ v236,v24,v25
C_ v237,v26,v27
C_ v238,v28,v29
C_ v239,v30,v31
global_store_dwordx4 v88,v[236:239],s[36:37] sc1
C_ v240,v32,v33
C_ v241,v34,v35
C_ v242,v36,v37
C_ v243,v38,v39
global_store_dwordx4 v88,v[240:243],s[36:37] offset:1024 sc1
C_ v224,v40,v41
C_ v225,v42,v43
C_ v226,v44,v45
C_ v227,v46,v47
global_store_dwordx4 v88,v[224:227],s[36:37] offset:2048 sc1
s_lshl_b32 s28,s26,2
s_add_u32 s28,s28,2
s_lshl_b32 s28,s28,2
s_add_u32 s28,s28,3
s_lshl_b32 s28,s28,2
s_add_u32 s28,s28,s3
s_mul_i32 s28,s28,3072
s_add_u32 s28,s28,0x5200000
s_add_u32 s36,s88,s28
s_addc_u32 s37,s89,0
C_ v228,v48,v49
C_ v229,v50,v51
C_ v230,v52,v53
C_ v231,v54,v55
global_store_dwordx4 v88,v[228:231],s[36:37] sc1
C_ v232,v56,v57
C_ v233,v58,v59
C_ v234,v60,v61
C_ v235,v62,v63
global_store_dwordx4 v88,v[232:235],s[36:37] offset:1024 sc1
LSD53e_%=:
s_waitcnt lgkmcnt(0)
s_barrier
v_add_u32 v215,0x1e000,v88
v_add_u32 v216,0xa000,v88
ds_read_b128 a[0:3],v215
R_ a[4:7],v215,1024
R_ a[8:11],v215,2048
R_ a[12:15],v215,3072
R_ a[16:19],v215,4096
s_waitcnt lgkmcnt(4)
M_ v[190:193],a[0:3],v[110:113],0
R_ a[20:23],v215,5120
s_waitcnt lgkmcnt(4)
M_ v[194:197],a[4:7],v[110:113],0
R_ a[24:27],v215,6144
s_waitcnt lgkmcnt(4)
M_ v[198:201],a[8:11],v[110:113],0
R_ a[28:31],v215,7168
s_waitcnt lgkmcnt(4)
M_ v[202:205],a[12:15],v[110:113],0
R_ a[32:35],v215,8192
s_waitcnt lgkmcnt(4)
M_ v[206:209],a[16:19],v[110:113],0
R_ a[36:39],v215,9216
s_waitcnt lgkmcnt(4)
M_ v[190:193],a[20:23],v[114:117],v[190:193]
R_ a[0:3],v215,10240
s_waitcnt lgkmcnt(4)
M_ v[194:197],a[24:27],v[114:117],v[194:197]
R_ a[4:7],v215,11264
s_waitcnt lgkmcnt(4)
M_ v[198:201],a[28:31],v[114:117],v[198:201]
R_ a[8:11],v215,12288
s_waitcnt lgkmcnt(4)
M_ v[202:205],a[32:35],v[114:117],v[202:205]
R_ a[12:15],v215,13312
s_waitcnt lgkmcnt(4)
M_ v[206:209],a[36:39],v[114:117],v[206:209]
R_ a[16:19],v215,14336
s_waitcnt lgkmcnt(4)
M_ v[190:193],a[0:3],v[118:121],v[190:193]
R_ a[20:23],v215,15360
s_waitcnt lgkmcnt(4)
M_ v[194:197],a[4:7],v[118:121],v[194:197]
R_ a[24:27],v215,16384
s_waitcnt lgkmcnt(4)
M_ v[198:201],a[8:11],v[118:121],v[198:201]
R_ a[28:31],v215,17408
s_waitcnt lgkmcnt(4)
M_ v[202:205],a[12:15],v[118:121],v[202:205]
R_ a[32:35],v215,18432
s_waitcnt lgkmcnt(4)
M_ v[206:209],a[16:19],v[118:121],v[206:209]
R_ a[36:39],v215,19456
s_waitcnt lgkmcnt(4)
M_ v[190:193],a[20:23],v[122:125],v[190:193]
R_ a[0:3],v215,20480
s_waitcnt lgkmcnt(4)
M_ v[194:197],a[24:27],v[122:125],v[194:197]
R_ a[4:7],v215,21504
s_waitcnt lgkmcnt(4)
M_ v[198:201],a[28:31],v[122:125],v[198:201]
R_ a[8:11],v215,22528
s_waitcnt lgkmcnt(4)
M_ v[202:205],a[32:35],v[122:125],v[202:205]
R_ a[12:15],v215,23552
s_waitcnt lgkmcnt(4)
M_ v[206:209],a[36:39],v[122:125],v[206:209]
R_ a[16:19],v215,24576
s_waitcnt lgkmcnt(4)
M_ v[190:193],a[0:3],v[126:129],v[190:193]
R_ a[20:23],v215,25600
s_waitcnt lgkmcnt(4)
M_ v[194:197],a[4:7],v[126:129],v[194:197]
R_ a[24:27],v215,26624
s_waitcnt lgkmcnt(4)
M_ v[198:201],a[8:11],v[126:129],v[198:201]
R_ a[28:31],v215,27648
s_waitcnt lgkmcnt(4)
M_ v[202:205],a[12:15],v[126:129],v[202:205]
R_ a[32:35],v215,28672
s_waitcnt lgkmcnt(4)
M_ v[206:209],a[16:19],v[126:129],v[206:209]
R_ a[36:39],v215,29696
s_waitcnt lgkmcnt(4)
M_ v[190:193],a[20:23],v[130:133],v[190:193]
R_ a[0:3],v215,30720
s_waitcnt lgkmcnt(4)
M_ v[194:197],a[24:27],v[130:133],v[194:197]
R_ a[4:7],v215,31744
s_waitcnt lgkmcnt(4)
M_ v[198:201],a[28:31],v[130:133],v[198:201]
R_ a[8:11],v215,32768
s_waitcnt lgkmcnt(4)
M_ v[202:205],a[32:35],v[130:133],v[202:205]
R_ a[12:15],v215,33792
s_waitcnt lgkmcnt(4)
M_ v[206:209],a[36:39],v[130:133],v[206:209]
R_ a[16:19],v215,34816
s_waitcnt lgkmcnt(4)
M_ v[190:193],a[0:3],v[134:137],v[190:193]
R_ a[20:23],v215,35840
s_waitcnt lgkmcnt(4)
M_ v[194:197],a[4:7],v[134:137],v[194:197]
R_ a[24:27],v215,36864
s_waitcnt lgkmcnt(4)
M_ v[198:201],a[8:11],v[134:137],v[198:201]
R_ a[28:31],v215,37888
s_waitcnt lgkmcnt(4)
M_ v[202:205],a[12:15],v[134:137],v[202:205]
R_ a[32:35],v215,38912
s_waitcnt lgkmcnt(4)
M_ v[206:209],a[16:19],v[134:137],v[206:209]
R_ a[36:39],v215,39936
s_waitcnt lgkmcnt(4)
M_ v[190:193],a[20:23],v[138:141],v[190:193]
ds_read_b128 a[0:3],v216
s_waitcnt lgkmcnt(4)
M_ v[194:197],a[24:27],v[138:141],v[194:197]
R_ a[4:7],v216,1024
s_waitcnt lgkmcnt(4)
M_ v[198:201],a[28:31],v[138:141],v[198:201]
R_ a[8:11],v216,2048
s_waitcnt lgkmcnt(4)
M_ v[202:205],a[32:35],v[138:141],v[202:205]
R_ a[12:15],v216,3072
s_waitcnt lgkmcnt(4)
M_ v[206:209],a[36:39],v[138:141],v[206:209]
R_ a[16:19],v216,4096
s_waitcnt lgkmcnt(4)
M_ v[190:193],a[0:3],v[142:145],v[190:193]
R_ a[20:23],v216,5120
s_waitcnt lgkmcnt(4)
M_ v[194:197],a[4:7],v[142:145],v[194:197]
R_ a[24:27],v216,6144
s_waitcnt lgkmcnt(4)
M_ v[198:201],a[8:11],v[142:145],v[198:201]
R_ a[28:31],v216,7168
s_waitcnt lgkmcnt(4)
M_ v[202:205],a[12:15],v[142:145],v[202:205]
R_ a[32:35],v216,8192
s_waitcnt lgkmcnt(4)
M_ v[206:209],a[16:19],v[142:145],v[206:209]
R_ a[36:39],v216,9216
s_waitcnt lgkmcnt(4)
M_ v[190:193],a[20:23],v[146:149],v[190:193]
R_ a[0:3],v216,10240
s_waitcnt lgkmcnt(4)
M_ v[194:197],a[24:27],v[146:149],v[194:197]
R_ a[4:7],v216,11264
s_waitcnt lgkmcnt(4)
M_ v[198:201],a[28:31],v[146:149],v[198:201]
R_ a[8:11],v216,12288
s_waitcnt lgkmcnt(4)
M_ v[202:205],a[32:35],v[146:149],v[202:205]
R_ a[12:15],v216,13312
s_waitcnt lgkmcnt(4)
M_ v[206:209],a[36:39],v[146:149],v[206:209]
R_ a[16:19],v216,14336
s_waitcnt lgkmcnt(4)
M_ v[190:193],a[0:3],v[150:153],v[190:193]
R_ a[20:23],v216,15360
s_waitcnt lgkmcnt(4)
M_ v[194:197],a[4:7],v[150:153],v[194:197]
R_ a[24:27],v216,16384
s_waitcnt lgkmcnt(4)
M_ v[198:201],a[8:11],v[150:153],v[198:201]
R_ a[28:31],v216,17408
s_waitcnt lgkmcnt(4)
M_ v[202:205],a[12:15],v[150:153],v[202:205]
R_ a[32:35],v216,18432
s_waitcnt lgkmcnt(4)
M_ v[206:209],a[16:19],v[150:153],v[206:209]
R_ a[36:39],v216,19456
s_waitcnt lgkmcnt(4)
M_ v[190:193],a[20:23],v[154:157],v[190:193]
R_ a[0:3],v216,20480
s_waitcnt lgkmcnt(4)
M_ v[194:197],a[24:27],v[154:157],v[194:197]
R_ a[4:7],v216,21504
s_waitcnt lgkmcnt(4)
M_ v[198:201],a[28:31],v[154:157],v[198:201]
R_ a[8:11],v216,22528
s_waitcnt lgkmcnt(4)
M_ v[202:205],a[32:35],v[154:157],v[202:205]
R_ a[12:15],v216,23552
s_waitcnt lgkmcnt(4)
M_ v[206:209],a[36:39],v[154:157],v[206:209]
R_ a[16:19],v216,24576
s_waitcnt lgkmcnt(4)
M_ v[190:193],a[0:3],v[158:161],v[190:193]
R_ a[20:23],v216,25600
s_waitcnt lgkmcnt(4)
M_ v[194:197],a[4:7],v[158:161],v[194:197]
R_ a[24:27],v216,26624
s_waitcnt lgkmcnt(4)
M_ v[198:201],a[8:11],v[158:161],v[198:201]
R_ a[28:31],v216,27648
s_waitcnt lgkmcnt(4)
M_ v[202:205],a[12:15],v[158:161],v[202:205]
R_ a[32:35],v216,28672
s_waitcnt lgkmcnt(4)
M_ v[206:209],a[16:19],v[158:161],v[206:209]
R_ a[36:39],v216,29696
s_waitcnt lgkmcnt(4)
M_ v[190:193],a[20:23],v[162:165],v[190:193]
R_ a[0:3],v216,30720
s_waitcnt lgkmcnt(4)
M_ v[194:197],a[24:27],v[162:165],v[194:197]
R_ a[4:7],v216,31744
s_waitcnt lgkmcnt(4)
M_ v[198:201],a[28:31],v[162:165],v[198:201]
R_ a[8:11],v216,32768
s_waitcnt lgkmcnt(4)
M_ v[202:205],a[32:35],v[162:165],v[202:205]
R_ a[12:15],v216,33792
s_waitcnt lgkmcnt(4)
M_ v[206:209],a[36:39],v[162:165],v[206:209]
R_ a[16:19],v216,34816
s_waitcnt vmcnt(0)
s_barrier
s_lshl_b32 s32,s26,2
s_add_u32 s32,s32,s18
s_cmp_lg_u32 s3,0
s_cbranch_scc1 LPB54_%=
s_lshl_b32 s54,s32,2
s_add_u32 s54,s54,0x6401800
v_mov_b32 v222,s54
v_mov_b32 v223,s86
s_mov_b64 exec,1
global_store_dword v222,v223,s[88:89] sc1
s_mov_b64 exec,-1
LPB54_%=:
s_lshl_b32 s32,s26,2
v_mbcnt_lo_u32_b32 v221,-1,0
v_mbcnt_hi_u32_b32 v221,-1,v221
v_min_u32 v221,3,v221
v_add_u32 v221,s32,v221
v_lshlrev_b32 v221,2,v221
v_add_u32 v221,0x6401800,v221
global_load_dword v222,v221,s[88:89] sc1
s_waitcnt lgkmcnt(0)
M_ v[190:193],a[0:3],v[166:169],v[190:193]
R_ a[20:23],v216,35840
M_ v[194:197],a[4:7],v[166:169],v[194:197]
R_ a[24:27],v216,36864
M_ v[198:201],a[8:11],v[166:169],v[198:201]
R_ a[28:31],v216,37888
M_ v[202:205],a[12:15],v[166:169],v[202:205]
R_ a[32:35],v216,38912
M_ v[206:209],a[16:19],v[166:169],v[206:209]
R_ a[36:39],v216,39936
s_waitcnt lgkmcnt(4)
M_ v[190:193],a[20:23],v[170:173],v[190:193]
R_ a[0:3],v216,40960
s_waitcnt lgkmcnt(4)
M_ v[194:197],a[24:27],v[170:173],v[194:197]
R_ a[4:7],v216,41984
s_waitcnt lgkmcnt(4)
M_ v[198:201],a[28:31],v[170:173],v[198:201]
R_ a[8:11],v216,43008
s_waitcnt lgkmcnt(4)
M_ v[202:205],a[32:35],v[170:173],v[202:205]
R_ a[12:15],v216,44032
s_waitcnt lgkmcnt(4)
M_ v[206:209],a[36:39],v[170:173],v[206:209]
R_ a[16:19],v216,45056
s_waitcnt lgkmcnt(4)
M_ v[190:193],a[0:3],v[174:177],v[190:193]
R_ a[20:23],v216,46080
s_waitcnt lgkmcnt(4)
M_ v[194:197],a[4:7],v[174:177],v[194:197]
R_ a[24:27],v216,47104
s_waitcnt lgkmcnt(4)
M_ v[198:201],a[8:11],v[174:177],v[198:201]
R_ a[28:31],v216,48128
s_waitcnt lgkmcnt(4)
M_ v[202:205],a[12:15],v[174:177],v[202:205]
R_ a[32:35],v216,49152
s_waitcnt lgkmcnt(4)
M_ v[206:209],a[16:19],v[174:177],v[206:209]
R_ a[36:39],v216,50176
s_waitcnt lgkmcnt(4)
M_ v[190:193],a[20:23],v[178:181],v[190:193]
R_ a[0:3],v216,51200
s_waitcnt lgkmcnt(4)
M_ v[194:197],a[24:27],v[178:181],v[194:197]
R_ a[4:7],v216,52224
s_waitcnt lgkmcnt(4)
M_ v[198:201],a[28:31],v[178:181],v[198:201]
R_ a[8:11],v216,53248
s_waitcnt lgkmcnt(4)
M_ v[202:205],a[32:35],v[178:181],v[202:205]
R_ a[12:15],v216,54272
s_waitcnt lgkmcnt(4)
M_ v[206:209],a[36:39],v[178:181],v[206:209]
R_ a[16:19],v216,55296
s_waitcnt lgkmcnt(4)
M_ v[190:193],a[0:3],v[182:185],v[190:193]
R_ a[20:23],v216,56320
s_waitcnt lgkmcnt(4)
M_ v[194:197],a[4:7],v[182:185],v[194:197]
R_ a[24:27],v216,57344
s_waitcnt lgkmcnt(4)
M_ v[198:201],a[8:11],v[182:185],v[198:201]
R_ a[28:31],v216,58368
s_waitcnt lgkmcnt(4)
M_ v[202:205],a[12:15],v[182:185],v[202:205]
R_ a[32:35],v216,59392
s_waitcnt lgkmcnt(4)
M_ v[206:209],a[16:19],v[182:185],v[206:209]
R_ a[36:39],v216,60416
s_waitcnt lgkmcnt(4)
M_ v[190:193],a[20:23],v[186:189],v[190:193]
s_waitcnt lgkmcnt(3)
M_ v[194:197],a[24:27],v[186:189],v[194:197]
s_waitcnt lgkmcnt(2)
M_ v[198:201],a[28:31],v[186:189],v[198:201]
s_waitcnt lgkmcnt(1)
M_ v[202:205],a[32:35],v[186:189],v[202:205]
s_waitcnt lgkmcnt(0)
M_ v[206:209],a[36:39],v[186:189],v[206:209]
v_and_b32 v95,15,v89
v_mul_u32_u24 v95,0xa00,v95
v_lshrrev_b32 v96,4,v89
v_lshl_add_u32 v95,v96,4,v95
s_lshl_b32 s54,s48,2
s_add_u32 s54,s54,s3
s_lshl_b32 s54,s54,4
s_mul_i32 s54,s54,2560
s_lshr_b32 s55,s49,1
s_mul_i32 s55,s55,320
s_add_u32 s54,s54,s55
s_add_u32 s54,s54,1280
s_add_u32 s76,s12,s54
s_addc_u32 s77,s13,0
s_and_b32 s56,s49,1
s_nop 3
s_cmp_lg_u32 s56,0
s_cbranch_scc1 LB3h55_%=
C_ v224,v190,v191
C_ v225,v192,v193
C_ v226,v194,v195
C_ v227,v196,v197
global_store_dwordx4 v95,v[224:227],s[76:77]
C_ v228,v198,v199
C_ v229,v200,v201
C_ v230,v202,v203
C_ v231,v204,v205
global_store_dwordx4 v95,v[228:231],s[76:77] offset:64
C_ v240,v206,v207
C_ v241,v208,v209
global_store_dwordx2 v95,v[240:241],s[76:77] offset:128
s_branch LB3E55_%=
LB3h55_%=:
C_ v240,v190,v191
C_ v241,v192,v193
global_store_dwordx2 v95,v[240:241],s[76:77] offset:136
C_ v224,v194,v195
C_ v225,v196,v197
C_ v226,v198,v199
C_ v227,v200,v201
global_store_dwordx4 v95,v[224:227],s[76:77] offset:192
C_ v228,v202,v203
C_ v229,v204,v205
C_ v230,v206,v207
C_ v231,v208,v209
global_store_dwordx4 v95,v[228:231],s[76:77] offset:256
LB3E55_%=:
s_waitcnt vmcnt(0)
v_cmp_ne_u32 vcc,s86,v222
s_cbranch_vccz LQD56_%=
s_mov_b32 s59,0x30d40
LQL56_%=:
s_sleep 2
global_load_dword v222,v221,s[88:89] sc1
s_waitcnt vmcnt(0)
v_cmp_ne_u32 vcc,s86,v222
s_cbranch_vccz LQD56_%=
s_sub_u32 s59,s59,1
s_cmp_lg_u32 s59,0
s_cbranch_scc1 LQL56_%=
LQD56_%=:
v_and_b32 v95,15,v89
v_mul_u32_u24 v95,0xa00,v95
v_lshrrev_b32 v96,4,v89
v_lshl_add_u32 v95,v96,3,v95
v_add_u32 v96,0xa000,v95
s_lshl_b32 s54,s16,7
s_lshl_b32 s55,s3,5
s_add_u32 s54,s54,s55
s_mul_i32 s54,s54,2560
s_mul_i32 s55,s17,320
s_add_u32 s54,s54,s55
s_add_u32 s72,s12,s54
s_addc_u32 s73,s13,0
s_cmp_lg_u32 s18,0
s_cbranch_scc1 LFN57n0_%=
s_lshl_b32 s28,s26,2
s_add_u32 s28,s28,0
s_lshl_b32 s28,s28,2
s_add_u32 s28,s28,1
s_lshl_b32 s28,s28,2
)A"
R"A(s_add_u32 s28,s28,s3
s_mul_i32 s28,s28,3072
s_add_u32 s28,s28,0x5200000
s_add_u32 s36,s88,s28
s_addc_u32 s37,s89,0
global_load_dwordx4 v[110:113],v88,s[36:37] sc1
global_load_dwordx4 v[114:117],v88,s[36:37] offset:1024 sc1
global_load_dwordx4 v[118:121],v88,s[36:37] offset:2048 sc1
s_lshl_b32 s28,s26,2
s_add_u32 s28,s28,0
s_lshl_b32 s28,s28,2
s_add_u32 s28,s28,2
s_lshl_b32 s28,s28,2
s_add_u32 s28,s28,s3
s_mul_i32 s28,s28,3072
s_add_u32 s28,s28,0x5200000
s_add_u32 s36,s88,s28
s_addc_u32 s37,s89,0
global_load_dwordx4 v[122:125],v88,s[36:37] sc1
global_load_dwordx4 v[126:129],v88,s[36:37] offset:1024 sc1
global_load_dwordx4 v[130:133],v88,s[36:37] offset:2048 sc1
s_lshl_b32 s28,s26,2
s_add_u32 s28,s28,0
s_lshl_b32 s28,s28,2
s_add_u32 s28,s28,3
s_lshl_b32 s28,s28,2
s_add_u32 s28,s28,s3
s_mul_i32 s28,s28,3072
s_add_u32 s28,s28,0x5200000
s_add_u32 s36,s88,s28
s_addc_u32 s37,s89,0
global_load_dwordx4 v[134:137],v88,s[36:37] sc1
global_load_dwordx4 v[138:141],v88,s[36:37] offset:1024 sc1
global_load_dwordx4 v[142:145],v88,s[36:37] offset:2048 sc1
s_waitcnt vmcnt(0)
v_lshlrev_b32 v98,16,v110
v_and_b32 v99,0xffff0000,v110
v_add_f32 v0,v0,v98
v_add_f32 v1,v1,v99
v_lshlrev_b32 v98,16,v111
v_and_b32 v99,0xffff0000,v111
v_add_f32 v2,v2,v98
v_add_f32 v3,v3,v99
v_lshlrev_b32 v98,16,v112
v_and_b32 v99,0xffff0000,v112
v_add_f32 v4,v4,v98
v_add_f32 v5,v5,v99
v_lshlrev_b32 v98,16,v113
v_and_b32 v99,0xffff0000,v113
v_add_f32 v6,v6,v98
v_add_f32 v7,v7,v99
v_lshlrev_b32 v98,16,v114
v_and_b32 v99,0xffff0000,v114
v_add_f32 v8,v8,v98
v_add_f32 v9,v9,v99
v_lshlrev_b32 v98,16,v115
v_and_b32 v99,0xffff0000,v115
v_add_f32 v10,v10,v98
v_add_f32 v11,v11,v99
v_lshlrev_b32 v98,16,v116
v_and_b32 v99,0xffff0000,v116
v_add_f32 v12,v12,v98
v_add_f32 v13,v13,v99
v_lshlrev_b32 v98,16,v117
v_and_b32 v99,0xffff0000,v117
v_add_f32 v14,v14,v98
v_add_f32 v15,v15,v99
v_lshlrev_b32 v98,16,v118
v_and_b32 v99,0xffff0000,v118
v_add_f32 v16,v16,v98
v_add_f32 v17,v17,v99
v_lshlrev_b32 v98,16,v119
v_and_b32 v99,0xffff0000,v119
v_add_f32 v18,v18,v98
v_add_f32 v19,v19,v99
v_lshlrev_b32 v98,16,v120
v_and_b32 v99,0xffff0000,v120
v_add_f32 v20,v20,v98
v_add_f32 v21,v21,v99
v_lshlrev_b32 v98,16,v121
v_and_b32 v99,0xffff0000,v121
v_add_f32 v22,v22,v98
v_add_f32 v23,v23,v99
v_lshlrev_b32 v98,16,v122
v_and_b32 v99,0xffff0000,v122
v_add_f32 v0,v0,v98
v_add_f32 v1,v1,v99
v_lshlrev_b32 v98,16,v123
v_and_b32 v99,0xffff0000,v123
v_add_f32 v2,v2,v98
v_add_f32 v3,v3,v99
v_lshlrev_b32 v98,16,v124
v_and_b32 v99,0xffff0000,v124
v_add_f32 v4,v4,v98
v_add_f32 v5,v5,v99
v_lshlrev_b32 v98,16,v125
v_and_b32 v99,0xffff0000,v125
v_add_f32 v6,v6,v98
v_add_f32 v7,v7,v99
v_lshlrev_b32 v98,16,v126
v_and_b32 v99,0xffff0000,v126
v_add_f32 v8,v8,v98
v_add_f32 v9,v9,v99
v_lshlrev_b32 v98,16,v127
v_and_b32 v99,0xffff0000,v127
v_add_f32 v10,v10,v98
v_add_f32 v11,v11,v99
v_lshlrev_b32 v98,16,v128
v_and_b32 v99,0xffff0000,v128
v_add_f32 v12,v12,v98
v_add_f32 v13,v13,v99
v_lshlrev_b32 v98,16,v129
v_and_b32 v99,0xffff0000,v129
v_add_f32 v14,v14,v98
v_add_f32 v15,v15,v99
v_lshlrev_b32 v98,16,v130
v_and_b32 v99,0xffff0000,v130
v_add_f32 v16,v16,v98
v_add_f32 v17,v17,v99
v_lshlrev_b32 v98,16,v131
v_and_b32 v99,0xffff0000,v131
v_add_f32 v18,v18,v98
v_add_f32 v19,v19,v99
v_lshlrev_b32 v98,16,v132
v_and_b32 v99,0xffff0000,v132
v_add_f32 v20,v20,v98
v_add_f32 v21,v21,v99
v_lshlrev_b32 v98,16,v133
v_and_b32 v99,0xffff0000,v133
v_add_f32 v22,v22,v98
v_add_f32 v23,v23,v99
v_lshlrev_b32 v98,16,v134
v_and_b32 v99,0xffff0000,v134
v_add_f32 v0,v0,v98
v_add_f32 v1,v1,v99
v_lshlrev_b32 v98,16,v135
v_and_b32 v99,0xffff0000,v135
v_add_f32 v2,v2,v98
v_add_f32 v3,v3,v99
v_lshlrev_b32 v98,16,v136
v_and_b32 v99,0xffff0000,v136
v_add_f32 v4,v4,v98
v_add_f32 v5,v5,v99
v_lshlrev_b32 v98,16,v137
v_and_b32 v99,0xffff0000,v137
v_add_f32 v6,v6,v98
v_add_f32 v7,v7,v99
v_lshlrev_b32 v98,16,v138
v_and_b32 v99,0xffff0000,v138
v_add_f32 v8,v8,v98
v_add_f32 v9,v9,v99
v_lshlrev_b32 v98,16,v139
v_and_b32 v99,0xffff0000,v139
v_add_f32 v10,v10,v98
v_add_f32 v11,v11,v99
v_lshlrev_b32 v98,16,v140
v_and_b32 v99,0xffff0000,v140
v_add_f32 v12,v12,v98
v_add_f32 v13,v13,v99
v_lshlrev_b32 v98,16,v141
v_and_b32 v99,0xffff0000,v141
v_add_f32 v14,v14,v98
v_add_f32 v15,v15,v99
v_lshlrev_b32 v98,16,v142
v_and_b32 v99,0xffff0000,v142
v_add_f32 v16,v16,v98
v_add_f32 v17,v17,v99
v_lshlrev_b32 v98,16,v143
v_and_b32 v99,0xffff0000,v143
v_add_f32 v18,v18,v98
v_add_f32 v19,v19,v99
v_lshlrev_b32 v98,16,v144
v_and_b32 v99,0xffff0000,v144
v_add_f32 v20,v20,v98
v_add_f32 v21,v21,v99
v_lshlrev_b32 v98,16,v145
v_and_b32 v99,0xffff0000,v145
v_add_f32 v22,v22,v98
v_add_f32 v23,v23,v99
C_ v150,v0,v1
C_ v151,v2,v3
global_store_dwordx2 v95,v[150:151],s[72:73]
C_ v152,v4,v5
C_ v153,v6,v7
global_store_dwordx2 v96,v[152:153],s[72:73]
C_ v154,v8,v9
C_ v155,v10,v11
global_store_dwordx2 v95,v[154:155],s[72:73] offset:32
C_ v156,v12,v13
C_ v157,v14,v15
global_store_dwordx2 v96,v[156:157],s[72:73] offset:32
C_ v158,v16,v17
C_ v159,v18,v19
global_store_dwordx2 v95,v[158:159],s[72:73] offset:64
C_ v160,v20,v21
C_ v161,v22,v23
global_store_dwordx2 v96,v[160:161],s[72:73] offset:64
s_branch LFN57e_%=
LFN57n0_%=:
s_cmp_lg_u32 s18,1
s_cbranch_scc1 LFN57n1_%=
s_lshl_b32 s28,s26,2
s_add_u32 s28,s28,1
s_lshl_b32 s28,s28,2
s_add_u32 s28,s28,2
s_lshl_b32 s28,s28,2
s_add_u32 s28,s28,s3
s_mul_i32 s28,s28,3072
s_add_u32 s28,s28,0x5200000
s_add_u32 s36,s88,s28
s_addc_u32 s37,s89,0
global_load_dwordx4 v[110:113],v88,s[36:37] sc1
global_load_dwordx4 v[114:117],v88,s[36:37] offset:1024 sc1
global_load_dwordx4 v[118:121],v88,s[36:37] offset:2048 sc1
s_lshl_b32 s28,s26,2
s_add_u32 s28,s28,1
s_lshl_b32 s28,s28,2
s_add_u32 s28,s28,3
s_lshl_b32 s28,s28,2
s_add_u32 s28,s28,s3
s_mul_i32 s28,s28,3072
s_add_u32 s28,s28,0x5200000
s_add_u32 s36,s88,s28
s_addc_u32 s37,s89,0
global_load_dwordx4 v[122:125],v88,s[36:37] sc1
global_load_dwordx4 v[126:129],v88,s[36:37] offset:1024 sc1
global_load_dwordx4 v[130:133],v88,s[36:37] offset:2048 sc1
s_lshl_b32 s28,s26,2
s_add_u32 s28,s28,1
s_lshl_b32 s28,s28,2
s_add_u32 s28,s28,0
s_lshl_b32 s28,s28,2
s_add_u32 s28,s28,s3
s_mul_i32 s28,s28,3072
s_add_u32 s28,s28,0x5200000
s_add_u32 s36,s88,s28
s_addc_u32 s37,s89,0
global_load_dwordx4 v[134:137],v88,s[36:37] sc1
global_load_dwordx4 v[138:141],v88,s[36:37] offset:1024 sc1
global_load_dwordx4 v[142:145],v88,s[36:37] offset:2048 sc1
s_waitcnt vmcnt(0)
v_lshlrev_b32 v98,16,v110
v_and_b32 v99,0xffff0000,v110
v_add_f32 v24,v24,v98
v_add_f32 v25,v25,v99
v_lshlrev_b32 v98,16,v111
v_and_b32 v99,0xffff0000,v111
v_add_f32 v26,v26,v98
v_add_f32 v27,v27,v99
v_lshlrev_b32 v98,16,v112
v_and_b32 v99,0xffff0000,v112
v_add_f32 v28,v28,v98
v_add_f32 v29,v29,v99
v_lshlrev_b32 v98,16,v113
v_and_b32 v99,0xffff0000,v113
v_add_f32 v30,v30,v98
v_add_f32 v31,v31,v99
v_lshlrev_b32 v98,16,v114
v_and_b32 v99,0xffff0000,v114
v_add_f32 v32,v32,v98
v_add_f32 v33,v33,v99
v_lshlrev_b32 v98,16,v115
v_and_b32 v99,0xffff0000,v115
v_add_f32 v34,v34,v98
v_add_f32 v35,v35,v99
v_lshlrev_b32 v98,16,v116
v_and_b32 v99,0xffff0000,v116
v_add_f32 v36,v36,v98
v_add_f32 v37,v37,v99
v_lshlrev_b32 v98,16,v117
v_and_b32 v99,0xffff0000,v117
v_add_f32 v38,v38,v98
v_add_f32 v39,v39,v99
v_lshlrev_b32 v98,16,v118
v_and_b32 v99,0xffff0000,v118
v_add_f32 v40,v40,v98
v_add_f32 v41,v41,v99
v_lshlrev_b32 v98,16,v119
v_and_b32 v99,0xffff0000,v119
v_add_f32 v42,v42,v98
v_add_f32 v43,v43,v99
v_lshlrev_b32 v98,16,v120
v_and_b32 v99,0xffff0000,v120
v_add_f32 v44,v44,v98
v_add_f32 v45,v45,v99
v_lshlrev_b32 v98,16,v121
v_and_b32 v99,0xffff0000,v121
v_add_f32 v46,v46,v98
v_add_f32 v47,v47,v99
v_lshlrev_b32 v98,16,v122
v_and_b32 v99,0xffff0000,v122
v_add_f32 v24,v24,v98
v_add_f32 v25,v25,v99
v_lshlrev_b32 v98,16,v123
v_and_b32 v99,0xffff0000,v123
v_add_f32 v26,v26,v98
v_add_f32 v27,v27,v99
v_lshlrev_b32 v98,16,v124
v_and_b32 v99,0xffff0000,v124
v_add_f32 v28,v28,v98
v_add_f32 v29,v29,v99
v_lshlrev_b32 v98,16,v125
v_and_b32 v99,0xffff0000,v125
v_add_f32 v30,v30,v98
v_add_f32 v31,v31,v99
v_lshlrev_b32 v98,16,v126
v_and_b32 v99,0xffff0000,v126
v_add_f32 v32,v32,v98
v_add_f32 v33,v33,v99
v_lshlrev_b32 v98,16,v127
v_and_b32 v99,0xffff0000,v127
v_add_f32 v34,v34,v98
v_add_f32 v35,v35,v99
v_lshlrev_b32 v98,16,v128
v_and_b32 v99,0xffff0000,v128
v_add_f32 v36,v36,v98
v_add_f32 v37,v37,v99
v_lshlrev_b32 v98,16,v129
v_and_b32 v99,0xffff0000,v129
v_add_f32 v38,v38,v98
v_add_f32 v39,v39,v99
v_lshlrev_b32 v98,16,v130
v_and_b32 v99,0xffff0000,v130
v_add_f32 v40,v40,v98
v_add_f32 v41,v41,v99
v_lshlrev_b32 v98,16,v131
v_and_b32 v99,0xffff0000,v131
v_add_f32 v42,v42,v98
v_add_f32 v43,v43,v99
v_lshlrev_b32 v98,16,v132
v_and_b32 v99,0xffff0000,v132
v_add_f32 v44,v44,v98
v_add_f32 v45,v45,v99
v_lshlrev_b32 v98,16,v133
v_and_b32 v99,0xffff0000,v133
v_add_f32 v46,v46,v98
v_add_f32 v47,v47,v99
v_lshlrev_b32 v98,16,v134
v_and_b32 v99,0xffff0000,v134
v_add_f32 v24,v24,v98
v_add_f32 v25,v25,v99
v_lshlrev_b32 v98,16,v135
v_and_b32 v99,0xffff0000,v135
v_add_f32 v26,v26,v98
v_add_f32 v27,v27,v99
v_lshlrev_b32 v98,16,v136
v_and_b32 v99,0xffff0000,v136
v_add_f32 v28,v28,v98
v_add_f32 v29,v29,v99
v_lshlrev_b32 v98,16,v137
v_and_b32 v99,0xffff0000,v137
v_add_f32 v30,v30,v98
v_add_f32 v31,v31,v99
v_lshlrev_b32 v98,16,v138
v_and_b32 v99,0xffff0000,v138
v_add_f32 v32,v32,v98
v_add_f32 v33,v33,v99
v_lshlrev_b32 v98,16,v139
v_and_b32 v99,0xffff0000,v139
v_add_f32 v34,v34,v98
v_add_f32 v35,v35,v99
v_lshlrev_b32 v98,16,v140
v_and_b32 v99,0xffff0000,v140
v_add_f32 v36,v36,v98
v_add_f32 v37,v37,v99
v_lshlrev_b32 v98,16,v141
v_and_b32 v99,0xffff0000,v141
v_add_f32 v38,v38,v98
v_add_f32 v39,v39,v99
v_lshlrev_b32 v98,16,v142
v_and_b32 v99,0xffff0000,v142
v_add_f32 v40,v40,v98
v_add_f32 v41,v41,v99
v_lshlrev_b32 v98,16,v143
v_and_b32 v99,0xffff0000,v143
v_add_f32 v42,v42,v98
v_add_f32 v43,v43,v99
v_lshlrev_b32 v98,16,v144
v_and_b32 v99,0xffff0000,v144
v_add_f32 v44,v44,v98
v_add_f32 v45,v45,v99
v_lshlrev_b32 v98,16,v145
v_and_b32 v99,0xffff0000,v145
v_add_f32 v46,v46,v98
v_add_f32 v47,v47,v99
C_ v150,v24,v25
C_ v151,v26,v27
global_store_dwordx2 v95,v[150:151],s[72:73] offset:96
C_ v152,v28,v29
C_ v153,v30,v31
global_store_dwordx2 v96,v[152:153],s[72:73] offset:96
C_ v154,v32,v33
C_ v155,v34,v35
global_store_dwordx2 v95,v[154:155],s[72:73] offset:128
C_ v156,v36,v37
C_ v157,v38,v39
global_store_dwordx2 v96,v[156:157],s[72:73] offset:128
C_ v158,v40,v41
C_ v159,v42,v43
global_store_dwordx2 v95,v[158:159],s[72:73] offset:160
C_ v160,v44,v45
C_ v161,v46,v47
global_store_dwordx2 v96,v[160:161],s[72:73] offset:160
s_branch LFN57e_%=
LFN57n1_%=:
s_cmp_lg_u32 s18,2
s_cbranch_scc1 LFN57n2_%=
s_lshl_b32 s28,s26,2
s_add_u32 s28,s28,2
s_lshl_b32 s28,s28,2
s_add_u32 s28,s28,3
s_lshl_b32 s28,s28,2
s_add_u32 s28,s28,s3
s_mul_i32 s28,s28,3072
s_add_u32 s28,s28,0x5200000
s_add_u32 s36,s88,s28
s_addc_u32 s37,s89,0
global_load_dwordx4 v[110:113],v88,s[36:37] sc1
global_load_dwordx4 v[114:117],v88,s[36:37] offset:1024 sc1
s_lshl_b32 s28,s26,2
s_add_u32 s28,s28,2
s_lshl_b32 s28,s28,2
s_add_u32 s28,s28,0
s_lshl_b32 s28,s28,2
s_add_u32 s28,s28,s3
s_mul_i32 s28,s28,3072
s_add_u32 s28,s28,0x5200000
s_add_u32 s36,s88,s28
s_addc_u32 s37,s89,0
global_load_dwordx4 v[122:125],v88,s[36:37] sc1
global_load_dwordx4 v[126:129],v88,s[36:37] offset:1024 sc1
s_lshl_b32 s28,s26,2
s_add_u32 s28,s28,2
s_lshl_b32 s28,s28,2
s_add_u32 s28,s28,1
s_lshl_b32 s28,s28,2
s_add_u32 s28,s28,s3
s_mul_i32 s28,s28,3072
s_add_u32 s28,s28,0x5200000
s_add_u32 s36,s88,s28
s_addc_u32 s37,s89,0
global_load_dwordx4 v[134:137],v88,s[36:37] sc1
global_load_dwordx4 v[138:141],v88,s[36:37] offset:1024 sc1
s_waitcnt vmcnt(0)
v_lshlrev_b32 v98,16,v110
v_and_b32 v99,0xffff0000,v110
v_add_f32 v48,v48,v98
v_add_f32 v49,v49,v99
v_lshlrev_b32 v98,16,v111
v_and_b32 v99,0xffff0000,v111
v_add_f32 v50,v50,v98
v_add_f32 v51,v51,v99
v_lshlrev_b32 v98,16,v112
v_and_b32 v99,0xffff0000,v112
v_add_f32 v52,v52,v98
v_add_f32 v53,v53,v99
v_lshlrev_b32 v98,16,v113
v_and_b32 v99,0xffff0000,v113
v_add_f32 v54,v54,v98
v_add_f32 v55,v55,v99
v_lshlrev_b32 v98,16,v114
v_and_b32 v99,0xffff0000,v114
v_add_f32 v56,v56,v98
v_add_f32 v57,v57,v99
v_lshlrev_b32 v98,16,v115
v_and_b32 v99,0xffff0000,v115
v_add_f32 v58,v58,v98
v_add_f32 v59,v59,v99
v_lshlrev_b32 v98,16,v116
v_and_b32 v99,0xffff0000,v116
v_add_f32 v60,v60,v98
v_add_f32 v61,v61,v99
v_lshlrev_b32 v98,16,v117
v_and_b32 v99,0xffff0000,v117
v_add_f32 v62,v62,v98
v_add_f32 v63,v63,v99
v_lshlrev_b32 v98,16,v122
v_and_b32 v99,0xffff0000,v122
v_add_f32 v48,v48,v98
v_add_f32 v49,v49,v99
v_lshlrev_b32 v98,16,v123
v_and_b32 v99,0xffff0000,v123
v_add_f32 v50,v50,v98
v_add_f32 v51,v51,v99
v_lshlrev_b32 v98,16,v124
v_and_b32 v99,0xffff0000,v124
v_add_f32 v52,v52,v98
v_add_f32 v53,v53,v99
v_lshlrev_b32 v98,16,v125
v_and_b32 v99,0xffff0000,v125
v_add_f32 v54,v54,v98
v_add_f32 v55,v55,v99
v_lshlrev_b32 v98,16,v126
v_and_b32 v99,0xffff0000,v126
v_add_f32 v56,v56,v98
v_add_f32 v57,v57,v99
v_lshlrev_b32 v98,16,v127
v_and_b32 v99,0xffff0000,v127
v_add_f32 v58,v58,v98
v_add_f32 v59,v59,v99
v_lshlrev_b32 v98,16,v128
v_and_b32 v99,0xffff0000,v128
v_add_f32 v60,v60,v98
v_add_f32 v61,v61,v99
v_lshlrev_b32 v98,16,v129
v_and_b32 v99,0xffff0000,v129
v_add_f32 v62,v62,v98
v_add_f32 v63,v63,v99
v_lshlrev_b32 v98,16,v134
v_and_b32 v99,0xffff0000,v134
v_add_f32 v48,v48,v98
v_add_f32 v49,v49,v99
v_lshlrev_b32 v98,16,v135
v_and_b32 v99,0xffff0000,v135
v_add_f32 v50,v50,v98
v_add_f32 v51,v51,v99
v_lshlrev_b32 v98,16,v136
v_and_b32 v99,0xffff0000,v136
v_add_f32 v52,v52,v98
v_add_f32 v53,v53,v99
v_lshlrev_b32 v98,16,v137
v_and_b32 v99,0xffff0000,v137
v_add_f32 v54,v54,v98
v_add_f32 v55,v55,v99
v_lshlrev_b32 v98,16,v138
v_and_b32 v99,0xffff0000,v138
v_add_f32 v56,v56,v98
v_add_f32 v57,v57,v99
v_lshlrev_b32 v98,16,v139
v_and_b32 v99,0xffff0000,v139
v_add_f32 v58,v58,v98
v_add_f32 v59,v59,v99
v_lshlrev_b32 v98,16,v140
v_and_b32 v99,0xffff0000,v140
v_add_f32 v60,v60,v98
v_add_f32 v61,v61,v99
v_lshlrev_b32 v98,16,v141
v_and_b32 v99,0xffff0000,v141
v_add_f32 v62,v62,v98
v_add_f32 v63,v63,v99
C_ v150,v48,v49
C_ v151,v50,v51
)A"
R"A(global_store_dwordx2 v95,v[150:151],s[72:73] offset:192
C_ v152,v52,v53
C_ v153,v54,v55
global_store_dwordx2 v96,v[152:153],s[72:73] offset:192
C_ v154,v56,v57
C_ v155,v58,v59
global_store_dwordx2 v95,v[154:155],s[72:73] offset:224
C_ v156,v60,v61
C_ v157,v62,v63
global_store_dwordx2 v96,v[156:157],s[72:73] offset:224
s_branch LFN57e_%=
LFN57n2_%=:
s_lshl_b32 s28,s26,2
s_add_u32 s28,s28,3
s_lshl_b32 s28,s28,2
s_add_u32 s28,s28,0
s_lshl_b32 s28,s28,2
s_add_u32 s28,s28,s3
s_mul_i32 s28,s28,3072
s_add_u32 s28,s28,0x5200000
s_add_u32 s36,s88,s28
s_addc_u32 s37,s89,0
global_load_dwordx4 v[110:113],v88,s[36:37] sc1
global_load_dwordx4 v[114:117],v88,s[36:37] offset:1024 sc1
s_lshl_b32 s28,s26,2
s_add_u32 s28,s28,3
s_lshl_b32 s28,s28,2
s_add_u32 s28,s28,1
s_lshl_b32 s28,s28,2
s_add_u32 s28,s28,s3
s_mul_i32 s28,s28,3072
s_add_u32 s28,s28,0x5200000
s_add_u32 s36,s88,s28
s_addc_u32 s37,s89,0
global_load_dwordx4 v[122:125],v88,s[36:37] sc1
global_load_dwordx4 v[126:129],v88,s[36:37] offset:1024 sc1
s_lshl_b32 s28,s26,2
s_add_u32 s28,s28,3
s_lshl_b32 s28,s28,2
s_add_u32 s28,s28,2
s_lshl_b32 s28,s28,2
s_add_u32 s28,s28,s3
s_mul_i32 s28,s28,3072
s_add_u32 s28,s28,0x5200000
s_add_u32 s36,s88,s28
s_addc_u32 s37,s89,0
global_load_dwordx4 v[134:137],v88,s[36:37] sc1
global_load_dwordx4 v[138:141],v88,s[36:37] offset:1024 sc1
s_waitcnt vmcnt(0)
v_lshlrev_b32 v98,16,v110
v_and_b32 v99,0xffff0000,v110
v_add_f32 v64,v64,v98
v_add_f32 v65,v65,v99
v_lshlrev_b32 v98,16,v111
v_and_b32 v99,0xffff0000,v111
v_add_f32 v66,v66,v98
v_add_f32 v67,v67,v99
v_lshlrev_b32 v98,16,v112
v_and_b32 v99,0xffff0000,v112
v_add_f32 v68,v68,v98
v_add_f32 v69,v69,v99
v_lshlrev_b32 v98,16,v113
v_and_b32 v99,0xffff0000,v113
v_add_f32 v70,v70,v98
v_add_f32 v71,v71,v99
v_lshlrev_b32 v98,16,v114
v_and_b32 v99,0xffff0000,v114
v_add_f32 v72,v72,v98
v_add_f32 v73,v73,v99
v_lshlrev_b32 v98,16,v115
v_and_b32 v99,0xffff0000,v115
v_add_f32 v74,v74,v98
v_add_f32 v75,v75,v99
v_lshlrev_b32 v98,16,v116
v_and_b32 v99,0xffff0000,v116
v_add_f32 v76,v76,v98
v_add_f32 v77,v77,v99
v_lshlrev_b32 v98,16,v117
v_and_b32 v99,0xffff0000,v117
v_add_f32 v78,v78,v98
v_add_f32 v79,v79,v99
v_lshlrev_b32 v98,16,v122
v_and_b32 v99,0xffff0000,v122
v_add_f32 v64,v64,v98
v_add_f32 v65,v65,v99
v_lshlrev_b32 v98,16,v123
v_and_b32 v99,0xffff0000,v123
v_add_f32 v66,v66,v98
v_add_f32 v67,v67,v99
v_lshlrev_b32 v98,16,v124
v_and_b32 v99,0xffff0000,v124
v_add_f32 v68,v68,v98
v_add_f32 v69,v69,v99
v_lshlrev_b32 v98,16,v125
v_and_b32 v99,0xffff0000,v125
v_add_f32 v70,v70,v98
v_add_f32 v71,v71,v99
v_lshlrev_b32 v98,16,v126
v_and_b32 v99,0xffff0000,v126
v_add_f32 v72,v72,v98
v_add_f32 v73,v73,v99
v_lshlrev_b32 v98,16,v127
v_and_b32 v99,0xffff0000,v127
v_add_f32 v74,v74,v98
v_add_f32 v75,v75,v99
v_lshlrev_b32 v98,16,v128
v_and_b32 v99,0xffff0000,v128
v_add_f32 v76,v76,v98
v_add_f32 v77,v77,v99
v_lshlrev_b32 v98,16,v129
v_and_b32 v99,0xffff0000,v129
v_add_f32 v78,v78,v98
v_add_f32 v79,v79,v99
v_lshlrev_b32 v98,16,v134
v_and_b32 v99,0xffff0000,v134
v_add_f32 v64,v64,v98
v_add_f32 v65,v65,v99
v_lshlrev_b32 v98,16,v135
v_and_b32 v99,0xffff0000,v135
v_add_f32 v66,v66,v98
v_add_f32 v67,v67,v99
v_lshlrev_b32 v98,16,v136
v_and_b32 v99,0xffff0000,v136
v_add_f32 v68,v68,v98
v_add_f32 v69,v69,v99
v_lshlrev_b32 v98,16,v137
v_and_b32 v99,0xffff0000,v137
v_add_f32 v70,v70,v98
v_add_f32 v71,v71,v99
v_lshlrev_b32 v98,16,v138
v_and_b32 v99,0xffff0000,v138
v_add_f32 v72,v72,v98
v_add_f32 v73,v73,v99
v_lshlrev_b32 v98,16,v139
v_and_b32 v99,0xffff0000,v139
v_add_f32 v74,v74,v98
v_add_f32 v75,v75,v99
v_lshlrev_b32 v98,16,v140
v_and_b32 v99,0xffff0000,v140
v_add_f32 v76,v76,v98
v_add_f32 v77,v77,v99
v_lshlrev_b32 v98,16,v141
v_and_b32 v99,0xffff0000,v141
v_add_f32 v78,v78,v98
v_add_f32 v79,v79,v99
C_ v150,v64,v65
C_ v151,v66,v67
global_store_dwordx2 v95,v[150:151],s[72:73] offset:256
C_ v152,v68,v69
C_ v153,v70,v71
global_store_dwordx2 v96,v[152:153],s[72:73] offset:256
C_ v154,v72,v73
C_ v155,v74,v75
global_store_dwordx2 v95,v[154:155],s[72:73] offset:288
C_ v156,v76,v77
C_ v157,v78,v79
global_store_dwordx2 v96,v[156:157],s[72:73] offset:288
LFN57e_%=:
s_cmp_lg_u32 s2,0
s_cbranch_scc1 LEP58_%=
s_cmp_lg_u32 s3,0
s_cbranch_scc1 LEP58_%=
v_mov_b32 v2,0x6404000
v_mov_b32 v3,s86
s_mov_b64 exec,1
global_store_dword v2,v3,s[88:89] sc1
s_mov_b64 exec,-1
LEP58_%=:
s_endpgm
LRF2_0_%=:
s_swappc_b64 s[44:45],s[36:37]
s_branch LBF2_0_%=
LRF2_1_%=:
s_swappc_b64 s[44:45],s[38:39]
s_branch LBF2_1_%=
LRF2_2_%=:
s_swappc_b64 s[44:45],s[40:41]
s_branch LBF2_2_%=
LRF2_3_%=:
s_swappc_b64 s[44:45],s[42:43]
s_branch LBF2_3_%=
LRF1_0_%=:
s_swappc_b64 s[44:45],s[36:37]
s_branch LBF1_0_%=
LRF1_1_%=:
s_swappc_b64 s[44:45],s[38:39]
s_branch LBF1_1_%=
LRF1_2_%=:
s_swappc_b64 s[44:45],s[40:41]
s_branch LBF1_2_%=
LRF1_3_%=:
s_swappc_b64 s[44:45],s[42:43]
s_branch LBF1_3_%=
LRF0_0_%=:
s_swappc_b64 s[44:45],s[36:37]
s_branch LBF0_0_%=
LRF0_1_%=:
s_swappc_b64 s[44:45],s[38:39]
s_branch LBF0_1_%=
LRF0_2_%=:
s_swappc_b64 s[44:45],s[40:41]
s_branch LBF0_2_%=
LRF0_3_%=:
s_swappc_b64 s[44:45],s[42:43]
s_branch LBF0_3_%=
LRD2_0_%=:
s_swappc_b64 s[44:45],s[36:37]
s_branch LBD2_0_%=
LRD2_1_%=:
s_swappc_b64 s[44:45],s[38:39]
s_branch LBD2_1_%=
LRD2_2_%=:
s_swappc_b64 s[44:45],s[40:41]
s_branch LBD2_2_%=
LRD2_3_%=:
s_swappc_b64 s[44:45],s[42:43]
s_branch LBD2_3_%=
LRD0_0_%=:
s_swappc_b64 s[44:45],s[36:37]
s_branch LBD0_0_%=
LRD0_1_%=:
s_swappc_b64 s[44:45],s[38:39]
s_branch LBD0_1_%=
LRD0_2_%=:
s_swappc_b64 s[44:45],s[40:41]
s_branch LBD0_2_%=
LRD0_3_%=:
s_swappc_b64 s[44:45],s[42:43]
s_branch LBD0_3_%=
LRNS2_3_%=:
s_swappc_b64 s[44:45],s[42:43]
s_branch LBNS2_3_%=
LRNS0_3_%=:
s_swappc_b64 s[44:45],s[42:43]
s_branch LBNS0_3_%=
LRN0_3_%=:
s_swappc_b64 s[44:45],s[42:43]
s_branch LBN0_3_%=
LRSW_3_%=:
s_swappc_b64 s[44:45],s[42:43]
s_branch LBSW_3_%=
LREND_3_%=:
s_swappc_b64 s[44:45],s[42:43]
s_branch LBEND_3_%=
LSLOW0_%=:
s_nop 7
v_max3_f32 v210,v128,v129,v130
v_max3_f32 v210,v210,v131,v132
v_max3_f32 v210,v210,v133,v134
v_max3_f32 v210,v210,v135,v136
v_max3_f32 v210,v210,v137,v138
v_max3_f32 v210,v210,v139,v140
v_max3_f32 v210,v210,v141,v142
v_max_f32 v210,v210,v143
v_mov_b32 v211,v210
s_nop 1
v_permlane32_swap_b32 v210,v211
v_max_f32 v210,v210,v211
v_mov_b32 v211,v210
s_nop 1
v_permlane16_swap_b32 v210,v211
v_max_f32 v210,v210,v211
v_max_f32 v214,0,v210
v_exp_f32 v212,-v214
v_sub_f32 v184,v184,v214
v_sub_f32 v185,v185,v214
v_sub_f32 v186,v186,v214
v_sub_f32 v187,v187,v214
v_pk_mul_f32 v[0:1],v[0:1],v[212:213] op_sel_hi:[1,0]
v_pk_mul_f32 v[2:3],v[2:3],v[212:213] op_sel_hi:[1,0]
v_pk_mul_f32 v[4:5],v[4:5],v[212:213] op_sel_hi:[1,0]
v_pk_mul_f32 v[6:7],v[6:7],v[212:213] op_sel_hi:[1,0]
v_pk_mul_f32 v[8:9],v[8:9],v[212:213] op_sel_hi:[1,0]
v_pk_mul_f32 v[10:11],v[10:11],v[212:213] op_sel_hi:[1,0]
v_pk_mul_f32 v[12:13],v[12:13],v[212:213] op_sel_hi:[1,0]
v_pk_mul_f32 v[14:15],v[14:15],v[212:213] op_sel_hi:[1,0]
v_pk_mul_f32 v[16:17],v[16:17],v[212:213] op_sel_hi:[1,0]
v_pk_mul_f32 v[18:19],v[18:19],v[212:213] op_sel_hi:[1,0]
v_pk_mul_f32 v[20:21],v[20:21],v[212:213] op_sel_hi:[1,0]
v_pk_mul_f32 v[22:23],v[22:23],v[212:213] op_sel_hi:[1,0]
v_pk_mul_f32 v[24:25],v[24:25],v[212:213] op_sel_hi:[1,0]
v_pk_mul_f32 v[26:27],v[26:27],v[212:213] op_sel_hi:[1,0]
v_pk_mul_f32 v[28:29],v[28:29],v[212:213] op_sel_hi:[1,0]
v_pk_mul_f32 v[30:31],v[30:31],v[212:213] op_sel_hi:[1,0]
v_mul_f32 v200,v200,v212
v_sub_f32 v128,v128,v214
v_sub_f32 v129,v129,v214
v_sub_f32 v130,v130,v214
v_sub_f32 v131,v131,v214
v_sub_f32 v132,v132,v214
v_sub_f32 v133,v133,v214
v_sub_f32 v134,v134,v214
v_sub_f32 v135,v135,v214
v_sub_f32 v136,v136,v214
v_sub_f32 v137,v137,v214
v_sub_f32 v138,v138,v214
v_sub_f32 v139,v139,v214
v_sub_f32 v140,v140,v214
v_sub_f32 v141,v141,v214
v_sub_f32 v142,v142,v214
v_sub_f32 v143,v143,v214
E_ v216,v128
E_ v217,v129
s_nop 0
v_add_f32 v204,v216,v217
C_ v160,v216,v217
E_ v218,v130
E_ v219,v131
s_nop 0
v_add_f32 v220,v218,v219
v_add_f32 v204,v204,v220
C_ v161,v218,v219
E_ v216,v132
E_ v217,v133
s_nop 0
v_add_f32 v220,v216,v217
v_add_f32 v204,v204,v220
C_ v162,v216,v217
E_ v218,v134
E_ v219,v135
s_nop 0
v_add_f32 v220,v218,v219
v_add_f32 v204,v204,v220
C_ v163,v218,v219
E_ v216,v136
E_ v217,v137
s_nop 0
v_add_f32 v220,v216,v217
v_add_f32 v204,v204,v220
C_ v164,v216,v217
E_ v218,v138
E_ v219,v139
s_nop 0
v_add_f32 v220,v218,v219
v_add_f32 v204,v204,v220
C_ v165,v218,v219
E_ v216,v140
E_ v217,v141
s_nop 0
v_add_f32 v220,v216,v217
v_add_f32 v204,v204,v220
C_ v166,v216,v217
E_ v218,v142
E_ v219,v143
s_nop 0
v_add_f32 v220,v218,v219
v_add_f32 v204,v204,v220
C_ v167,v218,v219
s_nop 1
s_setpc_b64 s[44:45]
LSLOW1_%=:
s_nop 7
v_max3_f32 v210,v144,v145,v146
v_max3_f32 v210,v210,v147,v148
v_max3_f32 v210,v210,v149,v150
v_max3_f32 v210,v210,v151,v152
v_max3_f32 v210,v210,v153,v154
v_max3_f32 v210,v210,v155,v156
v_max3_f32 v210,v210,v157,v158
v_max_f32 v210,v210,v159
v_mov_b32 v211,v210
s_nop 1
v_permlane32_swap_b32 v210,v211
v_max_f32 v210,v210,v211
v_mov_b32 v211,v210
s_nop 1
v_permlane16_swap_b32 v210,v211
v_max_f32 v210,v210,v211
v_max_f32 v214,0,v210
v_exp_f32 v212,-v214
v_sub_f32 v188,v188,v214
v_sub_f32 v189,v189,v214
v_sub_f32 v190,v190,v214
v_sub_f32 v191,v191,v214
v_pk_mul_f32 v[32:33],v[32:33],v[212:213] op_sel_hi:[1,0]
v_pk_mul_f32 v[34:35],v[34:35],v[212:213] op_sel_hi:[1,0]
v_pk_mul_f32 v[36:37],v[36:37],v[212:213] op_sel_hi:[1,0]
v_pk_mul_f32 v[38:39],v[38:39],v[212:213] op_sel_hi:[1,0]
v_pk_mul_f32 v[40:41],v[40:41],v[212:213] op_sel_hi:[1,0]
v_pk_mul_f32 v[42:43],v[42:43],v[212:213] op_sel_hi:[1,0]
v_pk_mul_f32 v[44:45],v[44:45],v[212:213] op_sel_hi:[1,0]
v_pk_mul_f32 v[46:47],v[46:47],v[212:213] op_sel_hi:[1,0]
v_pk_mul_f32 v[48:49],v[48:49],v[212:213] op_sel_hi:[1,0]
v_pk_mul_f32 v[50:51],v[50:51],v[212:213] op_sel_hi:[1,0]
v_pk_mul_f32 v[52:53],v[52:53],v[212:213] op_sel_hi:[1,0]
v_pk_mul_f32 v[54:55],v[54:55],v[212:213] op_sel_hi:[1,0]
v_pk_mul_f32 v[56:57],v[56:57],v[212:213] op_sel_hi:[1,0]
v_pk_mul_f32 v[58:59],v[58:59],v[212:213] op_sel_hi:[1,0]
v_pk_mul_f32 v[60:61],v[60:61],v[212:213] op_sel_hi:[1,0]
v_pk_mul_f32 v[62:63],v[62:63],v[212:213] op_sel_hi:[1,0]
v_mul_f32 v201,v201,v212
v_sub_f32 v144,v144,v214
v_sub_f32 v145,v145,v214
v_sub_f32 v146,v146,v214
v_sub_f32 v147,v147,v214
v_sub_f32 v148,v148,v214
v_sub_f32 v149,v149,v214
v_sub_f32 v150,v150,v214
v_sub_f32 v151,v151,v214
v_sub_f32 v152,v152,v214
v_sub_f32 v153,v153,v214
v_sub_f32 v154,v154,v214
v_sub_f32 v155,v155,v214
v_sub_f32 v156,v156,v214
v_sub_f32 v157,v157,v214
v_sub_f32 v158,v158,v214
v_sub_f32 v159,v159,v214
E_ v216,v144
E_ v217,v145
s_nop 0
v_add_f32 v205,v216,v217
C_ v168,v216,v217
E_ v218,v146
E_ v219,v147
s_nop 0
v_add_f32 v220,v218,v219
v_add_f32 v205,v205,v220
C_ v169,v218,v219
E_ v216,v148
E_ v217,v149
s_nop 0
v_add_f32 v220,v216,v217
v_add_f32 v205,v205,v220
C_ v170,v216,v217
E_ v218,v150
E_ v219,v151
s_nop 0
v_add_f32 v220,v218,v219
v_add_f32 v205,v205,v220
C_ v171,v218,v219
E_ v216,v152
E_ v217,v153
s_nop 0
v_add_f32 v220,v216,v217
v_add_f32 v205,v205,v220
C_ v172,v216,v217
E_ v218,v154
E_ v219,v155
s_nop 0
v_add_f32 v220,v218,v219
v_add_f32 v205,v205,v220
C_ v173,v218,v219
E_ v216,v156
E_ v217,v157
s_nop 0
v_add_f32 v220,v216,v217
v_add_f32 v205,v205,v220
C_ v174,v216,v217
E_ v218,v158
E_ v219,v159
s_nop 0
v_add_f32 v220,v218,v219
v_add_f32 v205,v205,v220
C_ v175,v218,v219
s_nop 1
s_setpc_b64 s[44:45]
LSLOW2_%=:
s_nop 7
v_max3_f32 v210,v128,v129,v130
v_max3_f32 v210,v210,v131,v132
v_max3_f32 v210,v210,v133,v134
v_max3_f32 v210,v210,v135,v136
v_max3_f32 v210,v210,v137,v138
v_max3_f32 v210,v210,v139,v140
v_max3_f32 v210,v210,v141,v142
v_max_f32 v210,v210,v143
v_mov_b32 v211,v210
s_nop 1
v_permlane32_swap_b32 v210,v211
v_max_f32 v210,v210,v211
v_mov_b32 v211,v210
s_nop 1
v_permlane16_swap_b32 v210,v211
v_max_f32 v210,v210,v211
v_max_f32 v214,0,v210
v_exp_f32 v212,-v214
v_sub_f32 v192,v192,v214
v_sub_f32 v193,v193,v214
v_sub_f32 v194,v194,v214
v_sub_f32 v195,v195,v214
v_pk_mul_f32 v[64:65],v[64:65],v[212:213] op_sel_hi:[1,0]
v_pk_mul_f32 v[66:67],v[66:67],v[212:213] op_sel_hi:[1,0]
v_pk_mul_f32 v[68:69],v[68:69],v[212:213] op_sel_hi:[1,0]
v_pk_mul_f32 v[70:71],v[70:71],v[212:213] op_sel_hi:[1,0]
v_pk_mul_f32 v[72:73],v[72:73],v[212:213] op_sel_hi:[1,0]
v_pk_mul_f32 v[74:75],v[74:75],v[212:213] op_sel_hi:[1,0]
v_pk_mul_f32 v[76:77],v[76:77],v[212:213] op_sel_hi:[1,0]
v_pk_mul_f32 v[78:79],v[78:79],v[212:213] op_sel_hi:[1,0]
v_pk_mul_f32 v[80:81],v[80:81],v[212:213] op_sel_hi:[1,0]
v_pk_mul_f32 v[82:83],v[82:83],v[212:213] op_sel_hi:[1,0]
v_pk_mul_f32 v[84:85],v[84:85],v[212:213] op_sel_hi:[1,0]
v_pk_mul_f32 v[86:87],v[86:87],v[212:213] op_sel_hi:[1,0]
v_pk_mul_f32 v[88:89],v[88:89],v[212:213] op_sel_hi:[1,0]
v_pk_mul_f32 v[90:91],v[90:91],v[212:213] op_sel_hi:[1,0]
v_pk_mul_f32 v[92:93],v[92:93],v[212:213] op_sel_hi:[1,0]
v_pk_mul_f32 v[94:95],v[94:95],v[212:213] op_sel_hi:[1,0]
v_mul_f32 v202,v202,v212
v_sub_f32 v128,v128,v214
v_sub_f32 v129,v129,v214
v_sub_f32 v130,v130,v214
v_sub_f32 v131,v131,v214
v_sub_f32 v132,v132,v214
v_sub_f32 v133,v133,v214
v_sub_f32 v134,v134,v214
v_sub_f32 v135,v135,v214
v_sub_f32 v136,v136,v214
v_sub_f32 v137,v137,v214
v_sub_f32 v138,v138,v214
v_sub_f32 v139,v139,v214
v_sub_f32 v140,v140,v214
v_sub_f32 v141,v141,v214
v_sub_f32 v142,v142,v214
v_sub_f32 v143,v143,v214
E_ v216,v128
E_ v217,v129
s_nop 0
v_add_f32 v204,v216,v217
C_ v160,v216,v217
E_ v218,v130
E_ v219,v131
s_nop 0
v_add_f32 v220,v218,v219
v_add_f32 v204,v204,v220
C_ v161,v218,v219
E_ v216,v132
E_ v217,v133
s_nop 0
v_add_f32 v220,v216,v217
v_add_f32 v204,v204,v220
C_ v162,v216,v217
E_ v218,v134
E_ v219,v135
s_nop 0
v_add_f32 v220,v218,v219
v_add_f32 v204,v204,v220
C_ v163,v218,v219
E_ v216,v136
E_ v217,v137
s_nop 0
v_add_f32 v220,v216,v217
v_add_f32 v204,v204,v220
C_ v164,v216,v217
E_ v218,v138
E_ v219,v139
s_nop 0
v_add_f32 v220,v218,v219
v_add_f32 v204,v204,v220
C_ v165,v218,v219
E_ v216,v140
E_ v217,v141
s_nop 0
v_add_f32 v220,v216,v217
v_add_f32 v204,v204,v220
C_ v166,v216,v217
E_ v218,v142
E_ v219,v143
s_nop 0
)A"
R"A(v_add_f32 v220,v218,v219
v_add_f32 v204,v204,v220
C_ v167,v218,v219
s_nop 1
s_setpc_b64 s[44:45]
LSLOW3_%=:
s_nop 7
v_max3_f32 v210,v144,v145,v146
v_max3_f32 v210,v210,v147,v148
v_max3_f32 v210,v210,v149,v150
v_max3_f32 v210,v210,v151,v152
v_max3_f32 v210,v210,v153,v154
v_max3_f32 v210,v210,v155,v156
v_max3_f32 v210,v210,v157,v158
v_max_f32 v210,v210,v159
v_mov_b32 v211,v210
s_nop 1
v_permlane32_swap_b32 v210,v211
v_max_f32 v210,v210,v211
v_mov_b32 v211,v210
s_nop 1
v_permlane16_swap_b32 v210,v211
v_max_f32 v210,v210,v211
v_max_f32 v214,0,v210
v_exp_f32 v212,-v214
v_sub_f32 v196,v196,v214
v_sub_f32 v197,v197,v214
v_sub_f32 v198,v198,v214
v_sub_f32 v199,v199,v214
v_pk_mul_f32 v[96:97],v[96:97],v[212:213] op_sel_hi:[1,0]
v_pk_mul_f32 v[98:99],v[98:99],v[212:213] op_sel_hi:[1,0]
v_pk_mul_f32 v[100:101],v[100:101],v[212:213] op_sel_hi:[1,0]
v_pk_mul_f32 v[102:103],v[102:103],v[212:213] op_sel_hi:[1,0]
v_pk_mul_f32 v[104:105],v[104:105],v[212:213] op_sel_hi:[1,0]
v_pk_mul_f32 v[106:107],v[106:107],v[212:213] op_sel_hi:[1,0]
v_pk_mul_f32 v[108:109],v[108:109],v[212:213] op_sel_hi:[1,0]
v_pk_mul_f32 v[110:111],v[110:111],v[212:213] op_sel_hi:[1,0]
v_pk_mul_f32 v[112:113],v[112:113],v[212:213] op_sel_hi:[1,0]
v_pk_mul_f32 v[114:115],v[114:115],v[212:213] op_sel_hi:[1,0]
v_pk_mul_f32 v[116:117],v[116:117],v[212:213] op_sel_hi:[1,0]
v_pk_mul_f32 v[118:119],v[118:119],v[212:213] op_sel_hi:[1,0]
v_pk_mul_f32 v[120:121],v[120:121],v[212:213] op_sel_hi:[1,0]
v_pk_mul_f32 v[122:123],v[122:123],v[212:213] op_sel_hi:[1,0]
v_pk_mul_f32 v[124:125],v[124:125],v[212:213] op_sel_hi:[1,0]
v_pk_mul_f32 v[126:127],v[126:127],v[212:213] op_sel_hi:[1,0]
v_mul_f32 v203,v203,v212
v_sub_f32 v144,v144,v214
v_sub_f32 v145,v145,v214
v_sub_f32 v146,v146,v214
v_sub_f32 v147,v147,v214
v_sub_f32 v148,v148,v214
v_sub_f32 v149,v149,v214
v_sub_f32 v150,v150,v214
v_sub_f32 v151,v151,v214
v_sub_f32 v152,v152,v214
v_sub_f32 v153,v153,v214
v_sub_f32 v154,v154,v214
v_sub_f32 v155,v155,v214
v_sub_f32 v156,v156,v214
v_sub_f32 v157,v157,v214
v_sub_f32 v158,v158,v214
v_sub_f32 v159,v159,v214
E_ v216,v144
E_ v217,v145
s_nop 0
v_add_f32 v205,v216,v217
C_ v168,v216,v217
E_ v218,v146
E_ v219,v147
s_nop 0
v_add_f32 v220,v218,v219
v_add_f32 v205,v205,v220
C_ v169,v218,v219
E_ v216,v148
E_ v217,v149
s_nop 0
v_add_f32 v220,v216,v217
v_add_f32 v205,v205,v220
C_ v170,v216,v217
E_ v218,v150
E_ v219,v151
s_nop 0
v_add_f32 v220,v218,v219
v_add_f32 v205,v205,v220
C_ v171,v218,v219
E_ v216,v152
E_ v217,v153
s_nop 0
v_add_f32 v220,v216,v217
v_add_f32 v205,v205,v220
C_ v172,v216,v217
E_ v218,v154
E_ v219,v155
s_nop 0
v_add_f32 v220,v218,v219
v_add_f32 v205,v205,v220
C_ v173,v218,v219
E_ v216,v156
E_ v217,v157
s_nop 0
v_add_f32 v220,v216,v217
v_add_f32 v205,v205,v220
C_ v174,v216,v217
E_ v218,v158
E_ v219,v159
s_nop 0
v_add_f32 v220,v218,v219
v_add_f32 v205,v205,v220
C_ v175,v218,v219
s_nop 1
s_setpc_b64 s[44:45]
LCODEEND_%=:
.purgem M_
.purgem C_
.purgem AR_
.purgem AW_
.purgem E_
.purgem R_
.purgem T_
)A"
    : "+{v0}"(tid), "+{s2}"(wg) : "{s[0:1]}"(kp), "s"(lb)
    : "v1","v2","v3","v4","v5","v6","v7","v8","v9","v10","v11","v12","v13","v14","v15","v16","v17","v18","v19","v20","v21","v22","v23","v24","v25","v26","v27","v28","v29","v30","v31","v32","v33","v34","v35","v36","v37","v38","v39","v40","v41","v42","v43","v44","v45","v46","v47","v48","v49","v50","v51","v52","v53","v54","v55","v56","v57","v58","v59","v60","v61","v62","v63","v64","v65","v66","v67","v68","v69","v70","v71","v72","v73","v74","v75","v76","v77","v78","v79","v80","v81","v82","v83","v84","v85","v86","v87","v88","v89","v90","v91","v92","v93","v94","v95","v96","v97","v98","v99","v100","v101","v102","v103","v104","v105","v106","v107","v108","v109","v110","v111","v112","v113","v114","v115","v116","v117","v118","v119","v120","v121","v122","v123","v124","v125","v126","v127","v128","v129","v130","v131","v132","v133","v134","v135","v136","v137","v138","v139","v140","v141","v142","v143","v144","v145","v146","v147","v148","v149","v150","v151","v152","v153","v154","v155","v156","v157","v158","v159","v160","v161","v162","v163","v164","v165","v166","v167","v168","v169","v170","v171","v172","v173","v174","v175","v176","v177","v178","v179","v180","v181","v182","v183","v184","v185","v186","v187","v188","v189","v190","v191","v192","v193","v194","v195","v196","v197","v198","v199","v200","v201","v202","v203","v204","v205","v206","v207","v208","v209","v210","v211","v212","v213","v214","v215","v216","v217","v218","v219","v220","v221","v222","v223","v224","v225","v226","v227","v228","v229","v230","v231","v232","v233","v234","v235","v236","v237","v238","v239","v240","v241","v242","v243","v244","v245","v246","v247","v248","v249","v250","v251","v252","v253","v254","v255","a0","a1","a2","a3","a4","a5","a6","a7","a8","a9","a10","a11","a12","a13","a14","a15","a16","a17","a18","a19","a20","a21","a22","a23","a24","a25","a26","a27","a28","a29","a30","a31","a32","a33","a34","a35","a36","a37","a38","a39","a40","a41","a42","a43","a44","a45","a46","a47","a48","a49","a50","a51","a52","a53","a54","a55","a56","a57","a58","a59","a60","a61","a62","a63","a64","a65","a66","a67","a68","a69","a70","a71","a72","a73","a74","a75","a76","a77","a78","a79","a80","a81","a82","a83","a84","a85","a86","a87","a88","a89","a90","a91","a92","a93","a94","a95","a96","a97","a98","a99","a100","a101","a102","a103","a104","a105","a106","a107","a108","a109","a110","a111","a112","a113","a114","a115","a116","a117","a118","a119","a120","a121","a122","a123","a124","a125","a126","a127","a128","a129","a130","a131","a132","a133","a134","a135","a136","a137","a138","a139","a140","a141","a142","a143","a144","a145","a146","a147","a148","a149","a150","a151","a152","a153","a154","a155","a156","a157","a158","a159","a160","a161","a162","a163","a164","a165","a166","a167","a168","a169","a170","a171","a172","a173","a174","a175","a176","a177","a178","a179","a180","a181","a182","a183","a184","a185","a186","a187","a188","a189","a190","a191","a192","a193","a194","a195","a196","a197","a198","a199","a200","a201","a202","a203","a204","a205","a206","a207","a208","a209","a210","a211","a212","a213","a214","a215","a216","a217","a218","a219","a220","a221","a222","a223","a224","a225","a226","a227","a228","a229","a230","a231","a232","a233","a234","a235","a236","a237","a238","a239","a240","a241","a242","a243","a244","a245","a246","a247","a248","a249","a250","a251","a252","a253","a254","a255","s3","s4","s5","s6","s7","s8","s9","s10","s11","s12","s13","s14","s15","s16","s17","s18","s19","s20","s21","s22","s23","s24","s25","s26","s27","s28","s29","s30","s31","s32","s33","s34","s35","s36","s37","s38","s39","s40","s41","s42","s43","s44","s45","s46","s47","s48","s49","s50","s51","s52","s53","s54","s55","s56","s57","s58","s59","s60","s61","s62","s63","s64","s65","s66","s67","s68","s69","s70","s71","s72","s73","s74","s75","s76","s77","s78","s79","s80","s81","s82","s83","s84","s85","s86","s87","s88","s89","s90","s91","s92","s93","s94","s95","vcc","memory");
}

extern "C" void kernel_launch(void* const* d_in, const int* in_sizes, int n_in, void* d_out, int out_size, void* d_ws, size_t ws_size, hipStream_t stream){
  k_all<<<256,256,0,stream>>>(d_in[0],d_in[1],d_in[2],d_in[3],d_in[4],d_in[5],d_in[6],d_in[7],d_in[8],d_in[9],d_out,d_ws);
}
```
